```python
import torch

def _softmax(x, axis=-1):
    return torch.softmax(x, dim=axis)

def _gelu(x):
    return torch.nn.functional.gelu(x, approximate='tanh')

def _silu(x):
    return torch.nn.functional.silu(x)

def _sigmoid(x):
    return torch.sigmoid(x)

def _layernorm(x, axis=-1, eps=1e-05):
    m = x.mean(dim=axis, keepdim=True)
    v = x.var(dim=axis, keepdim=True, unbiased=False)
    return (x - m) / torch.sqrt(v + eps)

def _scatter_add(tgt, idx, src, axis=0):
    return tgt.clone().index_add_(axis, idx, src)

def _segment_sum(src, seg, n):
    out = torch.zeros((n,) + src.shape[1:], dtype=src.dtype)
    return out.index_add_(0, seg, src)

def _pad(x, pw):
    flat = tuple((v for pair in reversed(pw) for v in pair))
    return torch.nn.functional.pad(x, flat)
kernel_name = 'adaLN_cond__compressed_kv_cla__moe_arch_mod__prompt'

def setup_inputs():

    _w = lambda a: (lambda t: t.to(torch.bfloat16) if t.is_floating_point() else t)(torch.as_tensor(a))

    def _A_setup():
        B, H, Sq, Sk, D, M = (1, 32, 2048, 2048, 128, 640)
        Dc = 4 * D
        return {'x': _w((lambda *s: torch.randn(*s, dtype=torch.bfloat16) * 0.1)(B, Sq, M)), 'c_kv': _w((lambda *s: torch.randn(*s, dtype=torch.bfloat16) * 1.6)(B, Sk, Dc)), 'w_q': _w((lambda *s: torch.randn(*s, dtype=torch.bfloat16) * 0.8)(M, H, D) * M ** (-0.5)), 'w_uk': _w((lambda *s: torch.randn(*s, dtype=torch.bfloat16) * 0.8)(Dc, H, D) * Dc ** (-0.5)), 'w_uv': _w((lambda *s: torch.randn(*s, dtype=torch.bfloat16) * 1.6)(Dc, H, D) * Dc ** (-0.5)), 'w_o': _w((lambda *s: torch.randn(*s, dtype=torch.bfloat16) * 1.6)(H, D, M) * (H * D) ** (-0.5))}
    _A_in = _A_setup()

    def _B_setup():
        B, S, D, F, C = (1, 2048, 640, 1728, 8)
        return {'x': _w((lambda *s: torch.randn(*s, dtype=torch.bfloat16) * 0.1)(B, S, D)), 'w_r': _w((lambda *s: torch.randn(*s, dtype=torch.bfloat16) * 0.1)(D)), 'w_gate': _w((lambda *s: torch.randn(*s, dtype=torch.bfloat16) * 0.1)(D, F)), 'w_up': _w((lambda *s: torch.randn(*s, dtype=torch.bfloat16) * 0.1)(D, F)), 'w_down': _w((lambda *s: torch.randn(*s, dtype=torch.bfloat16) * 0.1)(F, D))}
    _B_in = _B_setup()
    return {'A_x': _A_in['x'], 'A_c_kv': _A_in['c_kv'], 'A_w_q': _A_in['w_q'], 'A_w_uk': _A_in['w_uk'], 'A_w_uv': _A_in['w_uv'], 'A_w_o': _A_in['w_o'], 'B_x': _B_in['x'], 'B_w_r': _B_in['w_r'], 'B_w_gate': _B_in['w_gate'], 'B_w_up': _B_in['w_up'], 'B_w_down': _B_in['w_down'], 'c': _w((lambda *s: torch.randn(*s, dtype=torch.bfloat16) * 0.2)(1, 640) * 8.0), 'w_mod': _w((lambda *s: torch.randn(*s, dtype=torch.bfloat16) * 0.2)(640, 6 * 640) * 640 ** (-0.5) * 8.0)}

def reference(A_x, A_c_kv, A_w_q, A_w_uk, A_w_uv, A_w_o, B_x, B_w_r, B_w_gate, B_w_up, B_w_down, c, w_mod):

    def _A_ref(x, c_kv, w_q, w_uk, w_uv, w_o):
        B, Sq, M = (x.shape[0], x.shape[1], x.shape[2])
        Sk = c_kv.shape[1]
        H, D = (w_q.shape[1], w_q.shape[2])
        q = torch.einsum('bqm,mhd->bhqd', x, w_q)
        k = torch.einsum('bkc,chd->bhkd', c_kv, w_uk)
        v = torch.einsum('bkc,chd->bhkd', c_kv, w_uv)
        scores = torch.einsum('bhqd,bhkd->bhqk', q, k) * D ** (-0.5)
        off = Sk - Sq
        qi = torch.arange(Sq)[:, None]
        kj = torch.arange(Sk)[None, :]
        cmask = kj <= qi + off
        scores = torch.where(cmask[None, None], scores, -1000000000.0)
        p = _softmax(scores, axis=-1)
        ctx = torch.einsum('bhqk,bhkd->bhqd', p, v)
        return torch.einsum('bhqd,hdm->bqm', ctx, w_o)

    def _B_ref(x, w_r, w_gate, w_up, w_down):
        B, S, D = x.shape
        C = 8
        r = x @ w_r
        idx = torch.argsort(-r, axis=-1)[:, :C]
        idx_e = (lambda x, n, axis: torch.repeat_interleave(x, n, dim=axis))(idx.reshape(B, C, 1), D, axis=2)
        x_sel = (lambda x, i, axis: torch.take_along_dim(x, i, dim=axis))(x, idx_e, axis=1)
        g = _silu(x_sel @ w_gate)
        u = x_sel @ w_up
        y = g * u @ w_down
        r_sel = _sigmoid((lambda x, i, axis: torch.take_along_dim(x, i, dim=axis))(r, idx, axis=1))
        y = y * r_sel.reshape(B, C, 1)
        b_off = (torch.arange(B) * S).reshape(B, 1)
        flat_idx = (idx + b_off).reshape(B * C)
        zero = (x * 0.0).reshape(B * S, D)
        upd = _scatter_add(zero, flat_idx, y.reshape(B * C, D), axis=0)
        return x + upd.reshape(B, S, D)
    x = A_x
    mod = (_silu(c) @ w_mod).reshape(c.shape[0], 6, 1, -1)
    sa, ca, ga, sb, cb, gb = (mod[:, 0].clone(), mod[:, 1].clone(), mod[:, 2].clone(), mod[:, 3].clone(), mod[:, 4].clone(), mod[:, 5].clone())
    h = _layernorm(x, axis=-1) * (1 + ca) + sa
    x = x + ga * _A_ref(h, A_c_kv, A_w_q, A_w_uk, A_w_uv, A_w_o)
    h = _layernorm(x, axis=-1) * (1 + cb) + sb
    x = x + gb * _B_ref(h, B_w_r, B_w_gate, B_w_up, B_w_down)
    return x
```

```cpp
#include <hip/hip_runtime.h>
#include <hip/hip_bf16.h>
#include <stdint.h>
typedef unsigned short u16;
__device__ __forceinline__ float bf2f(u16 v){ return __uint_as_float(((unsigned)v)<<16); }
__device__ __forceinline__ u16 f2bf(float f){ unsigned u=__float_as_uint(f); u += 0x7FFFu + ((u>>16)&1u); return (u16)(u>>16); }
__device__ __forceinline__ float rbf(float f){ return bf2f(f2bf(f)); }
constexpr int S=2048, M=640, H=32, D=128, DC=512, HD=4096, F=1728;

__global__ void k_mod(const u16* c, const u16* wmod, float* mod){
  int n = blockIdx.x*blockDim.x+threadIdx.x; if(n>=3840) return;
  float acc=0; for(int k=0;k<640;k++){ float cv=bf2f(c[k]); float s=rbf(cv/(1.f+__expf(-cv))); acc += s*bf2f(wmod[k*3840+n]); }
  mod[n]=rbf(acc);
}
__device__ float wsum(float v){ for(int o=32;o>=1;o>>=1) v+=__shfl_xor(v,o); return v; }
__global__ void k_ln(const u16* x, const float* mod, int so, int co, u16* h){
  int row=blockIdx.x, l=threadIdx.x; float v[10]; float s=0;
  for(int i=0;i<10;i++){ v[i]=bf2f(x[row*M+l+64*i]); s+=v[i]; }
  float mean=wsum(s)/M; float q=0; for(int i=0;i<10;i++){ float d=v[i]-mean; q+=d*d; }
  float var=wsum(q)/M; float mb=rbf(mean); float den=rbf(sqrtf(rbf(rbf(var)+1e-5f)));
  for(int i=0;i<10;i++){ int k=l+64*i; float t=rbf(rbf(v[i]-mb)/den); float g=rbf(1.f+mod[co+k]); h[row*M+k]=f2bf(rbf(t*g)+mod[so+k]); }
}
__global__ void k_gemm(const u16* A, const u16* B, u16* C, int Mm, int N, int K){
  __shared__ float As[16][17], Bs[16][17];
  int tx=threadIdx.x, ty=threadIdx.y; int m=blockIdx.y*16+ty, n=blockIdx.x*16+tx; float acc=0;
  for(int k0=0;k0<K;k0+=16){ As[ty][tx]=bf2f(A[(size_t)m*K+k0+tx]); Bs[ty][tx]=bf2f(B[(size_t)(k0+ty)*N+n]); __syncthreads();
    for(int k=0;k<16;k++) acc+=As[ty][k]*Bs[k][tx]; __syncthreads(); }
  C[(size_t)m*N+n]=f2bf(acc);
}
__global__ void k_attn(const u16* q, const u16* k, const u16* v, u16* ctx){
  __shared__ float p[S]; __shared__ float qs[D]; __shared__ float red[2];
  int row=blockIdx.x, h=blockIdx.y, t=threadIdx.x;
  qs[t]=bf2f(q[(size_t)row*HD+h*D+t]); __syncthreads();
  float mx=-1e30f;
  for(int j=t;j<=row;j+=128){ float s=0; for(int d=0;d<D;d++) s+=qs[d]*bf2f(k[(size_t)j*HD+h*D+d]); s*=0.08838834764831845f; p[j]=s; mx=fmaxf(mx,s);}
  for(int o=32;o>=1;o>>=1) mx=fmaxf(mx,__shfl_xor(mx,o)); if((t&63)==0) red[t>>6]=mx; __syncthreads(); mx=fmaxf(red[0],red[1]); __syncthreads();
  float l=0; for(int j=t;j<=row;j+=128){ float e=__expf(p[j]-mx); p[j]=e; l+=e; }
  l=wsum(l); if((t&63)==0) red[t>>6]=l; __syncthreads(); l=red[0]+red[1];
  float acc=0; for(int j=0;j<=row;j++) acc+=p[j]*bf2f(v[(size_t)j*HD+h*D+t]);
  ctx[(size_t)row*HD+h*D+t]=f2bf(acc/l);
}
__global__ void k_res(const u16* x, const u16* a, const float* mod, u16* x1){
  int i=blockIdx.x*blockDim.x+threadIdx.x; int k=i%M; x1[i]=f2bf(bf2f(x[i])+rbf(mod[2*M+k]*bf2f(a[i])));
}
__global__ void k_r(const u16* h2, const u16* wr, float* r){
  int row=blockIdx.x,l=threadIdx.x; float s=0; for(int i=0;i<10;i++) s+=bf2f(h2[row*M+l+64*i])*bf2f(wr[l+64*i]); s=wsum(s); if(l==0) r[row]=rbf(s);
}
__global__ void k_topk(const float* r, int* idx){
  if(threadIdx.x==0){ for(int c=0;c<8;c++){ int best=-1; float bv=-INFINITY; for(int i=0;i<S;i++){ bool used=false; for(int j=0;j<c;j++) used|=(idx[j]==i); if(!used && (best<0 || r[i]>bv)){bv=r[i];best=i;} } idx[c]=best; } }
}
__global__ void k_gu(const u16* h2, const int* idx, const u16* wg, const u16* wu, float* act){
  int n=blockIdx.x*blockDim.x+threadIdx.x; int c=blockIdx.y; if(n>=F) return; int row=idx[c]; float g=0,u=0;
  for(int k=0;k<M;k++){ float xv=bf2f(h2[row*M+k]); g+=xv*bf2f(wg[k*F+n]); u+=xv*bf2f(wu[k*F+n]); }
  g=rbf(g); u=rbf(u); g=rbf(g/(1.f+__expf(-g))); act[c*F+n]=rbf(g*u);
}
__global__ void k_out(const u16* x1, const u16* h2, const float* mod, u16* out){
  int i=blockIdx.x*blockDim.x+threadIdx.x; int k=i%M; out[i]=f2bf(bf2f(x1[i])+rbf(mod[5*M+k]*bf2f(h2[i])));
}
__global__ void k_down(const float* act, const int* idx, const u16* wd, const float* r, const u16* x1, const u16* h2, const float* mod, u16* out){
  int n=blockIdx.x*blockDim.x+threadIdx.x; int c=blockIdx.y; if(n>=M) return; int row=idx[c]; float y=0;
  for(int k=0;k<F;k++) y+=act[c*F+k]*bf2f(wd[k*M+n]);
  y=rbf(y); float rs=rbf(1.f/(1.f+__expf(-r[row]))); y=rbf(y*rs);
  float b=rbf(bf2f(h2[row*M+n])+y); out[row*M+n]=f2bf(bf2f(x1[row*M+n])+rbf(mod[5*M+n]*b));
}
extern "C" void kernel_launch(void* const* d_in, const int* in_sizes, int n_in, void* d_out, int out_size, void* d_ws, size_t ws_size, hipStream_t st){
  const u16* x=(const u16*)d_in[0]; const u16* ckv=(const u16*)d_in[1]; const u16* wq=(const u16*)d_in[2]; const u16* wuk=(const u16*)d_in[3]; const u16* wuv=(const u16*)d_in[4]; const u16* wo=(const u16*)d_in[5];
  const u16* wr=(const u16*)d_in[7]; const u16* wg=(const u16*)d_in[8]; const u16* wu=(const u16*)d_in[9]; const u16* wd=(const u16*)d_in[10]; const u16* c=(const u16*)d_in[11]; const u16* wmod=(const u16*)d_in[12];
  char* ws=(char*)d_ws; size_t o=0; auto al=[&](size_t b){ char* p=ws+o; o+=(b+255)/256*256; return p; };
  float* mod=(float*)al(3840*4); u16* h=(u16*)al(S*M*2); u16* q=(u16*)al((size_t)S*HD*2); u16* k=(u16*)al((size_t)S*HD*2); u16* v=(u16*)al((size_t)S*HD*2); u16* ctx=(u16*)al((size_t)S*HD*2);
  u16* a=(u16*)al(S*M*2); u16* x1=(u16*)al(S*M*2); u16* h2=(u16*)al(S*M*2); float* r=(float*)al(S*4); int* idx=(int*)al(64); float* act=(float*)al(8*F*4);
  k_mod<<<15,256,0,st>>>(c,wmod,mod);
  k_ln<<<S,64,0,st>>>(x,mod,0,M,h);
  k_gemm<<<dim3(HD/16,S/16),dim3(16,16),0,st>>>(h,wq,q,S,HD,M);
  k_gemm<<<dim3(HD/16,S/16),dim3(16,16),0,st>>>(ckv,wuk,k,S,HD,DC);
  k_gemm<<<dim3(HD/16,S/16),dim3(16,16),0,st>>>(ckv,wuv,v,S,HD,DC);
  k_attn<<<dim3(S,H),128,0,st>>>(q,k,v,ctx);
  k_gemm<<<dim3(M/16,S/16),dim3(16,16),0,st>>>(ctx,wo,a,S,M,HD);
  k_res<<<S*M/256,256,0,st>>>(x,a,mod,x1);
  k_ln<<<S,64,0,st>>>(x1,mod,3*M,4*M,h2);
  k_r<<<S,64,0,st>>>(h2,wr,r);
  k_topk<<<1,64,0,st>>>(r,idx);
  k_gu<<<dim3((F+63)/64,8),64,0,st>>>(h2,idx,wg,wu,act);
  k_out<<<S*M/256,256,0,st>>>(x1,h2,mod,(u16*)d_out);
  k_down<<<dim3(M/64,8),64,0,st>>>(act,idx,wd,r,x1,h2,mod,(u16*)d_out);
}
```

```cpp
#include <hip/hip_runtime.h>
#include <stdint.h>

typedef unsigned short u16;
typedef short bf16x8 __attribute__((ext_vector_type(8)));
typedef short s16x4 __attribute__((ext_vector_type(4)));
typedef float f32x2 __attribute__((ext_vector_type(2)));
typedef float f32x4 __attribute__((ext_vector_type(4)));
typedef float f32x16 __attribute__((ext_vector_type(16)));
typedef unsigned u32x4 __attribute__((ext_vector_type(4)));
typedef unsigned u32x2 __attribute__((ext_vector_type(2)));
typedef __bf16 bf16x2_t __attribute__((ext_vector_type(2)));
#define LAS __attribute__((address_space(3)))
#define DI __device__ __forceinline__
typedef LAS char* lptr;
typedef LAS const char* lcptr;

constexpr int S = 2048, M = 640, H = 32, D = 128, DC = 512, HD = 4096, F = 1728;
constexpr size_t WS_KF = 0;
constexpr size_t WS_VF = WS_KF + (size_t)S * HD * 2;
constexpr size_t WS_CTX = WS_VF + (size_t)S * HD * 2;
constexpr size_t WS_XH = WS_CTX + (size_t)S * HD * 2;
constexpr size_t WS_MOD = WS_XH + (size_t)S * M * 2;
constexpr size_t WS_MODB = WS_MOD + 15360;
constexpr size_t WS_RST = WS_MOD + 24576;
constexpr size_t WS_PART = WS_MOD + 32768;
constexpr size_t WS_H2 = WS_PART + (size_t)4 * S * M * 2;
constexpr size_t WS_XN = WS_H2 + (size_t)S * M * 2;
constexpr size_t WS_WT = WS_XN + (size_t)S * M * 2;
constexpr size_t WS_ACT = WS_WT + (size_t)3 * M * F * 2;
constexpr size_t WS_KEY = WS_ACT + 8 * F * 2;
constexpr size_t WS_CNT = WS_KEY + S * 4;
constexpr size_t WS_END = WS_CNT + 65536;

static_assert(WS_ACT % 256 == 0 && WS_KEY % 256 == 0 && WS_CNT % 256 == 0 && WS_END < ((size_t)80 << 20), "ws map");
DI float bf2f(u16 v) { return __uint_as_float(((unsigned)v) << 16); }
DI float bflo(unsigned w) { return __uint_as_float(w << 16); }
DI float bfhi(unsigned w) { return __uint_as_float(w & 0xffff0000u); }
DI unsigned cvtpk(float lo, float hi) { f32x2 v = {lo, hi}; bf16x2_t b = __builtin_convertvector(v, bf16x2_t); return __builtin_bit_cast(unsigned, b); }
DI float rbf(float f) { return bflo(cvtpk(f, f)); }
DI u16 f2bf(float f) { return (u16)(cvtpk(f, f) & 0xffffu); }
template <int CTRL> DI float dppf(float v) { return __int_as_float(__builtin_amdgcn_update_dpp(0, __float_as_int(v), CTRL, 0xf, 0xf, true)); }
DI float sum_rows(float v) {
  { auto r = __builtin_amdgcn_permlane16_swap(__float_as_uint(v), __float_as_uint(v), false, false); v = __uint_as_float(r[0]) + __uint_as_float(r[1]); }
  { auto r = __builtin_amdgcn_permlane32_swap(__float_as_uint(v), __float_as_uint(v), false, false); v = __uint_as_float(r[0]) + __uint_as_float(r[1]); }
  return v;
}
DI float wave_sum_par(float v) {
  v += dppf<0x4E>(v); v += dppf<0x124>(v); v += dppf<0x128>(v); return sum_rows(v);
}
DI float wave_sum(float v) { v += dppf<0xB1>(v); return wave_sum_par(v); }
#define SBAR() __builtin_amdgcn_sched_barrier(0)
#define MFMA16(a, b, c) __builtin_amdgcn_mfma_f32_16x16x32_bf16(a, b, c, 0, 0, 0)
typedef short v4i16_t __attribute__((ext_vector_type(4)));
DI s16x4 ldtr(lcptr p) { return __builtin_bit_cast(s16x4, __builtin_amdgcn_ds_read_tr16_b64_v4i16((LAS v4i16_t*)p)); }
DI bf16x8 cat8(s16x4 lo, s16x4 hi) { return __builtin_shufflevector(lo, hi, 0, 1, 2, 3, 4, 5, 6, 7); }

DI void unpack8(const u32x4 w, float* v) {
#pragma unroll
  for (int e = 0; e < 4; ++e) { v[2 * e] = bflo(w[e]); v[2 * e + 1] = bfhi(w[e]); }
}
__global__ __launch_bounds__(512) void k0_pre(const u16* __restrict__ x, const u16* __restrict__ cvec, const u16* __restrict__ wmod, char* __restrict__ ws, const u16* __restrict__ wr) {
  __shared__ __attribute__((aligned(16))) float pmod[16 * 260];
  __shared__ float pst[16];
  const int tid = threadIdx.x, wid = __builtin_amdgcn_readfirstlane(tid >> 6), lane = tid & 63, b = blockIdx.x;
  const int mcg = b < 240 ? (b & 7) * 30 + (b >> 3) : 0;
  u32x4 wm[3]; u16 cvr[3];
  {
    const int kr = tid >> 1, hf = tid & 1;
#pragma unroll
    for (int i = 0; i < 3; ++i) { int k = kr + 256 * i; if (k >= 640) k = 0;

      wm[i] = *(const u32x4*)(wmod + (size_t)k * 3840 + mcg * 16 + hf * 8);
      cvr[i] = cvec[k]; }
  }
  const u16 wrv = wr[(mcg % 40) * 16 + (tid >> 5)];
  const int l40 = lane < 40 ? lane : 0; const u16* xp = x + (size_t)(b * 8 + wid) * M + l40 * 16;
  const u32x4 xr0 = *(const u32x4*)xp, xr1 = *(const u32x4*)(xp + 8);
  unsigned wa1 = 0;
  if (wid == 7) {
    unsigned long long pc;
    asm volatile("s_getpc_b64 s[40:41]\n\ts_add_u32 s40, s40, _Z7k2_attnPKtS0_S0_S0_PcS0_S0_S0_S0_@rel32@lo+4\n\ts_addc_u32 s41, s41, _Z7k2_attnPKtS0_S0_S0_PcS0_S0_S0_S0_@rel32@hi+12\n\ts_mov_b64 %0, s[40:41]" : "=s"(pc) :: "scc", "s40", "s41");
    wa1 = *(const unsigned*)((const char*)pc + (size_t)((b >> 3) * 41 + (lane < 41 ? lane : 0)) * 64);
  }
  if (tid == 0) ((unsigned*)(ws + WS_CNT))[b * 64] = 0u;
  if (tid < 8) ((unsigned*)(ws + WS_KEY))[b * 8 + tid] = 0u;
  if (tid >= 64 && tid < 64 + 27) ((unsigned*)(ws + WS_ACT))[b * 27 + tid - 64] = 0xffffffffu;
  {
    float v[16]; unpack8(xr0, v); unpack8(xr1, v + 8);
    float s = 0.f;
#pragma unroll
    for (int e = 0; e < 16; ++e) s += v[e];
    if (lane >= 40) s = 0.f;
    const float mean = wave_sum(s) * (1.f / M);
    float q = 0.f;
#pragma unroll
    for (int e = 0; e < 16; ++e) { const float dd = v[e] - mean; q += dd * dd; }
    if (lane >= 40) q = 0.f;
    const float var = wave_sum(q) * (1.f / M);
    const float mb = rbf(mean), rden = 1.f / rbf(sqrtf(rbf(rbf(var) + 1e-5f)));
    u32x4 o0, o1;
#pragma unroll
    for (int e = 0; e < 4; ++e) {
      const unsigned r0 = cvtpk(v[2 * e] - mb, v[2 * e + 1] - mb), r1 = cvtpk(v[8 + 2 * e] - mb, v[9 + 2 * e] - mb);
      o0[e] = cvtpk(bflo(r0) * rden, bfhi(r0) * rden); o1[e] = cvtpk(bflo(r1) * rden, bfhi(r1) * rden);
    }
    const int row = b * 8 + wid;
    if (lane < 40) {
      char* dst = ws + WS_XN + (size_t)(row >> 5) * 40960 + lane * 1024 + (row & 31) * 16;
      *(u32x4*)dst = o0; *(u32x4*)(dst + 512) = o1;
    }
  }
  {
    float a8[8];
#pragma unroll
    for (int e = 0; e < 8; ++e) a8[e] = 0.f;
#pragma unroll
    for (int i = 0; i < 3; ++i) {
      const float cv = bf2f(cvr[i]); float s = rbf(cv * __builtin_amdgcn_rcpf(1.f + __expf(-cv))); if ((tid >> 1) + 256 * i >= 640) s = 0.f;
#pragma unroll
      for (int e = 0; e < 4; ++e) { a8[2 * e] += s * bflo(wm[i][e]); a8[2 * e + 1] += s * bfhi(wm[i][e]); }
    }
#pragma unroll
    for (int e = 0; e < 8; ++e) pmod[((tid & 1) * 8 + e) * 260 + (tid >> 1)] = a8[e];
  }
  __syncthreads();
  float msum;
  { const f32x4 p0 = *(const f32x4*)(pmod + (tid >> 5) * 260 + (tid & 31) * 8), p1 = *(const f32x4*)(pmod + (tid >> 5) * 260 + (tid & 31) * 8 + 4);
    float t = ((p0[0] + p0[1]) + (p0[2] + p0[3])) + ((p1[0] + p1[1]) + (p1[2] + p1[3]));
    t += dppf<0xB1>(t); t += dppf<0x4E>(t); t += dppf<0x124>(t); t += dppf<0x128>(t);
    auto r = __builtin_amdgcn_permlane16_swap(__float_as_uint(t), __float_as_uint(t), false, false); msum = __uint_as_float(r[0]) + __uint_as_float(r[1]); }
  if (b < 240 && (tid & 31) == 0) {
    float s = msum;
    const int col = mcg * 16 + (tid >> 5), sec = mcg / 40; s = rbf(s); if (sec == 1 || sec == 4) s = rbf(1.f + s);
    ((u16*)(ws + WS_MODB))[col] = f2bf(s);
    const float t = s * bf2f(wrv); pst[tid >> 5] = sec == 3 ? t : t * t;
  }
  if (b < 240 && (mcg / 40 == 3 || mcg / 40 == 4)) {
    __syncthreads();
    if (tid == 0) { float t = 0.f;
#pragma unroll
      for (int i = 0; i < 16; ++i) t += pst[i];
      ((float*)(ws + WS_RST))[(mcg / 40 - 3) * 64 + mcg % 40] = t; }
  }
  asm volatile("" :: "v"(wa1));
}

constexpr int K2_LDS = 163840;
constexpr float QSCALE = 0.08838834764831845f * 1.4426950408889634f;
template <int LOFF, int GOFF> DI void dma1(const char* sbase, unsigned voff, unsigned lbase) {
  asm volatile("s_add_u32 m0, %2, %3\n\ts_nop 0\n\tglobal_load_lds_dwordx4 %0, %1 offset:%4" :: "v"(voff), "s"(sbase), "s"(lbase), "i"(LOFF), "i"(GOFF) : "memory", "scc");
}
typedef unsigned u32x16 __attribute__((ext_vector_type(16)));
#define ATTN_ASM \
  "s_mov_b32 s69, 0\n\t" \
  "s_mov_b32 s68, 1\n\t" \
  "s_mov_b32 s71, 0\n\t" \
  "v_readlane_b32 s53, %[prm], 10\n\t" \
  "v_readlane_b32 s48, %[prm], 4\n\t" \
  "v_readlane_b32 s49, %[prm], 5\n\t" \
  "v_readlane_b32 s50, %[prm], 7\n\t" \
  "v_readlane_b32 s64, %[prm], 0\n\t" \
  "v_readlane_b32 s65, %[prm], 1\n\t" \
  "v_readlane_b32 s66, %[prm], 2\n\t" \
  "v_readlane_b32 s67, %[prm], 3\n\t" \
  "v_readlane_b32 s80, %[prm], 6\n\t" \
  "v_readlane_b32 s51, %[prm], 8\n\t" \
  "v_readlane_b32 s52, %[prm], 9\n\t" \
  "v_readlane_b32 s70, %[prm], 11\n\t" \
  "s_mov_b32 s54, 0\n\t" \
  "v_mov_b32_e32 v248, 0xff800000\n\t" \
  "v_mov_b32_e32 v241, 0\n\t" \
  "v_mov_b32_e32 v243, 0\n\t" \
  "v_mov_b32_e32 v249, %[lin]\n\t" \
  "v_add_u32_e32 v250, 0x10000, %[lin]\n\t" \
  "v_add_u32_e32 v252, 0x1000, %[lin]\n\t" \
  "s_cmp_lg_u32 s53, 0\n\t" \
  "s_cbranch_scc1 LB_PRO%=\n\t" \
  "LA_PRO%=:\n\t" \
  "ds_read_b128 v[224:227], v249 offset:0\n\t" \
  "ds_read_b128 v[228:231], v249 offset:1024\n\t" \
  "ds_read_b128 v[232:235], v249 offset:2048\n\t" \
  "ds_read_b128 v[236:239], v249 offset:3072\n\t" \
  "ds_read_b128 v[208:211], v249 offset:4096\n\t" \
  "ds_read_b128 v[212:215], v249 offset:5120\n\t" \
  "ds_read_b128 v[216:219], v249 offset:6144\n\t" \
  "ds_read_b128 v[220:223], v249 offset:7168\n\t" \
  "s_waitcnt lgkmcnt(7)\n\t" \
  "v_mfma_f32_32x32x16_bf16 v[112:127], v[224:227], v[16:19], 0\n\t" \
  "ds_read_b128 v[224:227], v249 offset:8192\n\t" \
  "v_mov_b32_e32 v48, 0\n\t" \
  "v_mov_b32_e32 v49, 0\n\t" \
  "v_mov_b32_e32 v50, 0\n\t" \
  "v_mov_b32_e32 v51, 0\n\t" \
  "s_waitcnt lgkmcnt(7)\n\t" \
  "v_mfma_f32_32x32x16_bf16 v[112:127], v[228:231], v[20:23], v[112:127]\n\t" \
  "ds_read_b128 v[228:231], v249 offset:9216\n\t" \
  "v_mov_b32_e32 v52, 0\n\t" \
  "v_mov_b32_e32 v53, 0\n\t" \
  "v_mov_b32_e32 v54, 0\n\t" \
  "v_mov_b32_e32 v55, 0\n\t" \
  "s_waitcnt lgkmcnt(7)\n\t" \
  "v_mfma_f32_32x32x16_bf16 v[112:127], v[232:235], v[24:27], v[112:127]\n\t" \
  "ds_read_b128 v[232:235], v249 offset:10240\n\t" \
  "v_mov_b32_e32 v56, 0\n\t" \
  "v_mov_b32_e32 v57, 0\n\t" \
  "v_mov_b32_e32 v58, 0\n\t" \
  "v_mov_b32_e32 v59, 0\n\t" \
  "s_waitcnt lgkmcnt(7)\n\t" \
  "v_mfma_f32_32x32x16_bf16 v[112:127], v[236:239], v[28:31], v[112:127]\n\t" \
  "ds_read_b128 v[236:239], v249 offset:11264\n\t" \
  "v_mov_b32_e32 v60, 0\n\t" \
  "v_mov_b32_e32 v61, 0\n\t" \
  "v_mov_b32_e32 v62, 0\n\t" \
  "v_mov_b32_e32 v63, 0\n\t" \
  "s_waitcnt lgkmcnt(7)\n\t" \
  "v_mfma_f32_32x32x16_bf16 v[112:127], v[208:211], v[32:35], v[112:127]\n\t" \
  "ds_read_b128 v[208:211], v249 offset:12288\n\t" \
  "v_mov_b32_e32 v64, 0\n\t" \
  "v_mov_b32_e32 v65, 0\n\t" \
  "v_mov_b32_e32 v66, 0\n\t" \
  "v_mov_b32_e32 v67, 0\n\t" \
  "s_waitcnt lgkmcnt(7)\n\t" \
  "v_mfma_f32_32x32x16_bf16 v[112:127], v[212:215], v[36:39], v[112:127]\n\t" \
  "ds_read_b128 v[212:215], v249 offset:13312\n\t" \
  "v_mov_b32_e32 v68, 0\n\t" \
  "v_mov_b32_e32 v69, 0\n\t" \
  "v_mov_b32_e32 v70, 0\n\t" \
  "v_mov_b32_e32 v71, 0\n\t" \
  "s_waitcnt lgkmcnt(7)\n\t" \
  "v_mfma_f32_32x32x16_bf16 v[112:127], v[216:219], v[40:43], v[112:127]\n\t" \
  "ds_read_b128 v[216:219], v249 offset:14336\n\t" \
  "v_mov_b32_e32 v72, 0\n\t" \
  "v_mov_b32_e32 v73, 0\n\t" \
  "v_mov_b32_e32 v74, 0\n\t" \
  "v_mov_b32_e32 v75, 0\n\t" \
  "s_waitcnt lgkmcnt(7)\n\t" \
  "v_mfma_f32_32x32x16_bf16 v[112:127], v[220:223], v[44:47], v[112:127]\n\t" \
  "ds_read_b128 v[220:223], v249 offset:15360\n\t" \
  "v_mov_b32_e32 v76, 0\n\t" \
  "v_mov_b32_e32 v77, 0\n\t" \
  "v_mov_b32_e32 v78, 0\n\t" \
  "v_mov_b32_e32 v79, 0\n\t" \
  "s_waitcnt lgkmcnt(7)\n\t" \
  "v_mfma_f32_32x32x16_bf16 v[128:143], v[224:227], v[16:19], 0\n\t" \
  "v_mov_b32_e32 v80, 0\n\t" \
  "v_mov_b32_e32 v81, 0\n\t" \
  "v_mov_b32_e32 v82, 0\n\t" \
  "v_mov_b32_e32 v83, 0\n\t" \
  "s_waitcnt lgkmcnt(6)\n\t" \
  "v_mfma_f32_32x32x16_bf16 v[128:143], v[228:231], v[20:23], v[128:143]\n\t" \
  "v_mov_b32_e32 v84, 0\n\t" \
  "v_mov_b32_e32 v85, 0\n\t" \
  "v_mov_b32_e32 v86, 0\n\t" \
  "v_mov_b32_e32 v87, 0\n\t" \
  "s_waitcnt lgkmcnt(5)\n\t" \
  "v_mfma_f32_32x32x16_bf16 v[128:143], v[232:235], v[24:27], v[128:143]\n\t" \
  "v_mov_b32_e32 v88, 0\n\t" \
  "v_mov_b32_e32 v89, 0\n\t" \
  "v_mov_b32_e32 v90, 0\n\t" \
  "v_mov_b32_e32 v91, 0\n\t" \
  "s_waitcnt lgkmcnt(4)\n\t" \
  "v_mfma_f32_32x32x16_bf16 v[128:143], v[236:239], v[28:31], v[128:143]\n\t" \
  "v_mov_b32_e32 v92, 0\n\t" \
  "v_mov_b32_e32 v93, 0\n\t" \
  "v_mov_b32_e32 v94, 0\n\t" \
  "v_mov_b32_e32 v95, 0\n\t" \
  "s_waitcnt lgkmcnt(3)\n\t" \
  "v_mfma_f32_32x32x16_bf16 v[128:143], v[208:211], v[32:35], v[128:143]\n\t" \
  "v_mov_b32_e32 v96, 0\n\t" \
  "v_mov_b32_e32 v97, 0\n\t" \
  "v_mov_b32_e32 v98, 0\n\t" \
  "v_mov_b32_e32 v99, 0\n\t" \
  "s_waitcnt lgkmcnt(2)\n\t" \
  "v_mfma_f32_32x32x16_bf16 v[128:143], v[212:215], v[36:39], v[128:143]\n\t" \
  "v_mov_b32_e32 v100, 0\n\t" \
  "v_mov_b32_e32 v101, 0\n\t" \
  "v_mov_b32_e32 v102, 0\n\t" \
  "v_mov_b32_e32 v103, 0\n\t" \
  "s_waitcnt lgkmcnt(1)\n\t" \
  "v_mfma_f32_32x32x16_bf16 v[128:143], v[216:219], v[40:43], v[128:143]\n\t" \
  "v_mov_b32_e32 v104, 0\n\t" \
  "v_mov_b32_e32 v105, 0\n\t" \
  "v_mov_b32_e32 v106, 0\n\t" \
  "v_mov_b32_e32 v107, 0\n\t" \
  "s_waitcnt lgkmcnt(0)\n\t" \
  "v_mfma_f32_32x32x16_bf16 v[128:143], v[220:223], v[44:47], v[128:143]\n\t" \
  "v_mov_b32_e32 v108, 0\n\t" \
  "v_mov_b32_e32 v109, 0\n\t" \
  "v_mov_b32_e32 v110, 0\n\t" \
  "v_mov_b32_e32 v111, 0\n\t" \
  "ds_read_b128 v[224:227], v249 offset:16384\n\t" \
  "ds_read_b128 v[228:231], v249 offset:17408\n\t" \
  "ds_read_b128 v[232:235], v249 offset:18432\n\t" \
  "ds_read_b128 v[236:239], v249 offset:19456\n\t" \
  "v_mov_b32_e32 v208, 0\n\t" \
  "v_mov_b32_e32 v209, 0\n\t" \
  "v_mov_b32_e32 v210, 0\n\t" \
  "v_mov_b32_e32 v211, 0\n\t" \
  "v_mov_b32_e32 v212, 0\n\t" \
  "v_mov_b32_e32 v213, 0\n\t" \
  "v_mov_b32_e32 v214, 0\n\t" \
  "v_mov_b32_e32 v215, 0\n\t" \
  "v_mov_b32_e32 v216, 0\n\t" \
  "v_mov_b32_e32 v217, 0\n\t" \
  "v_mov_b32_e32 v218, 0\n\t" \
  "v_mov_b32_e32 v219, 0\n\t" \
  "v_mov_b32_e32 v220, 0\n\t" \
  "v_mov_b32_e32 v221, 0\n\t" \
  "v_mov_b32_e32 v222, 0\n\t" \
  "v_mov_b32_e32 v223, 0\n\t" \
  "v_mov_b32_e32 v188, 0\n\t" \
  "v_mov_b32_e32 v189, 0\n\t" \
  "v_mov_b32_e32 v190, 0\n\t" \
  "v_mov_b32_e32 v191, 0\n\t" \
  "s_cmp_eq_u32 s52, 1\n\t" \
  "s_cbranch_scc1 LA_PMASK%=\n\t" \
  "LA_PMASKB%=:\n\t" \
  "v_max_f32_e32 v244, v112, v113\n\t" \
  "v_max3_f32 v244, v244, v114, v115\n\t" \
  "v_max3_f32 v244, v244, v116, v117\n\t" \
  "v_max3_f32 v244, v244, v118, v119\n\t" \
  "v_max3_f32 v244, v244, v120, v121\n\t" \
  "v_max3_f32 v244, v244, v122, v123\n\t" \
  "v_max3_f32 v244, v244, v124, v125\n\t" \
  "v_max3_f32 v244, v244, v126, v127\n\t" \
  "v_max3_f32 v244, v244, v128, v129\n\t" \
  "v_max3_f32 v244, v244, v130, v131\n\t" \
  "v_max3_f32 v244, v244, v132, v133\n\t" \
  "v_max3_f32 v244, v244, v134, v135\n\t" \
  "v_max3_f32 v244, v244, v136, v137\n\t" \
  "v_max3_f32 v244, v244, v138, v139\n\t" \
  "v_max3_f32 v244, v244, v140, v141\n\t" \
  "v_max3_f32 v244, v244, v142, v143\n\t" \
  "v_mov_b32_e32 v246, v244\n\t" \
  "s_nop 1\n\t" \
  "v_permlane32_swap_b32_e32 v244, v246\n\t" \
  "s_nop 0\n\t" \
  "v_max_f32_e32 v240, v244, v246\n\t" \
  "v_sub_f32_e32 v112, v112, v240\n\t" \
  "v_sub_f32_e32 v128, v128, v240\n\t" \
  "v_xor_b32_e32 v192, 0x80000000, v240\n\t" \
  "v_sub_f32_e32 v113, v113, v240\n\t" \
  "v_sub_f32_e32 v129, v129, v240\n\t" \
  "v_xor_b32_e32 v193, 0x80000000, v240\n\t" \
  "v_sub_f32_e32 v114, v114, v240\n\t" \
  "v_sub_f32_e32 v130, v130, v240\n\t" \
  "v_xor_b32_e32 v194, 0x80000000, v240\n\t" \
  "v_sub_f32_e32 v115, v115, v240\n\t" \
  "v_sub_f32_e32 v131, v131, v240\n\t" \
  "v_xor_b32_e32 v195, 0x80000000, v240\n\t" \
  "v_sub_f32_e32 v116, v116, v240\n\t" \
  "v_sub_f32_e32 v132, v132, v240\n\t" \
  "v_xor_b32_e32 v196, 0x80000000, v240\n\t" \
  "v_sub_f32_e32 v117, v117, v240\n\t" \
  "v_sub_f32_e32 v133, v133, v240\n\t" \
  "v_xor_b32_e32 v197, 0x80000000, v240\n\t" \
  "v_sub_f32_e32 v118, v118, v240\n\t" \
  "v_sub_f32_e32 v134, v134, v240\n\t" \
  "v_xor_b32_e32 v198, 0x80000000, v240\n\t" \
  "v_sub_f32_e32 v119, v119, v240\n\t" \
  "v_sub_f32_e32 v135, v135, v240\n\t" \
  "v_xor_b32_e32 v199, 0x80000000, v240\n\t" \
  "v_sub_f32_e32 v120, v120, v240\n\t" \
  "v_sub_f32_e32 v136, v136, v240\n\t" \
  "v_xor_b32_e32 v200, 0x80000000, v240\n\t" \
  "v_sub_f32_e32 v121, v121, v240\n\t" \
  "v_sub_f32_e32 v137, v137, v240\n\t" \
  "v_xor_b32_e32 v201, 0x80000000, v240\n\t" \
  "v_sub_f32_e32 v122, v122, v240\n\t" \
  "v_sub_f32_e32 v138, v138, v240\n\t" \
  "v_xor_b32_e32 v202, 0x80000000, v240\n\t" \
  "v_sub_f32_e32 v123, v123, v240\n\t" \
  "v_sub_f32_e32 v139, v139, v240\n\t" \
  "v_xor_b32_e32 v203, 0x80000000, v240\n\t" \
  "v_sub_f32_e32 v124, v124, v240\n\t" \
  "v_sub_f32_e32 v140, v140, v240\n\t" \
  "v_xor_b32_e32 v204, 0x80000000, v240\n\t" \
  "v_sub_f32_e32 v125, v125, v240\n\t" \
  "v_sub_f32_e32 v141, v141, v240\n\t" \
  "v_xor_b32_e32 v205, 0x80000000, v240\n\t" \
  "v_sub_f32_e32 v126, v126, v240\n\t" \
  "v_sub_f32_e32 v142, v142, v240\n\t" \
  "v_xor_b32_e32 v206, 0x80000000, v240\n\t" \
  "v_sub_f32_e32 v127, v127, v240\n\t" \
  "v_sub_f32_e32 v143, v143, v240\n\t" \
  "v_xor_b32_e32 v207, 0x80000000, v240\n\t" \
  "v_exp_f32_e32 v112, v112\n\t" \
  "v_exp_f32_e32 v113, v113\n\t" \
  "v_exp_f32_e32 v114, v114\n\t" \
  "v_exp_f32_e32 v115, v115\n\t" \
  "v_add_f32_e32 v242, v112, v113\n\t" \
  "v_add_f32_e32 v242, v242, v114\n\t" \
  "s_waitcnt lgkmcnt(0)\n\t" \
  "s_barrier\n\t" \
  "s_add_u32 s71, s71, 1\n\t" \
  "s_cmp_eq_u32 s52, 1\n\t" \
  "s_cbranch_scc1 LA_DRAIN0%=\n\t" \
  "s_branch LA_STEP1%=\n\t" \
  "LA_STEP0%=:\n\t" \
  "ds_read_b128 v[224:227], v249 offset:0\n\t" \
  "ds_read_b128 v[228:231], v249 offset:1024\n\t" \
  "ds_read_b128 v[232:235], v249 offset:2048\n\t" \
  "ds_read_b128 v[236:239], v249 offset:3072\n\t" \
  "v_mfma_f32_32x32x16_bf16 v[48:63], v[208:211], v[188:191], v[48:63]\n\t" \
  "ds_read_b128 v[208:211], v249 offset:4096\n\t" \
  "v_exp_f32_e32 v148, v148\n\t" \
  "v_add_f32_e32 v243, v243, v147\n\t" \
  "v_cvt_pk_bf16_f32 v176, v144, v145\n\t" \
  "v_mfma_f32_32x32x16_bf16 v[64:79], v[212:215], v[188:191], v[64:79]\n\t" \
  "ds_read_b128 v[212:215], v249 offset:5120\n\t" \
  "v_exp_f32_e32 v149, v149\n\t" \
  "v_add_f32_e32 v243, v243, v148\n\t" \
  "v_mfma_f32_32x32x16_bf16 v[80:95], v[216:219], v[188:191], v[80:95]\n\t" \
  "ds_read_b128 v[216:219], v249 offset:6144\n\t" \
  "v_exp_f32_e32 v150, v150\n\t" \
  "v_add_f32_e32 v243, v243, v149\n\t" \
  "v_cvt_pk_bf16_f32 v177, v146, v147\n\t" \
  "v_mfma_f32_32x32x16_bf16 v[96:111], v[220:223], v[188:191], v[96:111]\n\t" \
  "ds_read_b128 v[220:223], v249 offset:7168\n\t" \
  "v_exp_f32_e32 v151, v151\n\t" \
  "v_add_f32_e32 v243, v243, v150\n\t" \
  "v_add_f32_e32 v241, v241, v242\n\t" \
  "s_cmp_lg_u32 s69, 0\n\t" \
  "s_cbranch_scc1 LA_ROS0%=\n\t" \
  "LA_ROBS0%=:\n\t" \
  "s_waitcnt lgkmcnt(4)\n\t" \
  "v_mfma_f32_32x32x16_bf16 v[112:127], v[224:227], v[16:19], v[192:207]\n\t" \
  "ds_read_b128 v[224:227], v249 offset:8192\n\t" \
  "v_exp_f32_e32 v152, v152\n\t" \
  "v_add_f32_e32 v243, v243, v151\n\t" \
  "v_cvt_pk_bf16_f32 v178, v148, v149\n\t" \
  "v_mfma_f32_32x32x16_bf16 v[112:127], v[228:231], v[20:23], v[112:127]\n\t" \
  "ds_read_b128 v[228:231], v249 offset:9216\n\t" \
  "v_exp_f32_e32 v153, v153\n\t" \
  "v_add_f32_e32 v243, v243, v152\n\t" \
  "s_add_u32 s68, s68, 1\n\t" \
  "v_mfma_f32_32x32x16_bf16 v[112:127], v[232:235], v[24:27], v[112:127]\n\t" \
  "ds_read_b128 v[232:235], v249 offset:10240\n\t" \
  "v_exp_f32_e32 v154, v154\n\t" \
  "v_add_f32_e32 v243, v243, v153\n\t" \
  "v_cvt_pk_bf16_f32 v179, v150, v151\n\t" \
  "v_mfma_f32_32x32x16_bf16 v[112:127], v[236:239], v[28:31], v[112:127]\n\t" \
  "ds_read_b128 v[236:239], v249 offset:11264\n\t" \
  "v_exp_f32_e32 v155, v155\n\t" \
  "v_add_f32_e32 v243, v243, v154\n\t" \
  "v_cvt_pk_bf16_f32 v180, v152, v153\n\t" \
  "s_waitcnt lgkmcnt(4)\n\t" \
  "v_mfma_f32_32x32x16_bf16 v[112:127], v[208:211], v[32:35], v[112:127]\n\t" \
  "ds_read_b128 v[208:211], v249 offset:12288\n\t" \
  "v_exp_f32_e32 v156, v156\n\t" \
  "v_add_f32_e32 v243, v243, v155\n\t" \
  "v_mfma_f32_32x32x16_bf16 v[112:127], v[212:215], v[36:39], v[112:127]\n\t" \
  "ds_read_b128 v[212:215], v249 offset:13312\n\t" \
  "v_exp_f32_e32 v157, v157\n\t" \
  "v_add_f32_e32 v243, v243, v156\n\t" \
  "v_cvt_pk_bf16_f32 v181, v154, v155\n\t" \
  "v_mfma_f32_32x32x16_bf16 v[112:127], v[216:219], v[40:43], v[112:127]\n\t" \
  "ds_read_b128 v[216:219], v249 offset:14336\n\t" \
  "v_exp_f32_e32 v158, v158\n\t" \
  "v_add_f32_e32 v243, v243, v157\n\t" \
  "v_cvt_pk_bf16_f32 v182, v156, v157\n\t" \
  "v_mfma_f32_32x32x16_bf16 v[112:127], v[220:223], v[44:47], v[112:127]\n\t" \
  "ds_read_b128 v[220:223], v249 offset:15360\n\t" \
  "v_exp_f32_e32 v159, v159\n\t" \
  "v_add_f32_e32 v243, v243, v158\n\t" \
  "s_waitcnt lgkmcnt(4)\n\t" \
  "v_mfma_f32_32x32x16_bf16 v[128:143], v[224:227], v[16:19], v[192:207]\n\t" \
  "ds_read_b128 v[224:227], v250 offset:0\n\t" \
  "v_exp_f32_e32 v160, v160\n\t" \
  "v_add_f32_e32 v243, v243, v159\n\t" \
  "v_cvt_pk_bf16_f32 v183, v158, v159\n\t" \
  "v_mfma_f32_32x32x16_bf16 v[128:143], v[228:231], v[20:23], v[128:143]\n\t" \
  "ds_read_b128 v[228:231], v250 offset:1024\n\t" \
  "v_exp_f32_e32 v161, v161\n\t" \
  "v_add_f32_e32 v243, v243, v160\n\t" \
  "s_cmp_eq_u32 s68, s52\n\t" \
  "s_cbranch_scc1 LA_MASK00%=\n\t" \
  "LA_MASKB00%=:\n\t" \
  "v_mfma_f32_32x32x16_bf16 v[128:143], v[232:235], v[24:27], v[128:143]\n\t" \
  "ds_read_b128 v[232:235], v250 offset:2048\n\t" \
  "v_exp_f32_e32 v162, v162\n\t" \
  "v_add_f32_e32 v243, v243, v161\n\t" \
  "v_cvt_pk_bf16_f32 v184, v160, v161\n\t" \
  "v_max_f32_e32 v244, v112, v113\n\t" \
  "v_mfma_f32_32x32x16_bf16 v[128:143], v[236:239], v[28:31], v[128:143]\n\t" \
  "ds_read_b128 v[236:239], v250 offset:3072\n\t" \
  "v_exp_f32_e32 v163, v163\n\t" \
  "v_add_f32_e32 v243, v243, v162\n\t" \
  "v_max3_f32 v244, v244, v114, v115\n\t" \
  "s_waitcnt lgkmcnt(4)\n\t" \
  "v_mfma_f32_32x32x16_bf16 v[128:143], v[208:211], v[32:35], v[128:143]\n\t" \
  "ds_read_b128 v[208:211], v250 offset:4096\n\t" \
  "v_exp_f32_e32 v164, v164\n\t" \
  "v_add_f32_e32 v243, v243, v163\n\t" \
  "v_max3_f32 v244, v244, v116, v117\n\t" \
  "v_mfma_f32_32x32x16_bf16 v[128:143], v[212:215], v[36:39], v[128:143]\n\t" \
  "ds_read_b128 v[212:215], v250 offset:5120\n\t" \
  "v_exp_f32_e32 v165, v165\n\t" \
  "v_add_f32_e32 v243, v243, v164\n\t" \
  "v_cvt_pk_bf16_f32 v185, v162, v163\n\t" \
  "v_max3_f32 v244, v244, v118, v119\n\t" \
  "v_mfma_f32_32x32x16_bf16 v[128:143], v[216:219], v[40:43], v[128:143]\n\t" \
  "ds_read_b128 v[216:219], v250 offset:6144\n\t" \
  "v_exp_f32_e32 v166, v166\n\t" \
  "v_add_f32_e32 v243, v243, v165\n\t" \
  "v_cvt_pk_bf16_f32 v186, v164, v165\n\t" \
  "v_max3_f32 v244, v244, v120, v121\n\t" \
  "v_mfma_f32_32x32x16_bf16 v[128:143], v[220:223], v[44:47], v[128:143]\n\t" \
  "ds_read_b128 v[220:223], v250 offset:7168\n\t" \
  "v_exp_f32_e32 v167, v167\n\t" \
  "v_add_f32_e32 v243, v243, v166\n\t" \
  "v_max3_f32 v244, v244, v122, v123\n\t" \
  "s_waitcnt lgkmcnt(4)\n\t" \
  "v_mfma_f32_32x32x16_bf16 v[48:63], v[224:227], v[176:179], v[48:63]\n\t" \
  "ds_read_b128 v[224:227], v250 offset:8192\n\t" \
  "v_exp_f32_e32 v168, v168\n\t" \
  "v_add_f32_e32 v243, v243, v167\n\t" \
  "v_cvt_pk_bf16_f32 v187, v166, v167\n\t" \
  "v_max3_f32 v244, v244, v124, v125\n\t" \
  "v_mfma_f32_32x32x16_bf16 v[64:79], v[228:231], v[176:179], v[64:79]\n\t" \
  "ds_read_b128 v[228:231], v250 offset:9216\n\t" \
  "v_exp_f32_e32 v169, v169\n\t" \
  "v_add_f32_e32 v243, v243, v168\n\t" \
  "s_cmp_eq_u32 s68, s52\n\t" \
  "s_cbranch_scc1 LA_MASK01%=\n\t" \
  "LA_MASKB01%=:\n\t" \
  "v_max3_f32 v244, v244, v126, v127\n\t" \
  "v_mfma_f32_32x32x16_bf16 v[80:95], v[232:235], v[176:179], v[80:95]\n\t" \
  "ds_read_b128 v[232:235], v250 offset:10240\n\t" \
  "v_exp_f32_e32 v170, v170\n\t" \
  "v_add_f32_e32 v243, v243, v169\n\t" \
  "v_max3_f32 v244, v244, v128, v129\n\t" \
  "v_max3_f32 v244, v244, v130, v131\n\t" \
  "v_mfma_f32_32x32x16_bf16 v[96:111], v[236:239], v[176:179], v[96:111]\n\t" \
  "ds_read_b128 v[236:239], v250 offset:11264\n\t" \
  "v_exp_f32_e32 v171, v171\n\t" \
  "v_add_f32_e32 v243, v243, v170\n\t" \
  "v_max3_f32 v244, v244, v132, v133\n\t" \
  "v_max3_f32 v244, v244, v134, v135\n\t" \
  "s_waitcnt lgkmcnt(4)\n\t" \
  "v_mfma_f32_32x32x16_bf16 v[48:63], v[208:211], v[180:183], v[48:63]\n\t" \
  "ds_read_b128 v[208:211], v250 offset:12288\n\t" \
  "v_exp_f32_e32 v172, v172\n\t" \
  "v_add_f32_e32 v243, v243, v171\n\t" \
  "v_max3_f32 v244, v244, v136, v137\n\t" \
  "v_max3_f32 v244, v244, v138, v139\n\t" \
  "v_mfma_f32_32x32x16_bf16 v[64:79], v[212:215], v[180:183], v[64:79]\n\t" \
  "ds_read_b128 v[212:215], v250 offset:13312\n\t" \
  "v_exp_f32_e32 v173, v173\n\t" \
  "v_add_f32_e32 v243, v243, v172\n\t" \
  "v_max3_f32 v244, v244, v140, v141\n\t" \
  "v_max3_f32 v244, v244, v142, v143\n\t" \
  "v_mov_b32_e32 v246, v244\n\t" \
  "v_mfma_f32_32x32x16_bf16 v[80:95], v[216:219], v[180:183], v[80:95]\n\t" \
  "ds_read_b128 v[216:219], v250 offset:14336\n\t" \
  "v_exp_f32_e32 v174, v174\n\t" \
  "v_add_f32_e32 v243, v243, v173\n\t" \
  "v_permlane32_swap_b32_e32 v244, v246\n\t" \
  "s_nop 0\n\t" \
  "v_max_f32_e32 v244, v244, v246\n\t" \
  "v_cmp_lt_f32_e32 vcc, 0x41c00000, v244\n\t" \
  "v_mfma_f32_32x32x16_bf16 v[96:111], v[220:223], v[180:183], v[96:111]\n\t" \
  "ds_read_b128 v[220:223], v250 offset:15360\n\t" \
  "s_cbranch_vccnz LA_RARE0%=\n\t" \
  "LA_RAREB0%=:\n\t" \
  "v_exp_f32_e32 v175, v175\n\t" \
  "v_add_f32_e32 v243, v243, v174\n\t" \
  "s_waitcnt lgkmcnt(4)\n\t" \
  "v_mfma_f32_32x32x16_bf16 v[48:63], v[224:227], v[184:187], v[48:63]\n\t" \
  "ds_read_b128 v[224:227], v249 offset:16384\n\t" \
  "v_exp_f32_e32 v112, v112\n\t" \
  "v_add_f32_e32 v243, v243, v175\n\t" \
  "v_cvt_pk_bf16_f32 v188, v168, v169\n\t" \
  "v_mfma_f32_32x32x16_bf16 v[64:79], v[228:231], v[184:187], v[64:79]\n\t" \
  "ds_read_b128 v[228:231], v249 offset:17408\n\t" \
  "v_exp_f32_e32 v113, v113\n\t" \
  "v_cvt_pk_bf16_f32 v189, v170, v171\n\t" \
  "v_mfma_f32_32x32x16_bf16 v[80:95], v[232:235], v[184:187], v[80:95]\n\t" \
  "ds_read_b128 v[232:235], v249 offset:18432\n\t" \
  "v_exp_f32_e32 v114, v114\n\t" \
  "v_add_f32_e32 v242, v112, v113\n\t" \
  "v_cvt_pk_bf16_f32 v190, v172, v173\n\t" \
  "v_mfma_f32_32x32x16_bf16 v[96:111], v[236:239], v[184:187], v[96:111]\n\t" \
  "ds_read_b128 v[236:239], v249 offset:19456\n\t" \
  "v_exp_f32_e32 v115, v115\n\t" \
  "v_add_f32_e32 v242, v242, v114\n\t" \
  "v_cvt_pk_bf16_f32 v191, v174, v175\n\t" \
  "s_cmp_eq_u32 s68, s52\n\t" \
  "s_cbranch_scc1 LA_DRAIN0%=\n\t" \
  "LA_STEP1%=:\n\t" \
  "s_waitcnt lgkmcnt(4)\n\t" \
  "v_mfma_f32_32x32x16_bf16 v[48:63], v[208:211], v[188:191], v[48:63]\n\t" \
  "ds_read_b128 v[208:211], v249 offset:20480\n\t" \
  "v_exp_f32_e32 v116, v116\n\t" \
  "v_add_f32_e32 v242, v242, v115\n\t" \
  "v_cvt_pk_bf16_f32 v176, v112, v113\n\t" \
  "v_mfma_f32_32x32x16_bf16 v[64:79], v[212:215], v[188:191], v[64:79]\n\t" \
  "ds_read_b128 v[212:215], v249 offset:21504\n\t" \
  "v_exp_f32_e32 v117, v117\n\t" \
  "v_add_f32_e32 v242, v242, v116\n\t" \
  "s_cmp_eq_u32 s71, s70\n\t" \
  "s_cbranch_scc1 L_WM%=\n\t" \
  "L_WMB%=:\n\t" \
  "v_mfma_f32_32x32x16_bf16 v[80:95], v[216:219], v[188:191], v[80:95]\n\t" \
  "ds_read_b128 v[216:219], v249 offset:22528\n\t" \
  "v_exp_f32_e32 v118, v118\n\t" \
  "v_add_f32_e32 v242, v242, v117\n\t" \
  "v_cvt_pk_bf16_f32 v177, v114, v115\n\t" \
  "v_mfma_f32_32x32x16_bf16 v[96:111], v[220:223], v[188:191], v[96:111]\n\t" \
  "ds_read_b128 v[220:223], v249 offset:23552\n\t" \
  "v_exp_f32_e32 v119, v119\n\t" \
  "v_add_f32_e32 v242, v242, v118\n\t" \
  "v_add_f32_e32 v241, v241, v243\n\t" \
  "s_cmp_lg_u32 s69, 0\n\t" \
  "s_cbranch_scc1 LA_ROS1%=\n\t" \
  "LA_ROBS1%=:\n\t" \
  "s_waitcnt lgkmcnt(4)\n\t" \
  "v_mfma_f32_32x32x16_bf16 v[144:159], v[224:227], v[16:19], v[192:207]\n\t" \
  "ds_read_b128 v[224:227], v249 offset:24576\n\t" \
  "v_exp_f32_e32 v120, v120\n\t" \
  "v_add_f32_e32 v242, v242, v119\n\t" \
  "v_cvt_pk_bf16_f32 v178, v116, v117\n\t" \
  "v_mfma_f32_32x32x16_bf16 v[144:159], v[228:231], v[20:23], v[144:159]\n\t" \
  "ds_read_b128 v[228:231], v249 offset:25600\n\t" \
  "v_exp_f32_e32 v121, v121\n\t" \
  "v_add_f32_e32 v242, v242, v120\n\t" \
  "s_add_u32 s68, s68, 1\n\t" \
  "v_mfma_f32_32x32x16_bf16 v[144:159], v[232:235], v[24:27], v[144:159]\n\t" \
  "ds_read_b128 v[232:235], v249 offset:26624\n\t" \
  "v_exp_f32_e32 v122, v122\n\t" \
  "v_add_f32_e32 v242, v242, v121\n\t" \
  "v_cvt_pk_bf16_f32 v179, v118, v119\n\t" \
  "v_mfma_f32_32x32x16_bf16 v[144:159], v[236:239], v[28:31], v[144:159]\n\t" \
  "ds_read_b128 v[236:239], v249 offset:27648\n\t" \
  "v_exp_f32_e32 v123, v123\n\t" \
  "v_add_f32_e32 v242, v242, v122\n\t" \
  "v_cvt_pk_bf16_f32 v180, v120, v121\n\t" \
  "s_waitcnt lgkmcnt(4)\n\t" \
  "v_mfma_f32_32x32x16_bf16 v[144:159], v[208:211], v[32:35], v[144:159]\n\t" \
  "ds_read_b128 v[208:211], v249 offset:28672\n\t" \
  "v_exp_f32_e32 v124, v124\n\t" \
  "v_add_f32_e32 v242, v242, v123\n\t" \
  "v_mfma_f32_32x32x16_bf16 v[144:159], v[212:215], v[36:39], v[144:159]\n\t" \
  "ds_read_b128 v[212:215], v249 offset:29696\n\t" \
  "v_exp_f32_e32 v125, v125\n\t" \
  "v_add_f32_e32 v242, v242, v124\n\t" \
  "v_cvt_pk_bf16_f32 v181, v122, v123\n\t" \
  "v_mfma_f32_32x32x16_bf16 v[144:159], v[216:219], v[40:43], v[144:159]\n\t" \
  "ds_read_b128 v[216:219], v249 offset:30720\n\t" \
  "v_exp_f32_e32 v126, v126\n\t" \
  "v_add_f32_e32 v242, v242, v125\n\t" \
  "v_cvt_pk_bf16_f32 v182, v124, v125\n\t" \
  "v_mfma_f32_32x32x16_bf16 v[144:159], v[220:223], v[44:47], v[144:159]\n\t" \
  "ds_read_b128 v[220:223], v249 offset:31744\n\t" \
  "v_exp_f32_e32 v127, v127\n\t" \
  "v_add_f32_e32 v242, v242, v126\n\t" \
  "s_waitcnt lgkmcnt(4)\n\t" \
  "v_mfma_f32_32x32x16_bf16 v[160:175], v[224:227], v[16:19], v[192:207]\n\t" \
  "ds_read_b128 v[224:227], v250 offset:16384\n\t" \
  "v_exp_f32_e32 v128, v128\n\t" \
  "v_add_f32_e32 v242, v242, v127\n\t" \
  "v_cvt_pk_bf16_f32 v183, v126, v127\n\t" \
  "v_mfma_f32_32x32x16_bf16 v[160:175], v[228:231], v[20:23], v[160:175]\n\t" \
  "ds_read_b128 v[228:231], v250 offset:17408\n\t" \
  "v_exp_f32_e32 v129, v129\n\t" \
  "v_add_f32_e32 v242, v242, v128\n\t" \
  "s_cmp_eq_u32 s68, s52\n\t" \
  "s_cbranch_scc1 LA_MASK10%=\n\t" \
  "LA_MASKB10%=:\n\t" \
  "v_mfma_f32_32x32x16_bf16 v[160:175], v[232:235], v[24:27], v[160:175]\n\t" \
  "ds_read_b128 v[232:235], v250 offset:18432\n\t" \
  "v_exp_f32_e32 v130, v130\n\t" \
  "v_add_f32_e32 v242, v242, v129\n\t" \
  "v_cvt_pk_bf16_f32 v184, v128, v129\n\t" \
  "v_max_f32_e32 v244, v144, v145\n\t" \
  "v_mfma_f32_32x32x16_bf16 v[160:175], v[236:239], v[28:31], v[160:175]\n\t" \
  "ds_read_b128 v[236:239], v250 offset:19456\n\t" \
  "v_exp_f32_e32 v131, v131\n\t" \
  "v_add_f32_e32 v242, v242, v130\n\t" \
  "v_max3_f32 v244, v244, v146, v147\n\t" \
  "s_waitcnt lgkmcnt(4)\n\t" \
  "v_mfma_f32_32x32x16_bf16 v[160:175], v[208:211], v[32:35], v[160:175]\n\t" \
  "ds_read_b128 v[208:211], v250 offset:20480\n\t" \
  "v_exp_f32_e32 v132, v132\n\t" \
  "v_add_f32_e32 v242, v242, v131\n\t" \
  "v_max3_f32 v244, v244, v148, v149\n\t" \
  "v_mfma_f32_32x32x16_bf16 v[160:175], v[212:215], v[36:39], v[160:175]\n\t" \
  "ds_read_b128 v[212:215], v250 offset:21504\n\t" \
  "v_exp_f32_e32 v133, v133\n\t" \
  "v_add_f32_e32 v242, v242, v132\n\t" \
  "v_cvt_pk_bf16_f32 v185, v130, v131\n\t" \
  "v_max3_f32 v244, v244, v150, v151\n\t" \
  "v_mfma_f32_32x32x16_bf16 v[160:175], v[216:219], v[40:43], v[160:175]\n\t" \
  "ds_read_b128 v[216:219], v250 offset:22528\n\t" \
  "v_exp_f32_e32 v134, v134\n\t" \
  "v_add_f32_e32 v242, v242, v133\n\t" \
  "v_cvt_pk_bf16_f32 v186, v132, v133\n\t" \
  "v_max3_f32 v244, v244, v152, v153\n\t" \
  "v_mfma_f32_32x32x16_bf16 v[160:175], v[220:223], v[44:47], v[160:175]\n\t" \
  "ds_read_b128 v[220:223], v250 offset:23552\n\t" \
  "v_exp_f32_e32 v135, v135\n\t" \
  "v_add_f32_e32 v242, v242, v134\n\t" \
  "v_max3_f32 v244, v244, v154, v155\n\t" \
  "s_waitcnt lgkmcnt(4)\n\t" \
  "v_mfma_f32_32x32x16_bf16 v[48:63], v[224:227], v[176:179], v[48:63]\n\t" \
  "ds_read_b128 v[224:227], v250 offset:24576\n\t" \
  "v_exp_f32_e32 v136, v136\n\t" \
  "v_add_f32_e32 v242, v242, v135\n\t" \
  "v_cvt_pk_bf16_f32 v187, v134, v135\n\t" \
  "v_max3_f32 v244, v244, v156, v157\n\t" \
  "v_mfma_f32_32x32x16_bf16 v[64:79], v[228:231], v[176:179], v[64:79]\n\t" \
  "ds_read_b128 v[228:231], v250 offset:25600\n\t" \
  "v_exp_f32_e32 v137, v137\n\t" \
  "v_add_f32_e32 v242, v242, v136\n\t" \
  "s_cmp_eq_u32 s68, s52\n\t" \
  "s_cbranch_scc1 LA_MASK11%=\n\t" \
  "LA_MASKB11%=:\n\t" \
  "v_max3_f32 v244, v244, v158, v159\n\t" \
  "v_mfma_f32_32x32x16_bf16 v[80:95], v[232:235], v[176:179], v[80:95]\n\t" \
  "ds_read_b128 v[232:235], v250 offset:26624\n\t" \
  "v_exp_f32_e32 v138, v138\n\t" \
  "v_add_f32_e32 v242, v242, v137\n\t" \
  "v_max3_f32 v244, v244, v160, v161\n\t" \
  "v_max3_f32 v244, v244, v162, v163\n\t" \
  "v_mfma_f32_32x32x16_bf16 v[96:111], v[236:239], v[176:179], v[96:111]\n\t" \
  "ds_read_b128 v[236:239], v250 offset:27648\n\t" \
  "v_exp_f32_e32 v139, v139\n\t" \
  "v_add_f32_e32 v242, v242, v138\n\t" \
  "v_max3_f32 v244, v244, v164, v165\n\t" \
  "v_max3_f32 v244, v244, v166, v167\n\t" \
  "s_waitcnt lgkmcnt(4)\n\t" \
  "v_mfma_f32_32x32x16_bf16 v[48:63], v[208:211], v[180:183], v[48:63]\n\t" \
  "ds_read_b128 v[208:211], v250 offset:28672\n\t" \
  "v_exp_f32_e32 v140, v140\n\t" \
  "v_add_f32_e32 v242, v242, v139\n\t" \
  "v_max3_f32 v244, v244, v168, v169\n\t" \
  "v_max3_f32 v244, v244, v170, v171\n\t" \
  "v_mfma_f32_32x32x16_bf16 v[64:79], v[212:215], v[180:183], v[64:79]\n\t" \
  "ds_read_b128 v[212:215], v250 offset:29696\n\t" \
  "v_exp_f32_e32 v141, v141\n\t" \
  "v_add_f32_e32 v242, v242, v140\n\t" \
  "v_max3_f32 v244, v244, v172, v173\n\t" \
  "v_max3_f32 v244, v244, v174, v175\n\t" \
  "v_mov_b32_e32 v246, v244\n\t" \
  "v_mfma_f32_32x32x16_bf16 v[80:95], v[216:219], v[180:183], v[80:95]\n\t" \
  "ds_read_b128 v[216:219], v250 offset:30720\n\t" \
  "v_exp_f32_e32 v142, v142\n\t" \
  "v_add_f32_e32 v242, v242, v141\n\t" \
  "v_permlane32_swap_b32_e32 v244, v246\n\t" \
  "s_nop 0\n\t" \
  "v_max_f32_e32 v244, v244, v246\n\t" \
  "v_cmp_lt_f32_e32 vcc, 0x41c00000, v244\n\t" \
  "v_mfma_f32_32x32x16_bf16 v[96:111], v[220:223], v[180:183], v[96:111]\n\t" \
  "ds_read_b128 v[220:223], v250 offset:31744\n\t" \
  "s_cbranch_vccnz LA_RARE1%=\n\t" \
  "LA_RAREB1%=:\n\t" \
  "v_exp_f32_e32 v143, v143\n\t" \
  "v_add_f32_e32 v242, v242, v142\n\t" \
  "s_waitcnt lgkmcnt(4)\n\t" \
  "v_mfma_f32_32x32x16_bf16 v[48:63], v[224:227], v[184:187], v[48:63]\n\t" \
  "v_exp_f32_e32 v144, v144\n\t" \
  "v_add_f32_e32 v242, v242, v143\n\t" \
  "v_cvt_pk_bf16_f32 v188, v136, v137\n\t" \
  "v_mfma_f32_32x32x16_bf16 v[64:79], v[228:231], v[184:187], v[64:79]\n\t" \
  "v_exp_f32_e32 v145, v145\n\t" \
  "v_cvt_pk_bf16_f32 v189, v138, v139\n\t" \
  "v_xor_b32_e32 v249, 0x8000, v249\n\t" \
  "v_mfma_f32_32x32x16_bf16 v[80:95], v[232:235], v[184:187], v[80:95]\n\t" \
  "v_exp_f32_e32 v146, v146\n\t" \
  "v_add_f32_e32 v243, v144, v145\n\t" \
  "v_cvt_pk_bf16_f32 v190, v140, v141\n\t" \
  "v_xor_b32_e32 v250, 0x8000, v250\n\t" \
  "v_mfma_f32_32x32x16_bf16 v[96:111], v[236:239], v[184:187], v[96:111]\n\t" \
  "v_exp_f32_e32 v147, v147\n\t" \
  "v_add_f32_e32 v243, v243, v146\n\t" \
  "v_cvt_pk_bf16_f32 v191, v142, v143\n\t" \
  "s_waitcnt lgkmcnt(0)\n\t" \
  "s_barrier\n\t" \
  "s_add_u32 s71, s71, 1\n\t" \
  "s_cmp_eq_u32 s68, s52\n\t" \
  "s_cbranch_scc0 LA_STEP0%=\n\t" \
  "s_branch LA_DRAIN1%=\n\t" \
  "LA_DRAIN0%=:\n\t" \
  "s_waitcnt lgkmcnt(0)\n\t" \
  "ds_read_b128 v[224:227], v250 offset:16384\n\t" \
  "ds_read_b128 v[228:231], v250 offset:17408\n\t" \
  "ds_read_b128 v[232:235], v250 offset:18432\n\t" \
  "ds_read_b128 v[236:239], v250 offset:19456\n\t" \
  "v_mfma_f32_32x32x16_bf16 v[48:63], v[208:211], v[188:191], v[48:63]\n\t" \
  "ds_read_b128 v[208:211], v250 offset:20480\n\t" \
  "v_cvt_pk_bf16_f32 v176, v112, v113\n\t" \
  "v_cvt_pk_bf16_f32 v177, v114, v115\n\t" \
  "v_exp_f32_e32 v116, v116\n\t" \
  "v_add_f32_e32 v242, v242, v115\n\t" \
  "v_exp_f32_e32 v117, v117\n\t" \
  "v_mfma_f32_32x32x16_bf16 v[64:79], v[212:215], v[188:191], v[64:79]\n\t" \
  "ds_read_b128 v[212:215], v250 offset:21504\n\t" \
  "v_cvt_pk_bf16_f32 v178, v116, v117\n\t" \
  "v_exp_f32_e32 v118, v118\n\t" \
  "v_add_f32_e32 v242, v242, v116\n\t" \
  "v_exp_f32_e32 v119, v119\n\t" \
  "v_add_f32_e32 v242, v242, v117\n\t" \
  "v_mfma_f32_32x32x16_bf16 v[80:95], v[216:219], v[188:191], v[80:95]\n\t" \
  "ds_read_b128 v[216:219], v250 offset:22528\n\t" \
  "v_cvt_pk_bf16_f32 v179, v118, v119\n\t" \
  "v_exp_f32_e32 v120, v120\n\t" \
  "v_add_f32_e32 v242, v242, v118\n\t" \
  "v_exp_f32_e32 v121, v121\n\t" \
  "v_add_f32_e32 v242, v242, v119\n\t" \
  "v_mfma_f32_32x32x16_bf16 v[96:111], v[220:223], v[188:191], v[96:111]\n\t" \
  "ds_read_b128 v[220:223], v250 offset:23552\n\t" \
  "v_cvt_pk_bf16_f32 v180, v120, v121\n\t" \
  "v_exp_f32_e32 v122, v122\n\t" \
  "v_add_f32_e32 v242, v242, v120\n\t" \
  "v_exp_f32_e32 v123, v123\n\t" \
  "v_add_f32_e32 v242, v242, v121\n\t" \
  "v_add_f32_e32 v241, v241, v243\n\t" \
  "s_cmp_lg_u32 s69, 0\n\t" \
  "s_cbranch_scc1 LA_ROD0%=\n\t" \
  "LA_ROBD0%=:\n\t" \
  "s_nop 0\n\t" \
  "s_waitcnt lgkmcnt(7)\n\t" \
  "v_mfma_f32_32x32x16_bf16 v[48:63], v[224:227], v[176:179], v[48:63]\n\t" \
  "ds_read_b128 v[224:227], v250 offset:24576\n\t" \
  "v_cvt_pk_bf16_f32 v181, v122, v123\n\t" \
  "v_exp_f32_e32 v124, v124\n\t" \
  "v_add_f32_e32 v242, v242, v122\n\t" \
  "v_exp_f32_e32 v125, v125\n\t" \
  "v_add_f32_e32 v242, v242, v123\n\t" \
  "s_waitcnt lgkmcnt(7)\n\t" \
  "v_mfma_f32_32x32x16_bf16 v[64:79], v[228:231], v[176:179], v[64:79]\n\t" \
  "ds_read_b128 v[228:231], v250 offset:25600\n\t" \
  "v_cvt_pk_bf16_f32 v182, v124, v125\n\t" \
  "v_exp_f32_e32 v126, v126\n\t" \
  "v_add_f32_e32 v242, v242, v124\n\t" \
  "v_exp_f32_e32 v127, v127\n\t" \
  "v_add_f32_e32 v242, v242, v125\n\t" \
  "s_waitcnt lgkmcnt(7)\n\t" \
  "v_mfma_f32_32x32x16_bf16 v[80:95], v[232:235], v[176:179], v[80:95]\n\t" \
  "ds_read_b128 v[232:235], v250 offset:26624\n\t" \
  "v_cvt_pk_bf16_f32 v183, v126, v127\n\t" \
  "v_exp_f32_e32 v128, v128\n\t" \
  "v_add_f32_e32 v242, v242, v126\n\t" \
  "v_exp_f32_e32 v129, v129\n\t" \
  "v_add_f32_e32 v242, v242, v127\n\t" \
  "s_waitcnt lgkmcnt(7)\n\t" \
  "v_mfma_f32_32x32x16_bf16 v[96:111], v[236:239], v[176:179], v[96:111]\n\t" \
  "ds_read_b128 v[236:239], v250 offset:27648\n\t" \
  "v_cvt_pk_bf16_f32 v184, v128, v129\n\t" \
  "v_exp_f32_e32 v130, v130\n\t" \
  "v_add_f32_e32 v242, v242, v128\n\t" \
  "v_exp_f32_e32 v131, v131\n\t" \
  "v_add_f32_e32 v242, v242, v129\n\t" \
  "s_waitcnt lgkmcnt(7)\n\t" \
  "v_mfma_f32_32x32x16_bf16 v[48:63], v[208:211], v[180:183], v[48:63]\n\t" \
  "ds_read_b128 v[208:211], v250 offset:28672\n\t" \
  "v_cvt_pk_bf16_f32 v185, v130, v131\n\t" \
  "v_exp_f32_e32 v132, v132\n\t" \
  "v_add_f32_e32 v242, v242, v130\n\t" \
  "v_exp_f32_e32 v133, v133\n\t" \
  "v_add_f32_e32 v242, v242, v131\n\t" \
  "s_waitcnt lgkmcnt(7)\n\t" \
  "v_mfma_f32_32x32x16_bf16 v[64:79], v[212:215], v[180:183], v[64:79]\n\t" \
  "ds_read_b128 v[212:215], v250 offset:29696\n\t" \
  "v_cvt_pk_bf16_f32 v186, v132, v133\n\t" \
  "v_exp_f32_e32 v134, v134\n\t" \
  "v_add_f32_e32 v242, v242, v132\n\t" \
  "v_exp_f32_e32 v135, v135\n\t" \
  "v_add_f32_e32 v242, v242, v133\n\t" \
  "s_waitcnt lgkmcnt(7)\n\t" \
  "v_mfma_f32_32x32x16_bf16 v[80:95], v[216:219], v[180:183], v[80:95]\n\t" \
  "ds_read_b128 v[216:219], v250 offset:30720\n\t" \
  "v_cvt_pk_bf16_f32 v187, v134, v135\n\t" \
  "v_exp_f32_e32 v136, v136\n\t" \
  "v_add_f32_e32 v242, v242, v134\n\t" \
  "v_exp_f32_e32 v137, v137\n\t" \
  "v_add_f32_e32 v242, v242, v135\n\t" \
  "s_waitcnt lgkmcnt(7)\n\t" \
  "v_mfma_f32_32x32x16_bf16 v[96:111], v[220:223], v[180:183], v[96:111]\n\t" \
  "ds_read_b128 v[220:223], v250 offset:31744\n\t" \
  "v_cvt_pk_bf16_f32 v188, v136, v137\n\t" \
  "v_exp_f32_e32 v138, v138\n\t" \
  "v_add_f32_e32 v242, v242, v136\n\t" \
  "v_exp_f32_e32 v139, v139\n\t" \
  "v_add_f32_e32 v242, v242, v137\n\t" \
  "s_waitcnt lgkmcnt(7)\n\t" \
  "v_mfma_f32_32x32x16_bf16 v[48:63], v[224:227], v[184:187], v[48:63]\n\t" \
  "v_cvt_pk_bf16_f32 v189, v138, v139\n\t" \
  "v_exp_f32_e32 v140, v140\n\t" \
  "v_add_f32_e32 v242, v242, v138\n\t" \
  "v_exp_f32_e32 v141, v141\n\t" \
  "v_add_f32_e32 v242, v242, v139\n\t" \
  "s_waitcnt lgkmcnt(6)\n\t" \
  "v_mfma_f32_32x32x16_bf16 v[64:79], v[228:231], v[184:187], v[64:79]\n\t" \
  "v_cvt_pk_bf16_f32 v190, v140, v141\n\t" \
  "v_exp_f32_e32 v142, v142\n\t" \
  "v_add_f32_e32 v242, v242, v140\n\t" \
  "v_exp_f32_e32 v143, v143\n\t" \
  "v_add_f32_e32 v242, v242, v141\n\t" \
  "s_waitcnt lgkmcnt(5)\n\t" \
  "v_mfma_f32_32x32x16_bf16 v[80:95], v[232:235], v[184:187], v[80:95]\n\t" \
  "v_cvt_pk_bf16_f32 v191, v142, v143\n\t" \
  "v_add_f32_e32 v242, v242, v142\n\t" \
  "v_add_f32_e32 v242, v242, v143\n\t" \
  "s_waitcnt lgkmcnt(4)\n\t" \
  "v_mfma_f32_32x32x16_bf16 v[96:111], v[236:239], v[184:187], v[96:111]\n\t" \
  "s_waitcnt lgkmcnt(3)\n\t" \
  "v_mfma_f32_32x32x16_bf16 v[48:63], v[208:211], v[188:191], v[48:63]\n\t" \
  "s_waitcnt lgkmcnt(2)\n\t" \
  "v_mfma_f32_32x32x16_bf16 v[64:79], v[212:215], v[188:191], v[64:79]\n\t" \
  "s_waitcnt lgkmcnt(1)\n\t" \
  "v_mfma_f32_32x32x16_bf16 v[80:95], v[216:219], v[188:191], v[80:95]\n\t" \
  "s_waitcnt lgkmcnt(0)\n\t" \
  "v_mfma_f32_32x32x16_bf16 v[96:111], v[220:223], v[188:191], v[96:111]\n\t" \
  "v_add_f32_e32 v241, v241, v242\n\t" \
  "s_branch L_EPI%=\n\t" \
  "LA_DRAIN1%=:\n\t" \
  "s_waitcnt lgkmcnt(0)\n\t" \
  "ds_read_b128 v[224:227], v250 offset:0\n\t" \
  "ds_read_b128 v[228:231], v250 offset:1024\n\t" \
  "ds_read_b128 v[232:235], v250 offset:2048\n\t" \
  "ds_read_b128 v[236:239], v250 offset:3072\n\t" \
  "v_mfma_f32_32x32x16_bf16 v[48:63], v[208:211], v[188:191], v[48:63]\n\t" \
  "ds_read_b128 v[208:211], v250 offset:4096\n\t" \
  "v_cvt_pk_bf16_f32 v176, v144, v145\n\t" \
  "v_cvt_pk_bf16_f32 v177, v146, v147\n\t" \
  "v_exp_f32_e32 v148, v148\n\t" \
  "v_add_f32_e32 v243, v243, v147\n\t" \
  "v_exp_f32_e32 v149, v149\n\t" \
  "v_mfma_f32_32x32x16_bf16 v[64:79], v[212:215], v[188:191], v[64:79]\n\t" \
  "ds_read_b128 v[212:215], v250 offset:5120\n\t" \
  "v_cvt_pk_bf16_f32 v178, v148, v149\n\t" \
  "v_exp_f32_e32 v150, v150\n\t" \
  "v_add_f32_e32 v243, v243, v148\n\t" \
  "v_exp_f32_e32 v151, v151\n\t" \
  "v_add_f32_e32 v243, v243, v149\n\t" \
  "v_mfma_f32_32x32x16_bf16 v[80:95], v[216:219], v[188:191], v[80:95]\n\t" \
  "ds_read_b128 v[216:219], v250 offset:6144\n\t" \
  "v_cvt_pk_bf16_f32 v179, v150, v151\n\t" \
  "v_exp_f32_e32 v152, v152\n\t" \
  "v_add_f32_e32 v243, v243, v150\n\t" \
  "v_exp_f32_e32 v153, v153\n\t" \
  "v_add_f32_e32 v243, v243, v151\n\t" \
  "v_mfma_f32_32x32x16_bf16 v[96:111], v[220:223], v[188:191], v[96:111]\n\t" \
  "ds_read_b128 v[220:223], v250 offset:7168\n\t" \
  "v_cvt_pk_bf16_f32 v180, v152, v153\n\t" \
  "v_exp_f32_e32 v154, v154\n\t" \
  "v_add_f32_e32 v243, v243, v152\n\t" \
  "v_exp_f32_e32 v155, v155\n\t" \
  "v_add_f32_e32 v243, v243, v153\n\t" \
  "v_add_f32_e32 v241, v241, v242\n\t" \
  "s_cmp_lg_u32 s69, 0\n\t" \
  "s_cbranch_scc1 LA_ROD1%=\n\t" \
  "LA_ROBD1%=:\n\t" \
  "s_nop 0\n\t" \
  "s_waitcnt lgkmcnt(7)\n\t" \
  "v_mfma_f32_32x32x16_bf16 v[48:63], v[224:227], v[176:179], v[48:63]\n\t" \
  "ds_read_b128 v[224:227], v250 offset:8192\n\t" \
  "v_cvt_pk_bf16_f32 v181, v154, v155\n\t" \
  "v_exp_f32_e32 v156, v156\n\t" \
  "v_add_f32_e32 v243, v243, v154\n\t" \
  "v_exp_f32_e32 v157, v157\n\t" \
  "v_add_f32_e32 v243, v243, v155\n\t" \
  "s_waitcnt lgkmcnt(7)\n\t" \
  "v_mfma_f32_32x32x16_bf16 v[64:79], v[228:231], v[176:179], v[64:79]\n\t" \
  "ds_read_b128 v[228:231], v250 offset:9216\n\t" \
  "v_cvt_pk_bf16_f32 v182, v156, v157\n\t" \
  "v_exp_f32_e32 v158, v158\n\t" \
  "v_add_f32_e32 v243, v243, v156\n\t" \
  "v_exp_f32_e32 v159, v159\n\t" \
  "v_add_f32_e32 v243, v243, v157\n\t" \
  "s_waitcnt lgkmcnt(7)\n\t" \
  "v_mfma_f32_32x32x16_bf16 v[80:95], v[232:235], v[176:179], v[80:95]\n\t" \
  "ds_read_b128 v[232:235], v250 offset:10240\n\t" \
  "v_cvt_pk_bf16_f32 v183, v158, v159\n\t" \
  "v_exp_f32_e32 v160, v160\n\t" \
  "v_add_f32_e32 v243, v243, v158\n\t" \
  "v_exp_f32_e32 v161, v161\n\t" \
  "v_add_f32_e32 v243, v243, v159\n\t" \
  "s_waitcnt lgkmcnt(7)\n\t" \
  "v_mfma_f32_32x32x16_bf16 v[96:111], v[236:239], v[176:179], v[96:111]\n\t" \
  "ds_read_b128 v[236:239], v250 offset:11264\n\t" \
  "v_cvt_pk_bf16_f32 v184, v160, v161\n\t" \
  "v_exp_f32_e32 v162, v162\n\t" \
  "v_add_f32_e32 v243, v243, v160\n\t" \
  "v_exp_f32_e32 v163, v163\n\t" \
  "v_add_f32_e32 v243, v243, v161\n\t" \
  "s_waitcnt lgkmcnt(7)\n\t" \
  "v_mfma_f32_32x32x16_bf16 v[48:63], v[208:211], v[180:183], v[48:63]\n\t" \
  "ds_read_b128 v[208:211], v250 offset:12288\n\t" \
  "v_cvt_pk_bf16_f32 v185, v162, v163\n\t" \
  "v_exp_f32_e32 v164, v164\n\t" \
  "v_add_f32_e32 v243, v243, v162\n\t" \
  "v_exp_f32_e32 v165, v165\n\t" \
  "v_add_f32_e32 v243, v243, v163\n\t" \
  "s_waitcnt lgkmcnt(7)\n\t" \
  "v_mfma_f32_32x32x16_bf16 v[64:79], v[212:215], v[180:183], v[64:79]\n\t" \
  "ds_read_b128 v[212:215], v250 offset:13312\n\t" \
  "v_cvt_pk_bf16_f32 v186, v164, v165\n\t" \
  "v_exp_f32_e32 v166, v166\n\t" \
  "v_add_f32_e32 v243, v243, v164\n\t" \
  "v_exp_f32_e32 v167, v167\n\t" \
  "v_add_f32_e32 v243, v243, v165\n\t" \
  "s_waitcnt lgkmcnt(7)\n\t" \
  "v_mfma_f32_32x32x16_bf16 v[80:95], v[216:219], v[180:183], v[80:95]\n\t" \
  "ds_read_b128 v[216:219], v250 offset:14336\n\t" \
  "v_cvt_pk_bf16_f32 v187, v166, v167\n\t" \
  "v_exp_f32_e32 v168, v168\n\t" \
  "v_add_f32_e32 v243, v243, v166\n\t" \
  "v_exp_f32_e32 v169, v169\n\t" \
  "v_add_f32_e32 v243, v243, v167\n\t" \
  "s_waitcnt lgkmcnt(7)\n\t" \
  "v_mfma_f32_32x32x16_bf16 v[96:111], v[220:223], v[180:183], v[96:111]\n\t" \
  "ds_read_b128 v[220:223], v250 offset:15360\n\t" \
  "v_cvt_pk_bf16_f32 v188, v168, v169\n\t" \
  "v_exp_f32_e32 v170, v170\n\t" \
  "v_add_f32_e32 v243, v243, v168\n\t" \
  "v_exp_f32_e32 v171, v171\n\t" \
  "v_add_f32_e32 v243, v243, v169\n\t" \
  "s_waitcnt lgkmcnt(7)\n\t" \
  "v_mfma_f32_32x32x16_bf16 v[48:63], v[224:227], v[184:187], v[48:63]\n\t" \
  "v_cvt_pk_bf16_f32 v189, v170, v171\n\t" \
  "v_exp_f32_e32 v172, v172\n\t" \
  "v_add_f32_e32 v243, v243, v170\n\t" \
  "v_exp_f32_e32 v173, v173\n\t" \
  "v_add_f32_e32 v243, v243, v171\n\t" \
  "s_waitcnt lgkmcnt(6)\n\t" \
  "v_mfma_f32_32x32x16_bf16 v[64:79], v[228:231], v[184:187], v[64:79]\n\t" \
  "v_cvt_pk_bf16_f32 v190, v172, v173\n\t" \
  "v_exp_f32_e32 v174, v174\n\t" \
  "v_add_f32_e32 v243, v243, v172\n\t" \
  "v_exp_f32_e32 v175, v175\n\t" \
  "v_add_f32_e32 v243, v243, v173\n\t" \
  "s_waitcnt lgkmcnt(5)\n\t" \
  "v_mfma_f32_32x32x16_bf16 v[80:95], v[232:235], v[184:187], v[80:95]\n\t" \
  "v_cvt_pk_bf16_f32 v191, v174, v175\n\t" \
  "v_add_f32_e32 v243, v243, v174\n\t" \
  "v_add_f32_e32 v243, v243, v175\n\t" \
  "s_waitcnt lgkmcnt(4)\n\t" \
  "v_mfma_f32_32x32x16_bf16 v[96:111], v[236:239], v[184:187], v[96:111]\n\t" \
  "s_waitcnt lgkmcnt(3)\n\t" \
  "v_mfma_f32_32x32x16_bf16 v[48:63], v[208:211], v[188:191], v[48:63]\n\t" \
  "s_waitcnt lgkmcnt(2)\n\t" \
  "v_mfma_f32_32x32x16_bf16 v[64:79], v[212:215], v[188:191], v[64:79]\n\t" \
  "s_waitcnt lgkmcnt(1)\n\t" \
  "v_mfma_f32_32x32x16_bf16 v[80:95], v[216:219], v[188:191], v[80:95]\n\t" \
  "s_waitcnt lgkmcnt(0)\n\t" \
  "v_mfma_f32_32x32x16_bf16 v[96:111], v[220:223], v[188:191], v[96:111]\n\t" \
  "v_add_f32_e32 v241, v241, v243\n\t" \
  "s_branch L_EPI%=\n\t" \
  "LB_PRO%=:\n\t" \
  "s_setprio 1\n\t" \
  "ds_read_b128 v[224:227], v249 offset:0\n\t" \
  "ds_read_b128 v[228:231], v249 offset:1024\n\t" \
  "ds_read_b128 v[232:235], v249 offset:2048\n\t" \
  "ds_read_b128 v[236:239], v249 offset:3072\n\t" \
  "ds_read_b128 v[208:211], v249 offset:4096\n\t" \
  "ds_read_b128 v[212:215], v249 offset:5120\n\t" \
  "ds_read_b128 v[216:219], v249 offset:6144\n\t" \
  "ds_read_b128 v[220:223], v249 offset:7168\n\t" \
  "s_add_u32 m0, s50, 0\n\t" \
  "s_nop 0\n\t" \
  "global_load_lds_dwordx4 %[lin], s[48:49] offset:0 sc1\n\t" \
  "global_load_lds_dwordx4 %[lin], s[48:49] offset:1024 sc1\n\t" \
  "global_load_lds_dwordx4 %[lin], s[48:49] offset:2048 sc1\n\t" \
  "global_load_lds_dwordx4 %[lin], s[48:49] offset:3072 sc1\n\t" \
  "s_add_u32 m0, s80, 0\n\t" \
  "s_add_u32 s64, s64, 0x8000\n\t" \
  "s_addc_u32 s65, s65, 0\n\t" \
  "global_load_lds_dwordx4 %[lin], s[64:65] offset:0 sc1\n\t" \
  "global_load_lds_dwordx4 %[lin], s[64:65] offset:1024 sc1\n\t" \
  "global_load_lds_dwordx4 %[lin], s[64:65] offset:2048 sc1\n\t" \
  "global_load_lds_dwordx4 %[lin], s[64:65] offset:3072 sc1\n\t" \
  "s_add_u32 m0, s80, 4096\n\t" \
  "s_nop 0\n\t" \
  "global_load_lds_dwordx4 v252, s[64:65] offset:0 sc1\n\t" \
  "global_load_lds_dwordx4 v252, s[64:65] offset:1024 sc1\n\t" \
  "global_load_lds_dwordx4 v252, s[64:65] offset:2048 sc1\n\t" \
  "global_load_lds_dwordx4 v252, s[64:65] offset:3072 sc1\n\t" \
  "s_add_u32 m0, s80, 65536\n\t" \
  "s_add_u32 s66, s66, 0x8000\n\t" \
  "s_addc_u32 s67, s67, 0\n\t" \
  "global_load_lds_dwordx4 %[lin], s[66:67] offset:0 sc1\n\t" \
  "global_load_lds_dwordx4 %[lin], s[66:67] offset:1024 sc1\n\t" \
  "global_load_lds_dwordx4 %[lin], s[66:67] offset:2048 sc1\n\t" \
  "global_load_lds_dwordx4 %[lin], s[66:67] offset:3072 sc1\n\t" \
  "s_add_u32 m0, s80, 69632\n\t" \
  "s_nop 0\n\t" \
  "global_load_lds_dwordx4 v252, s[66:67] offset:0 sc1\n\t" \
  "global_load_lds_dwordx4 v252, s[66:67] offset:1024 sc1\n\t" \
  "global_load_lds_dwordx4 v252, s[66:67] offset:2048 sc1\n\t" \
  "global_load_lds_dwordx4 v252, s[66:67] offset:3072 sc1\n\t" \
  "s_xor_b32 s80, s80, 0x8000\n\t" \
  "s_waitcnt lgkmcnt(7)\n\t" \
  "v_mfma_f32_32x32x16_bf16 v[112:127], v[224:227], v[16:19], 0\n\t" \
  "ds_read_b128 v[224:227], v249 offset:8192\n\t" \
  "v_mov_b32_e32 v48, 0\n\t" \
  "v_mov_b32_e32 v49, 0\n\t" \
  "v_mov_b32_e32 v50, 0\n\t" \
  "v_mov_b32_e32 v51, 0\n\t" \
  "s_waitcnt lgkmcnt(7)\n\t" \
  "v_mfma_f32_32x32x16_bf16 v[112:127], v[228:231], v[20:23], v[112:127]\n\t" \
  "ds_read_b128 v[228:231], v249 offset:9216\n\t" \
  "v_mov_b32_e32 v52, 0\n\t" \
  "v_mov_b32_e32 v53, 0\n\t" \
  "v_mov_b32_e32 v54, 0\n\t" \
  "v_mov_b32_e32 v55, 0\n\t" \
  "s_waitcnt lgkmcnt(7)\n\t" \
  "v_mfma_f32_32x32x16_bf16 v[112:127], v[232:235], v[24:27], v[112:127]\n\t" \
  "ds_read_b128 v[232:235], v249 offset:10240\n\t" \
  "v_mov_b32_e32 v56, 0\n\t" \
  "v_mov_b32_e32 v57, 0\n\t" \
  "v_mov_b32_e32 v58, 0\n\t" \
  "v_mov_b32_e32 v59, 0\n\t" \
  "s_waitcnt lgkmcnt(7)\n\t" \
  "v_mfma_f32_32x32x16_bf16 v[112:127], v[236:239], v[28:31], v[112:127]\n\t" \
  "ds_read_b128 v[236:239], v249 offset:11264\n\t" \
  "v_mov_b32_e32 v60, 0\n\t" \
  "v_mov_b32_e32 v61, 0\n\t" \
  "v_mov_b32_e32 v62, 0\n\t" \
  "v_mov_b32_e32 v63, 0\n\t" \
  "s_waitcnt lgkmcnt(7)\n\t" \
  "v_mfma_f32_32x32x16_bf16 v[112:127], v[208:211], v[32:35], v[112:127]\n\t" \
  "ds_read_b128 v[208:211], v249 offset:12288\n\t" \
  "v_mov_b32_e32 v64, 0\n\t" \
  "v_mov_b32_e32 v65, 0\n\t" \
  "v_mov_b32_e32 v66, 0\n\t" \
  "v_mov_b32_e32 v67, 0\n\t" \
  "s_waitcnt lgkmcnt(7)\n\t" \
  "v_mfma_f32_32x32x16_bf16 v[112:127], v[212:215], v[36:39], v[112:127]\n\t" \
  "ds_read_b128 v[212:215], v249 offset:13312\n\t" \
  "v_mov_b32_e32 v68, 0\n\t" \
  "v_mov_b32_e32 v69, 0\n\t" \
  "v_mov_b32_e32 v70, 0\n\t" \
  "v_mov_b32_e32 v71, 0\n\t" \
  "s_waitcnt lgkmcnt(7)\n\t" \
  "v_mfma_f32_32x32x16_bf16 v[112:127], v[216:219], v[40:43], v[112:127]\n\t" \
  "ds_read_b128 v[216:219], v249 offset:14336\n\t" \
  "v_mov_b32_e32 v72, 0\n\t" \
  "v_mov_b32_e32 v73, 0\n\t" \
  "v_mov_b32_e32 v74, 0\n\t" \
  "v_mov_b32_e32 v75, 0\n\t" \
  "s_waitcnt lgkmcnt(7)\n\t" \
  "v_mfma_f32_32x32x16_bf16 v[112:127], v[220:223], v[44:47], v[112:127]\n\t" \
  "ds_read_b128 v[220:223], v249 offset:15360\n\t" \
  "v_mov_b32_e32 v76, 0\n\t" \
  "v_mov_b32_e32 v77, 0\n\t" \
  "v_mov_b32_e32 v78, 0\n\t" \
  "v_mov_b32_e32 v79, 0\n\t" \
  "s_waitcnt lgkmcnt(7)\n\t" \
  "v_mfma_f32_32x32x16_bf16 v[128:143], v[224:227], v[16:19], 0\n\t" \
  "v_mov_b32_e32 v80, 0\n\t" \
  "v_mov_b32_e32 v81, 0\n\t" \
  "v_mov_b32_e32 v82, 0\n\t" \
  "v_mov_b32_e32 v83, 0\n\t" \
  "s_waitcnt lgkmcnt(6)\n\t" \
  "v_mfma_f32_32x32x16_bf16 v[128:143], v[228:231], v[20:23], v[128:143]\n\t" \
  "v_mov_b32_e32 v84, 0\n\t" \
  "v_mov_b32_e32 v85, 0\n\t" \
  "v_mov_b32_e32 v86, 0\n\t" \
  "v_mov_b32_e32 v87, 0\n\t" \
  "s_waitcnt lgkmcnt(5)\n\t" \
  "v_mfma_f32_32x32x16_bf16 v[128:143], v[232:235], v[24:27], v[128:143]\n\t" \
  "v_mov_b32_e32 v88, 0\n\t" \
  "v_mov_b32_e32 v89, 0\n\t" \
  "v_mov_b32_e32 v90, 0\n\t" \
  "v_mov_b32_e32 v91, 0\n\t" \
  "s_waitcnt lgkmcnt(4)\n\t" \
  "v_mfma_f32_32x32x16_bf16 v[128:143], v[236:239], v[28:31], v[128:143]\n\t" \
  "v_mov_b32_e32 v92, 0\n\t" \
  "v_mov_b32_e32 v93, 0\n\t" \
  "v_mov_b32_e32 v94, 0\n\t" \
  "v_mov_b32_e32 v95, 0\n\t" \
  "s_waitcnt lgkmcnt(3)\n\t" \
  "v_mfma_f32_32x32x16_bf16 v[128:143], v[208:211], v[32:35], v[128:143]\n\t" \
  "v_mov_b32_e32 v96, 0\n\t" \
  "v_mov_b32_e32 v97, 0\n\t" \
  "v_mov_b32_e32 v98, 0\n\t" \
  "v_mov_b32_e32 v99, 0\n\t" \
  "s_waitcnt lgkmcnt(2)\n\t" \
  "v_mfma_f32_32x32x16_bf16 v[128:143], v[212:215], v[36:39], v[128:143]\n\t" \
  "v_mov_b32_e32 v100, 0\n\t" \
  "v_mov_b32_e32 v101, 0\n\t" \
  "v_mov_b32_e32 v102, 0\n\t" \
  "v_mov_b32_e32 v103, 0\n\t" \
  "s_waitcnt lgkmcnt(1)\n\t" \
  "v_mfma_f32_32x32x16_bf16 v[128:143], v[216:219], v[40:43], v[128:143]\n\t" \
  "v_mov_b32_e32 v104, 0\n\t" \
  "v_mov_b32_e32 v105, 0\n\t" \
  "v_mov_b32_e32 v106, 0\n\t" \
  "v_mov_b32_e32 v107, 0\n\t" \
  "s_waitcnt lgkmcnt(0)\n\t" \
  "v_mfma_f32_32x32x16_bf16 v[128:143], v[220:223], v[44:47], v[128:143]\n\t" \
  "v_mov_b32_e32 v108, 0\n\t" \
  "v_mov_b32_e32 v109, 0\n\t" \
  "v_mov_b32_e32 v110, 0\n\t" \
  "v_mov_b32_e32 v111, 0\n\t" \
  "ds_read_b128 v[224:227], v249 offset:16384\n\t" \
  "ds_read_b128 v[228:231], v249 offset:17408\n\t" \
  "ds_read_b128 v[232:235], v249 offset:18432\n\t" \
  "ds_read_b128 v[236:239], v249 offset:19456\n\t" \
  "v_mov_b32_e32 v208, 0\n\t" \
  "v_mov_b32_e32 v209, 0\n\t" \
  "v_mov_b32_e32 v210, 0\n\t" \
  "v_mov_b32_e32 v211, 0\n\t" \
  "v_mov_b32_e32 v212, 0\n\t" \
  "v_mov_b32_e32 v213, 0\n\t" \
  "v_mov_b32_e32 v214, 0\n\t" \
  "v_mov_b32_e32 v215, 0\n\t" \
  "v_mov_b32_e32 v216, 0\n\t" \
  "v_mov_b32_e32 v217, 0\n\t" \
  "v_mov_b32_e32 v218, 0\n\t" \
  "v_mov_b32_e32 v219, 0\n\t" \
  "v_mov_b32_e32 v220, 0\n\t" \
  "v_mov_b32_e32 v221, 0\n\t" \
  "v_mov_b32_e32 v222, 0\n\t" \
  "v_mov_b32_e32 v223, 0\n\t" \
  "v_mov_b32_e32 v188, 0\n\t" \
  "v_mov_b32_e32 v189, 0\n\t" \
  "v_mov_b32_e32 v190, 0\n\t" \
  "v_mov_b32_e32 v191, 0\n\t" \
  "s_cmp_eq_u32 s52, 1\n\t" \
  "s_cbranch_scc1 LB_PMASK%=\n\t" \
  "LB_PMASKB%=:\n\t" \
  "v_max_f32_e32 v244, v112, v113\n\t" \
  "v_max3_f32 v244, v244, v114, v115\n\t" \
  "v_max3_f32 v244, v244, v116, v117\n\t" \
  "v_max3_f32 v244, v244, v118, v119\n\t" \
  "v_max3_f32 v244, v244, v120, v121\n\t" \
  "v_max3_f32 v244, v244, v122, v123\n\t" \
  "v_max3_f32 v244, v244, v124, v125\n\t" \
  "v_max3_f32 v244, v244, v126, v127\n\t" \
  "v_max3_f32 v244, v244, v128, v129\n\t" \
  "v_max3_f32 v244, v244, v130, v131\n\t" \
  "v_max3_f32 v244, v244, v132, v133\n\t" \
  "v_max3_f32 v244, v244, v134, v135\n\t" \
  "v_max3_f32 v244, v244, v136, v137\n\t" \
  "v_max3_f32 v244, v244, v138, v139\n\t" \
  "v_max3_f32 v244, v244, v140, v141\n\t" \
  "v_max3_f32 v244, v244, v142, v143\n\t" \
  "v_mov_b32_e32 v246, v244\n\t" \
  "s_nop 1\n\t" \
  "v_permlane32_swap_b32_e32 v244, v246\n\t" \
  "s_nop 0\n\t" \
  "v_max_f32_e32 v240, v244, v246\n\t" \
  "v_sub_f32_e32 v112, v112, v240\n\t" \
  "v_sub_f32_e32 v128, v128, v240\n\t" \
  "v_xor_b32_e32 v192, 0x80000000, v240\n\t" \
  "v_sub_f32_e32 v113, v113, v240\n\t" \
  "v_sub_f32_e32 v129, v129, v240\n\t" \
  "v_xor_b32_e32 v193, 0x80000000, v240\n\t" \
  "v_sub_f32_e32 v114, v114, v240\n\t" \
  "v_sub_f32_e32 v130, v130, v240\n\t" \
  "v_xor_b32_e32 v194, 0x80000000, v240\n\t" \
  "v_sub_f32_e32 v115, v115, v240\n\t" \
  "v_sub_f32_e32 v131, v131, v240\n\t" \
  "v_xor_b32_e32 v195, 0x80000000, v240\n\t" \
  "v_sub_f32_e32 v116, v116, v240\n\t" \
  "v_sub_f32_e32 v132, v132, v240\n\t" \
  "v_xor_b32_e32 v196, 0x80000000, v240\n\t" \
  "v_sub_f32_e32 v117, v117, v240\n\t" \
  "v_sub_f32_e32 v133, v133, v240\n\t" \
  "v_xor_b32_e32 v197, 0x80000000, v240\n\t" \
  "v_sub_f32_e32 v118, v118, v240\n\t" \
  "v_sub_f32_e32 v134, v134, v240\n\t" \
  "v_xor_b32_e32 v198, 0x80000000, v240\n\t" \
  "v_sub_f32_e32 v119, v119, v240\n\t" \
  "v_sub_f32_e32 v135, v135, v240\n\t" \
  "v_xor_b32_e32 v199, 0x80000000, v240\n\t" \
  "v_sub_f32_e32 v120, v120, v240\n\t" \
  "v_sub_f32_e32 v136, v136, v240\n\t" \
  "v_xor_b32_e32 v200, 0x80000000, v240\n\t" \
  "v_sub_f32_e32 v121, v121, v240\n\t" \
  "v_sub_f32_e32 v137, v137, v240\n\t" \
  "v_xor_b32_e32 v201, 0x80000000, v240\n\t" \
  "v_sub_f32_e32 v122, v122, v240\n\t" \
  "v_sub_f32_e32 v138, v138, v240\n\t" \
  "v_xor_b32_e32 v202, 0x80000000, v240\n\t" \
  "v_sub_f32_e32 v123, v123, v240\n\t" \
  "v_sub_f32_e32 v139, v139, v240\n\t" \
  "v_xor_b32_e32 v203, 0x80000000, v240\n\t" \
  "v_sub_f32_e32 v124, v124, v240\n\t" \
  "v_sub_f32_e32 v140, v140, v240\n\t" \
  "v_xor_b32_e32 v204, 0x80000000, v240\n\t" \
  "v_sub_f32_e32 v125, v125, v240\n\t" \
  "v_sub_f32_e32 v141, v141, v240\n\t" \
  "v_xor_b32_e32 v205, 0x80000000, v240\n\t" \
  "v_sub_f32_e32 v126, v126, v240\n\t" \
  "v_sub_f32_e32 v142, v142, v240\n\t" \
  "v_xor_b32_e32 v206, 0x80000000, v240\n\t" \
  "v_sub_f32_e32 v127, v127, v240\n\t" \
  "v_sub_f32_e32 v143, v143, v240\n\t" \
  "v_xor_b32_e32 v207, 0x80000000, v240\n\t" \
  "v_exp_f32_e32 v112, v112\n\t" \
  "v_exp_f32_e32 v113, v113\n\t" \
  "v_exp_f32_e32 v114, v114\n\t" \
  "v_exp_f32_e32 v115, v115\n\t" \
  "v_add_f32_e32 v242, v112, v113\n\t" \
  "v_add_f32_e32 v242, v242, v114\n\t" \
  "s_waitcnt vmcnt(16) lgkmcnt(0)\n\t" \
  "s_barrier\n\t" \
  "s_add_u32 s71, s71, 1\n\t" \
  "s_cmp_eq_u32 s52, 1\n\t" \
  "s_cbranch_scc1 LB_DRAIN0%=\n\t" \
  "s_branch LB_STEP1%=\n\t" \
  "LB_STEP0%=:\n\t" \
  "ds_read_b128 v[224:227], v249 offset:0\n\t" \
  "ds_read_b128 v[228:231], v249 offset:1024\n\t" \
  "ds_read_b128 v[232:235], v249 offset:2048\n\t" \
  "ds_read_b128 v[236:239], v249 offset:3072\n\t" \
  "v_mfma_f32_32x32x16_bf16 v[48:63], v[208:211], v[188:191], v[48:63]\n\t" \
  "ds_read_b128 v[208:211], v249 offset:4096\n\t" \
  "v_exp_f32_e32 v148, v148\n\t" \
  "v_add_f32_e32 v243, v243, v147\n\t" \
  "v_cvt_pk_bf16_f32 v176, v144, v145\n\t" \
  "v_mfma_f32_32x32x16_bf16 v[64:79], v[212:215], v[188:191], v[64:79]\n\t" \
  "ds_read_b128 v[212:215], v249 offset:5120\n\t" \
  "v_exp_f32_e32 v149, v149\n\t" \
  "v_add_f32_e32 v243, v243, v148\n\t" \
  "v_mfma_f32_32x32x16_bf16 v[80:95], v[216:219], v[188:191], v[80:95]\n\t" \
  "ds_read_b128 v[216:219], v249 offset:6144\n\t" \
  "v_exp_f32_e32 v150, v150\n\t" \
  "v_add_f32_e32 v243, v243, v149\n\t" \
  "v_cvt_pk_bf16_f32 v177, v146, v147\n\t" \
  "v_mfma_f32_32x32x16_bf16 v[96:111], v[220:223], v[188:191], v[96:111]\n\t" \
  "ds_read_b128 v[220:223], v249 offset:7168\n\t" \
  "v_exp_f32_e32 v151, v151\n\t" \
  "v_add_f32_e32 v243, v243, v150\n\t" \
  "v_add_f32_e32 v241, v241, v242\n\t" \
  "s_cmp_lg_u32 s69, 0\n\t" \
  "s_cbranch_scc1 LB_ROS0%=\n\t" \
  "LB_ROBS0%=:\n\t" \
  "s_waitcnt lgkmcnt(4)\n\t" \
  "v_mfma_f32_32x32x16_bf16 v[112:127], v[224:227], v[16:19], v[192:207]\n\t" \
  "ds_read_b128 v[224:227], v249 offset:8192\n\t" \
  "v_exp_f32_e32 v152, v152\n\t" \
  "v_add_f32_e32 v243, v243, v151\n\t" \
  "v_cvt_pk_bf16_f32 v178, v148, v149\n\t" \
  "s_add_u32 m0, s80, 0\n\t" \
  "s_add_u32 s64, s64, 0x8000\n\t" \
  "s_addc_u32 s65, s65, 0\n\t" \
  "global_load_lds_dwordx4 %[lin], s[64:65] offset:0 sc1\n\t" \
  "v_mfma_f32_32x32x16_bf16 v[112:127], v[228:231], v[20:23], v[112:127]\n\t" \
  "ds_read_b128 v[228:231], v249 offset:9216\n\t" \
  "v_exp_f32_e32 v153, v153\n\t" \
  "v_add_f32_e32 v243, v243, v152\n\t" \
  "s_add_u32 s68, s68, 1\n\t" \
  "global_load_lds_dwordx4 %[lin], s[64:65] offset:1024 sc1\n\t" \
  "v_mfma_f32_32x32x16_bf16 v[112:127], v[232:235], v[24:27], v[112:127]\n\t" \
  "ds_read_b128 v[232:235], v249 offset:10240\n\t" \
  "v_exp_f32_e32 v154, v154\n\t" \
  "v_add_f32_e32 v243, v243, v153\n\t" \
  "v_cvt_pk_bf16_f32 v179, v150, v151\n\t" \
  "global_load_lds_dwordx4 %[lin], s[64:65] offset:2048 sc1\n\t" \
  "v_mfma_f32_32x32x16_bf16 v[112:127], v[236:239], v[28:31], v[112:127]\n\t" \
  "ds_read_b128 v[236:239], v249 offset:11264\n\t" \
  "v_exp_f32_e32 v155, v155\n\t" \
  "v_add_f32_e32 v243, v243, v154\n\t" \
  "v_cvt_pk_bf16_f32 v180, v152, v153\n\t" \
  "global_load_lds_dwordx4 %[lin], s[64:65] offset:3072 sc1\n\t" \
  "s_waitcnt lgkmcnt(4)\n\t" \
  "v_mfma_f32_32x32x16_bf16 v[112:127], v[208:211], v[32:35], v[112:127]\n\t" \
  "ds_read_b128 v[208:211], v249 offset:12288\n\t" \
  "v_exp_f32_e32 v156, v156\n\t" \
  "v_add_f32_e32 v243, v243, v155\n\t" \
  "s_add_u32 m0, s80, 4096\n\t" \
  "s_nop 0\n\t" \
  "global_load_lds_dwordx4 v252, s[64:65] offset:0 sc1\n\t" \
  "v_mfma_f32_32x32x16_bf16 v[112:127], v[212:215], v[36:39], v[112:127]\n\t" \
  "ds_read_b128 v[212:215], v249 offset:13312\n\t" \
  "v_exp_f32_e32 v157, v157\n\t" \
  "v_add_f32_e32 v243, v243, v156\n\t" \
  "v_cvt_pk_bf16_f32 v181, v154, v155\n\t" \
  "global_load_lds_dwordx4 v252, s[64:65] offset:1024 sc1\n\t" \
  "v_mfma_f32_32x32x16_bf16 v[112:127], v[216:219], v[40:43], v[112:127]\n\t" \
  "ds_read_b128 v[216:219], v249 offset:14336\n\t" \
  "v_exp_f32_e32 v158, v158\n\t" \
  "v_add_f32_e32 v243, v243, v157\n\t" \
  "v_cvt_pk_bf16_f32 v182, v156, v157\n\t" \
  "global_load_lds_dwordx4 v252, s[64:65] offset:2048 sc1\n\t" \
  "v_mfma_f32_32x32x16_bf16 v[112:127], v[220:223], v[44:47], v[112:127]\n\t" \
  "ds_read_b128 v[220:223], v249 offset:15360\n\t" \
  "v_exp_f32_e32 v159, v159\n\t" \
  "v_add_f32_e32 v243, v243, v158\n\t" \
  "global_load_lds_dwordx4 v252, s[64:65] offset:3072 sc1\n\t" \
  "s_waitcnt lgkmcnt(4)\n\t" \
  "v_mfma_f32_32x32x16_bf16 v[128:143], v[224:227], v[16:19], v[192:207]\n\t" \
  "ds_read_b128 v[224:227], v250 offset:0\n\t" \
  "v_exp_f32_e32 v160, v160\n\t" \
  "v_add_f32_e32 v243, v243, v159\n\t" \
  "v_cvt_pk_bf16_f32 v183, v158, v159\n\t" \
  "s_add_u32 m0, s80, 65536\n\t" \
  "s_add_u32 s66, s66, 0x8000\n\t" \
  "s_addc_u32 s67, s67, 0\n\t" \
  "global_load_lds_dwordx4 %[lin], s[66:67] offset:0 sc1\n\t" \
  "v_mfma_f32_32x32x16_bf16 v[128:143], v[228:231], v[20:23], v[128:143]\n\t" \
  "ds_read_b128 v[228:231], v250 offset:1024\n\t" \
  "v_exp_f32_e32 v161, v161\n\t" \
  "v_add_f32_e32 v243, v243, v160\n\t" \
  "global_load_lds_dwordx4 %[lin], s[66:67] offset:1024 sc1\n\t" \
  "s_cmp_eq_u32 s68, s52\n\t" \
  "s_cbranch_scc1 LB_MASK00%=\n\t" \
  "LB_MASKB00%=:\n\t" \
  "v_mfma_f32_32x32x16_bf16 v[128:143], v[232:235], v[24:27], v[128:143]\n\t" \
  "ds_read_b128 v[232:235], v250 offset:2048\n\t" \
  "v_exp_f32_e32 v162, v162\n\t" \
  "v_add_f32_e32 v243, v243, v161\n\t" \
  "v_cvt_pk_bf16_f32 v184, v160, v161\n\t" \
  "global_load_lds_dwordx4 %[lin], s[66:67] offset:2048 sc1\n\t" \
  "v_max_f32_e32 v244, v112, v113\n\t" \
  "v_mfma_f32_32x32x16_bf16 v[128:143], v[236:239], v[28:31], v[128:143]\n\t" \
  "ds_read_b128 v[236:239], v250 offset:3072\n\t" \
  "v_exp_f32_e32 v163, v163\n\t" \
  "v_add_f32_e32 v243, v243, v162\n\t" \
  "global_load_lds_dwordx4 %[lin], s[66:67] offset:3072 sc1\n\t" \
  "v_max3_f32 v244, v244, v114, v115\n\t" \
  "s_waitcnt lgkmcnt(4)\n\t" \
  "v_mfma_f32_32x32x16_bf16 v[128:143], v[208:211], v[32:35], v[128:143]\n\t" \
  "ds_read_b128 v[208:211], v250 offset:4096\n\t" \
  "v_exp_f32_e32 v164, v164\n\t" \
  "v_add_f32_e32 v243, v243, v163\n\t" \
  "s_add_u32 m0, s80, 69632\n\t" \
  "s_nop 0\n\t" \
  "global_load_lds_dwordx4 v252, s[66:67] offset:0 sc1\n\t" \
  "v_max3_f32 v244, v244, v116, v117\n\t" \
  "v_mfma_f32_32x32x16_bf16 v[128:143], v[212:215], v[36:39], v[128:143]\n\t" \
  "ds_read_b128 v[212:215], v250 offset:5120\n\t" \
  "v_exp_f32_e32 v165, v165\n\t" \
  "v_add_f32_e32 v243, v243, v164\n\t" \
  "v_cvt_pk_bf16_f32 v185, v162, v163\n\t" \
  "global_load_lds_dwordx4 v252, s[66:67] offset:1024 sc1\n\t" \
  "v_max3_f32 v244, v244, v118, v119\n\t" \
  "v_mfma_f32_32x32x16_bf16 v[128:143], v[216:219], v[40:43], v[128:143]\n\t" \
  "ds_read_b128 v[216:219], v250 offset:6144\n\t" \
  "v_exp_f32_e32 v166, v166\n\t" \
  "v_add_f32_e32 v243, v243, v165\n\t" \
  "v_cvt_pk_bf16_f32 v186, v164, v165\n\t" \
  "global_load_lds_dwordx4 v252, s[66:67] offset:2048 sc1\n\t" \
  "v_max3_f32 v244, v244, v120, v121\n\t" \
  "v_mfma_f32_32x32x16_bf16 v[128:143], v[220:223], v[44:47], v[128:143]\n\t" \
  "ds_read_b128 v[220:223], v250 offset:7168\n\t" \
  "v_exp_f32_e32 v167, v167\n\t" \
  "v_add_f32_e32 v243, v243, v166\n\t" \
  "global_load_lds_dwordx4 v252, s[66:67] offset:3072 sc1\n\t" \
  "v_max3_f32 v244, v244, v122, v123\n\t" \
  "s_waitcnt lgkmcnt(4)\n\t" \
  "v_mfma_f32_32x32x16_bf16 v[48:63], v[224:227], v[176:179], v[48:63]\n\t" \
  "ds_read_b128 v[224:227], v250 offset:8192\n\t" \
  "v_exp_f32_e32 v168, v168\n\t" \
  "v_add_f32_e32 v243, v243, v167\n\t" \
  "v_cvt_pk_bf16_f32 v187, v166, v167\n\t" \
  "s_xor_b32 s80, s80, 0x8000\n\t" \
  "v_max3_f32 v244, v244, v124, v125\n\t" \
  "v_mfma_f32_32x32x16_bf16 v[64:79], v[228:231], v[176:179], v[64:79]\n\t" \
  "ds_read_b128 v[228:231], v250 offset:9216\n\t" \
  "v_exp_f32_e32 v169, v169\n\t" \
  "v_add_f32_e32 v243, v243, v168\n\t" \
  "s_cmp_eq_u32 s68, s52\n\t" \
  "s_cbranch_scc1 LB_MASK01%=\n\t" \
  "LB_MASKB01%=:\n\t" \
  "v_max3_f32 v244, v244, v126, v127\n\t" \
  "v_mfma_f32_32x32x16_bf16 v[80:95], v[232:235], v[176:179], v[80:95]\n\t" \
  "ds_read_b128 v[232:235], v250 offset:10240\n\t" \
  "v_exp_f32_e32 v170, v170\n\t" \
  "v_add_f32_e32 v243, v243, v169\n\t" \
  "v_max3_f32 v244, v244, v128, v129\n\t" \
  "v_max3_f32 v244, v244, v130, v131\n\t" \
  "v_mfma_f32_32x32x16_bf16 v[96:111], v[236:239], v[176:179], v[96:111]\n\t" \
  "ds_read_b128 v[236:239], v250 offset:11264\n\t" \
  "v_exp_f32_e32 v171, v171\n\t" \
  "v_add_f32_e32 v243, v243, v170\n\t" \
  "v_max3_f32 v244, v244, v132, v133\n\t" \
  "v_max3_f32 v244, v244, v134, v135\n\t" \
  "s_waitcnt lgkmcnt(4)\n\t" \
  "v_mfma_f32_32x32x16_bf16 v[48:63], v[208:211], v[180:183], v[48:63]\n\t" \
  "ds_read_b128 v[208:211], v250 offset:12288\n\t" \
  "v_exp_f32_e32 v172, v172\n\t" \
  "v_add_f32_e32 v243, v243, v171\n\t" \
  "v_max3_f32 v244, v244, v136, v137\n\t" \
  "v_max3_f32 v244, v244, v138, v139\n\t" \
  "v_mfma_f32_32x32x16_bf16 v[64:79], v[212:215], v[180:183], v[64:79]\n\t" \
  "ds_read_b128 v[212:215], v250 offset:13312\n\t" \
  "v_exp_f32_e32 v173, v173\n\t" \
  "v_add_f32_e32 v243, v243, v172\n\t" \
  "v_max3_f32 v244, v244, v140, v141\n\t" \
  "v_max3_f32 v244, v244, v142, v143\n\t" \
  "v_mov_b32_e32 v246, v244\n\t" \
  "v_mfma_f32_32x32x16_bf16 v[80:95], v[216:219], v[180:183], v[80:95]\n\t" \
  "ds_read_b128 v[216:219], v250 offset:14336\n\t" \
  "v_exp_f32_e32 v174, v174\n\t" \
  "v_add_f32_e32 v243, v243, v173\n\t" \
  "v_permlane32_swap_b32_e32 v244, v246\n\t" \
  "s_nop 0\n\t" \
  "v_max_f32_e32 v244, v244, v246\n\t" \
  "v_cmp_lt_f32_e32 vcc, 0x41c00000, v244\n\t" \
  "v_mfma_f32_32x32x16_bf16 v[96:111], v[220:223], v[180:183], v[96:111]\n\t" \
  "ds_read_b128 v[220:223], v250 offset:15360\n\t" \
  "s_cbranch_vccnz LB_RARE0%=\n\t" \
  "LB_RAREB0%=:\n\t" \
  "v_exp_f32_e32 v175, v175\n\t" \
  "v_add_f32_e32 v243, v243, v174\n\t" \
  "s_waitcnt lgkmcnt(4)\n\t" \
  "v_mfma_f32_32x32x16_bf16 v[48:63], v[224:227], v[184:187], v[48:63]\n\t" \
  "ds_read_b128 v[224:227], v249 offset:16384\n\t" \
  "v_exp_f32_e32 v112, v112\n\t" \
  "v_add_f32_e32 v243, v243, v175\n\t" \
  "v_cvt_pk_bf16_f32 v188, v168, v169\n\t" \
  "v_mfma_f32_32x32x16_bf16 v[64:79], v[228:231], v[184:187], v[64:79]\n\t" \
  "ds_read_b128 v[228:231], v249 offset:17408\n\t" \
  "v_exp_f32_e32 v113, v113\n\t" \
  "v_cvt_pk_bf16_f32 v189, v170, v171\n\t" \
  "v_mfma_f32_32x32x16_bf16 v[80:95], v[232:235], v[184:187], v[80:95]\n\t" \
  "ds_read_b128 v[232:235], v249 offset:18432\n\t" \
  "v_exp_f32_e32 v114, v114\n\t" \
  "v_add_f32_e32 v242, v112, v113\n\t" \
  "v_cvt_pk_bf16_f32 v190, v172, v173\n\t" \
  "v_mfma_f32_32x32x16_bf16 v[96:111], v[236:239], v[184:187], v[96:111]\n\t" \
  "ds_read_b128 v[236:239], v249 offset:19456\n\t" \
  "v_exp_f32_e32 v115, v115\n\t" \
  "v_add_f32_e32 v242, v242, v114\n\t" \
  "v_cvt_pk_bf16_f32 v191, v174, v175\n\t" \
  "s_cmp_eq_u32 s68, s52\n\t" \
  "s_cbranch_scc1 LB_DRAIN0%=\n\t" \
  "LB_STEP1%=:\n\t" \
  "s_waitcnt lgkmcnt(4)\n\t" \
  "v_mfma_f32_32x32x16_bf16 v[48:63], v[208:211], v[188:191], v[48:63]\n\t" \
  "ds_read_b128 v[208:211], v249 offset:20480\n\t" \
  "v_exp_f32_e32 v116, v116\n\t" \
  "v_add_f32_e32 v242, v242, v115\n\t" \
  "v_cvt_pk_bf16_f32 v176, v112, v113\n\t" \
  "v_mfma_f32_32x32x16_bf16 v[64:79], v[212:215], v[188:191], v[64:79]\n\t" \
  "ds_read_b128 v[212:215], v249 offset:21504\n\t" \
  "v_exp_f32_e32 v117, v117\n\t" \
  "v_add_f32_e32 v242, v242, v116\n\t" \
  "v_mfma_f32_32x32x16_bf16 v[80:95], v[216:219], v[188:191], v[80:95]\n\t" \
  "ds_read_b128 v[216:219], v249 offset:22528\n\t" \
  "v_exp_f32_e32 v118, v118\n\t" \
  "v_add_f32_e32 v242, v242, v117\n\t" \
  "v_cvt_pk_bf16_f32 v177, v114, v115\n\t" \
  "v_mfma_f32_32x32x16_bf16 v[96:111], v[220:223], v[188:191], v[96:111]\n\t" \
  "ds_read_b128 v[220:223], v249 offset:23552\n\t" \
  "v_exp_f32_e32 v119, v119\n\t" \
  "v_add_f32_e32 v242, v242, v118\n\t" \
  "v_add_f32_e32 v241, v241, v243\n\t" \
  "s_cmp_lg_u32 s69, 0\n\t" \
  "s_cbranch_scc1 LB_ROS1%=\n\t" \
  "LB_ROBS1%=:\n\t" \
  "s_waitcnt lgkmcnt(4)\n\t" \
  "v_mfma_f32_32x32x16_bf16 v[144:159], v[224:227], v[16:19], v[192:207]\n\t" \
  "ds_read_b128 v[224:227], v249 offset:24576\n\t" \
  "v_exp_f32_e32 v120, v120\n\t" \
  "v_add_f32_e32 v242, v242, v119\n\t" \
  "v_cvt_pk_bf16_f32 v178, v116, v117\n\t" \
  "v_mfma_f32_32x32x16_bf16 v[144:159], v[228:231], v[20:23], v[144:159]\n\t" \
  "ds_read_b128 v[228:231], v249 offset:25600\n\t" \
  "v_exp_f32_e32 v121, v121\n\t" \
  "v_add_f32_e32 v242, v242, v120\n\t" \
  "s_add_u32 s68, s68, 1\n\t" \
  "v_mfma_f32_32x32x16_bf16 v[144:159], v[232:235], v[24:27], v[144:159]\n\t" \
  "ds_read_b128 v[232:235], v249 offset:26624\n\t" \
  "v_exp_f32_e32 v122, v122\n\t" \
  "v_add_f32_e32 v242, v242, v121\n\t" \
  "v_cvt_pk_bf16_f32 v179, v118, v119\n\t" \
  "v_mfma_f32_32x32x16_bf16 v[144:159], v[236:239], v[28:31], v[144:159]\n\t" \
  "ds_read_b128 v[236:239], v249 offset:27648\n\t" \
  "v_exp_f32_e32 v123, v123\n\t" \
  "v_add_f32_e32 v242, v242, v122\n\t" \
  "v_cvt_pk_bf16_f32 v180, v120, v121\n\t" \
  "s_waitcnt lgkmcnt(4)\n\t" \
  "v_mfma_f32_32x32x16_bf16 v[144:159], v[208:211], v[32:35], v[144:159]\n\t" \
  "ds_read_b128 v[208:211], v249 offset:28672\n\t" \
  "v_exp_f32_e32 v124, v124\n\t" \
  "v_add_f32_e32 v242, v242, v123\n\t" \
  "v_mfma_f32_32x32x16_bf16 v[144:159], v[212:215], v[36:39], v[144:159]\n\t" \
  "ds_read_b128 v[212:215], v249 offset:29696\n\t" \
  "v_exp_f32_e32 v125, v125\n\t" \
  "v_add_f32_e32 v242, v242, v124\n\t" \
  "v_cvt_pk_bf16_f32 v181, v122, v123\n\t" \
  "v_mfma_f32_32x32x16_bf16 v[144:159], v[216:219], v[40:43], v[144:159]\n\t" \
  "ds_read_b128 v[216:219], v249 offset:30720\n\t" \
  "v_exp_f32_e32 v126, v126\n\t" \
  "v_add_f32_e32 v242, v242, v125\n\t" \
  "v_cvt_pk_bf16_f32 v182, v124, v125\n\t" \
  "v_mfma_f32_32x32x16_bf16 v[144:159], v[220:223], v[44:47], v[144:159]\n\t" \
  "ds_read_b128 v[220:223], v249 offset:31744\n\t" \
  "v_exp_f32_e32 v127, v127\n\t" \
  "v_add_f32_e32 v242, v242, v126\n\t" \
  "s_waitcnt lgkmcnt(4)\n\t" \
  "v_mfma_f32_32x32x16_bf16 v[160:175], v[224:227], v[16:19], v[192:207]\n\t" \
  "ds_read_b128 v[224:227], v250 offset:16384\n\t" \
  "v_exp_f32_e32 v128, v128\n\t" \
  "v_add_f32_e32 v242, v242, v127\n\t" \
  "v_cvt_pk_bf16_f32 v183, v126, v127\n\t" \
  "v_mfma_f32_32x32x16_bf16 v[160:175], v[228:231], v[20:23], v[160:175]\n\t" \
  "ds_read_b128 v[228:231], v250 offset:17408\n\t" \
  "v_exp_f32_e32 v129, v129\n\t" \
  "v_add_f32_e32 v242, v242, v128\n\t" \
  "s_cmp_eq_u32 s68, s52\n\t" \
  "s_cbranch_scc1 LB_MASK10%=\n\t" \
  "LB_MASKB10%=:\n\t" \
  "v_mfma_f32_32x32x16_bf16 v[160:175], v[232:235], v[24:27], v[160:175]\n\t" \
  "ds_read_b128 v[232:235], v250 offset:18432\n\t" \
  "v_exp_f32_e32 v130, v130\n\t" \
  "v_add_f32_e32 v242, v242, v129\n\t" \
  "v_cvt_pk_bf16_f32 v184, v128, v129\n\t" \
  "v_max_f32_e32 v244, v144, v145\n\t" \
  "v_mfma_f32_32x32x16_bf16 v[160:175], v[236:239], v[28:31], v[160:175]\n\t" \
  "ds_read_b128 v[236:239], v250 offset:19456\n\t" \
  "v_exp_f32_e32 v131, v131\n\t" \
  "v_add_f32_e32 v242, v242, v130\n\t" \
  "v_max3_f32 v244, v244, v146, v147\n\t" \
  "s_waitcnt lgkmcnt(4)\n\t" \
  "v_mfma_f32_32x32x16_bf16 v[160:175], v[208:211], v[32:35], v[160:175]\n\t" \
  "ds_read_b128 v[208:211], v250 offset:20480\n\t" \
  "v_exp_f32_e32 v132, v132\n\t" \
  "v_add_f32_e32 v242, v242, v131\n\t" \
  "v_max3_f32 v244, v244, v148, v149\n\t" \
  "v_mfma_f32_32x32x16_bf16 v[160:175], v[212:215], v[36:39], v[160:175]\n\t" \
  "ds_read_b128 v[212:215], v250 offset:21504\n\t" \
  "v_exp_f32_e32 v133, v133\n\t" \
  "v_add_f32_e32 v242, v242, v132\n\t" \
  "v_cvt_pk_bf16_f32 v185, v130, v131\n\t" \
  "v_max3_f32 v244, v244, v150, v151\n\t" \
  "v_mfma_f32_32x32x16_bf16 v[160:175], v[216:219], v[40:43], v[160:175]\n\t" \
  "ds_read_b128 v[216:219], v250 offset:22528\n\t" \
  "v_exp_f32_e32 v134, v134\n\t" \
  "v_add_f32_e32 v242, v242, v133\n\t" \
  "v_cvt_pk_bf16_f32 v186, v132, v133\n\t" \
  "v_max3_f32 v244, v244, v152, v153\n\t" \
  "v_mfma_f32_32x32x16_bf16 v[160:175], v[220:223], v[44:47], v[160:175]\n\t" \
  "ds_read_b128 v[220:223], v250 offset:23552\n\t" \
  "v_exp_f32_e32 v135, v135\n\t" \
  "v_add_f32_e32 v242, v242, v134\n\t" \
  "v_max3_f32 v244, v244, v154, v155\n\t" \
  "s_waitcnt lgkmcnt(4)\n\t" \
  "v_mfma_f32_32x32x16_bf16 v[48:63], v[224:227], v[176:179], v[48:63]\n\t" \
  "ds_read_b128 v[224:227], v250 offset:24576\n\t" \
  "v_exp_f32_e32 v136, v136\n\t" \
  "v_add_f32_e32 v242, v242, v135\n\t" \
  "v_cvt_pk_bf16_f32 v187, v134, v135\n\t" \
  "v_max3_f32 v244, v244, v156, v157\n\t" \
  "v_mfma_f32_32x32x16_bf16 v[64:79], v[228:231], v[176:179], v[64:79]\n\t" \
  "ds_read_b128 v[228:231], v250 offset:25600\n\t" \
  "v_exp_f32_e32 v137, v137\n\t" \
  "v_add_f32_e32 v242, v242, v136\n\t" \
  "s_cmp_eq_u32 s68, s52\n\t" \
  "s_cbranch_scc1 LB_MASK11%=\n\t" \
  "LB_MASKB11%=:\n\t" \
  "v_max3_f32 v244, v244, v158, v159\n\t" \
  "v_mfma_f32_32x32x16_bf16 v[80:95], v[232:235], v[176:179], v[80:95]\n\t" \
  "ds_read_b128 v[232:235], v250 offset:26624\n\t" \
  "v_exp_f32_e32 v138, v138\n\t" \
  "v_add_f32_e32 v242, v242, v137\n\t" \
  "v_max3_f32 v244, v244, v160, v161\n\t" \
  "v_max3_f32 v244, v244, v162, v163\n\t" \
  "v_mfma_f32_32x32x16_bf16 v[96:111], v[236:239], v[176:179], v[96:111]\n\t" \
  "ds_read_b128 v[236:239], v250 offset:27648\n\t" \
  "v_exp_f32_e32 v139, v139\n\t" \
  "v_add_f32_e32 v242, v242, v138\n\t" \
  "v_max3_f32 v244, v244, v164, v165\n\t" \
  "v_max3_f32 v244, v244, v166, v167\n\t" \
  "s_waitcnt lgkmcnt(4)\n\t" \
  "v_mfma_f32_32x32x16_bf16 v[48:63], v[208:211], v[180:183], v[48:63]\n\t" \
  "ds_read_b128 v[208:211], v250 offset:28672\n\t" \
  "v_exp_f32_e32 v140, v140\n\t" \
  "v_add_f32_e32 v242, v242, v139\n\t" \
  "v_max3_f32 v244, v244, v168, v169\n\t" \
  "v_max3_f32 v244, v244, v170, v171\n\t" \
  "v_mfma_f32_32x32x16_bf16 v[64:79], v[212:215], v[180:183], v[64:79]\n\t" \
  "ds_read_b128 v[212:215], v250 offset:29696\n\t" \
  "v_exp_f32_e32 v141, v141\n\t" \
  "v_add_f32_e32 v242, v242, v140\n\t" \
  "v_max3_f32 v244, v244, v172, v173\n\t" \
  "v_max3_f32 v244, v244, v174, v175\n\t" \
  "v_mov_b32_e32 v246, v244\n\t" \
  "v_mfma_f32_32x32x16_bf16 v[80:95], v[216:219], v[180:183], v[80:95]\n\t" \
  "ds_read_b128 v[216:219], v250 offset:30720\n\t" \
  "v_exp_f32_e32 v142, v142\n\t" \
  "v_add_f32_e32 v242, v242, v141\n\t" \
  "v_permlane32_swap_b32_e32 v244, v246\n\t" \
  "s_nop 0\n\t" \
  "v_max_f32_e32 v244, v244, v246\n\t" \
  "v_cmp_lt_f32_e32 vcc, 0x41c00000, v244\n\t" \
  "v_mfma_f32_32x32x16_bf16 v[96:111], v[220:223], v[180:183], v[96:111]\n\t" \
  "ds_read_b128 v[220:223], v250 offset:31744\n\t" \
  "s_cbranch_vccnz LB_RARE1%=\n\t" \
  "LB_RAREB1%=:\n\t" \
  "v_exp_f32_e32 v143, v143\n\t" \
  "v_add_f32_e32 v242, v242, v142\n\t" \
  "s_waitcnt lgkmcnt(4)\n\t" \
  "v_mfma_f32_32x32x16_bf16 v[48:63], v[224:227], v[184:187], v[48:63]\n\t" \
  "v_exp_f32_e32 v144, v144\n\t" \
  "v_add_f32_e32 v242, v242, v143\n\t" \
  "v_cvt_pk_bf16_f32 v188, v136, v137\n\t" \
  "v_mfma_f32_32x32x16_bf16 v[64:79], v[228:231], v[184:187], v[64:79]\n\t" \
  "v_exp_f32_e32 v145, v145\n\t" \
  "v_cvt_pk_bf16_f32 v189, v138, v139\n\t" \
  "v_xor_b32_e32 v249, 0x8000, v249\n\t" \
  "v_mfma_f32_32x32x16_bf16 v[80:95], v[232:235], v[184:187], v[80:95]\n\t" \
  "v_exp_f32_e32 v146, v146\n\t" \
  "v_add_f32_e32 v243, v144, v145\n\t" \
  "v_cvt_pk_bf16_f32 v190, v140, v141\n\t" \
  "v_xor_b32_e32 v250, 0x8000, v250\n\t" \
  "v_mfma_f32_32x32x16_bf16 v[96:111], v[236:239], v[184:187], v[96:111]\n\t" \
  "v_exp_f32_e32 v147, v147\n\t" \
  "v_add_f32_e32 v243, v243, v146\n\t" \
  "v_cvt_pk_bf16_f32 v191, v142, v143\n\t" \
  "s_waitcnt vmcnt(0) lgkmcnt(0)\n\t" \
  "s_barrier\n\t" \
  "s_add_u32 s71, s71, 1\n\t" \
  "s_cmp_eq_u32 s68, s52\n\t" \
  "s_cbranch_scc0 LB_STEP0%=\n\t" \
  "s_branch LB_DRAIN1%=\n\t" \
  "LB_DRAIN0%=:\n\t" \
  "s_waitcnt lgkmcnt(0)\n\t" \
  "ds_read_b128 v[224:227], v250 offset:16384\n\t" \
  "ds_read_b128 v[228:231], v250 offset:17408\n\t" \
  "ds_read_b128 v[232:235], v250 offset:18432\n\t" \
  "ds_read_b128 v[236:239], v250 offset:19456\n\t" \
  "v_mfma_f32_32x32x16_bf16 v[48:63], v[208:211], v[188:191], v[48:63]\n\t" \
  "ds_read_b128 v[208:211], v250 offset:20480\n\t" \
  "v_cvt_pk_bf16_f32 v176, v112, v113\n\t" \
  "v_cvt_pk_bf16_f32 v177, v114, v115\n\t" \
  "v_exp_f32_e32 v116, v116\n\t" \
  "v_add_f32_e32 v242, v242, v115\n\t" \
  "v_exp_f32_e32 v117, v117\n\t" \
  "v_mfma_f32_32x32x16_bf16 v[64:79], v[212:215], v[188:191], v[64:79]\n\t" \
  "ds_read_b128 v[212:215], v250 offset:21504\n\t" \
  "v_cvt_pk_bf16_f32 v178, v116, v117\n\t" \
  "v_exp_f32_e32 v118, v118\n\t" \
  "v_add_f32_e32 v242, v242, v116\n\t" \
  "v_exp_f32_e32 v119, v119\n\t" \
  "v_add_f32_e32 v242, v242, v117\n\t" \
  "v_mfma_f32_32x32x16_bf16 v[80:95], v[216:219], v[188:191], v[80:95]\n\t" \
  "ds_read_b128 v[216:219], v250 offset:22528\n\t" \
  "v_cvt_pk_bf16_f32 v179, v118, v119\n\t" \
  "v_exp_f32_e32 v120, v120\n\t" \
  "v_add_f32_e32 v242, v242, v118\n\t" \
  "v_exp_f32_e32 v121, v121\n\t" \
  "v_add_f32_e32 v242, v242, v119\n\t" \
  "v_mfma_f32_32x32x16_bf16 v[96:111], v[220:223], v[188:191], v[96:111]\n\t" \
  "ds_read_b128 v[220:223], v250 offset:23552\n\t" \
  "v_cvt_pk_bf16_f32 v180, v120, v121\n\t" \
  "v_exp_f32_e32 v122, v122\n\t" \
  "v_add_f32_e32 v242, v242, v120\n\t" \
  "v_exp_f32_e32 v123, v123\n\t" \
  "v_add_f32_e32 v242, v242, v121\n\t" \
  "v_add_f32_e32 v241, v241, v243\n\t" \
  "s_cmp_lg_u32 s69, 0\n\t" \
  "s_cbranch_scc1 LB_ROD0%=\n\t" \
  "LB_ROBD0%=:\n\t" \
  "s_nop 0\n\t" \
  "s_waitcnt lgkmcnt(7)\n\t" \
  "v_mfma_f32_32x32x16_bf16 v[48:63], v[224:227], v[176:179], v[48:63]\n\t" \
  "ds_read_b128 v[224:227], v250 offset:24576\n\t" \
  "v_cvt_pk_bf16_f32 v181, v122, v123\n\t" \
  "v_exp_f32_e32 v124, v124\n\t" \
  "v_add_f32_e32 v242, v242, v122\n\t" \
  "v_exp_f32_e32 v125, v125\n\t" \
  "v_add_f32_e32 v242, v242, v123\n\t" \
  "s_waitcnt lgkmcnt(7)\n\t" \
  "v_mfma_f32_32x32x16_bf16 v[64:79], v[228:231], v[176:179], v[64:79]\n\t" \
  "ds_read_b128 v[228:231], v250 offset:25600\n\t" \
  "v_cvt_pk_bf16_f32 v182, v124, v125\n\t" \
  "v_exp_f32_e32 v126, v126\n\t" \
  "v_add_f32_e32 v242, v242, v124\n\t" \
  "v_exp_f32_e32 v127, v127\n\t" \
  "v_add_f32_e32 v242, v242, v125\n\t" \
  "s_waitcnt lgkmcnt(7)\n\t" \
  "v_mfma_f32_32x32x16_bf16 v[80:95], v[232:235], v[176:179], v[80:95]\n\t" \
  "ds_read_b128 v[232:235], v250 offset:26624\n\t" \
  "v_cvt_pk_bf16_f32 v183, v126, v127\n\t" \
  "v_exp_f32_e32 v128, v128\n\t" \
  "v_add_f32_e32 v242, v242, v126\n\t" \
  "v_exp_f32_e32 v129, v129\n\t" \
  "v_add_f32_e32 v242, v242, v127\n\t" \
  "s_waitcnt lgkmcnt(7)\n\t" \
  "v_mfma_f32_32x32x16_bf16 v[96:111], v[236:239], v[176:179], v[96:111]\n\t" \
  "ds_read_b128 v[236:239], v250 offset:27648\n\t" \
  "v_cvt_pk_bf16_f32 v184, v128, v129\n\t" \
  "v_exp_f32_e32 v130, v130\n\t" \
  "v_add_f32_e32 v242, v242, v128\n\t" \
  "v_exp_f32_e32 v131, v131\n\t" \
  "v_add_f32_e32 v242, v242, v129\n\t" \
  "s_waitcnt lgkmcnt(7)\n\t" \
  "v_mfma_f32_32x32x16_bf16 v[48:63], v[208:211], v[180:183], v[48:63]\n\t" \
  "ds_read_b128 v[208:211], v250 offset:28672\n\t" \
  "v_cvt_pk_bf16_f32 v185, v130, v131\n\t" \
  "v_exp_f32_e32 v132, v132\n\t" \
  "v_add_f32_e32 v242, v242, v130\n\t" \
  "v_exp_f32_e32 v133, v133\n\t" \
  "v_add_f32_e32 v242, v242, v131\n\t" \
  "s_waitcnt lgkmcnt(7)\n\t" \
  "v_mfma_f32_32x32x16_bf16 v[64:79], v[212:215], v[180:183], v[64:79]\n\t" \
  "ds_read_b128 v[212:215], v250 offset:29696\n\t" \
  "v_cvt_pk_bf16_f32 v186, v132, v133\n\t" \
  "v_exp_f32_e32 v134, v134\n\t" \
  "v_add_f32_e32 v242, v242, v132\n\t" \
  "v_exp_f32_e32 v135, v135\n\t" \
  "v_add_f32_e32 v242, v242, v133\n\t" \
  "s_waitcnt lgkmcnt(7)\n\t" \
  "v_mfma_f32_32x32x16_bf16 v[80:95], v[216:219], v[180:183], v[80:95]\n\t" \
  "ds_read_b128 v[216:219], v250 offset:30720\n\t" \
  "v_cvt_pk_bf16_f32 v187, v134, v135\n\t" \
  "v_exp_f32_e32 v136, v136\n\t" \
  "v_add_f32_e32 v242, v242, v134\n\t" \
  "v_exp_f32_e32 v137, v137\n\t" \
  "v_add_f32_e32 v242, v242, v135\n\t" \
  "s_waitcnt lgkmcnt(7)\n\t" \
  "v_mfma_f32_32x32x16_bf16 v[96:111], v[220:223], v[180:183], v[96:111]\n\t" \
  "ds_read_b128 v[220:223], v250 offset:31744\n\t" \
  "v_cvt_pk_bf16_f32 v188, v136, v137\n\t" \
  "v_exp_f32_e32 v138, v138\n\t" \
  "v_add_f32_e32 v242, v242, v136\n\t" \
  "v_exp_f32_e32 v139, v139\n\t" \
  "v_add_f32_e32 v242, v242, v137\n\t" \
  "s_waitcnt lgkmcnt(7)\n\t" \
  "v_mfma_f32_32x32x16_bf16 v[48:63], v[224:227], v[184:187], v[48:63]\n\t" \
  "v_cvt_pk_bf16_f32 v189, v138, v139\n\t" \
  "v_exp_f32_e32 v140, v140\n\t" \
  "v_add_f32_e32 v242, v242, v138\n\t" \
  "v_exp_f32_e32 v141, v141\n\t" \
  "v_add_f32_e32 v242, v242, v139\n\t" \
  "s_waitcnt lgkmcnt(6)\n\t" \
  "v_mfma_f32_32x32x16_bf16 v[64:79], v[228:231], v[184:187], v[64:79]\n\t" \
  "v_cvt_pk_bf16_f32 v190, v140, v141\n\t" \
  "v_exp_f32_e32 v142, v142\n\t" \
  "v_add_f32_e32 v242, v242, v140\n\t" \
  "v_exp_f32_e32 v143, v143\n\t" \
  "v_add_f32_e32 v242, v242, v141\n\t" \
  "s_waitcnt lgkmcnt(5)\n\t" \
  "v_mfma_f32_32x32x16_bf16 v[80:95], v[232:235], v[184:187], v[80:95]\n\t" \
  "v_cvt_pk_bf16_f32 v191, v142, v143\n\t" \
  "v_add_f32_e32 v242, v242, v142\n\t" \
  "v_add_f32_e32 v242, v242, v143\n\t" \
  "s_waitcnt lgkmcnt(4)\n\t" \
  "v_mfma_f32_32x32x16_bf16 v[96:111], v[236:239], v[184:187], v[96:111]\n\t" \
  "s_waitcnt lgkmcnt(3)\n\t" \
  "v_mfma_f32_32x32x16_bf16 v[48:63], v[208:211], v[188:191], v[48:63]\n\t" \
  "s_waitcnt lgkmcnt(2)\n\t" \
  "v_mfma_f32_32x32x16_bf16 v[64:79], v[212:215], v[188:191], v[64:79]\n\t" \
  "s_waitcnt lgkmcnt(1)\n\t" \
  "v_mfma_f32_32x32x16_bf16 v[80:95], v[216:219], v[188:191], v[80:95]\n\t" \
  "s_waitcnt lgkmcnt(0)\n\t" \
  "v_mfma_f32_32x32x16_bf16 v[96:111], v[220:223], v[188:191], v[96:111]\n\t" \
  "v_add_f32_e32 v241, v241, v242\n\t" \
  "s_branch L_EPI%=\n\t" \
  "LB_DRAIN1%=:\n\t" \
  "s_waitcnt lgkmcnt(0)\n\t" \
  "ds_read_b128 v[224:227], v250 offset:0\n\t" \
  "ds_read_b128 v[228:231], v250 offset:1024\n\t" \
  "ds_read_b128 v[232:235], v250 offset:2048\n\t" \
  "ds_read_b128 v[236:239], v250 offset:3072\n\t" \
  "s_add_u32 m0, s80, 0\n\t" \
  "s_add_u32 s64, s64, 0x8000\n\t" \
  "s_addc_u32 s65, s65, 0\n\t" \
  "global_load_lds_dwordx4 %[lin], s[64:65] offset:0 sc1\n\t" \
  "global_load_lds_dwordx4 %[lin], s[64:65] offset:1024 sc1\n\t" \
  "global_load_lds_dwordx4 %[lin], s[64:65] offset:2048 sc1\n\t" \
  "global_load_lds_dwordx4 %[lin], s[64:65] offset:3072 sc1\n\t" \
  "s_add_u32 m0, s80, 4096\n\t" \
  "s_nop 0\n\t" \
  "global_load_lds_dwordx4 v252, s[64:65] offset:0 sc1\n\t" \
  "global_load_lds_dwordx4 v252, s[64:65] offset:1024 sc1\n\t" \
  "global_load_lds_dwordx4 v252, s[64:65] offset:2048 sc1\n\t" \
  "global_load_lds_dwordx4 v252, s[64:65] offset:3072 sc1\n\t" \
  "s_add_u32 m0, s80, 65536\n\t" \
  "s_add_u32 s66, s66, 0x8000\n\t" \
  "s_addc_u32 s67, s67, 0\n\t" \
  "global_load_lds_dwordx4 %[lin], s[66:67] offset:0 sc1\n\t" \
  "global_load_lds_dwordx4 %[lin], s[66:67] offset:1024 sc1\n\t" \
  "global_load_lds_dwordx4 %[lin], s[66:67] offset:2048 sc1\n\t" \
  "global_load_lds_dwordx4 %[lin], s[66:67] offset:3072 sc1\n\t" \
  "s_add_u32 m0, s80, 69632\n\t" \
  "s_nop 0\n\t" \
  "global_load_lds_dwordx4 v252, s[66:67] offset:0 sc1\n\t" \
  "global_load_lds_dwordx4 v252, s[66:67] offset:1024 sc1\n\t" \
  "global_load_lds_dwordx4 v252, s[66:67] offset:2048 sc1\n\t" \
  "global_load_lds_dwordx4 v252, s[66:67] offset:3072 sc1\n\t" \
  "s_xor_b32 s80, s80, 0x8000\n\t" \
  "v_mfma_f32_32x32x16_bf16 v[48:63], v[208:211], v[188:191], v[48:63]\n\t" \
  "ds_read_b128 v[208:211], v250 offset:4096\n\t" \
  "v_cvt_pk_bf16_f32 v176, v144, v145\n\t" \
  "v_cvt_pk_bf16_f32 v177, v146, v147\n\t" \
  "v_exp_f32_e32 v148, v148\n\t" \
  "v_add_f32_e32 v243, v243, v147\n\t" \
  "v_exp_f32_e32 v149, v149\n\t" \
  "v_mfma_f32_32x32x16_bf16 v[64:79], v[212:215], v[188:191], v[64:79]\n\t" \
  "ds_read_b128 v[212:215], v250 offset:5120\n\t" \
  "v_cvt_pk_bf16_f32 v178, v148, v149\n\t" \
  "v_exp_f32_e32 v150, v150\n\t" \
  "v_add_f32_e32 v243, v243, v148\n\t" \
  "v_exp_f32_e32 v151, v151\n\t" \
  "v_add_f32_e32 v243, v243, v149\n\t" \
  "v_mfma_f32_32x32x16_bf16 v[80:95], v[216:219], v[188:191], v[80:95]\n\t" \
  "ds_read_b128 v[216:219], v250 offset:6144\n\t" \
  "v_cvt_pk_bf16_f32 v179, v150, v151\n\t" \
  "v_exp_f32_e32 v152, v152\n\t" \
  "v_add_f32_e32 v243, v243, v150\n\t" \
  "v_exp_f32_e32 v153, v153\n\t" \
  "v_add_f32_e32 v243, v243, v151\n\t" \
  "v_mfma_f32_32x32x16_bf16 v[96:111], v[220:223], v[188:191], v[96:111]\n\t" \
  "ds_read_b128 v[220:223], v250 offset:7168\n\t" \
  "v_cvt_pk_bf16_f32 v180, v152, v153\n\t" \
  "v_exp_f32_e32 v154, v154\n\t" \
  "v_add_f32_e32 v243, v243, v152\n\t" \
  "v_exp_f32_e32 v155, v155\n\t" \
  "v_add_f32_e32 v243, v243, v153\n\t" \
  "v_add_f32_e32 v241, v241, v242\n\t" \
  "s_cmp_lg_u32 s69, 0\n\t" \
  "s_cbranch_scc1 LB_ROD1%=\n\t" \
  "LB_ROBD1%=:\n\t" \
  "s_nop 0\n\t" \
  "s_waitcnt lgkmcnt(7)\n\t" \
  "v_mfma_f32_32x32x16_bf16 v[48:63], v[224:227], v[176:179], v[48:63]\n\t" \
  "ds_read_b128 v[224:227], v250 offset:8192\n\t" \
  "v_cvt_pk_bf16_f32 v181, v154, v155\n\t" \
  "v_exp_f32_e32 v156, v156\n\t" \
  "v_add_f32_e32 v243, v243, v154\n\t" \
  "v_exp_f32_e32 v157, v157\n\t" \
  "v_add_f32_e32 v243, v243, v155\n\t" \
  "s_waitcnt lgkmcnt(7)\n\t" \
  "v_mfma_f32_32x32x16_bf16 v[64:79], v[228:231], v[176:179], v[64:79]\n\t" \
  "ds_read_b128 v[228:231], v250 offset:9216\n\t" \
  "v_cvt_pk_bf16_f32 v182, v156, v157\n\t" \
  "v_exp_f32_e32 v158, v158\n\t" \
  "v_add_f32_e32 v243, v243, v156\n\t" \
  "v_exp_f32_e32 v159, v159\n\t" \
  "v_add_f32_e32 v243, v243, v157\n\t" \
  "s_waitcnt lgkmcnt(7)\n\t" \
  "v_mfma_f32_32x32x16_bf16 v[80:95], v[232:235], v[176:179], v[80:95]\n\t" \
  "ds_read_b128 v[232:235], v250 offset:10240\n\t" \
  "v_cvt_pk_bf16_f32 v183, v158, v159\n\t" \
  "v_exp_f32_e32 v160, v160\n\t" \
  "v_add_f32_e32 v243, v243, v158\n\t" \
  "v_exp_f32_e32 v161, v161\n\t" \
  "v_add_f32_e32 v243, v243, v159\n\t" \
  "s_waitcnt lgkmcnt(7)\n\t" \
  "v_mfma_f32_32x32x16_bf16 v[96:111], v[236:239], v[176:179], v[96:111]\n\t" \
  "ds_read_b128 v[236:239], v250 offset:11264\n\t" \
  "v_cvt_pk_bf16_f32 v184, v160, v161\n\t" \
  "v_exp_f32_e32 v162, v162\n\t" \
  "v_add_f32_e32 v243, v243, v160\n\t" \
  "v_exp_f32_e32 v163, v163\n\t" \
  "v_add_f32_e32 v243, v243, v161\n\t" \
  "s_waitcnt lgkmcnt(7)\n\t" \
  "v_mfma_f32_32x32x16_bf16 v[48:63], v[208:211], v[180:183], v[48:63]\n\t" \
  "ds_read_b128 v[208:211], v250 offset:12288\n\t" \
  "v_cvt_pk_bf16_f32 v185, v162, v163\n\t" \
  "v_exp_f32_e32 v164, v164\n\t" \
  "v_add_f32_e32 v243, v243, v162\n\t" \
  "v_exp_f32_e32 v165, v165\n\t" \
  "v_add_f32_e32 v243, v243, v163\n\t" \
  "s_waitcnt lgkmcnt(7)\n\t" \
  "v_mfma_f32_32x32x16_bf16 v[64:79], v[212:215], v[180:183], v[64:79]\n\t" \
  "ds_read_b128 v[212:215], v250 offset:13312\n\t" \
  "v_cvt_pk_bf16_f32 v186, v164, v165\n\t" \
  "v_exp_f32_e32 v166, v166\n\t" \
  "v_add_f32_e32 v243, v243, v164\n\t" \
  "v_exp_f32_e32 v167, v167\n\t" \
  "v_add_f32_e32 v243, v243, v165\n\t" \
  "s_waitcnt lgkmcnt(7)\n\t" \
  "v_mfma_f32_32x32x16_bf16 v[80:95], v[216:219], v[180:183], v[80:95]\n\t" \
  "ds_read_b128 v[216:219], v250 offset:14336\n\t" \
  "v_cvt_pk_bf16_f32 v187, v166, v167\n\t" \
  "v_exp_f32_e32 v168, v168\n\t" \
  "v_add_f32_e32 v243, v243, v166\n\t" \
  "v_exp_f32_e32 v169, v169\n\t" \
  "v_add_f32_e32 v243, v243, v167\n\t" \
  "s_waitcnt lgkmcnt(7)\n\t" \
  "v_mfma_f32_32x32x16_bf16 v[96:111], v[220:223], v[180:183], v[96:111]\n\t" \
  "ds_read_b128 v[220:223], v250 offset:15360\n\t" \
  "v_cvt_pk_bf16_f32 v188, v168, v169\n\t" \
  "v_exp_f32_e32 v170, v170\n\t" \
  "v_add_f32_e32 v243, v243, v168\n\t" \
  "v_exp_f32_e32 v171, v171\n\t" \
  "v_add_f32_e32 v243, v243, v169\n\t" \
  "s_waitcnt lgkmcnt(7)\n\t" \
  "v_mfma_f32_32x32x16_bf16 v[48:63], v[224:227], v[184:187], v[48:63]\n\t" \
  "v_cvt_pk_bf16_f32 v189, v170, v171\n\t" \
  "v_exp_f32_e32 v172, v172\n\t" \
  "v_add_f32_e32 v243, v243, v170\n\t" \
  "v_exp_f32_e32 v173, v173\n\t" \
  "v_add_f32_e32 v243, v243, v171\n\t" \
  "s_waitcnt lgkmcnt(6)\n\t" \
  "v_mfma_f32_32x32x16_bf16 v[64:79], v[228:231], v[184:187], v[64:79]\n\t" \
  "v_cvt_pk_bf16_f32 v190, v172, v173\n\t" \
  "v_exp_f32_e32 v174, v174\n\t" \
  "v_add_f32_e32 v243, v243, v172\n\t" \
  "v_exp_f32_e32 v175, v175\n\t" \
  "v_add_f32_e32 v243, v243, v173\n\t" \
  "s_waitcnt lgkmcnt(5)\n\t" \
  "v_mfma_f32_32x32x16_bf16 v[80:95], v[232:235], v[184:187], v[80:95]\n\t" \
  "v_cvt_pk_bf16_f32 v191, v174, v175\n\t" \
  "v_add_f32_e32 v243, v243, v174\n\t" \
  "v_add_f32_e32 v243, v243, v175\n\t" \
  "s_waitcnt lgkmcnt(4)\n\t" \
  "v_mfma_f32_32x32x16_bf16 v[96:111], v[236:239], v[184:187], v[96:111]\n\t" \
  "s_waitcnt lgkmcnt(3)\n\t" \
  "v_mfma_f32_32x32x16_bf16 v[48:63], v[208:211], v[188:191], v[48:63]\n\t" \
  "s_waitcnt lgkmcnt(2)\n\t" \
  "v_mfma_f32_32x32x16_bf16 v[64:79], v[212:215], v[188:191], v[64:79]\n\t" \
  "s_waitcnt lgkmcnt(1)\n\t" \
  "v_mfma_f32_32x32x16_bf16 v[80:95], v[216:219], v[188:191], v[80:95]\n\t" \
  "s_waitcnt lgkmcnt(0)\n\t" \
  "v_mfma_f32_32x32x16_bf16 v[96:111], v[220:223], v[188:191], v[96:111]\n\t" \
  "v_add_f32_e32 v241, v241, v243\n\t" \
  "s_branch L_EPI%=\n\t" \
  "L_EPI%=:\n\t" \
  "s_cmp_ge_u32 s71, s51\n\t" \
  "s_cbranch_scc1 L_EPI2%=\n\t" \
  "s_waitcnt vmcnt(0)\n\t" \
  "s_barrier\n\t" \
  "s_add_u32 s71, s71, 1\n\t" \
  "s_cmp_ge_u32 s71, s51\n\t" \
  "s_cbranch_scc1 L_EPI2%=\n\t" \
  "s_add_u32 m0, s80, 0\n\t" \
  "s_add_u32 s64, s64, 0x8000\n\t" \
  "s_addc_u32 s65, s65, 0\n\t" \
  "global_load_lds_dwordx4 %[lin], s[64:65] offset:0 sc1\n\t" \
  "global_load_lds_dwordx4 %[lin], s[64:65] offset:1024 sc1\n\t" \
  "global_load_lds_dwordx4 %[lin], s[64:65] offset:2048 sc1\n\t" \
  "global_load_lds_dwordx4 %[lin], s[64:65] offset:3072 sc1\n\t" \
  "s_add_u32 m0, s80, 4096\n\t" \
  "s_nop 0\n\t" \
  "global_load_lds_dwordx4 v252, s[64:65] offset:0 sc1\n\t" \
  "global_load_lds_dwordx4 v252, s[64:65] offset:1024 sc1\n\t" \
  "global_load_lds_dwordx4 v252, s[64:65] offset:2048 sc1\n\t" \
  "global_load_lds_dwordx4 v252, s[64:65] offset:3072 sc1\n\t" \
  "s_add_u32 m0, s80, 65536\n\t" \
  "s_add_u32 s66, s66, 0x8000\n\t" \
  "s_addc_u32 s67, s67, 0\n\t" \
  "global_load_lds_dwordx4 %[lin], s[66:67] offset:0 sc1\n\t" \
  "global_load_lds_dwordx4 %[lin], s[66:67] offset:1024 sc1\n\t" \
  "global_load_lds_dwordx4 %[lin], s[66:67] offset:2048 sc1\n\t" \
  "global_load_lds_dwordx4 %[lin], s[66:67] offset:3072 sc1\n\t" \
  "s_add_u32 m0, s80, 69632\n\t" \
  "s_nop 0\n\t" \
  "global_load_lds_dwordx4 v252, s[66:67] offset:0 sc1\n\t" \
  "global_load_lds_dwordx4 v252, s[66:67] offset:1024 sc1\n\t" \
  "global_load_lds_dwordx4 v252, s[66:67] offset:2048 sc1\n\t" \
  "global_load_lds_dwordx4 v252, s[66:67] offset:3072 sc1\n\t" \
  "s_xor_b32 s80, s80, 0x8000\n\t" \
  "L_EPI2%=:\n\t" \
  "v_mov_b32_e32 v246, v241\n\t" \
  "s_nop 1\n\t" \
  "v_permlane32_swap_b32_e32 v241, v246\n\t" \
  "s_nop 0\n\t" \
  "v_add_f32_e32 v241, v241, v246\n\t" \
  "v_rcp_f32_e32 v241, v241\n\t" \
  "s_nop 5\n\t" \
  "v_readlane_b32 s72, %[prm], 12\n\t" \
  "v_readlane_b32 s73, %[prm], 13\n\t" \
  "v_mul_f32_e32 v48, v241, v48\n\t" \
  "v_mul_f32_e32 v49, v241, v49\n\t" \
  "v_mul_f32_e32 v50, v241, v50\n\t" \
  "v_mul_f32_e32 v51, v241, v51\n\t" \
  "v_mul_f32_e32 v52, v241, v52\n\t" \
  "v_mul_f32_e32 v53, v241, v53\n\t" \
  "v_mul_f32_e32 v54, v241, v54\n\t" \
  "v_mul_f32_e32 v55, v241, v55\n\t" \
  "v_mul_f32_e32 v56, v241, v56\n\t" \
  "v_mul_f32_e32 v57, v241, v57\n\t" \
  "v_mul_f32_e32 v58, v241, v58\n\t" \
  "v_mul_f32_e32 v59, v241, v59\n\t" \
  "v_mul_f32_e32 v60, v241, v60\n\t" \
  "v_mul_f32_e32 v61, v241, v61\n\t" \
  "v_mul_f32_e32 v62, v241, v62\n\t" \
  "v_mul_f32_e32 v63, v241, v63\n\t" \
  "v_cvt_pk_bf16_f32 v208, v48, v49\n\t" \
  "v_cvt_pk_bf16_f32 v209, v50, v51\n\t" \
  "v_cvt_pk_bf16_f32 v210, v52, v53\n\t" \
  "v_cvt_pk_bf16_f32 v211, v54, v55\n\t" \
  "global_store_dwordx4 %[lin], v[208:211], s[72:73] offset:0 sc1\n\t" \
  "v_cvt_pk_bf16_f32 v212, v56, v57\n\t" \
  "v_cvt_pk_bf16_f32 v213, v58, v59\n\t" \
  "v_cvt_pk_bf16_f32 v214, v60, v61\n\t" \
  "v_cvt_pk_bf16_f32 v215, v62, v63\n\t" \
  "global_store_dwordx4 %[lin], v[212:215], s[72:73] offset:1024 sc1\n\t" \
  "v_mul_f32_e32 v64, v241, v64\n\t" \
  "v_mul_f32_e32 v65, v241, v65\n\t" \
  "v_mul_f32_e32 v66, v241, v66\n\t" \
  "v_mul_f32_e32 v67, v241, v67\n\t" \
  "v_mul_f32_e32 v68, v241, v68\n\t" \
  "v_mul_f32_e32 v69, v241, v69\n\t" \
  "v_mul_f32_e32 v70, v241, v70\n\t" \
  "v_mul_f32_e32 v71, v241, v71\n\t" \
  "v_mul_f32_e32 v72, v241, v72\n\t" \
  "v_mul_f32_e32 v73, v241, v73\n\t" \
  "v_mul_f32_e32 v74, v241, v74\n\t" \
  "v_mul_f32_e32 v75, v241, v75\n\t" \
  "v_mul_f32_e32 v76, v241, v76\n\t" \
  "v_mul_f32_e32 v77, v241, v77\n\t" \
  "v_mul_f32_e32 v78, v241, v78\n\t" \
  "v_mul_f32_e32 v79, v241, v79\n\t" \
  "v_cvt_pk_bf16_f32 v216, v64, v65\n\t" \
  "v_cvt_pk_bf16_f32 v217, v66, v67\n\t" \
  "v_cvt_pk_bf16_f32 v218, v68, v69\n\t" \
  "v_cvt_pk_bf16_f32 v219, v70, v71\n\t" \
  "global_store_dwordx4 %[lin], v[216:219], s[72:73] offset:2048 sc1\n\t" \
  "v_cvt_pk_bf16_f32 v220, v72, v73\n\t" \
  "v_cvt_pk_bf16_f32 v221, v74, v75\n\t" \
  "v_cvt_pk_bf16_f32 v222, v76, v77\n\t" \
  "v_cvt_pk_bf16_f32 v223, v78, v79\n\t" \
  "global_store_dwordx4 %[lin], v[220:223], s[72:73] offset:3072 sc1\n\t" \
  "v_mul_f32_e32 v80, v241, v80\n\t" \
  "v_mul_f32_e32 v81, v241, v81\n\t" \
  "v_mul_f32_e32 v82, v241, v82\n\t" \
  "v_mul_f32_e32 v83, v241, v83\n\t" \
  "v_mul_f32_e32 v84, v241, v84\n\t" \
  "v_mul_f32_e32 v85, v241, v85\n\t" \
  "v_mul_f32_e32 v86, v241, v86\n\t" \
  "v_mul_f32_e32 v87, v241, v87\n\t" \
  "v_mul_f32_e32 v88, v241, v88\n\t" \
  "v_mul_f32_e32 v89, v241, v89\n\t" \
  "v_mul_f32_e32 v90, v241, v90\n\t" \
  "v_mul_f32_e32 v91, v241, v91\n\t" \
  "v_mul_f32_e32 v92, v241, v92\n\t" \
  "v_mul_f32_e32 v93, v241, v93\n\t" \
  "v_mul_f32_e32 v94, v241, v94\n\t" \
  "v_mul_f32_e32 v95, v241, v95\n\t" \
  "s_add_u32 s72, s72, 4096\n\t" \
  "s_addc_u32 s73, s73, 0\n\t" \
  "v_cvt_pk_bf16_f32 v224, v80, v81\n\t" \
  "v_cvt_pk_bf16_f32 v225, v82, v83\n\t" \
  "v_cvt_pk_bf16_f32 v226, v84, v85\n\t" \
  "v_cvt_pk_bf16_f32 v227, v86, v87\n\t" \
  "global_store_dwordx4 %[lin], v[224:227], s[72:73] offset:0 sc1\n\t" \
  "v_cvt_pk_bf16_f32 v228, v88, v89\n\t" \
  "v_cvt_pk_bf16_f32 v229, v90, v91\n\t" \
  "v_cvt_pk_bf16_f32 v230, v92, v93\n\t" \
  "v_cvt_pk_bf16_f32 v231, v94, v95\n\t" \
  "global_store_dwordx4 %[lin], v[228:231], s[72:73] offset:1024 sc1\n\t" \
  "v_mul_f32_e32 v96, v241, v96\n\t" \
  "v_mul_f32_e32 v97, v241, v97\n\t" \
  "v_mul_f32_e32 v98, v241, v98\n\t" \
  "v_mul_f32_e32 v99, v241, v99\n\t" \
  "v_mul_f32_e32 v100, v241, v100\n\t" \
  "v_mul_f32_e32 v101, v241, v101\n\t" \
  "v_mul_f32_e32 v102, v241, v102\n\t" \
  "v_mul_f32_e32 v103, v241, v103\n\t" \
  "v_mul_f32_e32 v104, v241, v104\n\t" \
  "v_mul_f32_e32 v105, v241, v105\n\t" \
  "v_mul_f32_e32 v106, v241, v106\n\t" \
  "v_mul_f32_e32 v107, v241, v107\n\t" \
  "v_mul_f32_e32 v108, v241, v108\n\t" \
  "v_mul_f32_e32 v109, v241, v109\n\t" \
  "v_mul_f32_e32 v110, v241, v110\n\t" \
  "v_mul_f32_e32 v111, v241, v111\n\t" \
  "v_cvt_pk_bf16_f32 v232, v96, v97\n\t" \
  "v_cvt_pk_bf16_f32 v233, v98, v99\n\t" \
  "v_cvt_pk_bf16_f32 v234, v100, v101\n\t" \
  "v_cvt_pk_bf16_f32 v235, v102, v103\n\t" \
  "global_store_dwordx4 %[lin], v[232:235], s[72:73] offset:2048 sc1\n\t" \
  "v_cvt_pk_bf16_f32 v236, v104, v105\n\t" \
  "v_cvt_pk_bf16_f32 v237, v106, v107\n\t" \
  "v_cvt_pk_bf16_f32 v238, v108, v109\n\t" \
  "v_cvt_pk_bf16_f32 v239, v110, v111\n\t" \
  "global_store_dwordx4 %[lin], v[236:239], s[72:73] offset:3072 sc1\n\t" \
  "s_waitcnt vmcnt(8)\n\t" \
  "s_mov_b32 s70, 999\n\t" \
  "s_cmp_eq_u32 s53, 0\n\t" \
  "s_cbranch_scc1 L_REST%=\n\t" \
  "v_readlane_b32 s72, %[prm], 14\n\t" \
  "v_readlane_b32 s73, %[prm], 15\n\t" \
  "v_mov_b32_e32 v176, -1\n\t" \
  "v_mov_b32_e32 v177, -1\n\t" \
  "v_mov_b32_e32 v178, -1\n\t" \
  "v_mov_b32_e32 v179, -1\n\t" \
  "s_nop 1\n\t" \
  "global_store_dwordx4 %[lin], v[176:179], s[72:73] offset:0 sc1\n\t" \
  "global_store_dwordx4 %[lin], v[176:179], s[72:73] offset:1024 sc1\n\t" \
  "global_store_dwordx4 %[lin], v[176:179], s[72:73] offset:2048 sc1\n\t" \
  "global_store_dwordx4 %[lin], v[176:179], s[72:73] offset:3072 sc1\n\t" \
  "s_add_u32 s72, s72, 4096\n\t" \
  "s_addc_u32 s73, s73, 0\n\t" \
  "global_store_dwordx4 %[lin], v[176:179], s[72:73] offset:0 sc1\n\t" \
  "global_store_dwordx4 %[lin], v[176:179], s[72:73] offset:1024 sc1\n\t" \
  "global_store_dwordx4 %[lin], v[176:179], s[72:73] offset:2048 sc1\n\t" \
  "global_store_dwordx4 %[lin], v[176:179], s[72:73] offset:3072 sc1\n\t" \
  "s_add_u32 s72, s72, 4096\n\t" \
  "s_addc_u32 s73, s73, 0\n\t" \
  "global_store_dwordx4 %[lin], v[176:179], s[72:73] offset:0 sc1\n\t" \
  "global_store_dwordx4 %[lin], v[176:179], s[72:73] offset:1024 sc1\n\t" \
  "v_readlane_b32 s70, %[prm], 20\n\t" \
  "s_nop 0\n\t" \
  "L_REST%=:\n\t" \
  "s_cmp_lt_u32 s71, s70\n\t" \
  "s_cbranch_scc1 L_NORT%=\n\t" \
  "s_mov_b32 s70, 999\n\t" \
  "v_readlane_b32 s72, %[prm], 16\n\t" \
  "v_readlane_b32 s73, %[prm], 17\n\t" \
  "v_readlane_b32 s74, %[prm], 18\n\t" \
  "v_readlane_b32 s75, %[prm], 19\n\t" \
  "v_readlane_b32 s55, %[prm], 21\n\t" \
  "v_readlane_b32 s56, %[prm], 22\n\t" \
  "v_lshrrev_b32_e32 v244, 7, %[lin]\n\t" \
  "v_and_b32_e32 v245, 0x70, %[lin]\n\t" \
  "v_mad_u32_u24 v246, v244, s55, v245\n\t" \
  "v_bfe_u32 v245, %[lin], 5, 2\n\t" \
  "v_and_b32_e32 v247, 16, %[lin]\n\t" \
  "v_lshl_add_u32 v247, v244, 5, v247\n\t" \
  "v_mad_u32_u24 v247, v245, s56, v247\n\t" \
  "s_lshl_b32 s55, s55, 3\n\t" \
  "global_load_dwordx4 v[48:51], v246, s[72:73]\n\t" \
  "s_add_u32 s72, s72, s55\n\t" \
  "s_addc_u32 s73, s73, 0\n\t" \
  "global_load_dwordx4 v[52:55], v246, s[72:73]\n\t" \
  "s_add_u32 s72, s72, s55\n\t" \
  "s_addc_u32 s73, s73, 0\n\t" \
  "global_load_dwordx4 v[56:59], v246, s[72:73]\n\t" \
  "s_add_u32 s72, s72, s55\n\t" \
  "s_addc_u32 s73, s73, 0\n\t" \
  "global_load_dwordx4 v[60:63], v246, s[72:73]\n\t" \
  "s_add_u32 s72, s72, s55\n\t" \
  "s_addc_u32 s73, s73, 0\n\t" \
  "global_load_dwordx4 v[64:67], v246, s[72:73]\n\t" \
  "s_add_u32 s72, s72, s55\n\t" \
  "s_addc_u32 s73, s73, 0\n\t" \
  "global_load_dwordx4 v[68:71], v246, s[72:73]\n\t" \
  "s_add_u32 s72, s72, s55\n\t" \
  "s_addc_u32 s73, s73, 0\n\t" \
  "global_load_dwordx4 v[72:75], v246, s[72:73]\n\t" \
  "s_add_u32 s72, s72, s55\n\t" \
  "s_addc_u32 s73, s73, 0\n\t" \
  "global_load_dwordx4 v[76:79], v246, s[72:73]\n\t" \
  "s_add_u32 s72, s72, s55\n\t" \
  "s_addc_u32 s73, s73, 0\n\t" \
  "global_load_dwordx4 v[80:83], v246, s[72:73]\n\t" \
  "s_add_u32 s72, s72, s55\n\t" \
  "s_addc_u32 s73, s73, 0\n\t" \
  "global_load_dwordx4 v[84:87], v246, s[72:73]\n\t" \
  "s_add_u32 s72, s72, s55\n\t" \
  "s_addc_u32 s73, s73, 0\n\t" \
  "s_waitcnt vmcnt(9)\n\t" \
  "global_store_dwordx4 v247, v[48:51], s[74:75] offset:0\n\t" \
  "s_waitcnt vmcnt(9)\n\t" \
  "global_store_dwordx4 v247, v[52:55], s[74:75] offset:256\n\t" \
  "s_waitcnt vmcnt(9)\n\t" \
  "global_store_dwordx4 v247, v[56:59], s[74:75] offset:512\n\t" \
  "s_waitcnt vmcnt(9)\n\t" \
  "global_store_dwordx4 v247, v[60:63], s[74:75] offset:768\n\t" \
  "s_waitcnt vmcnt(9)\n\t" \
  "global_store_dwordx4 v247, v[64:67], s[74:75] offset:1024\n\t" \
  "s_waitcnt vmcnt(9)\n\t" \
  "global_store_dwordx4 v247, v[68:71], s[74:75] offset:1280\n\t" \
  "s_waitcnt vmcnt(9)\n\t" \
  "global_store_dwordx4 v247, v[72:75], s[74:75] offset:1536\n\t" \
  "s_waitcnt vmcnt(9)\n\t" \
  "global_store_dwordx4 v247, v[76:79], s[74:75] offset:1792\n\t" \
  "s_waitcnt vmcnt(9)\n\t" \
  "global_store_dwordx4 v247, v[80:83], s[74:75] offset:2048\n\t" \
  "s_waitcnt vmcnt(9)\n\t" \
  "global_store_dwordx4 v247, v[84:87], s[74:75] offset:2304\n\t" \
  "L_NORT%=:\n\t" \
  "s_cmp_ge_u32 s71, s51\n\t" \
  "s_cbranch_scc1 L_END%=\n\t" \
  "s_barrier\n\t" \
  "s_add_u32 s71, s71, 1\n\t" \
  "s_cmp_ge_u32 s71, s51\n\t" \
  "s_cbranch_scc1 L_END%=\n\t" \
  "s_add_u32 m0, s80, 0\n\t" \
  "s_add_u32 s64, s64, 0x8000\n\t" \
  "s_addc_u32 s65, s65, 0\n\t" \
  "global_load_lds_dwordx4 %[lin], s[64:65] offset:0 sc1\n\t" \
  "global_load_lds_dwordx4 %[lin], s[64:65] offset:1024 sc1\n\t" \
  "global_load_lds_dwordx4 %[lin], s[64:65] offset:2048 sc1\n\t" \
  "global_load_lds_dwordx4 %[lin], s[64:65] offset:3072 sc1\n\t" \
  "s_add_u32 m0, s80, 4096\n\t" \
  "s_nop 0\n\t" \
  "global_load_lds_dwordx4 v252, s[64:65] offset:0 sc1\n\t" \
  "global_load_lds_dwordx4 v252, s[64:65] offset:1024 sc1\n\t" \
  "global_load_lds_dwordx4 v252, s[64:65] offset:2048 sc1\n\t" \
  "global_load_lds_dwordx4 v252, s[64:65] offset:3072 sc1\n\t" \
  "s_add_u32 m0, s80, 65536\n\t" \
  "s_add_u32 s66, s66, 0x8000\n\t" \
  "s_addc_u32 s67, s67, 0\n\t" \
  "global_load_lds_dwordx4 %[lin], s[66:67] offset:0 sc1\n\t" \
  "global_load_lds_dwordx4 %[lin], s[66:67] offset:1024 sc1\n\t" \
  "global_load_lds_dwordx4 %[lin], s[66:67] offset:2048 sc1\n\t" \
  "global_load_lds_dwordx4 %[lin], s[66:67] offset:3072 sc1\n\t" \
  "s_add_u32 m0, s80, 69632\n\t" \
  "s_nop 0\n\t" \
  "global_load_lds_dwordx4 v252, s[66:67] offset:0 sc1\n\t" \
  "global_load_lds_dwordx4 v252, s[66:67] offset:1024 sc1\n\t" \
  "global_load_lds_dwordx4 v252, s[66:67] offset:2048 sc1\n\t" \
  "global_load_lds_dwordx4 v252, s[66:67] offset:3072 sc1\n\t" \
  "s_xor_b32 s80, s80, 0x8000\n\t" \
  "s_waitcnt vmcnt(0)\n\t" \
  "s_branch L_REST%=\n\t" \
  "LA_PMASK%=:\n\t" \
  "v_cmp_gt_i32_e64 s[72:73], 0, %[dq]\n\t" \
  "v_cmp_gt_i32_e64 s[74:75], 1, %[dq]\n\t" \
  "v_cmp_gt_i32_e64 s[76:77], 2, %[dq]\n\t" \
  "v_cmp_gt_i32_e64 s[78:79], 3, %[dq]\n\t" \
  "v_cndmask_b32_e64 v112, v112, v248, s[72:73]\n\t" \
  "v_cmp_gt_i32_e64 s[72:73], 16, %[dq]\n\t" \
  "v_cndmask_b32_e64 v113, v113, v248, s[74:75]\n\t" \
  "v_cmp_gt_i32_e64 s[74:75], 17, %[dq]\n\t" \
  "v_cndmask_b32_e64 v114, v114, v248, s[76:77]\n\t" \
  "v_cmp_gt_i32_e64 s[76:77], 18, %[dq]\n\t" \
  "v_cndmask_b32_e64 v115, v115, v248, s[78:79]\n\t" \
  "v_cmp_gt_i32_e64 s[78:79], 19, %[dq]\n\t" \
  "v_cndmask_b32_e64 v116, v116, v248, s[72:73]\n\t" \
  "v_cmp_gt_i32_e64 s[72:73], 8, %[dq]\n\t" \
  "v_cndmask_b32_e64 v117, v117, v248, s[74:75]\n\t" \
  "v_cmp_gt_i32_e64 s[74:75], 9, %[dq]\n\t" \
  "v_cndmask_b32_e64 v118, v118, v248, s[76:77]\n\t" \
  "v_cmp_gt_i32_e64 s[76:77], 10, %[dq]\n\t" \
  "v_cndmask_b32_e64 v119, v119, v248, s[78:79]\n\t" \
  "v_cmp_gt_i32_e64 s[78:79], 11, %[dq]\n\t" \
  "v_cndmask_b32_e64 v120, v120, v248, s[72:73]\n\t" \
  "v_cmp_gt_i32_e64 s[72:73], 24, %[dq]\n\t" \
  "v_cndmask_b32_e64 v121, v121, v248, s[74:75]\n\t" \
  "v_cmp_gt_i32_e64 s[74:75], 25, %[dq]\n\t" \
  "v_cndmask_b32_e64 v122, v122, v248, s[76:77]\n\t" \
  "v_cmp_gt_i32_e64 s[76:77], 26, %[dq]\n\t" \
  "v_cndmask_b32_e64 v123, v123, v248, s[78:79]\n\t" \
  "v_cmp_gt_i32_e64 s[78:79], 27, %[dq]\n\t" \
  "v_cndmask_b32_e64 v124, v124, v248, s[72:73]\n\t" \
  "v_cndmask_b32_e64 v125, v125, v248, s[74:75]\n\t" \
  "v_cndmask_b32_e64 v126, v126, v248, s[76:77]\n\t" \
  "v_cndmask_b32_e64 v127, v127, v248, s[78:79]\n\t" \
  "v_cmp_gt_i32_e64 s[72:73], 32, %[dq]\n\t" \
  "v_cmp_gt_i32_e64 s[74:75], 33, %[dq]\n\t" \
  "v_cmp_gt_i32_e64 s[76:77], 34, %[dq]\n\t" \
  "v_cmp_gt_i32_e64 s[78:79], 35, %[dq]\n\t" \
  "v_cndmask_b32_e64 v128, v128, v248, s[72:73]\n\t" \
  "v_cmp_gt_i32_e64 s[72:73], 48, %[dq]\n\t" \
  "v_cndmask_b32_e64 v129, v129, v248, s[74:75]\n\t" \
  "v_cmp_gt_i32_e64 s[74:75], 49, %[dq]\n\t" \
  "v_cndmask_b32_e64 v130, v130, v248, s[76:77]\n\t" \
  "v_cmp_gt_i32_e64 s[76:77], 50, %[dq]\n\t" \
  "v_cndmask_b32_e64 v131, v131, v248, s[78:79]\n\t" \
  "v_cmp_gt_i32_e64 s[78:79], 51, %[dq]\n\t" \
  "v_cndmask_b32_e64 v132, v132, v248, s[72:73]\n\t" \
  "v_cmp_gt_i32_e64 s[72:73], 40, %[dq]\n\t" \
  "v_cndmask_b32_e64 v133, v133, v248, s[74:75]\n\t" \
  "v_cmp_gt_i32_e64 s[74:75], 41, %[dq]\n\t" \
  "v_cndmask_b32_e64 v134, v134, v248, s[76:77]\n\t" \
  "v_cmp_gt_i32_e64 s[76:77], 42, %[dq]\n\t" \
  "v_cndmask_b32_e64 v135, v135, v248, s[78:79]\n\t" \
  "v_cmp_gt_i32_e64 s[78:79], 43, %[dq]\n\t" \
  "v_cndmask_b32_e64 v136, v136, v248, s[72:73]\n\t" \
  "v_cmp_gt_i32_e64 s[72:73], 56, %[dq]\n\t" \
  "v_cndmask_b32_e64 v137, v137, v248, s[74:75]\n\t" \
  "v_cmp_gt_i32_e64 s[74:75], 57, %[dq]\n\t" \
  "v_cndmask_b32_e64 v138, v138, v248, s[76:77]\n\t" \
  "v_cmp_gt_i32_e64 s[76:77], 58, %[dq]\n\t" \
  "v_cndmask_b32_e64 v139, v139, v248, s[78:79]\n\t" \
  "v_cmp_gt_i32_e64 s[78:79], 59, %[dq]\n\t" \
  "v_cndmask_b32_e64 v140, v140, v248, s[72:73]\n\t" \
  "v_cndmask_b32_e64 v141, v141, v248, s[74:75]\n\t" \
  "v_cndmask_b32_e64 v142, v142, v248, s[76:77]\n\t" \
  "v_cndmask_b32_e64 v143, v143, v248, s[78:79]\n\t" \
  "s_nop 0\n\t" \
  "s_branch LA_PMASKB%=\n\t" \
  "LA_ROS0%=:\n\t" \
  "s_nop 11\n\t" \
  "v_mul_f32_e32 v48, v247, v48\n\t" \
  "v_mul_f32_e32 v49, v247, v49\n\t" \
  "v_mul_f32_e32 v50, v247, v50\n\t" \
  "v_mul_f32_e32 v51, v247, v51\n\t" \
  "v_mul_f32_e32 v52, v247, v52\n\t" \
  "v_mul_f32_e32 v53, v247, v53\n\t" \
  "v_mul_f32_e32 v54, v247, v54\n\t" \
  "v_mul_f32_e32 v55, v247, v55\n\t" \
  "v_mul_f32_e32 v56, v247, v56\n\t" \
  "v_mul_f32_e32 v57, v247, v57\n\t" \
  "v_mul_f32_e32 v58, v247, v58\n\t" \
  "v_mul_f32_e32 v59, v247, v59\n\t" \
  "v_mul_f32_e32 v60, v247, v60\n\t" \
  "v_mul_f32_e32 v61, v247, v61\n\t" \
  "v_mul_f32_e32 v62, v247, v62\n\t" \
  "v_mul_f32_e32 v63, v247, v63\n\t" \
  "v_mul_f32_e32 v64, v247, v64\n\t" \
  "v_mul_f32_e32 v65, v247, v65\n\t" \
  "v_mul_f32_e32 v66, v247, v66\n\t" \
  "v_mul_f32_e32 v67, v247, v67\n\t" \
  "v_mul_f32_e32 v68, v247, v68\n\t" \
  "v_mul_f32_e32 v69, v247, v69\n\t" \
  "v_mul_f32_e32 v70, v247, v70\n\t" \
  "v_mul_f32_e32 v71, v247, v71\n\t" \
  "v_mul_f32_e32 v72, v247, v72\n\t" \
  "v_mul_f32_e32 v73, v247, v73\n\t" \
  "v_mul_f32_e32 v74, v247, v74\n\t" \
  "v_mul_f32_e32 v75, v247, v75\n\t" \
  "v_mul_f32_e32 v76, v247, v76\n\t" \
  "v_mul_f32_e32 v77, v247, v77\n\t" \
  "v_mul_f32_e32 v78, v247, v78\n\t" \
  "v_mul_f32_e32 v79, v247, v79\n\t" \
  "v_mul_f32_e32 v80, v247, v80\n\t" \
  "v_mul_f32_e32 v81, v247, v81\n\t" \
  "v_mul_f32_e32 v82, v247, v82\n\t" \
  "v_mul_f32_e32 v83, v247, v83\n\t" \
  "v_mul_f32_e32 v84, v247, v84\n\t" \
  "v_mul_f32_e32 v85, v247, v85\n\t" \
  "v_mul_f32_e32 v86, v247, v86\n\t" \
  "v_mul_f32_e32 v87, v247, v87\n\t" \
  "v_mul_f32_e32 v88, v247, v88\n\t" \
  "v_mul_f32_e32 v89, v247, v89\n\t" \
  "v_mul_f32_e32 v90, v247, v90\n\t" \
  "v_mul_f32_e32 v91, v247, v91\n\t" \
  "v_mul_f32_e32 v92, v247, v92\n\t" \
  "v_mul_f32_e32 v93, v247, v93\n\t" \
  "v_mul_f32_e32 v94, v247, v94\n\t" \
  "v_mul_f32_e32 v95, v247, v95\n\t" \
  "v_mul_f32_e32 v96, v247, v96\n\t" \
  "v_mul_f32_e32 v97, v247, v97\n\t" \
  "v_mul_f32_e32 v98, v247, v98\n\t" \
  "v_mul_f32_e32 v99, v247, v99\n\t" \
  "v_mul_f32_e32 v100, v247, v100\n\t" \
  "v_mul_f32_e32 v101, v247, v101\n\t" \
  "v_mul_f32_e32 v102, v247, v102\n\t" \
  "v_mul_f32_e32 v103, v247, v103\n\t" \
  "v_mul_f32_e32 v104, v247, v104\n\t" \
  "v_mul_f32_e32 v105, v247, v105\n\t" \
  "v_mul_f32_e32 v106, v247, v106\n\t" \
  "v_mul_f32_e32 v107, v247, v107\n\t" \
  "v_mul_f32_e32 v108, v247, v108\n\t" \
  "v_mul_f32_e32 v109, v247, v109\n\t" \
  "v_mul_f32_e32 v110, v247, v110\n\t" \
  "v_mul_f32_e32 v111, v247, v111\n\t" \
  "v_mul_f32_e32 v241, v247, v241\n\t" \
  "s_mov_b32 s69, 0\n\t" \
  "s_nop 1\n\t" \
  "s_branch LA_ROBS0%=\n\t" \
  "LA_MASK00%=:\n\t" \
  "s_nop 3\n\t" \
  "v_cmp_gt_i32_e64 s[72:73], 0, %[dq]\n\t" \
  "v_cmp_gt_i32_e64 s[74:75], 1, %[dq]\n\t" \
  "v_cmp_gt_i32_e64 s[76:77], 2, %[dq]\n\t" \
  "v_cmp_gt_i32_e64 s[78:79], 3, %[dq]\n\t" \
  "v_cndmask_b32_e64 v112, v112, v248, s[72:73]\n\t" \
  "v_cmp_gt_i32_e64 s[72:73], 16, %[dq]\n\t" \
  "v_cndmask_b32_e64 v113, v113, v248, s[74:75]\n\t" \
  "v_cmp_gt_i32_e64 s[74:75], 17, %[dq]\n\t" \
  "v_cndmask_b32_e64 v114, v114, v248, s[76:77]\n\t" \
  "v_cmp_gt_i32_e64 s[76:77], 18, %[dq]\n\t" \
  "v_cndmask_b32_e64 v115, v115, v248, s[78:79]\n\t" \
  "v_cmp_gt_i32_e64 s[78:79], 19, %[dq]\n\t" \
  "v_cndmask_b32_e64 v116, v116, v248, s[72:73]\n\t" \
  "v_cmp_gt_i32_e64 s[72:73], 8, %[dq]\n\t" \
  "v_cndmask_b32_e64 v117, v117, v248, s[74:75]\n\t" \
  "v_cmp_gt_i32_e64 s[74:75], 9, %[dq]\n\t" \
  "v_cndmask_b32_e64 v118, v118, v248, s[76:77]\n\t" \
  "v_cmp_gt_i32_e64 s[76:77], 10, %[dq]\n\t" \
  "v_cndmask_b32_e64 v119, v119, v248, s[78:79]\n\t" \
  "v_cmp_gt_i32_e64 s[78:79], 11, %[dq]\n\t" \
  "v_cndmask_b32_e64 v120, v120, v248, s[72:73]\n\t" \
  "v_cmp_gt_i32_e64 s[72:73], 24, %[dq]\n\t" \
  "v_cndmask_b32_e64 v121, v121, v248, s[74:75]\n\t" \
  "v_cmp_gt_i32_e64 s[74:75], 25, %[dq]\n\t" \
  "v_cndmask_b32_e64 v122, v122, v248, s[76:77]\n\t" \
  "v_cmp_gt_i32_e64 s[76:77], 26, %[dq]\n\t" \
  "v_cndmask_b32_e64 v123, v123, v248, s[78:79]\n\t" \
  "v_cmp_gt_i32_e64 s[78:79], 27, %[dq]\n\t" \
  "v_cndmask_b32_e64 v124, v124, v248, s[72:73]\n\t" \
  "v_cndmask_b32_e64 v125, v125, v248, s[74:75]\n\t" \
  "v_cndmask_b32_e64 v126, v126, v248, s[76:77]\n\t" \
  "v_cndmask_b32_e64 v127, v127, v248, s[78:79]\n\t" \
  "s_nop 0\n\t" \
  "s_branch LA_MASKB00%=\n\t" \
  "LA_MASK01%=:\n\t" \
  "s_nop 3\n\t" \
  "v_cmp_gt_i32_e64 s[72:73], 32, %[dq]\n\t" \
  "v_cmp_gt_i32_e64 s[74:75], 33, %[dq]\n\t" \
  "v_cmp_gt_i32_e64 s[76:77], 34, %[dq]\n\t" \
  "v_cmp_gt_i32_e64 s[78:79], 35, %[dq]\n\t" \
  "v_cndmask_b32_e64 v128, v128, v248, s[72:73]\n\t" \
  "v_cmp_gt_i32_e64 s[72:73], 48, %[dq]\n\t" \
  "v_cndmask_b32_e64 v129, v129, v248, s[74:75]\n\t" \
  "v_cmp_gt_i32_e64 s[74:75], 49, %[dq]\n\t" \
  "v_cndmask_b32_e64 v130, v130, v248, s[76:77]\n\t" \
  "v_cmp_gt_i32_e64 s[76:77], 50, %[dq]\n\t" \
  "v_cndmask_b32_e64 v131, v131, v248, s[78:79]\n\t" \
  "v_cmp_gt_i32_e64 s[78:79], 51, %[dq]\n\t" \
  "v_cndmask_b32_e64 v132, v132, v248, s[72:73]\n\t" \
  "v_cmp_gt_i32_e64 s[72:73], 40, %[dq]\n\t" \
  "v_cndmask_b32_e64 v133, v133, v248, s[74:75]\n\t" \
  "v_cmp_gt_i32_e64 s[74:75], 41, %[dq]\n\t" \
  "v_cndmask_b32_e64 v134, v134, v248, s[76:77]\n\t" \
  "v_cmp_gt_i32_e64 s[76:77], 42, %[dq]\n\t" \
  "v_cndmask_b32_e64 v135, v135, v248, s[78:79]\n\t" \
  "v_cmp_gt_i32_e64 s[78:79], 43, %[dq]\n\t" \
  "v_cndmask_b32_e64 v136, v136, v248, s[72:73]\n\t" \
  "v_cmp_gt_i32_e64 s[72:73], 56, %[dq]\n\t" \
  "v_cndmask_b32_e64 v137, v137, v248, s[74:75]\n\t" \
  "v_cmp_gt_i32_e64 s[74:75], 57, %[dq]\n\t" \
  "v_cndmask_b32_e64 v138, v138, v248, s[76:77]\n\t" \
  "v_cmp_gt_i32_e64 s[76:77], 58, %[dq]\n\t" \
  "v_cndmask_b32_e64 v139, v139, v248, s[78:79]\n\t" \
  "v_cmp_gt_i32_e64 s[78:79], 59, %[dq]\n\t" \
  "v_cndmask_b32_e64 v140, v140, v248, s[72:73]\n\t" \
  "v_cndmask_b32_e64 v141, v141, v248, s[74:75]\n\t" \
  "v_cndmask_b32_e64 v142, v142, v248, s[76:77]\n\t" \
  "v_cndmask_b32_e64 v143, v143, v248, s[78:79]\n\t" \
  "s_nop 0\n\t" \
  "s_branch LA_MASKB01%=\n\t" \
  "LA_RARE0%=:\n\t" \
  "v_max_f32_e32 v246, 0, v244\n\t" \
  "v_add_f32_e32 v240, v240, v246\n\t" \
  "v_exp_f32_e64 v247, -v246\n\t" \
  "v_sub_f32_e32 v112, v112, v246\n\t" \
  "v_sub_f32_e32 v128, v128, v246\n\t" \
  "v_xor_b32_e32 v192, 0x80000000, v240\n\t" \
  "v_sub_f32_e32 v113, v113, v246\n\t" \
  "v_sub_f32_e32 v129, v129, v246\n\t" \
  "v_xor_b32_e32 v193, 0x80000000, v240\n\t" \
  "v_sub_f32_e32 v114, v114, v246\n\t" \
  "v_sub_f32_e32 v130, v130, v246\n\t" \
  "v_xor_b32_e32 v194, 0x80000000, v240\n\t" \
  "v_sub_f32_e32 v115, v115, v246\n\t" \
  "v_sub_f32_e32 v131, v131, v246\n\t" \
  "v_xor_b32_e32 v195, 0x80000000, v240\n\t" \
  "v_sub_f32_e32 v116, v116, v246\n\t" \
  "v_sub_f32_e32 v132, v132, v246\n\t" \
  "v_xor_b32_e32 v196, 0x80000000, v240\n\t" \
  "v_sub_f32_e32 v117, v117, v246\n\t" \
  "v_sub_f32_e32 v133, v133, v246\n\t" \
  "v_xor_b32_e32 v197, 0x80000000, v240\n\t" \
  "v_sub_f32_e32 v118, v118, v246\n\t" \
  "v_sub_f32_e32 v134, v134, v246\n\t" \
  "v_xor_b32_e32 v198, 0x80000000, v240\n\t" \
  "v_sub_f32_e32 v119, v119, v246\n\t" \
  "v_sub_f32_e32 v135, v135, v246\n\t" \
  "v_xor_b32_e32 v199, 0x80000000, v240\n\t" \
  "v_sub_f32_e32 v120, v120, v246\n\t" \
  "v_sub_f32_e32 v136, v136, v246\n\t" \
  "v_xor_b32_e32 v200, 0x80000000, v240\n\t" \
  "v_sub_f32_e32 v121, v121, v246\n\t" \
  "v_sub_f32_e32 v137, v137, v246\n\t" \
  "v_xor_b32_e32 v201, 0x80000000, v240\n\t" \
  "v_sub_f32_e32 v122, v122, v246\n\t" \
  "v_sub_f32_e32 v138, v138, v246\n\t" \
  "v_xor_b32_e32 v202, 0x80000000, v240\n\t" \
  "v_sub_f32_e32 v123, v123, v246\n\t" \
  "v_sub_f32_e32 v139, v139, v246\n\t" \
  "v_xor_b32_e32 v203, 0x80000000, v240\n\t" \
  "v_sub_f32_e32 v124, v124, v246\n\t" \
  "v_sub_f32_e32 v140, v140, v246\n\t" \
  "v_xor_b32_e32 v204, 0x80000000, v240\n\t" \
  "v_sub_f32_e32 v125, v125, v246\n\t" \
  "v_sub_f32_e32 v141, v141, v246\n\t" \
  "v_xor_b32_e32 v205, 0x80000000, v240\n\t" \
  "v_sub_f32_e32 v126, v126, v246\n\t" \
  "v_sub_f32_e32 v142, v142, v246\n\t" \
  "v_xor_b32_e32 v206, 0x80000000, v240\n\t" \
  "v_sub_f32_e32 v127, v127, v246\n\t" \
  "v_sub_f32_e32 v143, v143, v246\n\t" \
  "v_xor_b32_e32 v207, 0x80000000, v240\n\t" \
  "s_mov_b32 s69, 1\n\t" \
  "s_nop 1\n\t" \
  "s_branch LA_RAREB0%=\n\t" \
  "L_WM%=:\n\t" \
  "s_add_u32 m0, s54, 24\n\t" \
  "v_min_u32_e32 v254, 0x130, %[lin]\n\t" \
  "v_readlane_b32 s56, %[prm], m0\n\t" \
  "s_add_u32 m0, s54, 25\n\t" \
  "v_lshlrev_b32_e32 v254, 3, v254\n\t" \
  "v_readlane_b32 s57, %[prm], m0\n\t" \
  "s_add_u32 s54, s54, 2\n\t" \
  "s_add_u32 s70, s70, 1\n\t" \
  "s_cmp_ge_u32 s54, 10\n\t" \
  "s_cselect_b32 s70, 99, s70\n\t" \
  "s_nop 1\n\t" \
  "global_load_dword v253, v254, s[56:57]\n\t" \
  "s_branch L_WMB%=\n\t" \
  "LA_ROS1%=:\n\t" \
  "s_nop 11\n\t" \
  "v_mul_f32_e32 v48, v247, v48\n\t" \
  "v_mul_f32_e32 v49, v247, v49\n\t" \
  "v_mul_f32_e32 v50, v247, v50\n\t" \
  "v_mul_f32_e32 v51, v247, v51\n\t" \
  "v_mul_f32_e32 v52, v247, v52\n\t" \
  "v_mul_f32_e32 v53, v247, v53\n\t" \
  "v_mul_f32_e32 v54, v247, v54\n\t" \
  "v_mul_f32_e32 v55, v247, v55\n\t" \
  "v_mul_f32_e32 v56, v247, v56\n\t" \
  "v_mul_f32_e32 v57, v247, v57\n\t" \
  "v_mul_f32_e32 v58, v247, v58\n\t" \
  "v_mul_f32_e32 v59, v247, v59\n\t" \
  "v_mul_f32_e32 v60, v247, v60\n\t" \
  "v_mul_f32_e32 v61, v247, v61\n\t" \
  "v_mul_f32_e32 v62, v247, v62\n\t" \
  "v_mul_f32_e32 v63, v247, v63\n\t" \
  "v_mul_f32_e32 v64, v247, v64\n\t" \
  "v_mul_f32_e32 v65, v247, v65\n\t" \
  "v_mul_f32_e32 v66, v247, v66\n\t" \
  "v_mul_f32_e32 v67, v247, v67\n\t" \
  "v_mul_f32_e32 v68, v247, v68\n\t" \
  "v_mul_f32_e32 v69, v247, v69\n\t" \
  "v_mul_f32_e32 v70, v247, v70\n\t" \
  "v_mul_f32_e32 v71, v247, v71\n\t" \
  "v_mul_f32_e32 v72, v247, v72\n\t" \
  "v_mul_f32_e32 v73, v247, v73\n\t" \
  "v_mul_f32_e32 v74, v247, v74\n\t" \
  "v_mul_f32_e32 v75, v247, v75\n\t" \
  "v_mul_f32_e32 v76, v247, v76\n\t" \
  "v_mul_f32_e32 v77, v247, v77\n\t" \
  "v_mul_f32_e32 v78, v247, v78\n\t" \
  "v_mul_f32_e32 v79, v247, v79\n\t" \
  "v_mul_f32_e32 v80, v247, v80\n\t" \
  "v_mul_f32_e32 v81, v247, v81\n\t" \
  "v_mul_f32_e32 v82, v247, v82\n\t" \
  "v_mul_f32_e32 v83, v247, v83\n\t" \
  "v_mul_f32_e32 v84, v247, v84\n\t" \
  "v_mul_f32_e32 v85, v247, v85\n\t" \
  "v_mul_f32_e32 v86, v247, v86\n\t" \
  "v_mul_f32_e32 v87, v247, v87\n\t" \
  "v_mul_f32_e32 v88, v247, v88\n\t" \
  "v_mul_f32_e32 v89, v247, v89\n\t" \
  "v_mul_f32_e32 v90, v247, v90\n\t" \
  "v_mul_f32_e32 v91, v247, v91\n\t" \
  "v_mul_f32_e32 v92, v247, v92\n\t" \
  "v_mul_f32_e32 v93, v247, v93\n\t" \
  "v_mul_f32_e32 v94, v247, v94\n\t" \
  "v_mul_f32_e32 v95, v247, v95\n\t" \
  "v_mul_f32_e32 v96, v247, v96\n\t" \
  "v_mul_f32_e32 v97, v247, v97\n\t" \
  "v_mul_f32_e32 v98, v247, v98\n\t" \
  "v_mul_f32_e32 v99, v247, v99\n\t" \
  "v_mul_f32_e32 v100, v247, v100\n\t" \
  "v_mul_f32_e32 v101, v247, v101\n\t" \
  "v_mul_f32_e32 v102, v247, v102\n\t" \
  "v_mul_f32_e32 v103, v247, v103\n\t" \
  "v_mul_f32_e32 v104, v247, v104\n\t" \
  "v_mul_f32_e32 v105, v247, v105\n\t" \
  "v_mul_f32_e32 v106, v247, v106\n\t" \
  "v_mul_f32_e32 v107, v247, v107\n\t" \
  "v_mul_f32_e32 v108, v247, v108\n\t" \
  "v_mul_f32_e32 v109, v247, v109\n\t" \
  "v_mul_f32_e32 v110, v247, v110\n\t" \
  "v_mul_f32_e32 v111, v247, v111\n\t" \
  "v_mul_f32_e32 v241, v247, v241\n\t" \
  "s_mov_b32 s69, 0\n\t" \
  "s_nop 1\n\t" \
  "s_branch LA_ROBS1%=\n\t" \
  "LA_MASK10%=:\n\t" \
  "s_nop 3\n\t" \
  "v_cmp_gt_i32_e64 s[72:73], 0, %[dq]\n\t" \
  "v_cmp_gt_i32_e64 s[74:75], 1, %[dq]\n\t" \
  "v_cmp_gt_i32_e64 s[76:77], 2, %[dq]\n\t" \
  "v_cmp_gt_i32_e64 s[78:79], 3, %[dq]\n\t" \
  "v_cndmask_b32_e64 v144, v144, v248, s[72:73]\n\t" \
  "v_cmp_gt_i32_e64 s[72:73], 16, %[dq]\n\t" \
  "v_cndmask_b32_e64 v145, v145, v248, s[74:75]\n\t" \
  "v_cmp_gt_i32_e64 s[74:75], 17, %[dq]\n\t" \
  "v_cndmask_b32_e64 v146, v146, v248, s[76:77]\n\t" \
  "v_cmp_gt_i32_e64 s[76:77], 18, %[dq]\n\t" \
  "v_cndmask_b32_e64 v147, v147, v248, s[78:79]\n\t" \
  "v_cmp_gt_i32_e64 s[78:79], 19, %[dq]\n\t" \
  "v_cndmask_b32_e64 v148, v148, v248, s[72:73]\n\t" \
  "v_cmp_gt_i32_e64 s[72:73], 8, %[dq]\n\t" \
  "v_cndmask_b32_e64 v149, v149, v248, s[74:75]\n\t" \
  "v_cmp_gt_i32_e64 s[74:75], 9, %[dq]\n\t" \
  "v_cndmask_b32_e64 v150, v150, v248, s[76:77]\n\t" \
  "v_cmp_gt_i32_e64 s[76:77], 10, %[dq]\n\t" \
  "v_cndmask_b32_e64 v151, v151, v248, s[78:79]\n\t" \
  "v_cmp_gt_i32_e64 s[78:79], 11, %[dq]\n\t" \
  "v_cndmask_b32_e64 v152, v152, v248, s[72:73]\n\t" \
  "v_cmp_gt_i32_e64 s[72:73], 24, %[dq]\n\t" \
  "v_cndmask_b32_e64 v153, v153, v248, s[74:75]\n\t" \
  "v_cmp_gt_i32_e64 s[74:75], 25, %[dq]\n\t" \
  "v_cndmask_b32_e64 v154, v154, v248, s[76:77]\n\t" \
  "v_cmp_gt_i32_e64 s[76:77], 26, %[dq]\n\t" \
  "v_cndmask_b32_e64 v155, v155, v248, s[78:79]\n\t" \
  "v_cmp_gt_i32_e64 s[78:79], 27, %[dq]\n\t" \
  "v_cndmask_b32_e64 v156, v156, v248, s[72:73]\n\t" \
  "v_cndmask_b32_e64 v157, v157, v248, s[74:75]\n\t" \
  "v_cndmask_b32_e64 v158, v158, v248, s[76:77]\n\t" \
  "v_cndmask_b32_e64 v159, v159, v248, s[78:79]\n\t" \
  "s_nop 0\n\t" \
  "s_branch LA_MASKB10%=\n\t" \
  "LA_MASK11%=:\n\t" \
  "s_nop 3\n\t" \
  "v_cmp_gt_i32_e64 s[72:73], 32, %[dq]\n\t" \
  "v_cmp_gt_i32_e64 s[74:75], 33, %[dq]\n\t" \
  "v_cmp_gt_i32_e64 s[76:77], 34, %[dq]\n\t" \
  "v_cmp_gt_i32_e64 s[78:79], 35, %[dq]\n\t" \
  "v_cndmask_b32_e64 v160, v160, v248, s[72:73]\n\t" \
  "v_cmp_gt_i32_e64 s[72:73], 48, %[dq]\n\t" \
  "v_cndmask_b32_e64 v161, v161, v248, s[74:75]\n\t" \
  "v_cmp_gt_i32_e64 s[74:75], 49, %[dq]\n\t" \
  "v_cndmask_b32_e64 v162, v162, v248, s[76:77]\n\t" \
  "v_cmp_gt_i32_e64 s[76:77], 50, %[dq]\n\t" \
  "v_cndmask_b32_e64 v163, v163, v248, s[78:79]\n\t" \
  "v_cmp_gt_i32_e64 s[78:79], 51, %[dq]\n\t" \
  "v_cndmask_b32_e64 v164, v164, v248, s[72:73]\n\t" \
  "v_cmp_gt_i32_e64 s[72:73], 40, %[dq]\n\t" \
  "v_cndmask_b32_e64 v165, v165, v248, s[74:75]\n\t" \
  "v_cmp_gt_i32_e64 s[74:75], 41, %[dq]\n\t" \
  "v_cndmask_b32_e64 v166, v166, v248, s[76:77]\n\t" \
  "v_cmp_gt_i32_e64 s[76:77], 42, %[dq]\n\t" \
  "v_cndmask_b32_e64 v167, v167, v248, s[78:79]\n\t" \
  "v_cmp_gt_i32_e64 s[78:79], 43, %[dq]\n\t" \
  "v_cndmask_b32_e64 v168, v168, v248, s[72:73]\n\t" \
  "v_cmp_gt_i32_e64 s[72:73], 56, %[dq]\n\t" \
  "v_cndmask_b32_e64 v169, v169, v248, s[74:75]\n\t" \
  "v_cmp_gt_i32_e64 s[74:75], 57, %[dq]\n\t" \
  "v_cndmask_b32_e64 v170, v170, v248, s[76:77]\n\t" \
  "v_cmp_gt_i32_e64 s[76:77], 58, %[dq]\n\t" \
  "v_cndmask_b32_e64 v171, v171, v248, s[78:79]\n\t" \
  "v_cmp_gt_i32_e64 s[78:79], 59, %[dq]\n\t" \
  "v_cndmask_b32_e64 v172, v172, v248, s[72:73]\n\t" \
  "v_cndmask_b32_e64 v173, v173, v248, s[74:75]\n\t" \
  "v_cndmask_b32_e64 v174, v174, v248, s[76:77]\n\t" \
  "v_cndmask_b32_e64 v175, v175, v248, s[78:79]\n\t" \
  "s_nop 0\n\t" \
  "s_branch LA_MASKB11%=\n\t" \
  "LA_RARE1%=:\n\t" \
  "v_max_f32_e32 v246, 0, v244\n\t" \
  "v_add_f32_e32 v240, v240, v246\n\t" \
  "v_exp_f32_e64 v247, -v246\n\t" \
  "v_sub_f32_e32 v144, v144, v246\n\t" \
  "v_sub_f32_e32 v160, v160, v246\n\t" \
  "v_xor_b32_e32 v192, 0x80000000, v240\n\t" \
  "v_sub_f32_e32 v145, v145, v246\n\t" \
  "v_sub_f32_e32 v161, v161, v246\n\t" \
  "v_xor_b32_e32 v193, 0x80000000, v240\n\t" \
  "v_sub_f32_e32 v146, v146, v246\n\t" \
  "v_sub_f32_e32 v162, v162, v246\n\t" \
  "v_xor_b32_e32 v194, 0x80000000, v240\n\t" \
  "v_sub_f32_e32 v147, v147, v246\n\t" \
  "v_sub_f32_e32 v163, v163, v246\n\t" \
  "v_xor_b32_e32 v195, 0x80000000, v240\n\t" \
  "v_sub_f32_e32 v148, v148, v246\n\t" \
  "v_sub_f32_e32 v164, v164, v246\n\t" \
  "v_xor_b32_e32 v196, 0x80000000, v240\n\t" \
  "v_sub_f32_e32 v149, v149, v246\n\t" \
  "v_sub_f32_e32 v165, v165, v246\n\t" \
  "v_xor_b32_e32 v197, 0x80000000, v240\n\t" \
  "v_sub_f32_e32 v150, v150, v246\n\t" \
  "v_sub_f32_e32 v166, v166, v246\n\t" \
  "v_xor_b32_e32 v198, 0x80000000, v240\n\t" \
  "v_sub_f32_e32 v151, v151, v246\n\t" \
  "v_sub_f32_e32 v167, v167, v246\n\t" \
  "v_xor_b32_e32 v199, 0x80000000, v240\n\t" \
  "v_sub_f32_e32 v152, v152, v246\n\t" \
  "v_sub_f32_e32 v168, v168, v246\n\t" \
  "v_xor_b32_e32 v200, 0x80000000, v240\n\t" \
  "v_sub_f32_e32 v153, v153, v246\n\t" \
  "v_sub_f32_e32 v169, v169, v246\n\t" \
  "v_xor_b32_e32 v201, 0x80000000, v240\n\t" \
  "v_sub_f32_e32 v154, v154, v246\n\t" \
  "v_sub_f32_e32 v170, v170, v246\n\t" \
  "v_xor_b32_e32 v202, 0x80000000, v240\n\t" \
  "v_sub_f32_e32 v155, v155, v246\n\t" \
  "v_sub_f32_e32 v171, v171, v246\n\t" \
  "v_xor_b32_e32 v203, 0x80000000, v240\n\t" \
  "v_sub_f32_e32 v156, v156, v246\n\t" \
  "v_sub_f32_e32 v172, v172, v246\n\t" \
  "v_xor_b32_e32 v204, 0x80000000, v240\n\t" \
  "v_sub_f32_e32 v157, v157, v246\n\t" \
  "v_sub_f32_e32 v173, v173, v246\n\t" \
  "v_xor_b32_e32 v205, 0x80000000, v240\n\t" \
  "v_sub_f32_e32 v158, v158, v246\n\t" \
  "v_sub_f32_e32 v174, v174, v246\n\t" \
  "v_xor_b32_e32 v206, 0x80000000, v240\n\t" \
  "v_sub_f32_e32 v159, v159, v246\n\t" \
  "v_sub_f32_e32 v175, v175, v246\n\t" \
  "v_xor_b32_e32 v207, 0x80000000, v240\n\t" \
  "s_mov_b32 s69, 1\n\t" \
  "s_nop 1\n\t" \
  "s_branch LA_RAREB1%=\n\t" \
  "LA_ROD0%=:\n\t" \
  "s_nop 11\n\t" \
  "v_mul_f32_e32 v48, v247, v48\n\t" \
  "v_mul_f32_e32 v49, v247, v49\n\t" \
  "v_mul_f32_e32 v50, v247, v50\n\t" \
  "v_mul_f32_e32 v51, v247, v51\n\t" \
  "v_mul_f32_e32 v52, v247, v52\n\t" \
  "v_mul_f32_e32 v53, v247, v53\n\t" \
  "v_mul_f32_e32 v54, v247, v54\n\t" \
  "v_mul_f32_e32 v55, v247, v55\n\t" \
  "v_mul_f32_e32 v56, v247, v56\n\t" \
  "v_mul_f32_e32 v57, v247, v57\n\t" \
  "v_mul_f32_e32 v58, v247, v58\n\t" \
  "v_mul_f32_e32 v59, v247, v59\n\t" \
  "v_mul_f32_e32 v60, v247, v60\n\t" \
  "v_mul_f32_e32 v61, v247, v61\n\t" \
  "v_mul_f32_e32 v62, v247, v62\n\t" \
  "v_mul_f32_e32 v63, v247, v63\n\t" \
  "v_mul_f32_e32 v64, v247, v64\n\t" \
  "v_mul_f32_e32 v65, v247, v65\n\t" \
  "v_mul_f32_e32 v66, v247, v66\n\t" \
  "v_mul_f32_e32 v67, v247, v67\n\t" \
  "v_mul_f32_e32 v68, v247, v68\n\t" \
  "v_mul_f32_e32 v69, v247, v69\n\t" \
  "v_mul_f32_e32 v70, v247, v70\n\t" \
  "v_mul_f32_e32 v71, v247, v71\n\t" \
  "v_mul_f32_e32 v72, v247, v72\n\t" \
  "v_mul_f32_e32 v73, v247, v73\n\t" \
  "v_mul_f32_e32 v74, v247, v74\n\t" \
  "v_mul_f32_e32 v75, v247, v75\n\t" \
  "v_mul_f32_e32 v76, v247, v76\n\t" \
  "v_mul_f32_e32 v77, v247, v77\n\t" \
  "v_mul_f32_e32 v78, v247, v78\n\t" \
  "v_mul_f32_e32 v79, v247, v79\n\t" \
  "v_mul_f32_e32 v80, v247, v80\n\t" \
  "v_mul_f32_e32 v81, v247, v81\n\t" \
  "v_mul_f32_e32 v82, v247, v82\n\t" \
  "v_mul_f32_e32 v83, v247, v83\n\t" \
  "v_mul_f32_e32 v84, v247, v84\n\t" \
  "v_mul_f32_e32 v85, v247, v85\n\t" \
  "v_mul_f32_e32 v86, v247, v86\n\t" \
  "v_mul_f32_e32 v87, v247, v87\n\t" \
  "v_mul_f32_e32 v88, v247, v88\n\t" \
  "v_mul_f32_e32 v89, v247, v89\n\t" \
  "v_mul_f32_e32 v90, v247, v90\n\t" \
  "v_mul_f32_e32 v91, v247, v91\n\t" \
  "v_mul_f32_e32 v92, v247, v92\n\t" \
  "v_mul_f32_e32 v93, v247, v93\n\t" \
  "v_mul_f32_e32 v94, v247, v94\n\t" \
  "v_mul_f32_e32 v95, v247, v95\n\t" \
  "v_mul_f32_e32 v96, v247, v96\n\t" \
  "v_mul_f32_e32 v97, v247, v97\n\t" \
  "v_mul_f32_e32 v98, v247, v98\n\t" \
  "v_mul_f32_e32 v99, v247, v99\n\t" \
  "v_mul_f32_e32 v100, v247, v100\n\t" \
  "v_mul_f32_e32 v101, v247, v101\n\t" \
  "v_mul_f32_e32 v102, v247, v102\n\t" \
  "v_mul_f32_e32 v103, v247, v103\n\t" \
  "v_mul_f32_e32 v104, v247, v104\n\t" \
  "v_mul_f32_e32 v105, v247, v105\n\t" \
  "v_mul_f32_e32 v106, v247, v106\n\t" \
  "v_mul_f32_e32 v107, v247, v107\n\t" \
  "v_mul_f32_e32 v108, v247, v108\n\t" \
  "v_mul_f32_e32 v109, v247, v109\n\t" \
  "v_mul_f32_e32 v110, v247, v110\n\t" \
  "v_mul_f32_e32 v111, v247, v111\n\t" \
  "v_mul_f32_e32 v241, v247, v241\n\t" \
  "s_mov_b32 s69, 0\n\t" \
  "s_nop 1\n\t" \
  "s_branch LA_ROBD0%=\n\t" \
  "LA_ROD1%=:\n\t" \
  "s_nop 11\n\t" \
  "v_mul_f32_e32 v48, v247, v48\n\t" \
  "v_mul_f32_e32 v49, v247, v49\n\t" \
  "v_mul_f32_e32 v50, v247, v50\n\t" \
  "v_mul_f32_e32 v51, v247, v51\n\t" \
  "v_mul_f32_e32 v52, v247, v52\n\t" \
  "v_mul_f32_e32 v53, v247, v53\n\t" \
  "v_mul_f32_e32 v54, v247, v54\n\t" \
  "v_mul_f32_e32 v55, v247, v55\n\t" \
  "v_mul_f32_e32 v56, v247, v56\n\t" \
  "v_mul_f32_e32 v57, v247, v57\n\t" \
  "v_mul_f32_e32 v58, v247, v58\n\t" \
  "v_mul_f32_e32 v59, v247, v59\n\t" \
  "v_mul_f32_e32 v60, v247, v60\n\t" \
  "v_mul_f32_e32 v61, v247, v61\n\t" \
  "v_mul_f32_e32 v62, v247, v62\n\t" \
  "v_mul_f32_e32 v63, v247, v63\n\t" \
  "v_mul_f32_e32 v64, v247, v64\n\t" \
  "v_mul_f32_e32 v65, v247, v65\n\t" \
  "v_mul_f32_e32 v66, v247, v66\n\t" \
  "v_mul_f32_e32 v67, v247, v67\n\t" \
  "v_mul_f32_e32 v68, v247, v68\n\t" \
  "v_mul_f32_e32 v69, v247, v69\n\t" \
  "v_mul_f32_e32 v70, v247, v70\n\t" \
  "v_mul_f32_e32 v71, v247, v71\n\t" \
  "v_mul_f32_e32 v72, v247, v72\n\t" \
  "v_mul_f32_e32 v73, v247, v73\n\t" \
  "v_mul_f32_e32 v74, v247, v74\n\t" \
  "v_mul_f32_e32 v75, v247, v75\n\t" \
  "v_mul_f32_e32 v76, v247, v76\n\t" \
  "v_mul_f32_e32 v77, v247, v77\n\t" \
  "v_mul_f32_e32 v78, v247, v78\n\t" \
  "v_mul_f32_e32 v79, v247, v79\n\t" \
  "v_mul_f32_e32 v80, v247, v80\n\t" \
  "v_mul_f32_e32 v81, v247, v81\n\t" \
  "v_mul_f32_e32 v82, v247, v82\n\t" \
  "v_mul_f32_e32 v83, v247, v83\n\t" \
  "v_mul_f32_e32 v84, v247, v84\n\t" \
  "v_mul_f32_e32 v85, v247, v85\n\t" \
  "v_mul_f32_e32 v86, v247, v86\n\t" \
  "v_mul_f32_e32 v87, v247, v87\n\t" \
  "v_mul_f32_e32 v88, v247, v88\n\t" \
  "v_mul_f32_e32 v89, v247, v89\n\t" \
  "v_mul_f32_e32 v90, v247, v90\n\t" \
  "v_mul_f32_e32 v91, v247, v91\n\t" \
  "v_mul_f32_e32 v92, v247, v92\n\t" \
  "v_mul_f32_e32 v93, v247, v93\n\t" \
  "v_mul_f32_e32 v94, v247, v94\n\t" \
  "v_mul_f32_e32 v95, v247, v95\n\t" \
  "v_mul_f32_e32 v96, v247, v96\n\t" \
  "v_mul_f32_e32 v97, v247, v97\n\t" \
  "v_mul_f32_e32 v98, v247, v98\n\t" \
  "v_mul_f32_e32 v99, v247, v99\n\t" \
  "v_mul_f32_e32 v100, v247, v100\n\t" \
  "v_mul_f32_e32 v101, v247, v101\n\t" \
  "v_mul_f32_e32 v102, v247, v102\n\t" \
  "v_mul_f32_e32 v103, v247, v103\n\t" \
  "v_mul_f32_e32 v104, v247, v104\n\t" \
  "v_mul_f32_e32 v105, v247, v105\n\t" \
  "v_mul_f32_e32 v106, v247, v106\n\t" \
  "v_mul_f32_e32 v107, v247, v107\n\t" \
  "v_mul_f32_e32 v108, v247, v108\n\t" \
  "v_mul_f32_e32 v109, v247, v109\n\t" \
  "v_mul_f32_e32 v110, v247, v110\n\t" \
  "v_mul_f32_e32 v111, v247, v111\n\t" \
  "v_mul_f32_e32 v241, v247, v241\n\t" \
  "s_mov_b32 s69, 0\n\t" \
  "s_nop 1\n\t" \
  "s_branch LA_ROBD1%=\n\t" \
  "LB_PMASK%=:\n\t" \
  "v_cmp_gt_i32_e64 s[72:73], 0, %[dq]\n\t" \
  "v_cmp_gt_i32_e64 s[74:75], 1, %[dq]\n\t" \
  "v_cmp_gt_i32_e64 s[76:77], 2, %[dq]\n\t" \
  "v_cmp_gt_i32_e64 s[78:79], 3, %[dq]\n\t" \
  "v_cndmask_b32_e64 v112, v112, v248, s[72:73]\n\t" \
  "v_cmp_gt_i32_e64 s[72:73], 16, %[dq]\n\t" \
  "v_cndmask_b32_e64 v113, v113, v248, s[74:75]\n\t" \
  "v_cmp_gt_i32_e64 s[74:75], 17, %[dq]\n\t" \
  "v_cndmask_b32_e64 v114, v114, v248, s[76:77]\n\t" \
  "v_cmp_gt_i32_e64 s[76:77], 18, %[dq]\n\t" \
  "v_cndmask_b32_e64 v115, v115, v248, s[78:79]\n\t" \
  "v_cmp_gt_i32_e64 s[78:79], 19, %[dq]\n\t" \
  "v_cndmask_b32_e64 v116, v116, v248, s[72:73]\n\t" \
  "v_cmp_gt_i32_e64 s[72:73], 8, %[dq]\n\t" \
  "v_cndmask_b32_e64 v117, v117, v248, s[74:75]\n\t" \
  "v_cmp_gt_i32_e64 s[74:75], 9, %[dq]\n\t" \
  "v_cndmask_b32_e64 v118, v118, v248, s[76:77]\n\t" \
  "v_cmp_gt_i32_e64 s[76:77], 10, %[dq]\n\t" \
  "v_cndmask_b32_e64 v119, v119, v248, s[78:79]\n\t" \
  "v_cmp_gt_i32_e64 s[78:79], 11, %[dq]\n\t" \
  "v_cndmask_b32_e64 v120, v120, v248, s[72:73]\n\t" \
  "v_cmp_gt_i32_e64 s[72:73], 24, %[dq]\n\t" \
  "v_cndmask_b32_e64 v121, v121, v248, s[74:75]\n\t" \
  "v_cmp_gt_i32_e64 s[74:75], 25, %[dq]\n\t" \
  "v_cndmask_b32_e64 v122, v122, v248, s[76:77]\n\t" \
  "v_cmp_gt_i32_e64 s[76:77], 26, %[dq]\n\t" \
  "v_cndmask_b32_e64 v123, v123, v248, s[78:79]\n\t" \
  "v_cmp_gt_i32_e64 s[78:79], 27, %[dq]\n\t" \
  "v_cndmask_b32_e64 v124, v124, v248, s[72:73]\n\t" \
  "v_cndmask_b32_e64 v125, v125, v248, s[74:75]\n\t" \
  "v_cndmask_b32_e64 v126, v126, v248, s[76:77]\n\t" \
  "v_cndmask_b32_e64 v127, v127, v248, s[78:79]\n\t" \
  "v_cmp_gt_i32_e64 s[72:73], 32, %[dq]\n\t" \
  "v_cmp_gt_i32_e64 s[74:75], 33, %[dq]\n\t" \
  "v_cmp_gt_i32_e64 s[76:77], 34, %[dq]\n\t" \
  "v_cmp_gt_i32_e64 s[78:79], 35, %[dq]\n\t" \
  "v_cndmask_b32_e64 v128, v128, v248, s[72:73]\n\t" \
  "v_cmp_gt_i32_e64 s[72:73], 48, %[dq]\n\t" \
  "v_cndmask_b32_e64 v129, v129, v248, s[74:75]\n\t" \
  "v_cmp_gt_i32_e64 s[74:75], 49, %[dq]\n\t" \
  "v_cndmask_b32_e64 v130, v130, v248, s[76:77]\n\t" \
  "v_cmp_gt_i32_e64 s[76:77], 50, %[dq]\n\t" \
  "v_cndmask_b32_e64 v131, v131, v248, s[78:79]\n\t" \
  "v_cmp_gt_i32_e64 s[78:79], 51, %[dq]\n\t" \
  "v_cndmask_b32_e64 v132, v132, v248, s[72:73]\n\t" \
  "v_cmp_gt_i32_e64 s[72:73], 40, %[dq]\n\t" \
  "v_cndmask_b32_e64 v133, v133, v248, s[74:75]\n\t" \
  "v_cmp_gt_i32_e64 s[74:75], 41, %[dq]\n\t" \
  "v_cndmask_b32_e64 v134, v134, v248, s[76:77]\n\t" \
  "v_cmp_gt_i32_e64 s[76:77], 42, %[dq]\n\t" \
  "v_cndmask_b32_e64 v135, v135, v248, s[78:79]\n\t" \
  "v_cmp_gt_i32_e64 s[78:79], 43, %[dq]\n\t" \
  "v_cndmask_b32_e64 v136, v136, v248, s[72:73]\n\t" \
  "v_cmp_gt_i32_e64 s[72:73], 56, %[dq]\n\t" \
  "v_cndmask_b32_e64 v137, v137, v248, s[74:75]\n\t" \
  "v_cmp_gt_i32_e64 s[74:75], 57, %[dq]\n\t" \
  "v_cndmask_b32_e64 v138, v138, v248, s[76:77]\n\t" \
  "v_cmp_gt_i32_e64 s[76:77], 58, %[dq]\n\t" \
  "v_cndmask_b32_e64 v139, v139, v248, s[78:79]\n\t" \
  "v_cmp_gt_i32_e64 s[78:79], 59, %[dq]\n\t" \
  "v_cndmask_b32_e64 v140, v140, v248, s[72:73]\n\t" \
  "v_cndmask_b32_e64 v141, v141, v248, s[74:75]\n\t" \
  "v_cndmask_b32_e64 v142, v142, v248, s[76:77]\n\t" \
  "v_cndmask_b32_e64 v143, v143, v248, s[78:79]\n\t" \
  "s_nop 0\n\t" \
  "s_branch LB_PMASKB%=\n\t" \
  "LB_ROS0%=:\n\t" \
  "s_nop 11\n\t" \
  "v_mul_f32_e32 v48, v247, v48\n\t" \
  "v_mul_f32_e32 v49, v247, v49\n\t" \
  "v_mul_f32_e32 v50, v247, v50\n\t" \
  "v_mul_f32_e32 v51, v247, v51\n\t" \
  "v_mul_f32_e32 v52, v247, v52\n\t" \
  "v_mul_f32_e32 v53, v247, v53\n\t" \
  "v_mul_f32_e32 v54, v247, v54\n\t" \
  "v_mul_f32_e32 v55, v247, v55\n\t" \
  "v_mul_f32_e32 v56, v247, v56\n\t" \
  "v_mul_f32_e32 v57, v247, v57\n\t" \
  "v_mul_f32_e32 v58, v247, v58\n\t" \
  "v_mul_f32_e32 v59, v247, v59\n\t" \
  "v_mul_f32_e32 v60, v247, v60\n\t" \
  "v_mul_f32_e32 v61, v247, v61\n\t" \
  "v_mul_f32_e32 v62, v247, v62\n\t" \
  "v_mul_f32_e32 v63, v247, v63\n\t" \
  "v_mul_f32_e32 v64, v247, v64\n\t" \
  "v_mul_f32_e32 v65, v247, v65\n\t" \
  "v_mul_f32_e32 v66, v247, v66\n\t" \
  "v_mul_f32_e32 v67, v247, v67\n\t" \
  "v_mul_f32_e32 v68, v247, v68\n\t" \
  "v_mul_f32_e32 v69, v247, v69\n\t" \
  "v_mul_f32_e32 v70, v247, v70\n\t" \
  "v_mul_f32_e32 v71, v247, v71\n\t" \
  "v_mul_f32_e32 v72, v247, v72\n\t" \
  "v_mul_f32_e32 v73, v247, v73\n\t" \
  "v_mul_f32_e32 v74, v247, v74\n\t" \
  "v_mul_f32_e32 v75, v247, v75\n\t" \
  "v_mul_f32_e32 v76, v247, v76\n\t" \
  "v_mul_f32_e32 v77, v247, v77\n\t" \
  "v_mul_f32_e32 v78, v247, v78\n\t" \
  "v_mul_f32_e32 v79, v247, v79\n\t" \
  "v_mul_f32_e32 v80, v247, v80\n\t" \
  "v_mul_f32_e32 v81, v247, v81\n\t" \
  "v_mul_f32_e32 v82, v247, v82\n\t" \
  "v_mul_f32_e32 v83, v247, v83\n\t" \
  "v_mul_f32_e32 v84, v247, v84\n\t" \
  "v_mul_f32_e32 v85, v247, v85\n\t" \
  "v_mul_f32_e32 v86, v247, v86\n\t" \
  "v_mul_f32_e32 v87, v247, v87\n\t" \
  "v_mul_f32_e32 v88, v247, v88\n\t" \
  "v_mul_f32_e32 v89, v247, v89\n\t" \
  "v_mul_f32_e32 v90, v247, v90\n\t" \
  "v_mul_f32_e32 v91, v247, v91\n\t" \
  "v_mul_f32_e32 v92, v247, v92\n\t" \
  "v_mul_f32_e32 v93, v247, v93\n\t" \
  "v_mul_f32_e32 v94, v247, v94\n\t" \
  "v_mul_f32_e32 v95, v247, v95\n\t" \
  "v_mul_f32_e32 v96, v247, v96\n\t" \
  "v_mul_f32_e32 v97, v247, v97\n\t" \
  "v_mul_f32_e32 v98, v247, v98\n\t" \
  "v_mul_f32_e32 v99, v247, v99\n\t" \
  "v_mul_f32_e32 v100, v247, v100\n\t" \
  "v_mul_f32_e32 v101, v247, v101\n\t" \
  "v_mul_f32_e32 v102, v247, v102\n\t" \
  "v_mul_f32_e32 v103, v247, v103\n\t" \
  "v_mul_f32_e32 v104, v247, v104\n\t" \
  "v_mul_f32_e32 v105, v247, v105\n\t" \
  "v_mul_f32_e32 v106, v247, v106\n\t" \
  "v_mul_f32_e32 v107, v247, v107\n\t" \
  "v_mul_f32_e32 v108, v247, v108\n\t" \
  "v_mul_f32_e32 v109, v247, v109\n\t" \
  "v_mul_f32_e32 v110, v247, v110\n\t" \
  "v_mul_f32_e32 v111, v247, v111\n\t" \
  "v_mul_f32_e32 v241, v247, v241\n\t" \
  "s_mov_b32 s69, 0\n\t" \
  "s_nop 1\n\t" \
  "s_branch LB_ROBS0%=\n\t" \
  "LB_MASK00%=:\n\t" \
  "s_nop 3\n\t" \
  "v_cmp_gt_i32_e64 s[72:73], 0, %[dq]\n\t" \
  "v_cmp_gt_i32_e64 s[74:75], 1, %[dq]\n\t" \
  "v_cmp_gt_i32_e64 s[76:77], 2, %[dq]\n\t" \
  "v_cmp_gt_i32_e64 s[78:79], 3, %[dq]\n\t" \
  "v_cndmask_b32_e64 v112, v112, v248, s[72:73]\n\t" \
  "v_cmp_gt_i32_e64 s[72:73], 16, %[dq]\n\t" \
  "v_cndmask_b32_e64 v113, v113, v248, s[74:75]\n\t" \
  "v_cmp_gt_i32_e64 s[74:75], 17, %[dq]\n\t" \
  "v_cndmask_b32_e64 v114, v114, v248, s[76:77]\n\t" \
  "v_cmp_gt_i32_e64 s[76:77], 18, %[dq]\n\t" \
  "v_cndmask_b32_e64 v115, v115, v248, s[78:79]\n\t" \
  "v_cmp_gt_i32_e64 s[78:79], 19, %[dq]\n\t" \
  "v_cndmask_b32_e64 v116, v116, v248, s[72:73]\n\t" \
  "v_cmp_gt_i32_e64 s[72:73], 8, %[dq]\n\t" \
  "v_cndmask_b32_e64 v117, v117, v248, s[74:75]\n\t" \
  "v_cmp_gt_i32_e64 s[74:75], 9, %[dq]\n\t" \
  "v_cndmask_b32_e64 v118, v118, v248, s[76:77]\n\t" \
  "v_cmp_gt_i32_e64 s[76:77], 10, %[dq]\n\t" \
  "v_cndmask_b32_e64 v119, v119, v248, s[78:79]\n\t" \
  "v_cmp_gt_i32_e64 s[78:79], 11, %[dq]\n\t" \
  "v_cndmask_b32_e64 v120, v120, v248, s[72:73]\n\t" \
  "v_cmp_gt_i32_e64 s[72:73], 24, %[dq]\n\t" \
  "v_cndmask_b32_e64 v121, v121, v248, s[74:75]\n\t" \
  "v_cmp_gt_i32_e64 s[74:75], 25, %[dq]\n\t" \
  "v_cndmask_b32_e64 v122, v122, v248, s[76:77]\n\t" \
  "v_cmp_gt_i32_e64 s[76:77], 26, %[dq]\n\t" \
  "v_cndmask_b32_e64 v123, v123, v248, s[78:79]\n\t" \
  "v_cmp_gt_i32_e64 s[78:79], 27, %[dq]\n\t" \
  "v_cndmask_b32_e64 v124, v124, v248, s[72:73]\n\t" \
  "v_cndmask_b32_e64 v125, v125, v248, s[74:75]\n\t" \
  "v_cndmask_b32_e64 v126, v126, v248, s[76:77]\n\t" \
  "v_cndmask_b32_e64 v127, v127, v248, s[78:79]\n\t" \
  "s_nop 0\n\t" \
  "s_branch LB_MASKB00%=\n\t" \
  "LB_MASK01%=:\n\t" \
  "s_nop 3\n\t" \
  "v_cmp_gt_i32_e64 s[72:73], 32, %[dq]\n\t" \
  "v_cmp_gt_i32_e64 s[74:75], 33, %[dq]\n\t" \
  "v_cmp_gt_i32_e64 s[76:77], 34, %[dq]\n\t" \
  "v_cmp_gt_i32_e64 s[78:79], 35, %[dq]\n\t" \
  "v_cndmask_b32_e64 v128, v128, v248, s[72:73]\n\t" \
  "v_cmp_gt_i32_e64 s[72:73], 48, %[dq]\n\t" \
  "v_cndmask_b32_e64 v129, v129, v248, s[74:75]\n\t" \
  "v_cmp_gt_i32_e64 s[74:75], 49, %[dq]\n\t" \
  "v_cndmask_b32_e64 v130, v130, v248, s[76:77]\n\t" \
  "v_cmp_gt_i32_e64 s[76:77], 50, %[dq]\n\t" \
  "v_cndmask_b32_e64 v131, v131, v248, s[78:79]\n\t" \
  "v_cmp_gt_i32_e64 s[78:79], 51, %[dq]\n\t" \
  "v_cndmask_b32_e64 v132, v132, v248, s[72:73]\n\t" \
  "v_cmp_gt_i32_e64 s[72:73], 40, %[dq]\n\t" \
  "v_cndmask_b32_e64 v133, v133, v248, s[74:75]\n\t" \
  "v_cmp_gt_i32_e64 s[74:75], 41, %[dq]\n\t" \
  "v_cndmask_b32_e64 v134, v134, v248, s[76:77]\n\t" \
  "v_cmp_gt_i32_e64 s[76:77], 42, %[dq]\n\t" \
  "v_cndmask_b32_e64 v135, v135, v248, s[78:79]\n\t" \
  "v_cmp_gt_i32_e64 s[78:79], 43, %[dq]\n\t" \
  "v_cndmask_b32_e64 v136, v136, v248, s[72:73]\n\t" \
  "v_cmp_gt_i32_e64 s[72:73], 56, %[dq]\n\t" \
  "v_cndmask_b32_e64 v137, v137, v248, s[74:75]\n\t" \
  "v_cmp_gt_i32_e64 s[74:75], 57, %[dq]\n\t" \
  "v_cndmask_b32_e64 v138, v138, v248, s[76:77]\n\t" \
  "v_cmp_gt_i32_e64 s[76:77], 58, %[dq]\n\t" \
  "v_cndmask_b32_e64 v139, v139, v248, s[78:79]\n\t" \
  "v_cmp_gt_i32_e64 s[78:79], 59, %[dq]\n\t" \
  "v_cndmask_b32_e64 v140, v140, v248, s[72:73]\n\t" \
  "v_cndmask_b32_e64 v141, v141, v248, s[74:75]\n\t" \
  "v_cndmask_b32_e64 v142, v142, v248, s[76:77]\n\t" \
  "v_cndmask_b32_e64 v143, v143, v248, s[78:79]\n\t" \
  "s_nop 0\n\t" \
  "s_branch LB_MASKB01%=\n\t" \
  "LB_RARE0%=:\n\t" \
  "v_max_f32_e32 v246, 0, v244\n\t" \
  "v_add_f32_e32 v240, v240, v246\n\t" \
  "v_exp_f32_e64 v247, -v246\n\t" \
  "v_sub_f32_e32 v112, v112, v246\n\t" \
  "v_sub_f32_e32 v128, v128, v246\n\t" \
  "v_xor_b32_e32 v192, 0x80000000, v240\n\t" \
  "v_sub_f32_e32 v113, v113, v246\n\t" \
  "v_sub_f32_e32 v129, v129, v246\n\t" \
  "v_xor_b32_e32 v193, 0x80000000, v240\n\t" \
  "v_sub_f32_e32 v114, v114, v246\n\t" \
  "v_sub_f32_e32 v130, v130, v246\n\t" \
  "v_xor_b32_e32 v194, 0x80000000, v240\n\t" \
  "v_sub_f32_e32 v115, v115, v246\n\t" \
  "v_sub_f32_e32 v131, v131, v246\n\t" \
  "v_xor_b32_e32 v195, 0x80000000, v240\n\t" \
  "v_sub_f32_e32 v116, v116, v246\n\t" \
  "v_sub_f32_e32 v132, v132, v246\n\t" \
  "v_xor_b32_e32 v196, 0x80000000, v240\n\t" \
  "v_sub_f32_e32 v117, v117, v246\n\t" \
  "v_sub_f32_e32 v133, v133, v246\n\t" \
  "v_xor_b32_e32 v197, 0x80000000, v240\n\t" \
  "v_sub_f32_e32 v118, v118, v246\n\t" \
  "v_sub_f32_e32 v134, v134, v246\n\t" \
  "v_xor_b32_e32 v198, 0x80000000, v240\n\t" \
  "v_sub_f32_e32 v119, v119, v246\n\t" \
  "v_sub_f32_e32 v135, v135, v246\n\t" \
  "v_xor_b32_e32 v199, 0x80000000, v240\n\t" \
  "v_sub_f32_e32 v120, v120, v246\n\t" \
  "v_sub_f32_e32 v136, v136, v246\n\t" \
  "v_xor_b32_e32 v200, 0x80000000, v240\n\t" \
  "v_sub_f32_e32 v121, v121, v246\n\t" \
  "v_sub_f32_e32 v137, v137, v246\n\t" \
  "v_xor_b32_e32 v201, 0x80000000, v240\n\t" \
  "v_sub_f32_e32 v122, v122, v246\n\t" \
  "v_sub_f32_e32 v138, v138, v246\n\t" \
  "v_xor_b32_e32 v202, 0x80000000, v240\n\t" \
  "v_sub_f32_e32 v123, v123, v246\n\t" \
  "v_sub_f32_e32 v139, v139, v246\n\t" \
  "v_xor_b32_e32 v203, 0x80000000, v240\n\t" \
  "v_sub_f32_e32 v124, v124, v246\n\t" \
  "v_sub_f32_e32 v140, v140, v246\n\t" \
  "v_xor_b32_e32 v204, 0x80000000, v240\n\t" \
  "v_sub_f32_e32 v125, v125, v246\n\t" \
  "v_sub_f32_e32 v141, v141, v246\n\t" \
  "v_xor_b32_e32 v205, 0x80000000, v240\n\t" \
  "v_sub_f32_e32 v126, v126, v246\n\t" \
  "v_sub_f32_e32 v142, v142, v246\n\t" \
  "v_xor_b32_e32 v206, 0x80000000, v240\n\t" \
  "v_sub_f32_e32 v127, v127, v246\n\t" \
  "v_sub_f32_e32 v143, v143, v246\n\t" \
  "v_xor_b32_e32 v207, 0x80000000, v240\n\t" \
  "s_mov_b32 s69, 1\n\t" \
  "s_nop 1\n\t" \
  "s_branch LB_RAREB0%=\n\t" \
  "LB_ROS1%=:\n\t" \
  "s_nop 11\n\t" \
  "v_mul_f32_e32 v48, v247, v48\n\t" \
  "v_mul_f32_e32 v49, v247, v49\n\t" \
  "v_mul_f32_e32 v50, v247, v50\n\t" \
  "v_mul_f32_e32 v51, v247, v51\n\t" \
  "v_mul_f32_e32 v52, v247, v52\n\t" \
  "v_mul_f32_e32 v53, v247, v53\n\t" \
  "v_mul_f32_e32 v54, v247, v54\n\t" \
  "v_mul_f32_e32 v55, v247, v55\n\t" \
  "v_mul_f32_e32 v56, v247, v56\n\t" \
  "v_mul_f32_e32 v57, v247, v57\n\t" \
  "v_mul_f32_e32 v58, v247, v58\n\t" \
  "v_mul_f32_e32 v59, v247, v59\n\t" \
  "v_mul_f32_e32 v60, v247, v60\n\t" \
  "v_mul_f32_e32 v61, v247, v61\n\t" \
  "v_mul_f32_e32 v62, v247, v62\n\t" \
  "v_mul_f32_e32 v63, v247, v63\n\t" \
  "v_mul_f32_e32 v64, v247, v64\n\t" \
  "v_mul_f32_e32 v65, v247, v65\n\t" \
  "v_mul_f32_e32 v66, v247, v66\n\t" \
  "v_mul_f32_e32 v67, v247, v67\n\t" \
  "v_mul_f32_e32 v68, v247, v68\n\t" \
  "v_mul_f32_e32 v69, v247, v69\n\t" \
  "v_mul_f32_e32 v70, v247, v70\n\t" \
  "v_mul_f32_e32 v71, v247, v71\n\t" \
  "v_mul_f32_e32 v72, v247, v72\n\t" \
  "v_mul_f32_e32 v73, v247, v73\n\t" \
  "v_mul_f32_e32 v74, v247, v74\n\t" \
  "v_mul_f32_e32 v75, v247, v75\n\t" \
  "v_mul_f32_e32 v76, v247, v76\n\t" \
  "v_mul_f32_e32 v77, v247, v77\n\t" \
  "v_mul_f32_e32 v78, v247, v78\n\t" \
  "v_mul_f32_e32 v79, v247, v79\n\t" \
  "v_mul_f32_e32 v80, v247, v80\n\t" \
  "v_mul_f32_e32 v81, v247, v81\n\t" \
  "v_mul_f32_e32 v82, v247, v82\n\t" \
  "v_mul_f32_e32 v83, v247, v83\n\t" \
  "v_mul_f32_e32 v84, v247, v84\n\t" \
  "v_mul_f32_e32 v85, v247, v85\n\t" \
  "v_mul_f32_e32 v86, v247, v86\n\t" \
  "v_mul_f32_e32 v87, v247, v87\n\t" \
  "v_mul_f32_e32 v88, v247, v88\n\t" \
  "v_mul_f32_e32 v89, v247, v89\n\t" \
  "v_mul_f32_e32 v90, v247, v90\n\t" \
  "v_mul_f32_e32 v91, v247, v91\n\t" \
  "v_mul_f32_e32 v92, v247, v92\n\t" \
  "v_mul_f32_e32 v93, v247, v93\n\t" \
  "v_mul_f32_e32 v94, v247, v94\n\t" \
  "v_mul_f32_e32 v95, v247, v95\n\t" \
  "v_mul_f32_e32 v96, v247, v96\n\t" \
  "v_mul_f32_e32 v97, v247, v97\n\t" \
  "v_mul_f32_e32 v98, v247, v98\n\t" \
  "v_mul_f32_e32 v99, v247, v99\n\t" \
  "v_mul_f32_e32 v100, v247, v100\n\t" \
  "v_mul_f32_e32 v101, v247, v101\n\t" \
  "v_mul_f32_e32 v102, v247, v102\n\t" \
  "v_mul_f32_e32 v103, v247, v103\n\t" \
  "v_mul_f32_e32 v104, v247, v104\n\t" \
  "v_mul_f32_e32 v105, v247, v105\n\t" \
  "v_mul_f32_e32 v106, v247, v106\n\t" \
  "v_mul_f32_e32 v107, v247, v107\n\t" \
  "v_mul_f32_e32 v108, v247, v108\n\t" \
  "v_mul_f32_e32 v109, v247, v109\n\t" \
  "v_mul_f32_e32 v110, v247, v110\n\t" \
  "v_mul_f32_e32 v111, v247, v111\n\t" \
  "v_mul_f32_e32 v241, v247, v241\n\t" \
  "s_mov_b32 s69, 0\n\t" \
  "s_nop 1\n\t" \
  "s_branch LB_ROBS1%=\n\t" \
  "LB_MASK10%=:\n\t" \
  "s_nop 3\n\t" \
  "v_cmp_gt_i32_e64 s[72:73], 0, %[dq]\n\t" \
  "v_cmp_gt_i32_e64 s[74:75], 1, %[dq]\n\t" \
  "v_cmp_gt_i32_e64 s[76:77], 2, %[dq]\n\t" \
  "v_cmp_gt_i32_e64 s[78:79], 3, %[dq]\n\t" \
  "v_cndmask_b32_e64 v144, v144, v248, s[72:73]\n\t" \
  "v_cmp_gt_i32_e64 s[72:73], 16, %[dq]\n\t" \
  "v_cndmask_b32_e64 v145, v145, v248, s[74:75]\n\t" \
  "v_cmp_gt_i32_e64 s[74:75], 17, %[dq]\n\t" \
  "v_cndmask_b32_e64 v146, v146, v248, s[76:77]\n\t" \
  "v_cmp_gt_i32_e64 s[76:77], 18, %[dq]\n\t" \
  "v_cndmask_b32_e64 v147, v147, v248, s[78:79]\n\t" \
  "v_cmp_gt_i32_e64 s[78:79], 19, %[dq]\n\t" \
  "v_cndmask_b32_e64 v148, v148, v248, s[72:73]\n\t" \
  "v_cmp_gt_i32_e64 s[72:73], 8, %[dq]\n\t" \
  "v_cndmask_b32_e64 v149, v149, v248, s[74:75]\n\t" \
  "v_cmp_gt_i32_e64 s[74:75], 9, %[dq]\n\t" \
  "v_cndmask_b32_e64 v150, v150, v248, s[76:77]\n\t" \
  "v_cmp_gt_i32_e64 s[76:77], 10, %[dq]\n\t" \
  "v_cndmask_b32_e64 v151, v151, v248, s[78:79]\n\t" \
  "v_cmp_gt_i32_e64 s[78:79], 11, %[dq]\n\t" \
  "v_cndmask_b32_e64 v152, v152, v248, s[72:73]\n\t" \
  "v_cmp_gt_i32_e64 s[72:73], 24, %[dq]\n\t" \
  "v_cndmask_b32_e64 v153, v153, v248, s[74:75]\n\t" \
  "v_cmp_gt_i32_e64 s[74:75], 25, %[dq]\n\t" \
  "v_cndmask_b32_e64 v154, v154, v248, s[76:77]\n\t" \
  "v_cmp_gt_i32_e64 s[76:77], 26, %[dq]\n\t" \
  "v_cndmask_b32_e64 v155, v155, v248, s[78:79]\n\t" \
  "v_cmp_gt_i32_e64 s[78:79], 27, %[dq]\n\t" \
  "v_cndmask_b32_e64 v156, v156, v248, s[72:73]\n\t" \
  "v_cndmask_b32_e64 v157, v157, v248, s[74:75]\n\t" \
  "v_cndmask_b32_e64 v158, v158, v248, s[76:77]\n\t" \
  "v_cndmask_b32_e64 v159, v159, v248, s[78:79]\n\t" \
  "s_nop 0\n\t" \
  "s_branch LB_MASKB10%=\n\t" \
  "LB_MASK11%=:\n\t" \
  "s_nop 3\n\t" \
  "v_cmp_gt_i32_e64 s[72:73], 32, %[dq]\n\t" \
  "v_cmp_gt_i32_e64 s[74:75], 33, %[dq]\n\t" \
  "v_cmp_gt_i32_e64 s[76:77], 34, %[dq]\n\t" \
  "v_cmp_gt_i32_e64 s[78:79], 35, %[dq]\n\t" \
  "v_cndmask_b32_e64 v160, v160, v248, s[72:73]\n\t" \
  "v_cmp_gt_i32_e64 s[72:73], 48, %[dq]\n\t" \
  "v_cndmask_b32_e64 v161, v161, v248, s[74:75]\n\t" \
  "v_cmp_gt_i32_e64 s[74:75], 49, %[dq]\n\t" \
  "v_cndmask_b32_e64 v162, v162, v248, s[76:77]\n\t" \
  "v_cmp_gt_i32_e64 s[76:77], 50, %[dq]\n\t" \
  "v_cndmask_b32_e64 v163, v163, v248, s[78:79]\n\t" \
  "v_cmp_gt_i32_e64 s[78:79], 51, %[dq]\n\t" \
  "v_cndmask_b32_e64 v164, v164, v248, s[72:73]\n\t" \
  "v_cmp_gt_i32_e64 s[72:73], 40, %[dq]\n\t" \
  "v_cndmask_b32_e64 v165, v165, v248, s[74:75]\n\t" \
  "v_cmp_gt_i32_e64 s[74:75], 41, %[dq]\n\t" \
  "v_cndmask_b32_e64 v166, v166, v248, s[76:77]\n\t" \
  "v_cmp_gt_i32_e64 s[76:77], 42, %[dq]\n\t" \
  "v_cndmask_b32_e64 v167, v167, v248, s[78:79]\n\t" \
  "v_cmp_gt_i32_e64 s[78:79], 43, %[dq]\n\t" \
  "v_cndmask_b32_e64 v168, v168, v248, s[72:73]\n\t" \
  "v_cmp_gt_i32_e64 s[72:73], 56, %[dq]\n\t" \
  "v_cndmask_b32_e64 v169, v169, v248, s[74:75]\n\t" \
  "v_cmp_gt_i32_e64 s[74:75], 57, %[dq]\n\t" \
  "v_cndmask_b32_e64 v170, v170, v248, s[76:77]\n\t" \
  "v_cmp_gt_i32_e64 s[76:77], 58, %[dq]\n\t" \
  "v_cndmask_b32_e64 v171, v171, v248, s[78:79]\n\t" \
  "v_cmp_gt_i32_e64 s[78:79], 59, %[dq]\n\t" \
  "v_cndmask_b32_e64 v172, v172, v248, s[72:73]\n\t" \
  "v_cndmask_b32_e64 v173, v173, v248, s[74:75]\n\t" \
  "v_cndmask_b32_e64 v174, v174, v248, s[76:77]\n\t" \
  "v_cndmask_b32_e64 v175, v175, v248, s[78:79]\n\t" \
  "s_nop 0\n\t" \
  "s_branch LB_MASKB11%=\n\t" \
  "LB_RARE1%=:\n\t" \
  "v_max_f32_e32 v246, 0, v244\n\t" \
  "v_add_f32_e32 v240, v240, v246\n\t" \
  "v_exp_f32_e64 v247, -v246\n\t" \
  "v_sub_f32_e32 v144, v144, v246\n\t" \
  "v_sub_f32_e32 v160, v160, v246\n\t" \
  "v_xor_b32_e32 v192, 0x80000000, v240\n\t" \
  "v_sub_f32_e32 v145, v145, v246\n\t" \
  "v_sub_f32_e32 v161, v161, v246\n\t" \
  "v_xor_b32_e32 v193, 0x80000000, v240\n\t" \
  "v_sub_f32_e32 v146, v146, v246\n\t" \
  "v_sub_f32_e32 v162, v162, v246\n\t" \
  "v_xor_b32_e32 v194, 0x80000000, v240\n\t" \
  "v_sub_f32_e32 v147, v147, v246\n\t" \
  "v_sub_f32_e32 v163, v163, v246\n\t" \
  "v_xor_b32_e32 v195, 0x80000000, v240\n\t" \
  "v_sub_f32_e32 v148, v148, v246\n\t" \
  "v_sub_f32_e32 v164, v164, v246\n\t" \
  "v_xor_b32_e32 v196, 0x80000000, v240\n\t" \
  "v_sub_f32_e32 v149, v149, v246\n\t" \
  "v_sub_f32_e32 v165, v165, v246\n\t" \
  "v_xor_b32_e32 v197, 0x80000000, v240\n\t" \
  "v_sub_f32_e32 v150, v150, v246\n\t" \
  "v_sub_f32_e32 v166, v166, v246\n\t" \
  "v_xor_b32_e32 v198, 0x80000000, v240\n\t" \
  "v_sub_f32_e32 v151, v151, v246\n\t" \
  "v_sub_f32_e32 v167, v167, v246\n\t" \
  "v_xor_b32_e32 v199, 0x80000000, v240\n\t" \
  "v_sub_f32_e32 v152, v152, v246\n\t" \
  "v_sub_f32_e32 v168, v168, v246\n\t" \
  "v_xor_b32_e32 v200, 0x80000000, v240\n\t" \
  "v_sub_f32_e32 v153, v153, v246\n\t" \
  "v_sub_f32_e32 v169, v169, v246\n\t" \
  "v_xor_b32_e32 v201, 0x80000000, v240\n\t" \
  "v_sub_f32_e32 v154, v154, v246\n\t" \
  "v_sub_f32_e32 v170, v170, v246\n\t" \
  "v_xor_b32_e32 v202, 0x80000000, v240\n\t" \
  "v_sub_f32_e32 v155, v155, v246\n\t" \
  "v_sub_f32_e32 v171, v171, v246\n\t" \
  "v_xor_b32_e32 v203, 0x80000000, v240\n\t" \
  "v_sub_f32_e32 v156, v156, v246\n\t" \
  "v_sub_f32_e32 v172, v172, v246\n\t" \
  "v_xor_b32_e32 v204, 0x80000000, v240\n\t" \
  "v_sub_f32_e32 v157, v157, v246\n\t" \
  "v_sub_f32_e32 v173, v173, v246\n\t" \
  "v_xor_b32_e32 v205, 0x80000000, v240\n\t" \
  "v_sub_f32_e32 v158, v158, v246\n\t" \
  "v_sub_f32_e32 v174, v174, v246\n\t" \
  "v_xor_b32_e32 v206, 0x80000000, v240\n\t" \
  "v_sub_f32_e32 v159, v159, v246\n\t" \
  "v_sub_f32_e32 v175, v175, v246\n\t" \
  "v_xor_b32_e32 v207, 0x80000000, v240\n\t" \
  "s_mov_b32 s69, 1\n\t" \
  "s_nop 1\n\t" \
  "s_branch LB_RAREB1%=\n\t" \
  "LB_ROD0%=:\n\t" \
  "s_nop 11\n\t" \
  "v_mul_f32_e32 v48, v247, v48\n\t" \
  "v_mul_f32_e32 v49, v247, v49\n\t" \
  "v_mul_f32_e32 v50, v247, v50\n\t" \
  "v_mul_f32_e32 v51, v247, v51\n\t" \
  "v_mul_f32_e32 v52, v247, v52\n\t" \
  "v_mul_f32_e32 v53, v247, v53\n\t" \
  "v_mul_f32_e32 v54, v247, v54\n\t" \
  "v_mul_f32_e32 v55, v247, v55\n\t" \
  "v_mul_f32_e32 v56, v247, v56\n\t" \
  "v_mul_f32_e32 v57, v247, v57\n\t" \
  "v_mul_f32_e32 v58, v247, v58\n\t" \
  "v_mul_f32_e32 v59, v247, v59\n\t" \
  "v_mul_f32_e32 v60, v247, v60\n\t" \
  "v_mul_f32_e32 v61, v247, v61\n\t" \
  "v_mul_f32_e32 v62, v247, v62\n\t" \
  "v_mul_f32_e32 v63, v247, v63\n\t" \
  "v_mul_f32_e32 v64, v247, v64\n\t" \
  "v_mul_f32_e32 v65, v247, v65\n\t" \
  "v_mul_f32_e32 v66, v247, v66\n\t" \
  "v_mul_f32_e32 v67, v247, v67\n\t" \
  "v_mul_f32_e32 v68, v247, v68\n\t" \
  "v_mul_f32_e32 v69, v247, v69\n\t" \
  "v_mul_f32_e32 v70, v247, v70\n\t" \
  "v_mul_f32_e32 v71, v247, v71\n\t" \
  "v_mul_f32_e32 v72, v247, v72\n\t" \
  "v_mul_f32_e32 v73, v247, v73\n\t" \
  "v_mul_f32_e32 v74, v247, v74\n\t" \
  "v_mul_f32_e32 v75, v247, v75\n\t" \
  "v_mul_f32_e32 v76, v247, v76\n\t" \
  "v_mul_f32_e32 v77, v247, v77\n\t" \
  "v_mul_f32_e32 v78, v247, v78\n\t" \
  "v_mul_f32_e32 v79, v247, v79\n\t" \
  "v_mul_f32_e32 v80, v247, v80\n\t" \
  "v_mul_f32_e32 v81, v247, v81\n\t" \
  "v_mul_f32_e32 v82, v247, v82\n\t" \
  "v_mul_f32_e32 v83, v247, v83\n\t" \
  "v_mul_f32_e32 v84, v247, v84\n\t" \
  "v_mul_f32_e32 v85, v247, v85\n\t" \
  "v_mul_f32_e32 v86, v247, v86\n\t" \
  "v_mul_f32_e32 v87, v247, v87\n\t" \
  "v_mul_f32_e32 v88, v247, v88\n\t" \
  "v_mul_f32_e32 v89, v247, v89\n\t" \
  "v_mul_f32_e32 v90, v247, v90\n\t" \
  "v_mul_f32_e32 v91, v247, v91\n\t" \
  "v_mul_f32_e32 v92, v247, v92\n\t" \
  "v_mul_f32_e32 v93, v247, v93\n\t" \
  "v_mul_f32_e32 v94, v247, v94\n\t" \
  "v_mul_f32_e32 v95, v247, v95\n\t" \
  "v_mul_f32_e32 v96, v247, v96\n\t" \
  "v_mul_f32_e32 v97, v247, v97\n\t" \
  "v_mul_f32_e32 v98, v247, v98\n\t" \
  "v_mul_f32_e32 v99, v247, v99\n\t" \
  "v_mul_f32_e32 v100, v247, v100\n\t" \
  "v_mul_f32_e32 v101, v247, v101\n\t" \
  "v_mul_f32_e32 v102, v247, v102\n\t" \
  "v_mul_f32_e32 v103, v247, v103\n\t" \
  "v_mul_f32_e32 v104, v247, v104\n\t" \
  "v_mul_f32_e32 v105, v247, v105\n\t" \
  "v_mul_f32_e32 v106, v247, v106\n\t" \
  "v_mul_f32_e32 v107, v247, v107\n\t" \
  "v_mul_f32_e32 v108, v247, v108\n\t" \
  "v_mul_f32_e32 v109, v247, v109\n\t" \
  "v_mul_f32_e32 v110, v247, v110\n\t" \
  "v_mul_f32_e32 v111, v247, v111\n\t" \
  "v_mul_f32_e32 v241, v247, v241\n\t" \
  "s_mov_b32 s69, 0\n\t" \
  "s_nop 1\n\t" \
  "s_branch LB_ROBD0%=\n\t" \
  "LB_ROD1%=:\n\t" \
  "s_nop 11\n\t" \
  "v_mul_f32_e32 v48, v247, v48\n\t" \
  "v_mul_f32_e32 v49, v247, v49\n\t" \
  "v_mul_f32_e32 v50, v247, v50\n\t" \
  "v_mul_f32_e32 v51, v247, v51\n\t" \
  "v_mul_f32_e32 v52, v247, v52\n\t" \
  "v_mul_f32_e32 v53, v247, v53\n\t" \
  "v_mul_f32_e32 v54, v247, v54\n\t" \
  "v_mul_f32_e32 v55, v247, v55\n\t" \
  "v_mul_f32_e32 v56, v247, v56\n\t" \
  "v_mul_f32_e32 v57, v247, v57\n\t" \
  "v_mul_f32_e32 v58, v247, v58\n\t" \
  "v_mul_f32_e32 v59, v247, v59\n\t" \
  "v_mul_f32_e32 v60, v247, v60\n\t" \
  "v_mul_f32_e32 v61, v247, v61\n\t" \
  "v_mul_f32_e32 v62, v247, v62\n\t" \
  "v_mul_f32_e32 v63, v247, v63\n\t" \
  "v_mul_f32_e32 v64, v247, v64\n\t" \
  "v_mul_f32_e32 v65, v247, v65\n\t" \
  "v_mul_f32_e32 v66, v247, v66\n\t" \
  "v_mul_f32_e32 v67, v247, v67\n\t" \
  "v_mul_f32_e32 v68, v247, v68\n\t" \
  "v_mul_f32_e32 v69, v247, v69\n\t" \
  "v_mul_f32_e32 v70, v247, v70\n\t" \
  "v_mul_f32_e32 v71, v247, v71\n\t" \
  "v_mul_f32_e32 v72, v247, v72\n\t" \
  "v_mul_f32_e32 v73, v247, v73\n\t" \
  "v_mul_f32_e32 v74, v247, v74\n\t" \
  "v_mul_f32_e32 v75, v247, v75\n\t" \
  "v_mul_f32_e32 v76, v247, v76\n\t" \
  "v_mul_f32_e32 v77, v247, v77\n\t" \
  "v_mul_f32_e32 v78, v247, v78\n\t" \
  "v_mul_f32_e32 v79, v247, v79\n\t" \
  "v_mul_f32_e32 v80, v247, v80\n\t" \
  "v_mul_f32_e32 v81, v247, v81\n\t" \
  "v_mul_f32_e32 v82, v247, v82\n\t" \
  "v_mul_f32_e32 v83, v247, v83\n\t" \
  "v_mul_f32_e32 v84, v247, v84\n\t" \
  "v_mul_f32_e32 v85, v247, v85\n\t" \
  "v_mul_f32_e32 v86, v247, v86\n\t" \
  "v_mul_f32_e32 v87, v247, v87\n\t" \
  "v_mul_f32_e32 v88, v247, v88\n\t" \
  "v_mul_f32_e32 v89, v247, v89\n\t" \
  "v_mul_f32_e32 v90, v247, v90\n\t" \
  "v_mul_f32_e32 v91, v247, v91\n\t" \
  "v_mul_f32_e32 v92, v247, v92\n\t" \
  "v_mul_f32_e32 v93, v247, v93\n\t" \
  "v_mul_f32_e32 v94, v247, v94\n\t" \
  "v_mul_f32_e32 v95, v247, v95\n\t" \
  "v_mul_f32_e32 v96, v247, v96\n\t" \
  "v_mul_f32_e32 v97, v247, v97\n\t" \
  "v_mul_f32_e32 v98, v247, v98\n\t" \
  "v_mul_f32_e32 v99, v247, v99\n\t" \
  "v_mul_f32_e32 v100, v247, v100\n\t" \
  "v_mul_f32_e32 v101, v247, v101\n\t" \
  "v_mul_f32_e32 v102, v247, v102\n\t" \
  "v_mul_f32_e32 v103, v247, v103\n\t" \
  "v_mul_f32_e32 v104, v247, v104\n\t" \
  "v_mul_f32_e32 v105, v247, v105\n\t" \
  "v_mul_f32_e32 v106, v247, v106\n\t" \
  "v_mul_f32_e32 v107, v247, v107\n\t" \
  "v_mul_f32_e32 v108, v247, v108\n\t" \
  "v_mul_f32_e32 v109, v247, v109\n\t" \
  "v_mul_f32_e32 v110, v247, v110\n\t" \
  "v_mul_f32_e32 v111, v247, v111\n\t" \
  "v_mul_f32_e32 v241, v247, v241\n\t" \
  "s_mov_b32 s69, 0\n\t" \
  "s_nop 1\n\t" \
  "s_branch LB_ROBD1%=\n\t" \
  "L_END%=:\n\t" \
  "s_setprio 0\n\t"
#define ATTN_CLOB "v48", "v49", "v50", "v51", "v52", "v53", "v54", "v55", "v56", "v57", "v58", "v59", "v60", "v61", "v62", "v63", "v64", "v65", "v66", "v67", "v68", "v69", "v70", "v71", "v72", "v73", "v74", "v75", "v76", "v77", "v78", "v79", "v80", "v81", "v82", "v83", "v84", "v85", "v86", "v87", "v88", "v89", "v90", "v91", "v92", "v93", "v94", "v95", "v96", "v97", "v98", "v99", "v100", "v101", "v102", "v103", "v104", "v105", "v106", "v107", "v108", "v109", "v110", "v111", "v112", "v113", "v114", "v115", "v116", "v117", "v118", "v119", "v120", "v121", "v122", "v123", "v124", "v125", "v126", "v127", "v128", "v129", "v130", "v131", "v132", "v133", "v134", "v135", "v136", "v137", "v138", "v139", "v140", "v141", "v142", "v143", "v144", "v145", "v146", "v147", "v148", "v149", "v150", "v151", "v152", "v153", "v154", "v155", "v156", "v157", "v158", "v159", "v160", "v161", "v162", "v163", "v164", "v165", "v166", "v167", "v168", "v169", "v170", "v171", "v172", "v173", "v174", "v175", "v176", "v177", "v178", "v179", "v180", "v181", "v182", "v183", "v184", "v185", "v186", "v187", "v188", "v189", "v190", "v191", "v192", "v193", "v194", "v195", "v196", "v197", "v198", "v199", "v200", "v201", "v202", "v203", "v204", "v205", "v206", "v207", "v208", "v209", "v210", "v211", "v212", "v213", "v214", "v215", "v216", "v217", "v218", "v219", "v220", "v221", "v222", "v223", "v224", "v225", "v226", "v227", "v228", "v229", "v230", "v231", "v232", "v233", "v234", "v235", "v236", "v237", "v238", "v239", "v240", "v241", "v242", "v243", "v244", "v245", "v246", "v247", "v248", "v249", "v250", "v251", "v252", "v253", "v254", "v255", "s48", "s49", "s50", "s51", "s52", "s53", "s54", "s55", "s56", "s57", "s64", "s65", "s66", "s67", "s68", "s69", "s70", "s71", "s72", "s73", "s74", "s75", "s76", "s77", "s78", "s79", "s80", "vcc", "scc", "memory"

#define ATTNM_ASM \
  "s_mov_b32 s69, 0\n\t" \
  "s_mov_b32 s68, 1\n\t" \
  "s_mov_b32 s71, 0\n\t" \
  "v_readlane_b32 s53, %[prm], 10\n\t" \
  "v_readlane_b32 s48, %[prm], 4\n\t" \
  "v_readlane_b32 s49, %[prm], 5\n\t" \
  "v_readlane_b32 s50, %[prm], 7\n\t" \
  "v_readlane_b32 s64, %[prm], 0\n\t" \
  "v_readlane_b32 s65, %[prm], 1\n\t" \
  "v_readlane_b32 s66, %[prm], 2\n\t" \
  "v_readlane_b32 s67, %[prm], 3\n\t" \
  "v_readlane_b32 s80, %[prm], 6\n\t" \
  "v_readlane_b32 s51, %[prm], 8\n\t" \
  "v_readlane_b32 s52, %[prm], 9\n\t" \
  "v_readlane_b32 s70, %[prm], 11\n\t" \
  "s_mov_b32 s54, 0\n\t" \
  "s_mov_b32 s58, 0x3db504f3\n\t" \
  "s_mov_b32 s59, 0x3fb8aa3b\n\t" \
  "s_mov_b32 s60, 0x3e0293ee\n\t" \
  "s_mov_b32 s62, 0x3fb8aa3b\n\t" \
  "v_mov_b32_e32 v248, 0xff800000\n\t" \
  "v_mov_b32_e32 v192, 0\n\t" \
  "v_mov_b32_e32 v193, 0\n\t" \
  "v_mov_b32_e32 v194, 0\n\t" \
  "v_mov_b32_e32 v195, 0\n\t" \
  "v_mov_b32_e32 v196, 0\n\t" \
  "v_mov_b32_e32 v197, 0\n\t" \
  "v_mov_b32_e32 v198, 0\n\t" \
  "v_mov_b32_e32 v199, 0\n\t" \
  "v_mov_b32_e32 v200, 0\n\t" \
  "v_mov_b32_e32 v201, 0\n\t" \
  "v_mov_b32_e32 v202, 0\n\t" \
  "v_mov_b32_e32 v203, 0\n\t" \
  "v_mov_b32_e32 v204, 0\n\t" \
  "v_mov_b32_e32 v205, 0\n\t" \
  "v_mov_b32_e32 v206, 0\n\t" \
  "v_mov_b32_e32 v207, 0\n\t" \
  "v_mov_b32_e32 v242, 0x3f803f80\n\t" \
  "v_mov_b32_e32 v243, 0x3f803f80\n\t" \
  "v_mov_b32_e32 v244, 0x3f803f80\n\t" \
  "v_mov_b32_e32 v245, 0x3f803f80\n\t" \
  "v_mov_b32_e32 v249, %[lin]\n\t" \
  "v_add_u32_e32 v250, 0x10000, %[lin]\n\t" \
  "v_add_u32_e32 v252, 0x1000, %[lin]\n\t" \
  "s_cmp_lg_u32 s53, 0\n\t" \
  "s_cbranch_scc1 LB_PRO%=\n\t" \
  "LA_PRO%=:\n\t" \
  "ds_read_b128 v[224:227], v249 offset:0\n\t" \
  "ds_read_b128 v[228:231], v249 offset:1024\n\t" \
  "ds_read_b128 v[232:235], v249 offset:2048\n\t" \
  "ds_read_b128 v[236:239], v249 offset:3072\n\t" \
  "ds_read_b128 v[208:211], v249 offset:4096\n\t" \
  "ds_read_b128 v[212:215], v249 offset:5120\n\t" \
  "ds_read_b128 v[216:219], v249 offset:6144\n\t" \
  "ds_read_b128 v[220:223], v249 offset:7168\n\t" \
  "s_waitcnt lgkmcnt(7)\n\t" \
  "v_mfma_f32_32x32x16_bf16 v[112:127], v[224:227], v[16:19], 0\n\t" \
  "ds_read_b128 v[224:227], v249 offset:8192\n\t" \
  "v_mov_b32_e32 v48, 0\n\t" \
  "v_mov_b32_e32 v49, 0\n\t" \
  "v_mov_b32_e32 v50, 0\n\t" \
  "v_mov_b32_e32 v51, 0\n\t" \
  "s_waitcnt lgkmcnt(7)\n\t" \
  "v_mfma_f32_32x32x16_bf16 v[112:127], v[228:231], v[20:23], v[112:127]\n\t" \
  "ds_read_b128 v[228:231], v249 offset:9216\n\t" \
  "v_mov_b32_e32 v52, 0\n\t" \
  "v_mov_b32_e32 v53, 0\n\t" \
  "v_mov_b32_e32 v54, 0\n\t" \
  "v_mov_b32_e32 v55, 0\n\t" \
  "s_waitcnt lgkmcnt(7)\n\t" \
  "v_mfma_f32_32x32x16_bf16 v[112:127], v[232:235], v[24:27], v[112:127]\n\t" \
  "ds_read_b128 v[232:235], v249 offset:10240\n\t" \
  "v_mov_b32_e32 v56, 0\n\t" \
  "v_mov_b32_e32 v57, 0\n\t" \
  "v_mov_b32_e32 v58, 0\n\t" \
  "v_mov_b32_e32 v59, 0\n\t" \
  "s_waitcnt lgkmcnt(7)\n\t" \
  "v_mfma_f32_32x32x16_bf16 v[112:127], v[236:239], v[28:31], v[112:127]\n\t" \
  "ds_read_b128 v[236:239], v249 offset:11264\n\t" \
  "v_mov_b32_e32 v60, 0\n\t" \
  "v_mov_b32_e32 v61, 0\n\t" \
  "v_mov_b32_e32 v62, 0\n\t" \
  "v_mov_b32_e32 v63, 0\n\t" \
  "s_waitcnt lgkmcnt(7)\n\t" \
  "v_mfma_f32_32x32x16_bf16 v[112:127], v[208:211], v[32:35], v[112:127]\n\t" \
  "ds_read_b128 v[208:211], v249 offset:12288\n\t" \
  "v_mov_b32_e32 v64, 0\n\t" \
  "v_mov_b32_e32 v65, 0\n\t" \
  "v_mov_b32_e32 v66, 0\n\t" \
  "v_mov_b32_e32 v67, 0\n\t" \
  "s_waitcnt lgkmcnt(7)\n\t" \
  "v_mfma_f32_32x32x16_bf16 v[112:127], v[212:215], v[36:39], v[112:127]\n\t" \
  "ds_read_b128 v[212:215], v249 offset:13312\n\t" \
  "v_mov_b32_e32 v68, 0\n\t" \
  "v_mov_b32_e32 v69, 0\n\t" \
  "v_mov_b32_e32 v70, 0\n\t" \
  "v_mov_b32_e32 v71, 0\n\t" \
  "s_waitcnt lgkmcnt(7)\n\t" \
  "v_mfma_f32_32x32x16_bf16 v[112:127], v[216:219], v[40:43], v[112:127]\n\t" \
  "ds_read_b128 v[216:219], v249 offset:14336\n\t" \
  "v_mov_b32_e32 v72, 0\n\t" \
  "v_mov_b32_e32 v73, 0\n\t" \
  "v_mov_b32_e32 v74, 0\n\t" \
  "v_mov_b32_e32 v75, 0\n\t" \
  "s_waitcnt lgkmcnt(7)\n\t" \
  "v_mfma_f32_32x32x16_bf16 v[112:127], v[220:223], v[44:47], v[112:127]\n\t" \
  "ds_read_b128 v[220:223], v249 offset:15360\n\t" \
  "v_mov_b32_e32 v76, 0\n\t" \
  "v_mov_b32_e32 v77, 0\n\t" \
  "v_mov_b32_e32 v78, 0\n\t" \
  "v_mov_b32_e32 v79, 0\n\t" \
  "s_waitcnt lgkmcnt(7)\n\t" \
  "v_mfma_f32_32x32x16_bf16 v[128:143], v[224:227], v[16:19], 0\n\t" \
  "v_mov_b32_e32 v80, 0\n\t" \
  "v_mov_b32_e32 v81, 0\n\t" \
  "v_mov_b32_e32 v82, 0\n\t" \
  "v_mov_b32_e32 v83, 0\n\t" \
  "s_waitcnt lgkmcnt(6)\n\t" \
  "v_mfma_f32_32x32x16_bf16 v[128:143], v[228:231], v[20:23], v[128:143]\n\t" \
  "v_mov_b32_e32 v84, 0\n\t" \
  "v_mov_b32_e32 v85, 0\n\t" \
  "v_mov_b32_e32 v86, 0\n\t" \
  "v_mov_b32_e32 v87, 0\n\t" \
  "s_waitcnt lgkmcnt(5)\n\t" \
  "v_mfma_f32_32x32x16_bf16 v[128:143], v[232:235], v[24:27], v[128:143]\n\t" \
  "v_mov_b32_e32 v88, 0\n\t" \
  "v_mov_b32_e32 v89, 0\n\t" \
  "v_mov_b32_e32 v90, 0\n\t" \
  "v_mov_b32_e32 v91, 0\n\t" \
  "s_waitcnt lgkmcnt(4)\n\t" \
  "v_mfma_f32_32x32x16_bf16 v[128:143], v[236:239], v[28:31], v[128:143]\n\t" \
  "v_mov_b32_e32 v92, 0\n\t" \
  "v_mov_b32_e32 v93, 0\n\t" \
  "v_mov_b32_e32 v94, 0\n\t" \
  "v_mov_b32_e32 v95, 0\n\t" \
  "s_waitcnt lgkmcnt(3)\n\t" \
  "v_mfma_f32_32x32x16_bf16 v[128:143], v[208:211], v[32:35], v[128:143]\n\t" \
  "v_mov_b32_e32 v96, 0\n\t" \
  "v_mov_b32_e32 v97, 0\n\t" \
  "v_mov_b32_e32 v98, 0\n\t" \
  "v_mov_b32_e32 v99, 0\n\t" \
  "s_waitcnt lgkmcnt(2)\n\t" \
  "v_mfma_f32_32x32x16_bf16 v[128:143], v[212:215], v[36:39], v[128:143]\n\t" \
  "v_mov_b32_e32 v100, 0\n\t" \
  "v_mov_b32_e32 v101, 0\n\t" \
  "v_mov_b32_e32 v102, 0\n\t" \
  "v_mov_b32_e32 v103, 0\n\t" \
  "s_waitcnt lgkmcnt(1)\n\t" \
  "v_mfma_f32_32x32x16_bf16 v[128:143], v[216:219], v[40:43], v[128:143]\n\t" \
  "v_mov_b32_e32 v104, 0\n\t" \
  "v_mov_b32_e32 v105, 0\n\t" \
  "v_mov_b32_e32 v106, 0\n\t" \
  "v_mov_b32_e32 v107, 0\n\t" \
  "s_waitcnt lgkmcnt(0)\n\t" \
  "v_mfma_f32_32x32x16_bf16 v[128:143], v[220:223], v[44:47], v[128:143]\n\t" \
  "v_mov_b32_e32 v108, 0\n\t" \
  "v_mov_b32_e32 v109, 0\n\t" \
  "v_mov_b32_e32 v110, 0\n\t" \
  "v_mov_b32_e32 v111, 0\n\t" \
  "ds_read_b128 v[224:227], v249 offset:16384\n\t" \
  "ds_read_b128 v[228:231], v249 offset:17408\n\t" \
  "ds_read_b128 v[232:235], v249 offset:18432\n\t" \
  "ds_read_b128 v[236:239], v249 offset:19456\n\t" \
  "v_mov_b32_e32 v208, 0\n\t" \
  "v_mov_b32_e32 v209, 0\n\t" \
  "v_mov_b32_e32 v210, 0\n\t" \
  "v_mov_b32_e32 v211, 0\n\t" \
  "v_mov_b32_e32 v212, 0\n\t" \
  "v_mov_b32_e32 v213, 0\n\t" \
  "v_mov_b32_e32 v214, 0\n\t" \
  "v_mov_b32_e32 v215, 0\n\t" \
  "v_mov_b32_e32 v216, 0\n\t" \
  "v_mov_b32_e32 v217, 0\n\t" \
  "v_mov_b32_e32 v218, 0\n\t" \
  "v_mov_b32_e32 v219, 0\n\t" \
  "v_mov_b32_e32 v220, 0\n\t" \
  "v_mov_b32_e32 v221, 0\n\t" \
  "v_mov_b32_e32 v222, 0\n\t" \
  "v_mov_b32_e32 v223, 0\n\t" \
  "v_mov_b32_e32 v188, 0\n\t" \
  "v_mov_b32_e32 v189, 0\n\t" \
  "v_mov_b32_e32 v190, 0\n\t" \
  "v_mov_b32_e32 v191, 0\n\t" \
  "s_cmp_eq_u32 s52, 1\n\t" \
  "s_cbranch_scc1 LA_PMASK%=\n\t" \
  "LA_PMASKB%=:\n\t" \
  "v_max_f32_e32 v241, v112, v113\n\t" \
  "v_max3_f32 v241, v241, v114, v115\n\t" \
  "v_max3_f32 v241, v241, v116, v117\n\t" \
  "v_max3_f32 v241, v241, v118, v119\n\t" \
  "v_max3_f32 v241, v241, v120, v121\n\t" \
  "v_max3_f32 v241, v241, v122, v123\n\t" \
  "v_max3_f32 v241, v241, v124, v125\n\t" \
  "v_max3_f32 v241, v241, v126, v127\n\t" \
  "v_max3_f32 v241, v241, v128, v129\n\t" \
  "v_max3_f32 v241, v241, v130, v131\n\t" \
  "v_max3_f32 v241, v241, v132, v133\n\t" \
  "v_max3_f32 v241, v241, v134, v135\n\t" \
  "v_max3_f32 v241, v241, v136, v137\n\t" \
  "v_max3_f32 v241, v241, v138, v139\n\t" \
  "v_max3_f32 v241, v241, v140, v141\n\t" \
  "v_max3_f32 v241, v241, v142, v143\n\t" \
  "v_mov_b32_e32 v246, v241\n\t" \
  "s_nop 1\n\t" \
  "v_permlane32_swap_b32_e32 v241, v246\n\t" \
  "s_nop 0\n\t" \
  "v_max_f32_e32 v241, v241, v246\n\t" \
  "v_mul_f32_e64 v240, -v241, s60\n\t" \
  "v_cvt_pk_bf16_f32 v112, 0, v112\n\t" \
  "v_cvt_pk_bf16_f32 v113, 0, v113\n\t" \
  "v_cvt_pk_bf16_f32 v114, 0, v114\n\t" \
  "v_cvt_pk_bf16_f32 v115, 0, v115\n\t" \
  "v_cvt_pk_bf16_f32 v116, 0, v116\n\t" \
  "v_cvt_pk_bf16_f32 v117, 0, v117\n\t" \
  "v_cvt_pk_bf16_f32 v118, 0, v118\n\t" \
  "v_cvt_pk_bf16_f32 v119, 0, v119\n\t" \
  "v_mul_f32_e32 v112, s58, v112\n\t" \
  "v_mul_f32_e32 v113, s58, v113\n\t" \
  "v_mul_f32_e32 v114, s58, v114\n\t" \
  "v_mul_f32_e32 v115, s58, v115\n\t" \
  "v_mul_f32_e32 v116, s58, v116\n\t" \
  "v_mul_f32_e32 v117, s58, v117\n\t" \
  "v_mul_f32_e32 v118, s58, v118\n\t" \
  "v_cvt_pk_bf16_f32 v112, 0, v112\n\t" \
  "v_cvt_pk_bf16_f32 v113, 0, v113\n\t" \
  "v_cvt_pk_bf16_f32 v114, 0, v114\n\t" \
  "v_cvt_pk_bf16_f32 v115, 0, v115\n\t" \
  "v_cvt_pk_bf16_f32 v116, 0, v116\n\t" \
  "v_cvt_pk_bf16_f32 v117, 0, v117\n\t" \
  "v_fma_f32 v112, v112, s59, v240\n\t" \
  "v_fma_f32 v113, v113, s59, v240\n\t" \
  "v_fma_f32 v114, v114, s59, v240\n\t" \
  "v_fma_f32 v115, v115, s59, v240\n\t" \
  "v_fma_f32 v116, v116, s59, v240\n\t" \
  "v_exp_f32_e32 v112, v112\n\t" \
  "v_exp_f32_e32 v113, v113\n\t" \
  "v_exp_f32_e32 v114, v114\n\t" \
  "v_exp_f32_e32 v115, v115\n\t" \
  "s_waitcnt lgkmcnt(0)\n\t" \
  "s_barrier\n\t" \
  "s_add_u32 s71, s71, 1\n\t" \
  "s_cmp_eq_u32 s52, 1\n\t" \
  "s_cbranch_scc1 LA_DRAIN0%=\n\t" \
  "s_branch LA_STEP1%=\n\t" \
  "LA_STEP0%=:\n\t" \
  "ds_read_b128 v[224:227], v249 offset:0\n\t" \
  "ds_read_b128 v[228:231], v249 offset:1024\n\t" \
  "ds_read_b128 v[232:235], v249 offset:2048\n\t" \
  "ds_read_b128 v[236:239], v249 offset:3072\n\t" \
  "v_mfma_f32_32x32x16_bf16 v[48:63], v[208:211], v[188:191], v[48:63]\n\t" \
  "ds_read_b128 v[208:211], v249 offset:4096\n\t" \
  "v_exp_f32_e32 v148, v148\n\t" \
  "v_fma_f32 v149, v149, s59, v240\n\t" \
  "v_cvt_pk_bf16_f32 v150, 0, v150\n\t" \
  "v_mul_f32_e32 v151, s58, v151\n\t" \
  "v_cvt_pk_bf16_f32 v152, 0, v152\n\t" \
  "v_cvt_pk_bf16_f32 v176, v144, v145\n\t" \
  "v_mfma_f32_32x32x16_bf16 v[64:79], v[212:215], v[188:191], v[64:79]\n\t" \
  "ds_read_b128 v[212:215], v249 offset:5120\n\t" \
  "v_exp_f32_e32 v149, v149\n\t" \
  "v_fma_f32 v150, v150, s59, v240\n\t" \
  "v_cvt_pk_bf16_f32 v151, 0, v151\n\t" \
  "v_mul_f32_e32 v152, s58, v152\n\t" \
  "v_cvt_pk_bf16_f32 v153, 0, v153\n\t" \
  "v_mfma_f32_32x32x16_bf16 v[80:95], v[216:219], v[188:191], v[80:95]\n\t" \
  "ds_read_b128 v[216:219], v249 offset:6144\n\t" \
  "v_exp_f32_e32 v150, v150\n\t" \
  "v_fma_f32 v151, v151, s59, v240\n\t" \
  "v_cvt_pk_bf16_f32 v152, 0, v152\n\t" \
  "v_mul_f32_e32 v153, s58, v153\n\t" \
  "v_cvt_pk_bf16_f32 v154, 0, v154\n\t" \
  "v_cvt_pk_bf16_f32 v177, v146, v147\n\t" \
  "v_mfma_f32_32x32x16_bf16 v[96:111], v[220:223], v[188:191], v[96:111]\n\t" \
  "ds_read_b128 v[220:223], v249 offset:7168\n\t" \
  "v_exp_f32_e32 v151, v151\n\t" \
  "v_fma_f32 v152, v152, s59, v240\n\t" \
  "v_cvt_pk_bf16_f32 v153, 0, v153\n\t" \
  "v_mfma_f32_32x32x16_bf16 v[192:207], v[242:245], v[188:191], v[192:207]\n\t" \
  "v_mul_f32_e32 v154, s58, v154\n\t" \
  "v_cvt_pk_bf16_f32 v155, 0, v155\n\t" \
  "s_cmp_lg_u32 s69, 0\n\t" \
  "s_cbranch_scc1 LA_ROS0%=\n\t" \
  "LA_ROBS0%=:\n\t" \
  "s_waitcnt lgkmcnt(4)\n\t" \
  "v_mfma_f32_32x32x16_bf16 v[112:127], v[224:227], v[16:19], 0\n\t" \
  "ds_read_b128 v[224:227], v249 offset:8192\n\t" \
  "v_exp_f32_e32 v152, v152\n\t" \
  "v_fma_f32 v153, v153, s59, v240\n\t" \
  "v_cvt_pk_bf16_f32 v154, 0, v154\n\t" \
  "v_mul_f32_e32 v155, s58, v155\n\t" \
  "v_cvt_pk_bf16_f32 v156, 0, v156\n\t" \
  "v_cvt_pk_bf16_f32 v178, v148, v149\n\t" \
  "v_mfma_f32_32x32x16_bf16 v[112:127], v[228:231], v[20:23], v[112:127]\n\t" \
  "ds_read_b128 v[228:231], v249 offset:9216\n\t" \
  "v_exp_f32_e32 v153, v153\n\t" \
  "v_fma_f32 v154, v154, s59, v240\n\t" \
  "v_cvt_pk_bf16_f32 v155, 0, v155\n\t" \
  "v_mul_f32_e32 v156, s58, v156\n\t" \
  "v_cvt_pk_bf16_f32 v157, 0, v157\n\t" \
  "s_add_u32 s68, s68, 1\n\t" \
  "v_mfma_f32_32x32x16_bf16 v[112:127], v[232:235], v[24:27], v[112:127]\n\t" \
  "ds_read_b128 v[232:235], v249 offset:10240\n\t" \
  "v_exp_f32_e32 v154, v154\n\t" \
  "v_fma_f32 v155, v155, s59, v240\n\t" \
  "v_cvt_pk_bf16_f32 v156, 0, v156\n\t" \
  "v_mul_f32_e32 v157, s58, v157\n\t" \
  "v_cvt_pk_bf16_f32 v158, 0, v158\n\t" \
  "v_cvt_pk_bf16_f32 v179, v150, v151\n\t" \
  "v_mfma_f32_32x32x16_bf16 v[112:127], v[236:239], v[28:31], v[112:127]\n\t" \
  "ds_read_b128 v[236:239], v249 offset:11264\n\t" \
  "v_exp_f32_e32 v155, v155\n\t" \
  "v_fma_f32 v156, v156, s59, v240\n\t" \
  "v_cvt_pk_bf16_f32 v157, 0, v157\n\t" \
  "v_mul_f32_e32 v158, s58, v158\n\t" \
  "v_cvt_pk_bf16_f32 v159, 0, v159\n\t" \
  "v_cvt_pk_bf16_f32 v180, v152, v153\n\t" \
  "s_waitcnt lgkmcnt(4)\n\t" \
  "v_mfma_f32_32x32x16_bf16 v[112:127], v[208:211], v[32:35], v[112:127]\n\t" \
  "ds_read_b128 v[208:211], v249 offset:12288\n\t" \
  "v_exp_f32_e32 v156, v156\n\t" \
  "v_fma_f32 v157, v157, s59, v240\n\t" \
  "v_cvt_pk_bf16_f32 v158, 0, v158\n\t" \
  "v_mul_f32_e32 v159, s58, v159\n\t" \
  "v_cvt_pk_bf16_f32 v160, 0, v160\n\t" \
  "v_mfma_f32_32x32x16_bf16 v[112:127], v[212:215], v[36:39], v[112:127]\n\t" \
  "ds_read_b128 v[212:215], v249 offset:13312\n\t" \
  "v_exp_f32_e32 v157, v157\n\t" \
  "v_fma_f32 v158, v158, s59, v240\n\t" \
  "v_cvt_pk_bf16_f32 v159, 0, v159\n\t" \
  "v_mul_f32_e32 v160, s58, v160\n\t" \
  "v_cvt_pk_bf16_f32 v161, 0, v161\n\t" \
  "v_cvt_pk_bf16_f32 v181, v154, v155\n\t" \
  "v_mfma_f32_32x32x16_bf16 v[112:127], v[216:219], v[40:43], v[112:127]\n\t" \
  "ds_read_b128 v[216:219], v249 offset:14336\n\t" \
  "v_exp_f32_e32 v158, v158\n\t" \
  "v_fma_f32 v159, v159, s59, v240\n\t" \
  "v_cvt_pk_bf16_f32 v160, 0, v160\n\t" \
  "v_mul_f32_e32 v161, s58, v161\n\t" \
  "v_cvt_pk_bf16_f32 v162, 0, v162\n\t" \
  "v_cvt_pk_bf16_f32 v182, v156, v157\n\t" \
  "v_mfma_f32_32x32x16_bf16 v[112:127], v[220:223], v[44:47], v[112:127]\n\t" \
  "ds_read_b128 v[220:223], v249 offset:15360\n\t" \
  "v_exp_f32_e32 v159, v159\n\t" \
  "v_fma_f32 v160, v160, s59, v240\n\t" \
  "v_cvt_pk_bf16_f32 v161, 0, v161\n\t" \
  "v_mul_f32_e32 v162, s58, v162\n\t" \
  "v_cvt_pk_bf16_f32 v163, 0, v163\n\t" \
  "s_waitcnt lgkmcnt(4)\n\t" \
  "v_mfma_f32_32x32x16_bf16 v[128:143], v[224:227], v[16:19], 0\n\t" \
  "ds_read_b128 v[224:227], v250 offset:0\n\t" \
  "v_exp_f32_e32 v160, v160\n\t" \
  "v_fma_f32 v161, v161, s59, v240\n\t" \
  "v_cvt_pk_bf16_f32 v162, 0, v162\n\t" \
  "v_mul_f32_e32 v163, s58, v163\n\t" \
  "v_cvt_pk_bf16_f32 v164, 0, v164\n\t" \
  "v_cvt_pk_bf16_f32 v183, v158, v159\n\t" \
  "v_mfma_f32_32x32x16_bf16 v[128:143], v[228:231], v[20:23], v[128:143]\n\t" \
  "ds_read_b128 v[228:231], v250 offset:1024\n\t" \
  "v_exp_f32_e32 v161, v161\n\t" \
  "v_fma_f32 v162, v162, s59, v240\n\t" \
  "v_cvt_pk_bf16_f32 v163, 0, v163\n\t" \
  "v_mul_f32_e32 v164, s58, v164\n\t" \
  "v_cvt_pk_bf16_f32 v165, 0, v165\n\t" \
  "s_cmp_eq_u32 s68, s52\n\t" \
  "s_cbranch_scc1 LA_MASK00%=\n\t" \
  "LA_MASKB00%=:\n\t" \
  "v_mfma_f32_32x32x16_bf16 v[128:143], v[232:235], v[24:27], v[128:143]\n\t" \
  "ds_read_b128 v[232:235], v250 offset:2048\n\t" \
  "v_exp_f32_e32 v162, v162\n\t" \
  "v_fma_f32 v163, v163, s59, v240\n\t" \
  "v_cvt_pk_bf16_f32 v164, 0, v164\n\t" \
  "v_mul_f32_e32 v165, s58, v165\n\t" \
  "v_cvt_pk_bf16_f32 v166, 0, v166\n\t" \
  "v_cvt_pk_bf16_f32 v184, v160, v161\n\t" \
  "v_max_f32_e32 v241, v112, v113\n\t" \
  "v_mfma_f32_32x32x16_bf16 v[128:143], v[236:239], v[28:31], v[128:143]\n\t" \
  "ds_read_b128 v[236:239], v250 offset:3072\n\t" \
  "v_exp_f32_e32 v163, v163\n\t" \
  "v_fma_f32 v164, v164, s59, v240\n\t" \
  "v_cvt_pk_bf16_f32 v165, 0, v165\n\t" \
  "v_mul_f32_e32 v166, s58, v166\n\t" \
  "v_cvt_pk_bf16_f32 v167, 0, v167\n\t" \
  "v_max3_f32 v241, v241, v114, v115\n\t" \
  "s_waitcnt lgkmcnt(4)\n\t" \
  "v_mfma_f32_32x32x16_bf16 v[128:143], v[208:211], v[32:35], v[128:143]\n\t" \
  "ds_read_b128 v[208:211], v250 offset:4096\n\t" \
  "v_exp_f32_e32 v164, v164\n\t" \
  "v_fma_f32 v165, v165, s59, v240\n\t" \
  "v_cvt_pk_bf16_f32 v166, 0, v166\n\t" \
  "v_mul_f32_e32 v167, s58, v167\n\t" \
  "v_cvt_pk_bf16_f32 v168, 0, v168\n\t" \
  "v_max3_f32 v241, v241, v116, v117\n\t" \
  "v_mfma_f32_32x32x16_bf16 v[128:143], v[212:215], v[36:39], v[128:143]\n\t" \
  "ds_read_b128 v[212:215], v250 offset:5120\n\t" \
  "v_exp_f32_e32 v165, v165\n\t" \
  "v_fma_f32 v166, v166, s59, v240\n\t" \
  "v_cvt_pk_bf16_f32 v167, 0, v167\n\t" \
  "v_mul_f32_e32 v168, s58, v168\n\t" \
  "v_cvt_pk_bf16_f32 v169, 0, v169\n\t" \
  "v_cvt_pk_bf16_f32 v185, v162, v163\n\t" \
  "v_max3_f32 v241, v241, v118, v119\n\t" \
  "v_mfma_f32_32x32x16_bf16 v[128:143], v[216:219], v[40:43], v[128:143]\n\t" \
  "ds_read_b128 v[216:219], v250 offset:6144\n\t" \
  "v_exp_f32_e32 v166, v166\n\t" \
  "v_fma_f32 v167, v167, s59, v240\n\t" \
  "v_cvt_pk_bf16_f32 v168, 0, v168\n\t" \
  "v_mul_f32_e32 v169, s58, v169\n\t" \
  "v_cvt_pk_bf16_f32 v170, 0, v170\n\t" \
  "v_cvt_pk_bf16_f32 v186, v164, v165\n\t" \
  "v_max3_f32 v241, v241, v120, v121\n\t" \
  "v_mfma_f32_32x32x16_bf16 v[128:143], v[220:223], v[44:47], v[128:143]\n\t" \
  "ds_read_b128 v[220:223], v250 offset:7168\n\t" \
  "v_exp_f32_e32 v167, v167\n\t" \
  "v_fma_f32 v168, v168, s59, v240\n\t" \
  "v_cvt_pk_bf16_f32 v169, 0, v169\n\t" \
  "v_mul_f32_e32 v170, s58, v170\n\t" \
  "v_cvt_pk_bf16_f32 v171, 0, v171\n\t" \
  "v_max3_f32 v241, v241, v122, v123\n\t" \
  "s_waitcnt lgkmcnt(4)\n\t" \
  "v_mfma_f32_32x32x16_bf16 v[48:63], v[224:227], v[176:179], v[48:63]\n\t" \
  "ds_read_b128 v[224:227], v250 offset:8192\n\t" \
  "v_exp_f32_e32 v168, v168\n\t" \
  "v_fma_f32 v169, v169, s59, v240\n\t" \
  "v_cvt_pk_bf16_f32 v170, 0, v170\n\t" \
  "v_mul_f32_e32 v171, s58, v171\n\t" \
  "v_cvt_pk_bf16_f32 v172, 0, v172\n\t" \
  "v_cvt_pk_bf16_f32 v187, v166, v167\n\t" \
  "v_max3_f32 v241, v241, v124, v125\n\t" \
  "v_mfma_f32_32x32x16_bf16 v[64:79], v[228:231], v[176:179], v[64:79]\n\t" \
  "ds_read_b128 v[228:231], v250 offset:9216\n\t" \
  "v_exp_f32_e32 v169, v169\n\t" \
  "v_fma_f32 v170, v170, s59, v240\n\t" \
  "v_cvt_pk_bf16_f32 v171, 0, v171\n\t" \
  "v_mfma_f32_32x32x16_bf16 v[192:207], v[242:245], v[184:187], v[192:207]\n\t" \
  "v_mul_f32_e32 v172, s58, v172\n\t" \
  "v_cvt_pk_bf16_f32 v173, 0, v173\n\t" \
  "s_cmp_eq_u32 s68, s52\n\t" \
  "s_cbranch_scc1 LA_MASK01%=\n\t" \
  "LA_MASKB01%=:\n\t" \
  "v_max3_f32 v241, v241, v126, v127\n\t" \
  "v_mfma_f32_32x32x16_bf16 v[80:95], v[232:235], v[176:179], v[80:95]\n\t" \
  "ds_read_b128 v[232:235], v250 offset:10240\n\t" \
  "v_exp_f32_e32 v170, v170\n\t" \
  "v_fma_f32 v171, v171, s59, v240\n\t" \
  "v_cvt_pk_bf16_f32 v172, 0, v172\n\t" \
  "v_mul_f32_e32 v173, s58, v173\n\t" \
  "v_cvt_pk_bf16_f32 v174, 0, v174\n\t" \
  "v_max3_f32 v241, v241, v128, v129\n\t" \
  "v_max3_f32 v241, v241, v130, v131\n\t" \
  "v_mfma_f32_32x32x16_bf16 v[96:111], v[236:239], v[176:179], v[96:111]\n\t" \
  "ds_read_b128 v[236:239], v250 offset:11264\n\t" \
  "v_exp_f32_e32 v171, v171\n\t" \
  "v_fma_f32 v172, v172, s59, v240\n\t" \
  "v_cvt_pk_bf16_f32 v173, 0, v173\n\t" \
  "v_mfma_f32_32x32x16_bf16 v[192:207], v[242:245], v[176:179], v[192:207]\n\t" \
  "v_mul_f32_e32 v174, s58, v174\n\t" \
  "v_cvt_pk_bf16_f32 v175, 0, v175\n\t" \
  "v_max3_f32 v241, v241, v132, v133\n\t" \
  "v_max3_f32 v241, v241, v134, v135\n\t" \
  "s_waitcnt lgkmcnt(4)\n\t" \
  "v_mfma_f32_32x32x16_bf16 v[48:63], v[208:211], v[180:183], v[48:63]\n\t" \
  "ds_read_b128 v[208:211], v250 offset:12288\n\t" \
  "v_exp_f32_e32 v172, v172\n\t" \
  "v_fma_f32 v173, v173, s59, v240\n\t" \
  "v_cvt_pk_bf16_f32 v174, 0, v174\n\t" \
  "v_mul_f32_e32 v175, s58, v175\n\t" \
  "v_cvt_pk_bf16_f32 v112, 0, v112\n\t" \
  "v_max3_f32 v241, v241, v136, v137\n\t" \
  "v_max3_f32 v241, v241, v138, v139\n\t" \
  "v_mfma_f32_32x32x16_bf16 v[64:79], v[212:215], v[180:183], v[64:79]\n\t" \
  "ds_read_b128 v[212:215], v250 offset:13312\n\t" \
  "v_exp_f32_e32 v173, v173\n\t" \
  "v_fma_f32 v174, v174, s59, v240\n\t" \
  "v_cvt_pk_bf16_f32 v175, 0, v175\n\t" \
  "v_mul_f32_e32 v112, s58, v112\n\t" \
  "v_cvt_pk_bf16_f32 v113, 0, v113\n\t" \
  "v_max3_f32 v241, v241, v140, v141\n\t" \
  "v_max3_f32 v241, v241, v142, v143\n\t" \
  "v_mov_b32_e32 v246, v241\n\t" \
  "v_mfma_f32_32x32x16_bf16 v[80:95], v[216:219], v[180:183], v[80:95]\n\t" \
  "ds_read_b128 v[216:219], v250 offset:14336\n\t" \
  "v_exp_f32_e32 v174, v174\n\t" \
  "v_fma_f32 v175, v175, s59, v240\n\t" \
  "v_cvt_pk_bf16_f32 v112, 0, v112\n\t" \
  "v_mfma_f32_32x32x16_bf16 v[192:207], v[242:245], v[180:183], v[192:207]\n\t" \
  "v_mul_f32_e32 v113, s58, v113\n\t" \
  "v_cvt_pk_bf16_f32 v114, 0, v114\n\t" \
  "v_permlane32_swap_b32_e32 v241, v246\n\t" \
  "s_nop 0\n\t" \
  "v_max_f32_e32 v241, v241, v246\n\t" \
  "v_fma_f32 v241, v241, s60, v240\n\t" \
  "v_cmp_lt_f32_e32 vcc, 0x41c00000, v241\n\t" \
  "v_mfma_f32_32x32x16_bf16 v[96:111], v[220:223], v[180:183], v[96:111]\n\t" \
  "ds_read_b128 v[220:223], v250 offset:15360\n\t" \
  "s_cbranch_vccnz LA_RARE0%=\n\t" \
  "LA_RAREB0%=:\n\t" \
  "v_fma_f32 v112, v112, s59, v240\n\t" \
  "v_exp_f32_e32 v175, v175\n\t" \
  "v_cvt_pk_bf16_f32 v113, 0, v113\n\t" \
  "v_mul_f32_e32 v114, s58, v114\n\t" \
  "v_cvt_pk_bf16_f32 v115, 0, v115\n\t" \
  "s_waitcnt lgkmcnt(4)\n\t" \
  "v_mfma_f32_32x32x16_bf16 v[48:63], v[224:227], v[184:187], v[48:63]\n\t" \
  "ds_read_b128 v[224:227], v249 offset:16384\n\t" \
  "v_exp_f32_e32 v112, v112\n\t" \
  "v_fma_f32 v113, v113, s59, v240\n\t" \
  "v_cvt_pk_bf16_f32 v114, 0, v114\n\t" \
  "v_mul_f32_e32 v115, s58, v115\n\t" \
  "v_cvt_pk_bf16_f32 v116, 0, v116\n\t" \
  "v_cvt_pk_bf16_f32 v188, v168, v169\n\t" \
  "v_mfma_f32_32x32x16_bf16 v[64:79], v[228:231], v[184:187], v[64:79]\n\t" \
  "ds_read_b128 v[228:231], v249 offset:17408\n\t" \
  "v_exp_f32_e32 v113, v113\n\t" \
  "v_fma_f32 v114, v114, s59, v240\n\t" \
  "v_cvt_pk_bf16_f32 v115, 0, v115\n\t" \
  "v_mul_f32_e32 v116, s58, v116\n\t" \
  "v_cvt_pk_bf16_f32 v117, 0, v117\n\t" \
  "v_cvt_pk_bf16_f32 v189, v170, v171\n\t" \
  "v_mfma_f32_32x32x16_bf16 v[80:95], v[232:235], v[184:187], v[80:95]\n\t" \
  "ds_read_b128 v[232:235], v249 offset:18432\n\t" \
  "v_exp_f32_e32 v114, v114\n\t" \
  "v_fma_f32 v115, v115, s59, v240\n\t" \
  "v_cvt_pk_bf16_f32 v116, 0, v116\n\t" \
  "v_mul_f32_e32 v117, s58, v117\n\t" \
  "v_cvt_pk_bf16_f32 v118, 0, v118\n\t" \
  "v_cvt_pk_bf16_f32 v190, v172, v173\n\t" \
  "v_mfma_f32_32x32x16_bf16 v[96:111], v[236:239], v[184:187], v[96:111]\n\t" \
  "ds_read_b128 v[236:239], v249 offset:19456\n\t" \
  "v_exp_f32_e32 v115, v115\n\t" \
  "v_fma_f32 v116, v116, s59, v240\n\t" \
  "v_cvt_pk_bf16_f32 v117, 0, v117\n\t" \
  "v_mul_f32_e32 v118, s58, v118\n\t" \
  "v_cvt_pk_bf16_f32 v119, 0, v119\n\t" \
  "v_cvt_pk_bf16_f32 v191, v174, v175\n\t" \
  "s_cmp_eq_u32 s68, s52\n\t" \
  "s_cbranch_scc1 LA_DRAIN0%=\n\t" \
  "LA_STEP1%=:\n\t" \
  "s_waitcnt lgkmcnt(4)\n\t" \
  "v_mfma_f32_32x32x16_bf16 v[48:63], v[208:211], v[188:191], v[48:63]\n\t" \
  "ds_read_b128 v[208:211], v249 offset:20480\n\t" \
  "v_exp_f32_e32 v116, v116\n\t" \
  "v_fma_f32 v117, v117, s59, v240\n\t" \
  "v_cvt_pk_bf16_f32 v118, 0, v118\n\t" \
  "v_mul_f32_e32 v119, s58, v119\n\t" \
  "v_cvt_pk_bf16_f32 v120, 0, v120\n\t" \
  "v_cvt_pk_bf16_f32 v176, v112, v113\n\t" \
  "v_mfma_f32_32x32x16_bf16 v[64:79], v[212:215], v[188:191], v[64:79]\n\t" \
  "ds_read_b128 v[212:215], v249 offset:21504\n\t" \
  "v_exp_f32_e32 v117, v117\n\t" \
  "v_fma_f32 v118, v118, s59, v240\n\t" \
  "v_cvt_pk_bf16_f32 v119, 0, v119\n\t" \
  "v_mul_f32_e32 v120, s58, v120\n\t" \
  "v_cvt_pk_bf16_f32 v121, 0, v121\n\t" \
  "s_cmp_eq_u32 s71, s70\n\t" \
  "s_cbranch_scc1 L_WM%=\n\t" \
  "L_WMB%=:\n\t" \
  "v_mfma_f32_32x32x16_bf16 v[80:95], v[216:219], v[188:191], v[80:95]\n\t" \
  "ds_read_b128 v[216:219], v249 offset:22528\n\t" \
  "v_exp_f32_e32 v118, v118\n\t" \
  "v_fma_f32 v119, v119, s59, v240\n\t" \
  "v_cvt_pk_bf16_f32 v120, 0, v120\n\t" \
  "v_mul_f32_e32 v121, s58, v121\n\t" \
  "v_cvt_pk_bf16_f32 v122, 0, v122\n\t" \
  "v_cvt_pk_bf16_f32 v177, v114, v115\n\t" \
  "v_mfma_f32_32x32x16_bf16 v[96:111], v[220:223], v[188:191], v[96:111]\n\t" \
  "ds_read_b128 v[220:223], v249 offset:23552\n\t" \
  "v_exp_f32_e32 v119, v119\n\t" \
  "v_fma_f32 v120, v120, s59, v240\n\t" \
  "v_cvt_pk_bf16_f32 v121, 0, v121\n\t" \
  "v_mfma_f32_32x32x16_bf16 v[192:207], v[242:245], v[188:191], v[192:207]\n\t" \
  "v_mul_f32_e32 v122, s58, v122\n\t" \
  "v_cvt_pk_bf16_f32 v123, 0, v123\n\t" \
  "s_cmp_lg_u32 s69, 0\n\t" \
  "s_cbranch_scc1 LA_ROS1%=\n\t" \
  "LA_ROBS1%=:\n\t" \
  "s_waitcnt lgkmcnt(4)\n\t" \
  "v_mfma_f32_32x32x16_bf16 v[144:159], v[224:227], v[16:19], 0\n\t" \
  "ds_read_b128 v[224:227], v249 offset:24576\n\t" \
  "v_exp_f32_e32 v120, v120\n\t" \
  "v_fma_f32 v121, v121, s59, v240\n\t" \
  "v_cvt_pk_bf16_f32 v122, 0, v122\n\t" \
  "v_mul_f32_e32 v123, s58, v123\n\t" \
  "v_cvt_pk_bf16_f32 v124, 0, v124\n\t" \
  "v_cvt_pk_bf16_f32 v178, v116, v117\n\t" \
  "v_mfma_f32_32x32x16_bf16 v[144:159], v[228:231], v[20:23], v[144:159]\n\t" \
  "ds_read_b128 v[228:231], v249 offset:25600\n\t" \
  "v_exp_f32_e32 v121, v121\n\t" \
  "v_fma_f32 v122, v122, s59, v240\n\t" \
  "v_cvt_pk_bf16_f32 v123, 0, v123\n\t" \
  "v_mul_f32_e32 v124, s58, v124\n\t" \
  "v_cvt_pk_bf16_f32 v125, 0, v125\n\t" \
  "s_add_u32 s68, s68, 1\n\t" \
  "v_mfma_f32_32x32x16_bf16 v[144:159], v[232:235], v[24:27], v[144:159]\n\t" \
  "ds_read_b128 v[232:235], v249 offset:26624\n\t" \
  "v_exp_f32_e32 v122, v122\n\t" \
  "v_fma_f32 v123, v123, s59, v240\n\t" \
  "v_cvt_pk_bf16_f32 v124, 0, v124\n\t" \
  "v_mul_f32_e32 v125, s58, v125\n\t" \
  "v_cvt_pk_bf16_f32 v126, 0, v126\n\t" \
  "v_cvt_pk_bf16_f32 v179, v118, v119\n\t" \
  "v_mfma_f32_32x32x16_bf16 v[144:159], v[236:239], v[28:31], v[144:159]\n\t" \
  "ds_read_b128 v[236:239], v249 offset:27648\n\t" \
  "v_exp_f32_e32 v123, v123\n\t" \
  "v_fma_f32 v124, v124, s59, v240\n\t" \
  "v_cvt_pk_bf16_f32 v125, 0, v125\n\t" \
  "v_mul_f32_e32 v126, s58, v126\n\t" \
  "v_cvt_pk_bf16_f32 v127, 0, v127\n\t" \
  "v_cvt_pk_bf16_f32 v180, v120, v121\n\t" \
  "s_waitcnt lgkmcnt(4)\n\t" \
  "v_mfma_f32_32x32x16_bf16 v[144:159], v[208:211], v[32:35], v[144:159]\n\t" \
  "ds_read_b128 v[208:211], v249 offset:28672\n\t" \
  "v_exp_f32_e32 v124, v124\n\t" \
  "v_fma_f32 v125, v125, s59, v240\n\t" \
  "v_cvt_pk_bf16_f32 v126, 0, v126\n\t" \
  "v_mul_f32_e32 v127, s58, v127\n\t" \
  "v_cvt_pk_bf16_f32 v128, 0, v128\n\t" \
  "v_mfma_f32_32x32x16_bf16 v[144:159], v[212:215], v[36:39], v[144:159]\n\t" \
  "ds_read_b128 v[212:215], v249 offset:29696\n\t" \
  "v_exp_f32_e32 v125, v125\n\t" \
  "v_fma_f32 v126, v126, s59, v240\n\t" \
  "v_cvt_pk_bf16_f32 v127, 0, v127\n\t" \
  "v_mul_f32_e32 v128, s58, v128\n\t" \
  "v_cvt_pk_bf16_f32 v129, 0, v129\n\t" \
  "v_cvt_pk_bf16_f32 v181, v122, v123\n\t" \
  "v_mfma_f32_32x32x16_bf16 v[144:159], v[216:219], v[40:43], v[144:159]\n\t" \
  "ds_read_b128 v[216:219], v249 offset:30720\n\t" \
  "v_exp_f32_e32 v126, v126\n\t" \
  "v_fma_f32 v127, v127, s59, v240\n\t" \
  "v_cvt_pk_bf16_f32 v128, 0, v128\n\t" \
  "v_mul_f32_e32 v129, s58, v129\n\t" \
  "v_cvt_pk_bf16_f32 v130, 0, v130\n\t" \
  "v_cvt_pk_bf16_f32 v182, v124, v125\n\t" \
  "v_mfma_f32_32x32x16_bf16 v[144:159], v[220:223], v[44:47], v[144:159]\n\t" \
  "ds_read_b128 v[220:223], v249 offset:31744\n\t" \
  "v_exp_f32_e32 v127, v127\n\t" \
  "v_fma_f32 v128, v128, s59, v240\n\t" \
  "v_cvt_pk_bf16_f32 v129, 0, v129\n\t" \
  "v_mul_f32_e32 v130, s58, v130\n\t" \
  "v_cvt_pk_bf16_f32 v131, 0, v131\n\t" \
  "s_waitcnt lgkmcnt(4)\n\t" \
  "v_mfma_f32_32x32x16_bf16 v[160:175], v[224:227], v[16:19], 0\n\t" \
  "ds_read_b128 v[224:227], v250 offset:16384\n\t" \
  "v_exp_f32_e32 v128, v128\n\t" \
  "v_fma_f32 v129, v129, s59, v240\n\t" \
  "v_cvt_pk_bf16_f32 v130, 0, v130\n\t" \
  "v_mul_f32_e32 v131, s58, v131\n\t" \
  "v_cvt_pk_bf16_f32 v132, 0, v132\n\t" \
  "v_cvt_pk_bf16_f32 v183, v126, v127\n\t" \
  "v_mfma_f32_32x32x16_bf16 v[160:175], v[228:231], v[20:23], v[160:175]\n\t" \
  "ds_read_b128 v[228:231], v250 offset:17408\n\t" \
  "v_exp_f32_e32 v129, v129\n\t" \
  "v_fma_f32 v130, v130, s59, v240\n\t" \
  "v_cvt_pk_bf16_f32 v131, 0, v131\n\t" \
  "v_mul_f32_e32 v132, s58, v132\n\t" \
  "v_cvt_pk_bf16_f32 v133, 0, v133\n\t" \
  "s_cmp_eq_u32 s68, s52\n\t" \
  "s_cbranch_scc1 LA_MASK10%=\n\t" \
  "LA_MASKB10%=:\n\t" \
  "v_mfma_f32_32x32x16_bf16 v[160:175], v[232:235], v[24:27], v[160:175]\n\t" \
  "ds_read_b128 v[232:235], v250 offset:18432\n\t" \
  "v_exp_f32_e32 v130, v130\n\t" \
  "v_fma_f32 v131, v131, s59, v240\n\t" \
  "v_cvt_pk_bf16_f32 v132, 0, v132\n\t" \
  "v_mul_f32_e32 v133, s58, v133\n\t" \
  "v_cvt_pk_bf16_f32 v134, 0, v134\n\t" \
  "v_cvt_pk_bf16_f32 v184, v128, v129\n\t" \
  "v_max_f32_e32 v241, v144, v145\n\t" \
  "v_mfma_f32_32x32x16_bf16 v[160:175], v[236:239], v[28:31], v[160:175]\n\t" \
  "ds_read_b128 v[236:239], v250 offset:19456\n\t" \
  "v_exp_f32_e32 v131, v131\n\t" \
  "v_fma_f32 v132, v132, s59, v240\n\t" \
  "v_cvt_pk_bf16_f32 v133, 0, v133\n\t" \
  "v_mul_f32_e32 v134, s58, v134\n\t" \
  "v_cvt_pk_bf16_f32 v135, 0, v135\n\t" \
  "v_max3_f32 v241, v241, v146, v147\n\t" \
  "s_waitcnt lgkmcnt(4)\n\t" \
  "v_mfma_f32_32x32x16_bf16 v[160:175], v[208:211], v[32:35], v[160:175]\n\t" \
  "ds_read_b128 v[208:211], v250 offset:20480\n\t" \
  "v_exp_f32_e32 v132, v132\n\t" \
  "v_fma_f32 v133, v133, s59, v240\n\t" \
  "v_cvt_pk_bf16_f32 v134, 0, v134\n\t" \
  "v_mul_f32_e32 v135, s58, v135\n\t" \
  "v_cvt_pk_bf16_f32 v136, 0, v136\n\t" \
  "v_max3_f32 v241, v241, v148, v149\n\t" \
  "v_mfma_f32_32x32x16_bf16 v[160:175], v[212:215], v[36:39], v[160:175]\n\t" \
  "ds_read_b128 v[212:215], v250 offset:21504\n\t" \
  "v_exp_f32_e32 v133, v133\n\t" \
  "v_fma_f32 v134, v134, s59, v240\n\t" \
  "v_cvt_pk_bf16_f32 v135, 0, v135\n\t" \
  "v_mul_f32_e32 v136, s58, v136\n\t" \
  "v_cvt_pk_bf16_f32 v137, 0, v137\n\t" \
  "v_cvt_pk_bf16_f32 v185, v130, v131\n\t" \
  "v_max3_f32 v241, v241, v150, v151\n\t" \
  "v_mfma_f32_32x32x16_bf16 v[160:175], v[216:219], v[40:43], v[160:175]\n\t" \
  "ds_read_b128 v[216:219], v250 offset:22528\n\t" \
  "v_exp_f32_e32 v134, v134\n\t" \
  "v_fma_f32 v135, v135, s59, v240\n\t" \
  "v_cvt_pk_bf16_f32 v136, 0, v136\n\t" \
  "v_mul_f32_e32 v137, s58, v137\n\t" \
  "v_cvt_pk_bf16_f32 v138, 0, v138\n\t" \
  "v_cvt_pk_bf16_f32 v186, v132, v133\n\t" \
  "v_max3_f32 v241, v241, v152, v153\n\t" \
  "v_mfma_f32_32x32x16_bf16 v[160:175], v[220:223], v[44:47], v[160:175]\n\t" \
  "ds_read_b128 v[220:223], v250 offset:23552\n\t" \
  "v_exp_f32_e32 v135, v135\n\t" \
  "v_fma_f32 v136, v136, s59, v240\n\t" \
  "v_cvt_pk_bf16_f32 v137, 0, v137\n\t" \
  "v_mul_f32_e32 v138, s58, v138\n\t" \
  "v_cvt_pk_bf16_f32 v139, 0, v139\n\t" \
  "v_max3_f32 v241, v241, v154, v155\n\t" \
  "s_waitcnt lgkmcnt(4)\n\t" \
  "v_mfma_f32_32x32x16_bf16 v[48:63], v[224:227], v[176:179], v[48:63]\n\t" \
  "ds_read_b128 v[224:227], v250 offset:24576\n\t" \
  "v_exp_f32_e32 v136, v136\n\t" \
  "v_fma_f32 v137, v137, s59, v240\n\t" \
  "v_cvt_pk_bf16_f32 v138, 0, v138\n\t" \
  "v_mul_f32_e32 v139, s58, v139\n\t" \
  "v_cvt_pk_bf16_f32 v140, 0, v140\n\t" \
  "v_cvt_pk_bf16_f32 v187, v134, v135\n\t" \
  "v_max3_f32 v241, v241, v156, v157\n\t" \
  "v_mfma_f32_32x32x16_bf16 v[64:79], v[228:231], v[176:179], v[64:79]\n\t" \
  "ds_read_b128 v[228:231], v250 offset:25600\n\t" \
  "v_exp_f32_e32 v137, v137\n\t" \
  "v_fma_f32 v138, v138, s59, v240\n\t" \
  "v_cvt_pk_bf16_f32 v139, 0, v139\n\t" \
  "v_mfma_f32_32x32x16_bf16 v[192:207], v[242:245], v[184:187], v[192:207]\n\t" \
  "v_mul_f32_e32 v140, s58, v140\n\t" \
  "v_cvt_pk_bf16_f32 v141, 0, v141\n\t" \
  "s_cmp_eq_u32 s68, s52\n\t" \
  "s_cbranch_scc1 LA_MASK11%=\n\t" \
  "LA_MASKB11%=:\n\t" \
  "v_max3_f32 v241, v241, v158, v159\n\t" \
  "v_mfma_f32_32x32x16_bf16 v[80:95], v[232:235], v[176:179], v[80:95]\n\t" \
  "ds_read_b128 v[232:235], v250 offset:26624\n\t" \
  "v_exp_f32_e32 v138, v138\n\t" \
  "v_fma_f32 v139, v139, s59, v240\n\t" \
  "v_cvt_pk_bf16_f32 v140, 0, v140\n\t" \
  "v_mul_f32_e32 v141, s58, v141\n\t" \
  "v_cvt_pk_bf16_f32 v142, 0, v142\n\t" \
  "v_max3_f32 v241, v241, v160, v161\n\t" \
  "v_max3_f32 v241, v241, v162, v163\n\t" \
  "v_mfma_f32_32x32x16_bf16 v[96:111], v[236:239], v[176:179], v[96:111]\n\t" \
  "ds_read_b128 v[236:239], v250 offset:27648\n\t" \
  "v_exp_f32_e32 v139, v139\n\t" \
  "v_fma_f32 v140, v140, s59, v240\n\t" \
  "v_cvt_pk_bf16_f32 v141, 0, v141\n\t" \
  "v_mfma_f32_32x32x16_bf16 v[192:207], v[242:245], v[176:179], v[192:207]\n\t" \
  "v_mul_f32_e32 v142, s58, v142\n\t" \
  "v_cvt_pk_bf16_f32 v143, 0, v143\n\t" \
  "v_max3_f32 v241, v241, v164, v165\n\t" \
  "v_max3_f32 v241, v241, v166, v167\n\t" \
  "s_waitcnt lgkmcnt(4)\n\t" \
  "v_mfma_f32_32x32x16_bf16 v[48:63], v[208:211], v[180:183], v[48:63]\n\t" \
  "ds_read_b128 v[208:211], v250 offset:28672\n\t" \
  "v_exp_f32_e32 v140, v140\n\t" \
  "v_fma_f32 v141, v141, s59, v240\n\t" \
  "v_cvt_pk_bf16_f32 v142, 0, v142\n\t" \
  "v_mul_f32_e32 v143, s58, v143\n\t" \
  "v_cvt_pk_bf16_f32 v144, 0, v144\n\t" \
  "v_max3_f32 v241, v241, v168, v169\n\t" \
  "v_max3_f32 v241, v241, v170, v171\n\t" \
  "v_mfma_f32_32x32x16_bf16 v[64:79], v[212:215], v[180:183], v[64:79]\n\t" \
  "ds_read_b128 v[212:215], v250 offset:29696\n\t" \
  "v_exp_f32_e32 v141, v141\n\t" \
  "v_fma_f32 v142, v142, s59, v240\n\t" \
  "v_cvt_pk_bf16_f32 v143, 0, v143\n\t" \
  "v_mul_f32_e32 v144, s58, v144\n\t" \
  "v_cvt_pk_bf16_f32 v145, 0, v145\n\t" \
  "v_max3_f32 v241, v241, v172, v173\n\t" \
  "v_max3_f32 v241, v241, v174, v175\n\t" \
  "v_mov_b32_e32 v246, v241\n\t" \
  "v_mfma_f32_32x32x16_bf16 v[80:95], v[216:219], v[180:183], v[80:95]\n\t" \
  "ds_read_b128 v[216:219], v250 offset:30720\n\t" \
  "v_exp_f32_e32 v142, v142\n\t" \
  "v_fma_f32 v143, v143, s59, v240\n\t" \
  "v_cvt_pk_bf16_f32 v144, 0, v144\n\t" \
  "v_mfma_f32_32x32x16_bf16 v[192:207], v[242:245], v[180:183], v[192:207]\n\t" \
  "v_mul_f32_e32 v145, s58, v145\n\t" \
  "v_cvt_pk_bf16_f32 v146, 0, v146\n\t" \
  "v_permlane32_swap_b32_e32 v241, v246\n\t" \
  "s_nop 0\n\t" \
  "v_max_f32_e32 v241, v241, v246\n\t" \
  "v_fma_f32 v241, v241, s60, v240\n\t" \
  "v_cmp_lt_f32_e32 vcc, 0x41c00000, v241\n\t" \
  "v_mfma_f32_32x32x16_bf16 v[96:111], v[220:223], v[180:183], v[96:111]\n\t" \
  "ds_read_b128 v[220:223], v250 offset:31744\n\t" \
  "s_cbranch_vccnz LA_RARE1%=\n\t" \
  "LA_RAREB1%=:\n\t" \
  "v_fma_f32 v144, v144, s59, v240\n\t" \
  "v_exp_f32_e32 v143, v143\n\t" \
  "v_cvt_pk_bf16_f32 v145, 0, v145\n\t" \
  "v_mul_f32_e32 v146, s58, v146\n\t" \
  "v_cvt_pk_bf16_f32 v147, 0, v147\n\t" \
  "s_waitcnt lgkmcnt(4)\n\t" \
  "v_mfma_f32_32x32x16_bf16 v[48:63], v[224:227], v[184:187], v[48:63]\n\t" \
  "v_exp_f32_e32 v144, v144\n\t" \
  "v_fma_f32 v145, v145, s59, v240\n\t" \
  "v_cvt_pk_bf16_f32 v146, 0, v146\n\t" \
  "v_mul_f32_e32 v147, s58, v147\n\t" \
  "v_cvt_pk_bf16_f32 v148, 0, v148\n\t" \
  "v_cvt_pk_bf16_f32 v188, v136, v137\n\t" \
  "v_mfma_f32_32x32x16_bf16 v[64:79], v[228:231], v[184:187], v[64:79]\n\t" \
  "v_exp_f32_e32 v145, v145\n\t" \
  "v_fma_f32 v146, v146, s59, v240\n\t" \
  "v_cvt_pk_bf16_f32 v147, 0, v147\n\t" \
  "v_mul_f32_e32 v148, s58, v148\n\t" \
  "v_cvt_pk_bf16_f32 v149, 0, v149\n\t" \
  "v_cvt_pk_bf16_f32 v189, v138, v139\n\t" \
  "v_xor_b32_e32 v249, 0x8000, v249\n\t" \
  "v_mfma_f32_32x32x16_bf16 v[80:95], v[232:235], v[184:187], v[80:95]\n\t" \
  "v_exp_f32_e32 v146, v146\n\t" \
  "v_fma_f32 v147, v147, s59, v240\n\t" \
  "v_cvt_pk_bf16_f32 v148, 0, v148\n\t" \
  "v_mul_f32_e32 v149, s58, v149\n\t" \
  "v_cvt_pk_bf16_f32 v150, 0, v150\n\t" \
  "v_cvt_pk_bf16_f32 v190, v140, v141\n\t" \
  "v_xor_b32_e32 v250, 0x8000, v250\n\t" \
  "v_mfma_f32_32x32x16_bf16 v[96:111], v[236:239], v[184:187], v[96:111]\n\t" \
  "v_exp_f32_e32 v147, v147\n\t" \
  "v_fma_f32 v148, v148, s59, v240\n\t" \
  "v_cvt_pk_bf16_f32 v149, 0, v149\n\t" \
  "v_mul_f32_e32 v150, s58, v150\n\t" \
  "v_cvt_pk_bf16_f32 v151, 0, v151\n\t" \
  "v_cvt_pk_bf16_f32 v191, v142, v143\n\t" \
  "s_waitcnt lgkmcnt(0)\n\t" \
  "s_barrier\n\t" \
  "s_add_u32 s71, s71, 1\n\t" \
  "s_cmp_eq_u32 s68, s52\n\t" \
  "s_cbranch_scc0 LA_STEP0%=\n\t" \
  "s_branch LA_DRAIN1%=\n\t" \
  "LA_DRAIN0%=:\n\t" \
  "s_waitcnt lgkmcnt(0)\n\t" \
  "ds_read_b128 v[224:227], v250 offset:16384\n\t" \
  "ds_read_b128 v[228:231], v250 offset:17408\n\t" \
  "ds_read_b128 v[232:235], v250 offset:18432\n\t" \
  "ds_read_b128 v[236:239], v250 offset:19456\n\t" \
  "v_mfma_f32_32x32x16_bf16 v[48:63], v[208:211], v[188:191], v[48:63]\n\t" \
  "ds_read_b128 v[208:211], v250 offset:20480\n\t" \
  "v_cvt_pk_bf16_f32 v176, v112, v113\n\t" \
  "v_cvt_pk_bf16_f32 v177, v114, v115\n\t" \
  "v_exp_f32_e32 v116, v116\n\t" \
  "v_fma_f32 v117, v117, s59, v240\n\t" \
  "v_cvt_pk_bf16_f32 v118, 0, v118\n\t" \
  "v_mul_f32_e32 v119, s58, v119\n\t" \
  "v_cvt_pk_bf16_f32 v120, 0, v120\n\t" \
  "v_exp_f32_e32 v117, v117\n\t" \
  "v_fma_f32 v118, v118, s59, v240\n\t" \
  "v_cvt_pk_bf16_f32 v119, 0, v119\n\t" \
  "v_mul_f32_e32 v120, s58, v120\n\t" \
  "v_cvt_pk_bf16_f32 v121, 0, v121\n\t" \
  "v_mfma_f32_32x32x16_bf16 v[64:79], v[212:215], v[188:191], v[64:79]\n\t" \
  "ds_read_b128 v[212:215], v250 offset:21504\n\t" \
  "v_cvt_pk_bf16_f32 v178, v116, v117\n\t" \
  "v_exp_f32_e32 v118, v118\n\t" \
  "v_fma_f32 v119, v119, s59, v240\n\t" \
  "v_cvt_pk_bf16_f32 v120, 0, v120\n\t" \
  "v_mul_f32_e32 v121, s58, v121\n\t" \
  "v_cvt_pk_bf16_f32 v122, 0, v122\n\t" \
  "v_exp_f32_e32 v119, v119\n\t" \
  "v_fma_f32 v120, v120, s59, v240\n\t" \
  "v_cvt_pk_bf16_f32 v121, 0, v121\n\t" \
  "v_mul_f32_e32 v122, s58, v122\n\t" \
  "v_cvt_pk_bf16_f32 v123, 0, v123\n\t" \
  "v_mfma_f32_32x32x16_bf16 v[80:95], v[216:219], v[188:191], v[80:95]\n\t" \
  "ds_read_b128 v[216:219], v250 offset:22528\n\t" \
  "v_cvt_pk_bf16_f32 v179, v118, v119\n\t" \
  "v_exp_f32_e32 v120, v120\n\t" \
  "v_fma_f32 v121, v121, s59, v240\n\t" \
  "v_cvt_pk_bf16_f32 v122, 0, v122\n\t" \
  "v_mul_f32_e32 v123, s58, v123\n\t" \
  "v_cvt_pk_bf16_f32 v124, 0, v124\n\t" \
  "v_exp_f32_e32 v121, v121\n\t" \
  "v_fma_f32 v122, v122, s59, v240\n\t" \
  "v_cvt_pk_bf16_f32 v123, 0, v123\n\t" \
  "v_mul_f32_e32 v124, s58, v124\n\t" \
  "v_cvt_pk_bf16_f32 v125, 0, v125\n\t" \
  "v_mfma_f32_32x32x16_bf16 v[96:111], v[220:223], v[188:191], v[96:111]\n\t" \
  "ds_read_b128 v[220:223], v250 offset:23552\n\t" \
  "v_cvt_pk_bf16_f32 v180, v120, v121\n\t" \
  "v_exp_f32_e32 v122, v122\n\t" \
  "v_fma_f32 v123, v123, s59, v240\n\t" \
  "v_cvt_pk_bf16_f32 v124, 0, v124\n\t" \
  "v_mul_f32_e32 v125, s58, v125\n\t" \
  "v_cvt_pk_bf16_f32 v126, 0, v126\n\t" \
  "v_exp_f32_e32 v123, v123\n\t" \
  "v_fma_f32 v124, v124, s59, v240\n\t" \
  "v_cvt_pk_bf16_f32 v125, 0, v125\n\t" \
  "v_mul_f32_e32 v126, s58, v126\n\t" \
  "v_cvt_pk_bf16_f32 v127, 0, v127\n\t" \
  "v_mfma_f32_32x32x16_bf16 v[192:207], v[242:245], v[188:191], v[192:207]\n\t" \
  "s_cmp_lg_u32 s69, 0\n\t" \
  "s_cbranch_scc1 LA_ROD0%=\n\t" \
  "LA_ROBD0%=:\n\t" \
  "s_nop 0\n\t" \
  "s_waitcnt lgkmcnt(7)\n\t" \
  "v_mfma_f32_32x32x16_bf16 v[48:63], v[224:227], v[176:179], v[48:63]\n\t" \
  "ds_read_b128 v[224:227], v250 offset:24576\n\t" \
  "v_cvt_pk_bf16_f32 v181, v122, v123\n\t" \
  "v_exp_f32_e32 v124, v124\n\t" \
  "v_fma_f32 v125, v125, s59, v240\n\t" \
  "v_cvt_pk_bf16_f32 v126, 0, v126\n\t" \
  "v_mul_f32_e32 v127, s58, v127\n\t" \
  "v_cvt_pk_bf16_f32 v128, 0, v128\n\t" \
  "v_exp_f32_e32 v125, v125\n\t" \
  "v_fma_f32 v126, v126, s59, v240\n\t" \
  "v_cvt_pk_bf16_f32 v127, 0, v127\n\t" \
  "v_mul_f32_e32 v128, s58, v128\n\t" \
  "v_cvt_pk_bf16_f32 v129, 0, v129\n\t" \
  "s_waitcnt lgkmcnt(7)\n\t" \
  "v_mfma_f32_32x32x16_bf16 v[64:79], v[228:231], v[176:179], v[64:79]\n\t" \
  "ds_read_b128 v[228:231], v250 offset:25600\n\t" \
  "v_cvt_pk_bf16_f32 v182, v124, v125\n\t" \
  "v_exp_f32_e32 v126, v126\n\t" \
  "v_fma_f32 v127, v127, s59, v240\n\t" \
  "v_cvt_pk_bf16_f32 v128, 0, v128\n\t" \
  "v_mul_f32_e32 v129, s58, v129\n\t" \
  "v_cvt_pk_bf16_f32 v130, 0, v130\n\t" \
  "v_exp_f32_e32 v127, v127\n\t" \
  "v_fma_f32 v128, v128, s59, v240\n\t" \
  "v_cvt_pk_bf16_f32 v129, 0, v129\n\t" \
  "v_mul_f32_e32 v130, s58, v130\n\t" \
  "v_cvt_pk_bf16_f32 v131, 0, v131\n\t" \
  "s_waitcnt lgkmcnt(7)\n\t" \
  "v_mfma_f32_32x32x16_bf16 v[80:95], v[232:235], v[176:179], v[80:95]\n\t" \
  "ds_read_b128 v[232:235], v250 offset:26624\n\t" \
  "v_cvt_pk_bf16_f32 v183, v126, v127\n\t" \
  "v_exp_f32_e32 v128, v128\n\t" \
  "v_fma_f32 v129, v129, s59, v240\n\t" \
  "v_cvt_pk_bf16_f32 v130, 0, v130\n\t" \
  "v_mul_f32_e32 v131, s58, v131\n\t" \
  "v_cvt_pk_bf16_f32 v132, 0, v132\n\t" \
  "v_exp_f32_e32 v129, v129\n\t" \
  "v_fma_f32 v130, v130, s59, v240\n\t" \
  "v_cvt_pk_bf16_f32 v131, 0, v131\n\t" \
  "v_mul_f32_e32 v132, s58, v132\n\t" \
  "v_cvt_pk_bf16_f32 v133, 0, v133\n\t" \
  "s_waitcnt lgkmcnt(7)\n\t" \
  "v_mfma_f32_32x32x16_bf16 v[96:111], v[236:239], v[176:179], v[96:111]\n\t" \
  "ds_read_b128 v[236:239], v250 offset:27648\n\t" \
  "v_mfma_f32_32x32x16_bf16 v[192:207], v[242:245], v[176:179], v[192:207]\n\t" \
  "v_cvt_pk_bf16_f32 v184, v128, v129\n\t" \
  "v_exp_f32_e32 v130, v130\n\t" \
  "v_fma_f32 v131, v131, s59, v240\n\t" \
  "v_cvt_pk_bf16_f32 v132, 0, v132\n\t" \
  "v_mul_f32_e32 v133, s58, v133\n\t" \
  "v_cvt_pk_bf16_f32 v134, 0, v134\n\t" \
  "v_exp_f32_e32 v131, v131\n\t" \
  "v_fma_f32 v132, v132, s59, v240\n\t" \
  "v_cvt_pk_bf16_f32 v133, 0, v133\n\t" \
  "v_mul_f32_e32 v134, s58, v134\n\t" \
  "v_cvt_pk_bf16_f32 v135, 0, v135\n\t" \
  "s_waitcnt lgkmcnt(7)\n\t" \
  "v_mfma_f32_32x32x16_bf16 v[48:63], v[208:211], v[180:183], v[48:63]\n\t" \
  "ds_read_b128 v[208:211], v250 offset:28672\n\t" \
  "v_cvt_pk_bf16_f32 v185, v130, v131\n\t" \
  "v_exp_f32_e32 v132, v132\n\t" \
  "v_fma_f32 v133, v133, s59, v240\n\t" \
  "v_cvt_pk_bf16_f32 v134, 0, v134\n\t" \
  "v_mul_f32_e32 v135, s58, v135\n\t" \
  "v_cvt_pk_bf16_f32 v136, 0, v136\n\t" \
  "v_exp_f32_e32 v133, v133\n\t" \
  "v_fma_f32 v134, v134, s59, v240\n\t" \
  "v_cvt_pk_bf16_f32 v135, 0, v135\n\t" \
  "v_mul_f32_e32 v136, s58, v136\n\t" \
  "v_cvt_pk_bf16_f32 v137, 0, v137\n\t" \
  "s_waitcnt lgkmcnt(7)\n\t" \
  "v_mfma_f32_32x32x16_bf16 v[64:79], v[212:215], v[180:183], v[64:79]\n\t" \
  "ds_read_b128 v[212:215], v250 offset:29696\n\t" \
  "v_cvt_pk_bf16_f32 v186, v132, v133\n\t" \
  "v_exp_f32_e32 v134, v134\n\t" \
  "v_fma_f32 v135, v135, s59, v240\n\t" \
  "v_cvt_pk_bf16_f32 v136, 0, v136\n\t" \
  "v_mul_f32_e32 v137, s58, v137\n\t" \
  "v_cvt_pk_bf16_f32 v138, 0, v138\n\t" \
  "v_exp_f32_e32 v135, v135\n\t" \
  "v_fma_f32 v136, v136, s59, v240\n\t" \
  "v_cvt_pk_bf16_f32 v137, 0, v137\n\t" \
  "v_mul_f32_e32 v138, s58, v138\n\t" \
  "v_cvt_pk_bf16_f32 v139, 0, v139\n\t" \
  "s_waitcnt lgkmcnt(7)\n\t" \
  "v_mfma_f32_32x32x16_bf16 v[80:95], v[216:219], v[180:183], v[80:95]\n\t" \
  "ds_read_b128 v[216:219], v250 offset:30720\n\t" \
  "v_cvt_pk_bf16_f32 v187, v134, v135\n\t" \
  "v_exp_f32_e32 v136, v136\n\t" \
  "v_fma_f32 v137, v137, s59, v240\n\t" \
  "v_cvt_pk_bf16_f32 v138, 0, v138\n\t" \
  "v_mul_f32_e32 v139, s58, v139\n\t" \
  "v_cvt_pk_bf16_f32 v140, 0, v140\n\t" \
  "v_exp_f32_e32 v137, v137\n\t" \
  "v_fma_f32 v138, v138, s59, v240\n\t" \
  "v_cvt_pk_bf16_f32 v139, 0, v139\n\t" \
  "v_mul_f32_e32 v140, s58, v140\n\t" \
  "v_cvt_pk_bf16_f32 v141, 0, v141\n\t" \
  "s_waitcnt lgkmcnt(7)\n\t" \
  "v_mfma_f32_32x32x16_bf16 v[96:111], v[220:223], v[180:183], v[96:111]\n\t" \
  "ds_read_b128 v[220:223], v250 offset:31744\n\t" \
  "v_mfma_f32_32x32x16_bf16 v[192:207], v[242:245], v[180:183], v[192:207]\n\t" \
  "v_cvt_pk_bf16_f32 v188, v136, v137\n\t" \
  "v_exp_f32_e32 v138, v138\n\t" \
  "v_fma_f32 v139, v139, s59, v240\n\t" \
  "v_cvt_pk_bf16_f32 v140, 0, v140\n\t" \
  "v_mul_f32_e32 v141, s58, v141\n\t" \
  "v_cvt_pk_bf16_f32 v142, 0, v142\n\t" \
  "v_exp_f32_e32 v139, v139\n\t" \
  "v_fma_f32 v140, v140, s59, v240\n\t" \
  "v_cvt_pk_bf16_f32 v141, 0, v141\n\t" \
  "v_mul_f32_e32 v142, s58, v142\n\t" \
  "v_cvt_pk_bf16_f32 v143, 0, v143\n\t" \
  "s_waitcnt lgkmcnt(7)\n\t" \
  "v_mfma_f32_32x32x16_bf16 v[48:63], v[224:227], v[184:187], v[48:63]\n\t" \
  "v_cvt_pk_bf16_f32 v189, v138, v139\n\t" \
  "v_exp_f32_e32 v140, v140\n\t" \
  "v_fma_f32 v141, v141, s59, v240\n\t" \
  "v_cvt_pk_bf16_f32 v142, 0, v142\n\t" \
  "v_mul_f32_e32 v143, s58, v143\n\t" \
  "v_exp_f32_e32 v141, v141\n\t" \
  "v_fma_f32 v142, v142, s59, v240\n\t" \
  "v_cvt_pk_bf16_f32 v143, 0, v143\n\t" \
  "s_waitcnt lgkmcnt(6)\n\t" \
  "v_mfma_f32_32x32x16_bf16 v[64:79], v[228:231], v[184:187], v[64:79]\n\t" \
  "v_cvt_pk_bf16_f32 v190, v140, v141\n\t" \
  "v_exp_f32_e32 v142, v142\n\t" \
  "v_fma_f32 v143, v143, s59, v240\n\t" \
  "v_exp_f32_e32 v143, v143\n\t" \
  "s_waitcnt lgkmcnt(5)\n\t" \
  "v_mfma_f32_32x32x16_bf16 v[80:95], v[232:235], v[184:187], v[80:95]\n\t" \
  "v_cvt_pk_bf16_f32 v191, v142, v143\n\t" \
  "s_waitcnt lgkmcnt(4)\n\t" \
  "v_mfma_f32_32x32x16_bf16 v[96:111], v[236:239], v[184:187], v[96:111]\n\t" \
  "v_mfma_f32_32x32x16_bf16 v[192:207], v[242:245], v[184:187], v[192:207]\n\t" \
  "s_waitcnt lgkmcnt(3)\n\t" \
  "v_mfma_f32_32x32x16_bf16 v[48:63], v[208:211], v[188:191], v[48:63]\n\t" \
  "s_waitcnt lgkmcnt(2)\n\t" \
  "v_mfma_f32_32x32x16_bf16 v[64:79], v[212:215], v[188:191], v[64:79]\n\t" \
  "s_waitcnt lgkmcnt(1)\n\t" \
  "v_mfma_f32_32x32x16_bf16 v[80:95], v[216:219], v[188:191], v[80:95]\n\t" \
  "s_waitcnt lgkmcnt(0)\n\t" \
  "v_mfma_f32_32x32x16_bf16 v[96:111], v[220:223], v[188:191], v[96:111]\n\t" \
  "v_mfma_f32_32x32x16_bf16 v[192:207], v[242:245], v[188:191], v[192:207]\n\t" \
  "s_branch L_EPI%=\n\t" \
  "LA_DRAIN1%=:\n\t" \
  "s_waitcnt lgkmcnt(0)\n\t" \
  "ds_read_b128 v[224:227], v250 offset:0\n\t" \
  "ds_read_b128 v[228:231], v250 offset:1024\n\t" \
  "ds_read_b128 v[232:235], v250 offset:2048\n\t" \
  "ds_read_b128 v[236:239], v250 offset:3072\n\t" \
  "v_mfma_f32_32x32x16_bf16 v[48:63], v[208:211], v[188:191], v[48:63]\n\t" \
  "ds_read_b128 v[208:211], v250 offset:4096\n\t" \
  "v_cvt_pk_bf16_f32 v176, v144, v145\n\t" \
  "v_cvt_pk_bf16_f32 v177, v146, v147\n\t" \
  "v_exp_f32_e32 v148, v148\n\t" \
  "v_fma_f32 v149, v149, s59, v240\n\t" \
  "v_cvt_pk_bf16_f32 v150, 0, v150\n\t" \
  "v_mul_f32_e32 v151, s58, v151\n\t" \
  "v_cvt_pk_bf16_f32 v152, 0, v152\n\t" \
  "v_exp_f32_e32 v149, v149\n\t" \
  "v_fma_f32 v150, v150, s59, v240\n\t" \
  "v_cvt_pk_bf16_f32 v151, 0, v151\n\t" \
  "v_mul_f32_e32 v152, s58, v152\n\t" \
  "v_cvt_pk_bf16_f32 v153, 0, v153\n\t" \
  "v_mfma_f32_32x32x16_bf16 v[64:79], v[212:215], v[188:191], v[64:79]\n\t" \
  "ds_read_b128 v[212:215], v250 offset:5120\n\t" \
  "v_cvt_pk_bf16_f32 v178, v148, v149\n\t" \
  "v_exp_f32_e32 v150, v150\n\t" \
  "v_fma_f32 v151, v151, s59, v240\n\t" \
  "v_cvt_pk_bf16_f32 v152, 0, v152\n\t" \
  "v_mul_f32_e32 v153, s58, v153\n\t" \
  "v_cvt_pk_bf16_f32 v154, 0, v154\n\t" \
  "v_exp_f32_e32 v151, v151\n\t" \
  "v_fma_f32 v152, v152, s59, v240\n\t" \
  "v_cvt_pk_bf16_f32 v153, 0, v153\n\t" \
  "v_mul_f32_e32 v154, s58, v154\n\t" \
  "v_cvt_pk_bf16_f32 v155, 0, v155\n\t" \
  "v_mfma_f32_32x32x16_bf16 v[80:95], v[216:219], v[188:191], v[80:95]\n\t" \
  "ds_read_b128 v[216:219], v250 offset:6144\n\t" \
  "v_cvt_pk_bf16_f32 v179, v150, v151\n\t" \
  "v_exp_f32_e32 v152, v152\n\t" \
  "v_fma_f32 v153, v153, s59, v240\n\t" \
  "v_cvt_pk_bf16_f32 v154, 0, v154\n\t" \
  "v_mul_f32_e32 v155, s58, v155\n\t" \
  "v_cvt_pk_bf16_f32 v156, 0, v156\n\t" \
  "v_exp_f32_e32 v153, v153\n\t" \
  "v_fma_f32 v154, v154, s59, v240\n\t" \
  "v_cvt_pk_bf16_f32 v155, 0, v155\n\t" \
  "v_mul_f32_e32 v156, s58, v156\n\t" \
  "v_cvt_pk_bf16_f32 v157, 0, v157\n\t" \
  "v_mfma_f32_32x32x16_bf16 v[96:111], v[220:223], v[188:191], v[96:111]\n\t" \
  "ds_read_b128 v[220:223], v250 offset:7168\n\t" \
  "v_cvt_pk_bf16_f32 v180, v152, v153\n\t" \
  "v_exp_f32_e32 v154, v154\n\t" \
  "v_fma_f32 v155, v155, s59, v240\n\t" \
  "v_cvt_pk_bf16_f32 v156, 0, v156\n\t" \
  "v_mul_f32_e32 v157, s58, v157\n\t" \
  "v_cvt_pk_bf16_f32 v158, 0, v158\n\t" \
  "v_exp_f32_e32 v155, v155\n\t" \
  "v_fma_f32 v156, v156, s59, v240\n\t" \
  "v_cvt_pk_bf16_f32 v157, 0, v157\n\t" \
  "v_mul_f32_e32 v158, s58, v158\n\t" \
  "v_cvt_pk_bf16_f32 v159, 0, v159\n\t" \
  "v_mfma_f32_32x32x16_bf16 v[192:207], v[242:245], v[188:191], v[192:207]\n\t" \
  "s_cmp_lg_u32 s69, 0\n\t" \
  "s_cbranch_scc1 LA_ROD1%=\n\t" \
  "LA_ROBD1%=:\n\t" \
  "s_nop 0\n\t" \
  "s_waitcnt lgkmcnt(7)\n\t" \
  "v_mfma_f32_32x32x16_bf16 v[48:63], v[224:227], v[176:179], v[48:63]\n\t" \
  "ds_read_b128 v[224:227], v250 offset:8192\n\t" \
  "v_cvt_pk_bf16_f32 v181, v154, v155\n\t" \
  "v_exp_f32_e32 v156, v156\n\t" \
  "v_fma_f32 v157, v157, s59, v240\n\t" \
  "v_cvt_pk_bf16_f32 v158, 0, v158\n\t" \
  "v_mul_f32_e32 v159, s58, v159\n\t" \
  "v_cvt_pk_bf16_f32 v160, 0, v160\n\t" \
  "v_exp_f32_e32 v157, v157\n\t" \
  "v_fma_f32 v158, v158, s59, v240\n\t" \
  "v_cvt_pk_bf16_f32 v159, 0, v159\n\t" \
  "v_mul_f32_e32 v160, s58, v160\n\t" \
  "v_cvt_pk_bf16_f32 v161, 0, v161\n\t" \
  "s_waitcnt lgkmcnt(7)\n\t" \
  "v_mfma_f32_32x32x16_bf16 v[64:79], v[228:231], v[176:179], v[64:79]\n\t" \
  "ds_read_b128 v[228:231], v250 offset:9216\n\t" \
  "v_cvt_pk_bf16_f32 v182, v156, v157\n\t" \
  "v_exp_f32_e32 v158, v158\n\t" \
  "v_fma_f32 v159, v159, s59, v240\n\t" \
  "v_cvt_pk_bf16_f32 v160, 0, v160\n\t" \
  "v_mul_f32_e32 v161, s58, v161\n\t" \
  "v_cvt_pk_bf16_f32 v162, 0, v162\n\t" \
  "v_exp_f32_e32 v159, v159\n\t" \
  "v_fma_f32 v160, v160, s59, v240\n\t" \
  "v_cvt_pk_bf16_f32 v161, 0, v161\n\t" \
  "v_mul_f32_e32 v162, s58, v162\n\t" \
  "v_cvt_pk_bf16_f32 v163, 0, v163\n\t" \
  "s_waitcnt lgkmcnt(7)\n\t" \
  "v_mfma_f32_32x32x16_bf16 v[80:95], v[232:235], v[176:179], v[80:95]\n\t" \
  "ds_read_b128 v[232:235], v250 offset:10240\n\t" \
  "v_cvt_pk_bf16_f32 v183, v158, v159\n\t" \
  "v_exp_f32_e32 v160, v160\n\t" \
  "v_fma_f32 v161, v161, s59, v240\n\t" \
  "v_cvt_pk_bf16_f32 v162, 0, v162\n\t" \
  "v_mul_f32_e32 v163, s58, v163\n\t" \
  "v_cvt_pk_bf16_f32 v164, 0, v164\n\t" \
  "v_exp_f32_e32 v161, v161\n\t" \
  "v_fma_f32 v162, v162, s59, v240\n\t" \
  "v_cvt_pk_bf16_f32 v163, 0, v163\n\t" \
  "v_mul_f32_e32 v164, s58, v164\n\t" \
  "v_cvt_pk_bf16_f32 v165, 0, v165\n\t" \
  "s_waitcnt lgkmcnt(7)\n\t" \
  "v_mfma_f32_32x32x16_bf16 v[96:111], v[236:239], v[176:179], v[96:111]\n\t" \
  "ds_read_b128 v[236:239], v250 offset:11264\n\t" \
  "v_mfma_f32_32x32x16_bf16 v[192:207], v[242:245], v[176:179], v[192:207]\n\t" \
  "v_cvt_pk_bf16_f32 v184, v160, v161\n\t" \
  "v_exp_f32_e32 v162, v162\n\t" \
  "v_fma_f32 v163, v163, s59, v240\n\t" \
  "v_cvt_pk_bf16_f32 v164, 0, v164\n\t" \
  "v_mul_f32_e32 v165, s58, v165\n\t" \
  "v_cvt_pk_bf16_f32 v166, 0, v166\n\t" \
  "v_exp_f32_e32 v163, v163\n\t" \
  "v_fma_f32 v164, v164, s59, v240\n\t" \
  "v_cvt_pk_bf16_f32 v165, 0, v165\n\t" \
  "v_mul_f32_e32 v166, s58, v166\n\t" \
  "v_cvt_pk_bf16_f32 v167, 0, v167\n\t" \
  "s_waitcnt lgkmcnt(7)\n\t" \
  "v_mfma_f32_32x32x16_bf16 v[48:63], v[208:211], v[180:183], v[48:63]\n\t" \
  "ds_read_b128 v[208:211], v250 offset:12288\n\t" \
  "v_cvt_pk_bf16_f32 v185, v162, v163\n\t" \
  "v_exp_f32_e32 v164, v164\n\t" \
  "v_fma_f32 v165, v165, s59, v240\n\t" \
  "v_cvt_pk_bf16_f32 v166, 0, v166\n\t" \
  "v_mul_f32_e32 v167, s58, v167\n\t" \
  "v_cvt_pk_bf16_f32 v168, 0, v168\n\t" \
  "v_exp_f32_e32 v165, v165\n\t" \
  "v_fma_f32 v166, v166, s59, v240\n\t" \
  "v_cvt_pk_bf16_f32 v167, 0, v167\n\t" \
  "v_mul_f32_e32 v168, s58, v168\n\t" \
  "v_cvt_pk_bf16_f32 v169, 0, v169\n\t" \
  "s_waitcnt lgkmcnt(7)\n\t" \
  "v_mfma_f32_32x32x16_bf16 v[64:79], v[212:215], v[180:183], v[64:79]\n\t" \
  "ds_read_b128 v[212:215], v250 offset:13312\n\t" \
  "v_cvt_pk_bf16_f32 v186, v164, v165\n\t" \
  "v_exp_f32_e32 v166, v166\n\t" \
  "v_fma_f32 v167, v167, s59, v240\n\t" \
  "v_cvt_pk_bf16_f32 v168, 0, v168\n\t" \
  "v_mul_f32_e32 v169, s58, v169\n\t" \
  "v_cvt_pk_bf16_f32 v170, 0, v170\n\t" \
  "v_exp_f32_e32 v167, v167\n\t" \
  "v_fma_f32 v168, v168, s59, v240\n\t" \
  "v_cvt_pk_bf16_f32 v169, 0, v169\n\t" \
  "v_mul_f32_e32 v170, s58, v170\n\t" \
  "v_cvt_pk_bf16_f32 v171, 0, v171\n\t" \
  "s_waitcnt lgkmcnt(7)\n\t" \
  "v_mfma_f32_32x32x16_bf16 v[80:95], v[216:219], v[180:183], v[80:95]\n\t" \
  "ds_read_b128 v[216:219], v250 offset:14336\n\t" \
  "v_cvt_pk_bf16_f32 v187, v166, v167\n\t" \
  "v_exp_f32_e32 v168, v168\n\t" \
  "v_fma_f32 v169, v169, s59, v240\n\t" \
  "v_cvt_pk_bf16_f32 v170, 0, v170\n\t" \
  "v_mul_f32_e32 v171, s58, v171\n\t" \
  "v_cvt_pk_bf16_f32 v172, 0, v172\n\t" \
  "v_exp_f32_e32 v169, v169\n\t" \
  "v_fma_f32 v170, v170, s59, v240\n\t" \
  "v_cvt_pk_bf16_f32 v171, 0, v171\n\t" \
  "v_mul_f32_e32 v172, s58, v172\n\t" \
  "v_cvt_pk_bf16_f32 v173, 0, v173\n\t" \
  "s_waitcnt lgkmcnt(7)\n\t" \
  "v_mfma_f32_32x32x16_bf16 v[96:111], v[220:223], v[180:183], v[96:111]\n\t" \
  "ds_read_b128 v[220:223], v250 offset:15360\n\t" \
  "v_mfma_f32_32x32x16_bf16 v[192:207], v[242:245], v[180:183], v[192:207]\n\t" \
  "v_cvt_pk_bf16_f32 v188, v168, v169\n\t" \
  "v_exp_f32_e32 v170, v170\n\t" \
  "v_fma_f32 v171, v171, s59, v240\n\t" \
  "v_cvt_pk_bf16_f32 v172, 0, v172\n\t" \
  "v_mul_f32_e32 v173, s58, v173\n\t" \
  "v_cvt_pk_bf16_f32 v174, 0, v174\n\t" \
  "v_exp_f32_e32 v171, v171\n\t" \
  "v_fma_f32 v172, v172, s59, v240\n\t" \
  "v_cvt_pk_bf16_f32 v173, 0, v173\n\t" \
  "v_mul_f32_e32 v174, s58, v174\n\t" \
  "v_cvt_pk_bf16_f32 v175, 0, v175\n\t" \
  "s_waitcnt lgkmcnt(7)\n\t" \
  "v_mfma_f32_32x32x16_bf16 v[48:63], v[224:227], v[184:187], v[48:63]\n\t" \
  "v_cvt_pk_bf16_f32 v189, v170, v171\n\t" \
  "v_exp_f32_e32 v172, v172\n\t" \
  "v_fma_f32 v173, v173, s59, v240\n\t" \
  "v_cvt_pk_bf16_f32 v174, 0, v174\n\t" \
  "v_mul_f32_e32 v175, s58, v175\n\t" \
  "v_exp_f32_e32 v173, v173\n\t" \
  "v_fma_f32 v174, v174, s59, v240\n\t" \
  "v_cvt_pk_bf16_f32 v175, 0, v175\n\t" \
  "s_waitcnt lgkmcnt(6)\n\t" \
  "v_mfma_f32_32x32x16_bf16 v[64:79], v[228:231], v[184:187], v[64:79]\n\t" \
  "v_cvt_pk_bf16_f32 v190, v172, v173\n\t" \
  "v_exp_f32_e32 v174, v174\n\t" \
  "v_fma_f32 v175, v175, s59, v240\n\t" \
  "v_exp_f32_e32 v175, v175\n\t" \
  "s_waitcnt lgkmcnt(5)\n\t" \
  "v_mfma_f32_32x32x16_bf16 v[80:95], v[232:235], v[184:187], v[80:95]\n\t" \
  "v_cvt_pk_bf16_f32 v191, v174, v175\n\t" \
  "s_waitcnt lgkmcnt(4)\n\t" \
  "v_mfma_f32_32x32x16_bf16 v[96:111], v[236:239], v[184:187], v[96:111]\n\t" \
  "v_mfma_f32_32x32x16_bf16 v[192:207], v[242:245], v[184:187], v[192:207]\n\t" \
  "s_waitcnt lgkmcnt(3)\n\t" \
  "v_mfma_f32_32x32x16_bf16 v[48:63], v[208:211], v[188:191], v[48:63]\n\t" \
  "s_waitcnt lgkmcnt(2)\n\t" \
  "v_mfma_f32_32x32x16_bf16 v[64:79], v[212:215], v[188:191], v[64:79]\n\t" \
  "s_waitcnt lgkmcnt(1)\n\t" \
  "v_mfma_f32_32x32x16_bf16 v[80:95], v[216:219], v[188:191], v[80:95]\n\t" \
  "s_waitcnt lgkmcnt(0)\n\t" \
  "v_mfma_f32_32x32x16_bf16 v[96:111], v[220:223], v[188:191], v[96:111]\n\t" \
  "v_mfma_f32_32x32x16_bf16 v[192:207], v[242:245], v[188:191], v[192:207]\n\t" \
  "s_branch L_EPI%=\n\t" \
  "LB_PRO%=:\n\t" \
  "s_setprio 1\n\t" \
  "ds_read_b128 v[224:227], v249 offset:0\n\t" \
  "ds_read_b128 v[228:231], v249 offset:1024\n\t" \
  "ds_read_b128 v[232:235], v249 offset:2048\n\t" \
  "ds_read_b128 v[236:239], v249 offset:3072\n\t" \
  "ds_read_b128 v[208:211], v249 offset:4096\n\t" \
  "ds_read_b128 v[212:215], v249 offset:5120\n\t" \
  "ds_read_b128 v[216:219], v249 offset:6144\n\t" \
  "ds_read_b128 v[220:223], v249 offset:7168\n\t" \
  "s_add_u32 m0, s50, 0\n\t" \
  "s_nop 0\n\t" \
  "global_load_lds_dwordx4 %[lin], s[48:49] offset:0 sc1\n\t" \
  "global_load_lds_dwordx4 %[lin], s[48:49] offset:1024 sc1\n\t" \
  "global_load_lds_dwordx4 %[lin], s[48:49] offset:2048 sc1\n\t" \
  "global_load_lds_dwordx4 %[lin], s[48:49] offset:3072 sc1\n\t" \
  "s_add_u32 m0, s80, 0\n\t" \
  "s_add_u32 s64, s64, 0x8000\n\t" \
  "s_addc_u32 s65, s65, 0\n\t" \
  "global_load_lds_dwordx4 %[lin], s[64:65] offset:0 sc1\n\t" \
  "global_load_lds_dwordx4 %[lin], s[64:65] offset:1024 sc1\n\t" \
  "global_load_lds_dwordx4 %[lin], s[64:65] offset:2048 sc1\n\t" \
  "global_load_lds_dwordx4 %[lin], s[64:65] offset:3072 sc1\n\t" \
  "s_add_u32 m0, s80, 4096\n\t" \
  "s_nop 0\n\t" \
  "global_load_lds_dwordx4 v252, s[64:65] offset:0 sc1\n\t" \
  "global_load_lds_dwordx4 v252, s[64:65] offset:1024 sc1\n\t" \
  "global_load_lds_dwordx4 v252, s[64:65] offset:2048 sc1\n\t" \
  "global_load_lds_dwordx4 v252, s[64:65] offset:3072 sc1\n\t" \
  "s_add_u32 m0, s80, 65536\n\t" \
  "s_add_u32 s66, s66, 0x8000\n\t" \
  "s_addc_u32 s67, s67, 0\n\t" \
  "global_load_lds_dwordx4 %[lin], s[66:67] offset:0 sc1\n\t" \
  "global_load_lds_dwordx4 %[lin], s[66:67] offset:1024 sc1\n\t" \
  "global_load_lds_dwordx4 %[lin], s[66:67] offset:2048 sc1\n\t" \
  "global_load_lds_dwordx4 %[lin], s[66:67] offset:3072 sc1\n\t" \
  "s_add_u32 m0, s80, 69632\n\t" \
  "s_nop 0\n\t" \
  "global_load_lds_dwordx4 v252, s[66:67] offset:0 sc1\n\t" \
  "global_load_lds_dwordx4 v252, s[66:67] offset:1024 sc1\n\t" \
  "global_load_lds_dwordx4 v252, s[66:67] offset:2048 sc1\n\t" \
  "global_load_lds_dwordx4 v252, s[66:67] offset:3072 sc1\n\t" \
  "s_xor_b32 s80, s80, 0x8000\n\t" \
  "s_waitcnt lgkmcnt(7)\n\t" \
  "v_mfma_f32_32x32x16_bf16 v[112:127], v[224:227], v[16:19], 0\n\t" \
  "ds_read_b128 v[224:227], v249 offset:8192\n\t" \
  "v_mov_b32_e32 v48, 0\n\t" \
  "v_mov_b32_e32 v49, 0\n\t" \
  "v_mov_b32_e32 v50, 0\n\t" \
  "v_mov_b32_e32 v51, 0\n\t" \
  "s_waitcnt lgkmcnt(7)\n\t" \
  "v_mfma_f32_32x32x16_bf16 v[112:127], v[228:231], v[20:23], v[112:127]\n\t" \
  "ds_read_b128 v[228:231], v249 offset:9216\n\t" \
  "v_mov_b32_e32 v52, 0\n\t" \
  "v_mov_b32_e32 v53, 0\n\t" \
  "v_mov_b32_e32 v54, 0\n\t" \
  "v_mov_b32_e32 v55, 0\n\t" \
  "s_waitcnt lgkmcnt(7)\n\t" \
  "v_mfma_f32_32x32x16_bf16 v[112:127], v[232:235], v[24:27], v[112:127]\n\t" \
  "ds_read_b128 v[232:235], v249 offset:10240\n\t" \
  "v_mov_b32_e32 v56, 0\n\t" \
  "v_mov_b32_e32 v57, 0\n\t" \
  "v_mov_b32_e32 v58, 0\n\t" \
  "v_mov_b32_e32 v59, 0\n\t" \
  "s_waitcnt lgkmcnt(7)\n\t" \
  "v_mfma_f32_32x32x16_bf16 v[112:127], v[236:239], v[28:31], v[112:127]\n\t" \
  "ds_read_b128 v[236:239], v249 offset:11264\n\t" \
  "v_mov_b32_e32 v60, 0\n\t" \
  "v_mov_b32_e32 v61, 0\n\t" \
  "v_mov_b32_e32 v62, 0\n\t" \
  "v_mov_b32_e32 v63, 0\n\t" \
  "s_waitcnt lgkmcnt(7)\n\t" \
  "v_mfma_f32_32x32x16_bf16 v[112:127], v[208:211], v[32:35], v[112:127]\n\t" \
  "ds_read_b128 v[208:211], v249 offset:12288\n\t" \
  "v_mov_b32_e32 v64, 0\n\t" \
  "v_mov_b32_e32 v65, 0\n\t" \
  "v_mov_b32_e32 v66, 0\n\t" \
  "v_mov_b32_e32 v67, 0\n\t" \
  "s_waitcnt lgkmcnt(7)\n\t" \
  "v_mfma_f32_32x32x16_bf16 v[112:127], v[212:215], v[36:39], v[112:127]\n\t" \
  "ds_read_b128 v[212:215], v249 offset:13312\n\t" \
  "v_mov_b32_e32 v68, 0\n\t" \
  "v_mov_b32_e32 v69, 0\n\t" \
  "v_mov_b32_e32 v70, 0\n\t" \
  "v_mov_b32_e32 v71, 0\n\t" \
  "s_waitcnt lgkmcnt(7)\n\t" \
  "v_mfma_f32_32x32x16_bf16 v[112:127], v[216:219], v[40:43], v[112:127]\n\t" \
  "ds_read_b128 v[216:219], v249 offset:14336\n\t" \
  "v_mov_b32_e32 v72, 0\n\t" \
  "v_mov_b32_e32 v73, 0\n\t" \
  "v_mov_b32_e32 v74, 0\n\t" \
  "v_mov_b32_e32 v75, 0\n\t" \
  "s_waitcnt lgkmcnt(7)\n\t" \
  "v_mfma_f32_32x32x16_bf16 v[112:127], v[220:223], v[44:47], v[112:127]\n\t" \
  "ds_read_b128 v[220:223], v249 offset:15360\n\t" \
  "v_mov_b32_e32 v76, 0\n\t" \
  "v_mov_b32_e32 v77, 0\n\t" \
  "v_mov_b32_e32 v78, 0\n\t" \
  "v_mov_b32_e32 v79, 0\n\t" \
  "s_waitcnt lgkmcnt(7)\n\t" \
  "v_mfma_f32_32x32x16_bf16 v[128:143], v[224:227], v[16:19], 0\n\t" \
  "v_mov_b32_e32 v80, 0\n\t" \
  "v_mov_b32_e32 v81, 0\n\t" \
  "v_mov_b32_e32 v82, 0\n\t" \
  "v_mov_b32_e32 v83, 0\n\t" \
  "s_waitcnt lgkmcnt(6)\n\t" \
  "v_mfma_f32_32x32x16_bf16 v[128:143], v[228:231], v[20:23], v[128:143]\n\t" \
  "v_mov_b32_e32 v84, 0\n\t" \
  "v_mov_b32_e32 v85, 0\n\t" \
  "v_mov_b32_e32 v86, 0\n\t" \
  "v_mov_b32_e32 v87, 0\n\t" \
  "s_waitcnt lgkmcnt(5)\n\t" \
  "v_mfma_f32_32x32x16_bf16 v[128:143], v[232:235], v[24:27], v[128:143]\n\t" \
  "v_mov_b32_e32 v88, 0\n\t" \
  "v_mov_b32_e32 v89, 0\n\t" \
  "v_mov_b32_e32 v90, 0\n\t" \
  "v_mov_b32_e32 v91, 0\n\t" \
  "s_waitcnt lgkmcnt(4)\n\t" \
  "v_mfma_f32_32x32x16_bf16 v[128:143], v[236:239], v[28:31], v[128:143]\n\t" \
  "v_mov_b32_e32 v92, 0\n\t" \
  "v_mov_b32_e32 v93, 0\n\t" \
  "v_mov_b32_e32 v94, 0\n\t" \
  "v_mov_b32_e32 v95, 0\n\t" \
  "s_waitcnt lgkmcnt(3)\n\t" \
  "v_mfma_f32_32x32x16_bf16 v[128:143], v[208:211], v[32:35], v[128:143]\n\t" \
  "v_mov_b32_e32 v96, 0\n\t" \
  "v_mov_b32_e32 v97, 0\n\t" \
  "v_mov_b32_e32 v98, 0\n\t" \
  "v_mov_b32_e32 v99, 0\n\t" \
  "s_waitcnt lgkmcnt(2)\n\t" \
  "v_mfma_f32_32x32x16_bf16 v[128:143], v[212:215], v[36:39], v[128:143]\n\t" \
  "v_mov_b32_e32 v100, 0\n\t" \
  "v_mov_b32_e32 v101, 0\n\t" \
  "v_mov_b32_e32 v102, 0\n\t" \
  "v_mov_b32_e32 v103, 0\n\t" \
  "s_waitcnt lgkmcnt(1)\n\t" \
  "v_mfma_f32_32x32x16_bf16 v[128:143], v[216:219], v[40:43], v[128:143]\n\t" \
  "v_mov_b32_e32 v104, 0\n\t" \
  "v_mov_b32_e32 v105, 0\n\t" \
  "v_mov_b32_e32 v106, 0\n\t" \
  "v_mov_b32_e32 v107, 0\n\t" \
  "s_waitcnt lgkmcnt(0)\n\t" \
  "v_mfma_f32_32x32x16_bf16 v[128:143], v[220:223], v[44:47], v[128:143]\n\t" \
  "v_mov_b32_e32 v108, 0\n\t" \
  "v_mov_b32_e32 v109, 0\n\t" \
  "v_mov_b32_e32 v110, 0\n\t" \
  "v_mov_b32_e32 v111, 0\n\t" \
  "ds_read_b128 v[224:227], v249 offset:16384\n\t" \
  "ds_read_b128 v[228:231], v249 offset:17408\n\t" \
  "ds_read_b128 v[232:235], v249 offset:18432\n\t" \
  "ds_read_b128 v[236:239], v249 offset:19456\n\t" \
  "v_mov_b32_e32 v208, 0\n\t" \
  "v_mov_b32_e32 v209, 0\n\t" \
  "v_mov_b32_e32 v210, 0\n\t" \
  "v_mov_b32_e32 v211, 0\n\t" \
  "v_mov_b32_e32 v212, 0\n\t" \
  "v_mov_b32_e32 v213, 0\n\t" \
  "v_mov_b32_e32 v214, 0\n\t" \
  "v_mov_b32_e32 v215, 0\n\t" \
  "v_mov_b32_e32 v216, 0\n\t" \
  "v_mov_b32_e32 v217, 0\n\t" \
  "v_mov_b32_e32 v218, 0\n\t" \
  "v_mov_b32_e32 v219, 0\n\t" \
  "v_mov_b32_e32 v220, 0\n\t" \
  "v_mov_b32_e32 v221, 0\n\t" \
  "v_mov_b32_e32 v222, 0\n\t" \
  "v_mov_b32_e32 v223, 0\n\t" \
  "v_mov_b32_e32 v188, 0\n\t" \
  "v_mov_b32_e32 v189, 0\n\t" \
  "v_mov_b32_e32 v190, 0\n\t" \
  "v_mov_b32_e32 v191, 0\n\t" \
  "s_cmp_eq_u32 s52, 1\n\t" \
  "s_cbranch_scc1 LB_PMASK%=\n\t" \
  "LB_PMASKB%=:\n\t" \
  "v_max_f32_e32 v241, v112, v113\n\t" \
  "v_max3_f32 v241, v241, v114, v115\n\t" \
  "v_max3_f32 v241, v241, v116, v117\n\t" \
  "v_max3_f32 v241, v241, v118, v119\n\t" \
  "v_max3_f32 v241, v241, v120, v121\n\t" \
  "v_max3_f32 v241, v241, v122, v123\n\t" \
  "v_max3_f32 v241, v241, v124, v125\n\t" \
  "v_max3_f32 v241, v241, v126, v127\n\t" \
  "v_max3_f32 v241, v241, v128, v129\n\t" \
  "v_max3_f32 v241, v241, v130, v131\n\t" \
  "v_max3_f32 v241, v241, v132, v133\n\t" \
  "v_max3_f32 v241, v241, v134, v135\n\t" \
  "v_max3_f32 v241, v241, v136, v137\n\t" \
  "v_max3_f32 v241, v241, v138, v139\n\t" \
  "v_max3_f32 v241, v241, v140, v141\n\t" \
  "v_max3_f32 v241, v241, v142, v143\n\t" \
  "v_mov_b32_e32 v246, v241\n\t" \
  "s_nop 1\n\t" \
  "v_permlane32_swap_b32_e32 v241, v246\n\t" \
  "s_nop 0\n\t" \
  "v_max_f32_e32 v241, v241, v246\n\t" \
  "v_mul_f32_e64 v240, -v241, s60\n\t" \
  "v_cvt_pk_bf16_f32 v112, 0, v112\n\t" \
  "v_cvt_pk_bf16_f32 v113, 0, v113\n\t" \
  "v_cvt_pk_bf16_f32 v114, 0, v114\n\t" \
  "v_cvt_pk_bf16_f32 v115, 0, v115\n\t" \
  "v_cvt_pk_bf16_f32 v116, 0, v116\n\t" \
  "v_cvt_pk_bf16_f32 v117, 0, v117\n\t" \
  "v_cvt_pk_bf16_f32 v118, 0, v118\n\t" \
  "v_cvt_pk_bf16_f32 v119, 0, v119\n\t" \
  "v_mul_f32_e32 v112, s58, v112\n\t" \
  "v_mul_f32_e32 v113, s58, v113\n\t" \
  "v_mul_f32_e32 v114, s58, v114\n\t" \
  "v_mul_f32_e32 v115, s58, v115\n\t" \
  "v_mul_f32_e32 v116, s58, v116\n\t" \
  "v_mul_f32_e32 v117, s58, v117\n\t" \
  "v_mul_f32_e32 v118, s58, v118\n\t" \
  "v_cvt_pk_bf16_f32 v112, 0, v112\n\t" \
  "v_cvt_pk_bf16_f32 v113, 0, v113\n\t" \
  "v_cvt_pk_bf16_f32 v114, 0, v114\n\t" \
  "v_cvt_pk_bf16_f32 v115, 0, v115\n\t" \
  "v_cvt_pk_bf16_f32 v116, 0, v116\n\t" \
  "v_cvt_pk_bf16_f32 v117, 0, v117\n\t" \
  "v_fma_f32 v112, v112, s59, v240\n\t" \
  "v_fma_f32 v113, v113, s59, v240\n\t" \
  "v_fma_f32 v114, v114, s59, v240\n\t" \
  "v_fma_f32 v115, v115, s59, v240\n\t" \
  "v_fma_f32 v116, v116, s59, v240\n\t" \
  "v_exp_f32_e32 v112, v112\n\t" \
  "v_exp_f32_e32 v113, v113\n\t" \
  "v_exp_f32_e32 v114, v114\n\t" \
  "v_exp_f32_e32 v115, v115\n\t" \
  "s_waitcnt vmcnt(16) lgkmcnt(0)\n\t" \
  "s_barrier\n\t" \
  "s_add_u32 s71, s71, 1\n\t" \
  "s_cmp_eq_u32 s52, 1\n\t" \
  "s_cbranch_scc1 LB_DRAIN0%=\n\t" \
  "s_branch LB_STEP1%=\n\t" \
  "LB_STEP0%=:\n\t" \
  "ds_read_b128 v[224:227], v249 offset:0\n\t" \
  "ds_read_b128 v[228:231], v249 offset:1024\n\t" \
  "ds_read_b128 v[232:235], v249 offset:2048\n\t" \
  "ds_read_b128 v[236:239], v249 offset:3072\n\t" \
  "v_mfma_f32_32x32x16_bf16 v[48:63], v[208:211], v[188:191], v[48:63]\n\t" \
  "ds_read_b128 v[208:211], v249 offset:4096\n\t" \
  "v_exp_f32_e32 v148, v148\n\t" \
  "v_fma_f32 v149, v149, s59, v240\n\t" \
  "v_cvt_pk_bf16_f32 v150, 0, v150\n\t" \
  "v_mul_f32_e32 v151, s58, v151\n\t" \
  "v_cvt_pk_bf16_f32 v152, 0, v152\n\t" \
  "v_cvt_pk_bf16_f32 v176, v144, v145\n\t" \
  "v_mfma_f32_32x32x16_bf16 v[64:79], v[212:215], v[188:191], v[64:79]\n\t" \
  "ds_read_b128 v[212:215], v249 offset:5120\n\t" \
  "v_exp_f32_e32 v149, v149\n\t" \
  "v_fma_f32 v150, v150, s59, v240\n\t" \
  "v_cvt_pk_bf16_f32 v151, 0, v151\n\t" \
  "v_mul_f32_e32 v152, s58, v152\n\t" \
  "v_cvt_pk_bf16_f32 v153, 0, v153\n\t" \
  "v_mfma_f32_32x32x16_bf16 v[80:95], v[216:219], v[188:191], v[80:95]\n\t" \
  "ds_read_b128 v[216:219], v249 offset:6144\n\t" \
  "v_exp_f32_e32 v150, v150\n\t" \
  "v_fma_f32 v151, v151, s59, v240\n\t" \
  "v_cvt_pk_bf16_f32 v152, 0, v152\n\t" \
  "v_mul_f32_e32 v153, s58, v153\n\t" \
  "v_cvt_pk_bf16_f32 v154, 0, v154\n\t" \
  "v_cvt_pk_bf16_f32 v177, v146, v147\n\t" \
  "v_mfma_f32_32x32x16_bf16 v[96:111], v[220:223], v[188:191], v[96:111]\n\t" \
  "ds_read_b128 v[220:223], v249 offset:7168\n\t" \
  "v_exp_f32_e32 v151, v151\n\t" \
  "v_fma_f32 v152, v152, s59, v240\n\t" \
  "v_cvt_pk_bf16_f32 v153, 0, v153\n\t" \
  "v_mfma_f32_32x32x16_bf16 v[192:207], v[242:245], v[188:191], v[192:207]\n\t" \
  "v_mul_f32_e32 v154, s58, v154\n\t" \
  "v_cvt_pk_bf16_f32 v155, 0, v155\n\t" \
  "s_cmp_lg_u32 s69, 0\n\t" \
  "s_cbranch_scc1 LB_ROS0%=\n\t" \
  "LB_ROBS0%=:\n\t" \
  "s_waitcnt lgkmcnt(4)\n\t" \
  "v_mfma_f32_32x32x16_bf16 v[112:127], v[224:227], v[16:19], 0\n\t" \
  "ds_read_b128 v[224:227], v249 offset:8192\n\t" \
  "v_exp_f32_e32 v152, v152\n\t" \
  "v_fma_f32 v153, v153, s59, v240\n\t" \
  "v_cvt_pk_bf16_f32 v154, 0, v154\n\t" \
  "v_mul_f32_e32 v155, s58, v155\n\t" \
  "v_cvt_pk_bf16_f32 v156, 0, v156\n\t" \
  "v_cvt_pk_bf16_f32 v178, v148, v149\n\t" \
  "s_add_u32 m0, s80, 0\n\t" \
  "s_add_u32 s64, s64, 0x8000\n\t" \
  "s_addc_u32 s65, s65, 0\n\t" \
  "global_load_lds_dwordx4 %[lin], s[64:65] offset:0 sc1\n\t" \
  "v_mfma_f32_32x32x16_bf16 v[112:127], v[228:231], v[20:23], v[112:127]\n\t" \
  "ds_read_b128 v[228:231], v249 offset:9216\n\t" \
  "v_exp_f32_e32 v153, v153\n\t" \
  "v_fma_f32 v154, v154, s59, v240\n\t" \
  "v_cvt_pk_bf16_f32 v155, 0, v155\n\t" \
  "v_mul_f32_e32 v156, s58, v156\n\t" \
  "v_cvt_pk_bf16_f32 v157, 0, v157\n\t" \
  "s_add_u32 s68, s68, 1\n\t" \
  "global_load_lds_dwordx4 %[lin], s[64:65] offset:1024 sc1\n\t" \
  "v_mfma_f32_32x32x16_bf16 v[112:127], v[232:235], v[24:27], v[112:127]\n\t" \
  "ds_read_b128 v[232:235], v249 offset:10240\n\t" \
  "v_exp_f32_e32 v154, v154\n\t" \
  "v_fma_f32 v155, v155, s59, v240\n\t" \
  "v_cvt_pk_bf16_f32 v156, 0, v156\n\t" \
  "v_mul_f32_e32 v157, s58, v157\n\t" \
  "v_cvt_pk_bf16_f32 v158, 0, v158\n\t" \
  "v_cvt_pk_bf16_f32 v179, v150, v151\n\t" \
  "global_load_lds_dwordx4 %[lin], s[64:65] offset:2048 sc1\n\t" \
  "v_mfma_f32_32x32x16_bf16 v[112:127], v[236:239], v[28:31], v[112:127]\n\t" \
  "ds_read_b128 v[236:239], v249 offset:11264\n\t" \
  "v_exp_f32_e32 v155, v155\n\t" \
  "v_fma_f32 v156, v156, s59, v240\n\t" \
  "v_cvt_pk_bf16_f32 v157, 0, v157\n\t" \
  "v_mul_f32_e32 v158, s58, v158\n\t" \
  "v_cvt_pk_bf16_f32 v159, 0, v159\n\t" \
  "v_cvt_pk_bf16_f32 v180, v152, v153\n\t" \
  "global_load_lds_dwordx4 %[lin], s[64:65] offset:3072 sc1\n\t" \
  "s_waitcnt lgkmcnt(4)\n\t" \
  "v_mfma_f32_32x32x16_bf16 v[112:127], v[208:211], v[32:35], v[112:127]\n\t" \
  "ds_read_b128 v[208:211], v249 offset:12288\n\t" \
  "v_exp_f32_e32 v156, v156\n\t" \
  "v_fma_f32 v157, v157, s59, v240\n\t" \
  "v_cvt_pk_bf16_f32 v158, 0, v158\n\t" \
  "v_mul_f32_e32 v159, s58, v159\n\t" \
  "v_cvt_pk_bf16_f32 v160, 0, v160\n\t" \
  "s_add_u32 m0, s80, 4096\n\t" \
  "s_nop 0\n\t" \
  "global_load_lds_dwordx4 v252, s[64:65] offset:0 sc1\n\t" \
  "v_mfma_f32_32x32x16_bf16 v[112:127], v[212:215], v[36:39], v[112:127]\n\t" \
  "ds_read_b128 v[212:215], v249 offset:13312\n\t" \
  "v_exp_f32_e32 v157, v157\n\t" \
  "v_fma_f32 v158, v158, s59, v240\n\t" \
  "v_cvt_pk_bf16_f32 v159, 0, v159\n\t" \
  "v_mul_f32_e32 v160, s58, v160\n\t" \
  "v_cvt_pk_bf16_f32 v161, 0, v161\n\t" \
  "v_cvt_pk_bf16_f32 v181, v154, v155\n\t" \
  "global_load_lds_dwordx4 v252, s[64:65] offset:1024 sc1\n\t" \
  "v_mfma_f32_32x32x16_bf16 v[112:127], v[216:219], v[40:43], v[112:127]\n\t" \
  "ds_read_b128 v[216:219], v249 offset:14336\n\t" \
  "v_exp_f32_e32 v158, v158\n\t" \
  "v_fma_f32 v159, v159, s59, v240\n\t" \
  "v_cvt_pk_bf16_f32 v160, 0, v160\n\t" \
  "v_mul_f32_e32 v161, s58, v161\n\t" \
  "v_cvt_pk_bf16_f32 v162, 0, v162\n\t" \
  "v_cvt_pk_bf16_f32 v182, v156, v157\n\t" \
  "global_load_lds_dwordx4 v252, s[64:65] offset:2048 sc1\n\t" \
  "v_mfma_f32_32x32x16_bf16 v[112:127], v[220:223], v[44:47], v[112:127]\n\t" \
  "ds_read_b128 v[220:223], v249 offset:15360\n\t" \
  "v_exp_f32_e32 v159, v159\n\t" \
  "v_fma_f32 v160, v160, s59, v240\n\t" \
  "v_cvt_pk_bf16_f32 v161, 0, v161\n\t" \
  "v_mul_f32_e32 v162, s58, v162\n\t" \
  "v_cvt_pk_bf16_f32 v163, 0, v163\n\t" \
  "global_load_lds_dwordx4 v252, s[64:65] offset:3072 sc1\n\t" \
  "s_waitcnt lgkmcnt(4)\n\t" \
  "v_mfma_f32_32x32x16_bf16 v[128:143], v[224:227], v[16:19], 0\n\t" \
  "ds_read_b128 v[224:227], v250 offset:0\n\t" \
  "v_exp_f32_e32 v160, v160\n\t" \
  "v_fma_f32 v161, v161, s59, v240\n\t" \
  "v_cvt_pk_bf16_f32 v162, 0, v162\n\t" \
  "v_mul_f32_e32 v163, s58, v163\n\t" \
  "v_cvt_pk_bf16_f32 v164, 0, v164\n\t" \
  "v_cvt_pk_bf16_f32 v183, v158, v159\n\t" \
  "s_add_u32 m0, s80, 65536\n\t" \
  "s_add_u32 s66, s66, 0x8000\n\t" \
  "s_addc_u32 s67, s67, 0\n\t" \
  "global_load_lds_dwordx4 %[lin], s[66:67] offset:0 sc1\n\t" \
  "v_mfma_f32_32x32x16_bf16 v[128:143], v[228:231], v[20:23], v[128:143]\n\t" \
  "ds_read_b128 v[228:231], v250 offset:1024\n\t" \
  "v_exp_f32_e32 v161, v161\n\t" \
  "v_fma_f32 v162, v162, s59, v240\n\t" \
  "v_cvt_pk_bf16_f32 v163, 0, v163\n\t" \
  "v_mul_f32_e32 v164, s58, v164\n\t" \
  "v_cvt_pk_bf16_f32 v165, 0, v165\n\t" \
  "global_load_lds_dwordx4 %[lin], s[66:67] offset:1024 sc1\n\t" \
  "s_cmp_eq_u32 s68, s52\n\t" \
  "s_cbranch_scc1 LB_MASK00%=\n\t" \
  "LB_MASKB00%=:\n\t" \
  "v_mfma_f32_32x32x16_bf16 v[128:143], v[232:235], v[24:27], v[128:143]\n\t" \
  "ds_read_b128 v[232:235], v250 offset:2048\n\t" \
  "v_exp_f32_e32 v162, v162\n\t" \
  "v_fma_f32 v163, v163, s59, v240\n\t" \
  "v_cvt_pk_bf16_f32 v164, 0, v164\n\t" \
  "v_mul_f32_e32 v165, s58, v165\n\t" \
  "v_cvt_pk_bf16_f32 v166, 0, v166\n\t" \
  "v_cvt_pk_bf16_f32 v184, v160, v161\n\t" \
  "global_load_lds_dwordx4 %[lin], s[66:67] offset:2048 sc1\n\t" \
  "v_max_f32_e32 v241, v112, v113\n\t" \
  "v_mfma_f32_32x32x16_bf16 v[128:143], v[236:239], v[28:31], v[128:143]\n\t" \
  "ds_read_b128 v[236:239], v250 offset:3072\n\t" \
  "v_exp_f32_e32 v163, v163\n\t" \
  "v_fma_f32 v164, v164, s59, v240\n\t" \
  "v_cvt_pk_bf16_f32 v165, 0, v165\n\t" \
  "v_mul_f32_e32 v166, s58, v166\n\t" \
  "v_cvt_pk_bf16_f32 v167, 0, v167\n\t" \
  "global_load_lds_dwordx4 %[lin], s[66:67] offset:3072 sc1\n\t" \
  "v_max3_f32 v241, v241, v114, v115\n\t" \
  "s_waitcnt lgkmcnt(4)\n\t" \
  "v_mfma_f32_32x32x16_bf16 v[128:143], v[208:211], v[32:35], v[128:143]\n\t" \
  "ds_read_b128 v[208:211], v250 offset:4096\n\t" \
  "v_exp_f32_e32 v164, v164\n\t" \
  "v_fma_f32 v165, v165, s59, v240\n\t" \
  "v_cvt_pk_bf16_f32 v166, 0, v166\n\t" \
  "v_mul_f32_e32 v167, s58, v167\n\t" \
  "v_cvt_pk_bf16_f32 v168, 0, v168\n\t" \
  "s_add_u32 m0, s80, 69632\n\t" \
  "s_nop 0\n\t" \
  "global_load_lds_dwordx4 v252, s[66:67] offset:0 sc1\n\t" \
  "v_max3_f32 v241, v241, v116, v117\n\t" \
  "v_mfma_f32_32x32x16_bf16 v[128:143], v[212:215], v[36:39], v[128:143]\n\t" \
  "ds_read_b128 v[212:215], v250 offset:5120\n\t" \
  "v_exp_f32_e32 v165, v165\n\t" \
  "v_fma_f32 v166, v166, s59, v240\n\t" \
  "v_cvt_pk_bf16_f32 v167, 0, v167\n\t" \
  "v_mul_f32_e32 v168, s58, v168\n\t" \
  "v_cvt_pk_bf16_f32 v169, 0, v169\n\t" \
  "v_cvt_pk_bf16_f32 v185, v162, v163\n\t" \
  "global_load_lds_dwordx4 v252, s[66:67] offset:1024 sc1\n\t" \
  "v_max3_f32 v241, v241, v118, v119\n\t" \
  "v_mfma_f32_32x32x16_bf16 v[128:143], v[216:219], v[40:43], v[128:143]\n\t" \
  "ds_read_b128 v[216:219], v250 offset:6144\n\t" \
  "v_exp_f32_e32 v166, v166\n\t" \
  "v_fma_f32 v167, v167, s59, v240\n\t" \
  "v_cvt_pk_bf16_f32 v168, 0, v168\n\t" \
  "v_mul_f32_e32 v169, s58, v169\n\t" \
  "v_cvt_pk_bf16_f32 v170, 0, v170\n\t" \
  "v_cvt_pk_bf16_f32 v186, v164, v165\n\t" \
  "global_load_lds_dwordx4 v252, s[66:67] offset:2048 sc1\n\t" \
  "v_max3_f32 v241, v241, v120, v121\n\t" \
  "v_mfma_f32_32x32x16_bf16 v[128:143], v[220:223], v[44:47], v[128:143]\n\t" \
  "ds_read_b128 v[220:223], v250 offset:7168\n\t" \
  "v_exp_f32_e32 v167, v167\n\t" \
  "v_fma_f32 v168, v168, s59, v240\n\t" \
  "v_cvt_pk_bf16_f32 v169, 0, v169\n\t" \
  "v_mul_f32_e32 v170, s58, v170\n\t" \
  "v_cvt_pk_bf16_f32 v171, 0, v171\n\t" \
  "global_load_lds_dwordx4 v252, s[66:67] offset:3072 sc1\n\t" \
  "v_max3_f32 v241, v241, v122, v123\n\t" \
  "s_waitcnt lgkmcnt(4)\n\t" \
  "v_mfma_f32_32x32x16_bf16 v[48:63], v[224:227], v[176:179], v[48:63]\n\t" \
  "ds_read_b128 v[224:227], v250 offset:8192\n\t" \
  "v_exp_f32_e32 v168, v168\n\t" \
  "v_fma_f32 v169, v169, s59, v240\n\t" \
  "v_cvt_pk_bf16_f32 v170, 0, v170\n\t" \
  "v_mul_f32_e32 v171, s58, v171\n\t" \
  "v_cvt_pk_bf16_f32 v172, 0, v172\n\t" \
  "v_cvt_pk_bf16_f32 v187, v166, v167\n\t" \
  "s_xor_b32 s80, s80, 0x8000\n\t" \
  "v_max3_f32 v241, v241, v124, v125\n\t" \
  "v_mfma_f32_32x32x16_bf16 v[64:79], v[228:231], v[176:179], v[64:79]\n\t" \
  "ds_read_b128 v[228:231], v250 offset:9216\n\t" \
  "v_exp_f32_e32 v169, v169\n\t" \
  "v_fma_f32 v170, v170, s59, v240\n\t" \
  "v_cvt_pk_bf16_f32 v171, 0, v171\n\t" \
  "v_mfma_f32_32x32x16_bf16 v[192:207], v[242:245], v[184:187], v[192:207]\n\t" \
  "v_mul_f32_e32 v172, s58, v172\n\t" \
  "v_cvt_pk_bf16_f32 v173, 0, v173\n\t" \
  "s_cmp_eq_u32 s68, s52\n\t" \
  "s_cbranch_scc1 LB_MASK01%=\n\t" \
  "LB_MASKB01%=:\n\t" \
  "v_max3_f32 v241, v241, v126, v127\n\t" \
  "v_mfma_f32_32x32x16_bf16 v[80:95], v[232:235], v[176:179], v[80:95]\n\t" \
  "ds_read_b128 v[232:235], v250 offset:10240\n\t" \
  "v_exp_f32_e32 v170, v170\n\t" \
  "v_fma_f32 v171, v171, s59, v240\n\t" \
  "v_cvt_pk_bf16_f32 v172, 0, v172\n\t" \
  "v_mul_f32_e32 v173, s58, v173\n\t" \
  "v_cvt_pk_bf16_f32 v174, 0, v174\n\t" \
  "v_max3_f32 v241, v241, v128, v129\n\t" \
  "v_max3_f32 v241, v241, v130, v131\n\t" \
  "v_mfma_f32_32x32x16_bf16 v[96:111], v[236:239], v[176:179], v[96:111]\n\t" \
  "ds_read_b128 v[236:239], v250 offset:11264\n\t" \
  "v_exp_f32_e32 v171, v171\n\t" \
  "v_fma_f32 v172, v172, s59, v240\n\t" \
  "v_cvt_pk_bf16_f32 v173, 0, v173\n\t" \
  "v_mfma_f32_32x32x16_bf16 v[192:207], v[242:245], v[176:179], v[192:207]\n\t" \
  "v_mul_f32_e32 v174, s58, v174\n\t" \
  "v_cvt_pk_bf16_f32 v175, 0, v175\n\t" \
  "v_max3_f32 v241, v241, v132, v133\n\t" \
  "v_max3_f32 v241, v241, v134, v135\n\t" \
  "s_waitcnt lgkmcnt(4)\n\t" \
  "v_mfma_f32_32x32x16_bf16 v[48:63], v[208:211], v[180:183], v[48:63]\n\t" \
  "ds_read_b128 v[208:211], v250 offset:12288\n\t" \
  "v_exp_f32_e32 v172, v172\n\t" \
  "v_fma_f32 v173, v173, s59, v240\n\t" \
  "v_cvt_pk_bf16_f32 v174, 0, v174\n\t" \
  "v_mul_f32_e32 v175, s58, v175\n\t" \
  "v_cvt_pk_bf16_f32 v112, 0, v112\n\t" \
  "v_max3_f32 v241, v241, v136, v137\n\t" \
  "v_max3_f32 v241, v241, v138, v139\n\t" \
  "v_mfma_f32_32x32x16_bf16 v[64:79], v[212:215], v[180:183], v[64:79]\n\t" \
  "ds_read_b128 v[212:215], v250 offset:13312\n\t" \
  "v_exp_f32_e32 v173, v173\n\t" \
  "v_fma_f32 v174, v174, s59, v240\n\t" \
  "v_cvt_pk_bf16_f32 v175, 0, v175\n\t" \
  "v_mul_f32_e32 v112, s58, v112\n\t" \
  "v_cvt_pk_bf16_f32 v113, 0, v113\n\t" \
  "v_max3_f32 v241, v241, v140, v141\n\t" \
  "v_max3_f32 v241, v241, v142, v143\n\t" \
  "v_mov_b32_e32 v246, v241\n\t" \
  "v_mfma_f32_32x32x16_bf16 v[80:95], v[216:219], v[180:183], v[80:95]\n\t" \
  "ds_read_b128 v[216:219], v250 offset:14336\n\t" \
  "v_exp_f32_e32 v174, v174\n\t" \
  "v_fma_f32 v175, v175, s59, v240\n\t" \
  "v_cvt_pk_bf16_f32 v112, 0, v112\n\t" \
  "v_mfma_f32_32x32x16_bf16 v[192:207], v[242:245], v[180:183], v[192:207]\n\t" \
  "v_mul_f32_e32 v113, s58, v113\n\t" \
  "v_cvt_pk_bf16_f32 v114, 0, v114\n\t" \
  "v_permlane32_swap_b32_e32 v241, v246\n\t" \
  "s_nop 0\n\t" \
  "v_max_f32_e32 v241, v241, v246\n\t" \
  "v_fma_f32 v241, v241, s60, v240\n\t" \
  "v_cmp_lt_f32_e32 vcc, 0x41c00000, v241\n\t" \
  "v_mfma_f32_32x32x16_bf16 v[96:111], v[220:223], v[180:183], v[96:111]\n\t" \
  "ds_read_b128 v[220:223], v250 offset:15360\n\t" \
  "s_cbranch_vccnz LB_RARE0%=\n\t" \
  "LB_RAREB0%=:\n\t" \
  "v_fma_f32 v112, v112, s59, v240\n\t" \
  "v_exp_f32_e32 v175, v175\n\t" \
  "v_cvt_pk_bf16_f32 v113, 0, v113\n\t" \
  "v_mul_f32_e32 v114, s58, v114\n\t" \
  "v_cvt_pk_bf16_f32 v115, 0, v115\n\t" \
  "s_waitcnt lgkmcnt(4)\n\t" \
  "v_mfma_f32_32x32x16_bf16 v[48:63], v[224:227], v[184:187], v[48:63]\n\t" \
  "ds_read_b128 v[224:227], v249 offset:16384\n\t" \
  "v_exp_f32_e32 v112, v112\n\t" \
  "v_fma_f32 v113, v113, s59, v240\n\t" \
  "v_cvt_pk_bf16_f32 v114, 0, v114\n\t" \
  "v_mul_f32_e32 v115, s58, v115\n\t" \
  "v_cvt_pk_bf16_f32 v116, 0, v116\n\t" \
  "v_cvt_pk_bf16_f32 v188, v168, v169\n\t" \
  "v_mfma_f32_32x32x16_bf16 v[64:79], v[228:231], v[184:187], v[64:79]\n\t" \
  "ds_read_b128 v[228:231], v249 offset:17408\n\t" \
  "v_exp_f32_e32 v113, v113\n\t" \
  "v_fma_f32 v114, v114, s59, v240\n\t" \
  "v_cvt_pk_bf16_f32 v115, 0, v115\n\t" \
  "v_mul_f32_e32 v116, s58, v116\n\t" \
  "v_cvt_pk_bf16_f32 v117, 0, v117\n\t" \
  "v_cvt_pk_bf16_f32 v189, v170, v171\n\t" \
  "v_mfma_f32_32x32x16_bf16 v[80:95], v[232:235], v[184:187], v[80:95]\n\t" \
  "ds_read_b128 v[232:235], v249 offset:18432\n\t" \
  "v_exp_f32_e32 v114, v114\n\t" \
  "v_fma_f32 v115, v115, s59, v240\n\t" \
  "v_cvt_pk_bf16_f32 v116, 0, v116\n\t" \
  "v_mul_f32_e32 v117, s58, v117\n\t" \
  "v_cvt_pk_bf16_f32 v118, 0, v118\n\t" \
  "v_cvt_pk_bf16_f32 v190, v172, v173\n\t" \
  "v_mfma_f32_32x32x16_bf16 v[96:111], v[236:239], v[184:187], v[96:111]\n\t" \
  "ds_read_b128 v[236:239], v249 offset:19456\n\t" \
  "v_exp_f32_e32 v115, v115\n\t" \
  "v_fma_f32 v116, v116, s59, v240\n\t" \
  "v_cvt_pk_bf16_f32 v117, 0, v117\n\t" \
  "v_mul_f32_e32 v118, s58, v118\n\t" \
  "v_cvt_pk_bf16_f32 v119, 0, v119\n\t" \
  "v_cvt_pk_bf16_f32 v191, v174, v175\n\t" \
  "s_cmp_eq_u32 s68, s52\n\t" \
  "s_cbranch_scc1 LB_DRAIN0%=\n\t" \
  "LB_STEP1%=:\n\t" \
  "s_waitcnt lgkmcnt(4)\n\t" \
  "v_mfma_f32_32x32x16_bf16 v[48:63], v[208:211], v[188:191], v[48:63]\n\t" \
  "ds_read_b128 v[208:211], v249 offset:20480\n\t" \
  "v_exp_f32_e32 v116, v116\n\t" \
  "v_fma_f32 v117, v117, s59, v240\n\t" \
  "v_cvt_pk_bf16_f32 v118, 0, v118\n\t" \
  "v_mul_f32_e32 v119, s58, v119\n\t" \
  "v_cvt_pk_bf16_f32 v120, 0, v120\n\t" \
  "v_cvt_pk_bf16_f32 v176, v112, v113\n\t" \
  "v_mfma_f32_32x32x16_bf16 v[64:79], v[212:215], v[188:191], v[64:79]\n\t" \
  "ds_read_b128 v[212:215], v249 offset:21504\n\t" \
  "v_exp_f32_e32 v117, v117\n\t" \
  "v_fma_f32 v118, v118, s59, v240\n\t" \
  "v_cvt_pk_bf16_f32 v119, 0, v119\n\t" \
  "v_mul_f32_e32 v120, s58, v120\n\t" \
  "v_cvt_pk_bf16_f32 v121, 0, v121\n\t" \
  "v_mfma_f32_32x32x16_bf16 v[80:95], v[216:219], v[188:191], v[80:95]\n\t" \
  "ds_read_b128 v[216:219], v249 offset:22528\n\t" \
  "v_exp_f32_e32 v118, v118\n\t" \
  "v_fma_f32 v119, v119, s59, v240\n\t" \
  "v_cvt_pk_bf16_f32 v120, 0, v120\n\t" \
  "v_mul_f32_e32 v121, s58, v121\n\t" \
  "v_cvt_pk_bf16_f32 v122, 0, v122\n\t" \
  "v_cvt_pk_bf16_f32 v177, v114, v115\n\t" \
  "v_mfma_f32_32x32x16_bf16 v[96:111], v[220:223], v[188:191], v[96:111]\n\t" \
  "ds_read_b128 v[220:223], v249 offset:23552\n\t" \
  "v_exp_f32_e32 v119, v119\n\t" \
  "v_fma_f32 v120, v120, s59, v240\n\t" \
  "v_cvt_pk_bf16_f32 v121, 0, v121\n\t" \
  "v_mfma_f32_32x32x16_bf16 v[192:207], v[242:245], v[188:191], v[192:207]\n\t" \
  "v_mul_f32_e32 v122, s58, v122\n\t" \
  "v_cvt_pk_bf16_f32 v123, 0, v123\n\t" \
  "s_cmp_lg_u32 s69, 0\n\t" \
  "s_cbranch_scc1 LB_ROS1%=\n\t" \
  "LB_ROBS1%=:\n\t" \
  "s_waitcnt lgkmcnt(4)\n\t" \
  "v_mfma_f32_32x32x16_bf16 v[144:159], v[224:227], v[16:19], 0\n\t" \
  "ds_read_b128 v[224:227], v249 offset:24576\n\t" \
  "v_exp_f32_e32 v120, v120\n\t" \
  "v_fma_f32 v121, v121, s59, v240\n\t" \
  "v_cvt_pk_bf16_f32 v122, 0, v122\n\t" \
  "v_mul_f32_e32 v123, s58, v123\n\t" \
  "v_cvt_pk_bf16_f32 v124, 0, v124\n\t" \
  "v_cvt_pk_bf16_f32 v178, v116, v117\n\t" \
  "v_mfma_f32_32x32x16_bf16 v[144:159], v[228:231], v[20:23], v[144:159]\n\t" \
  "ds_read_b128 v[228:231], v249 offset:25600\n\t" \
  "v_exp_f32_e32 v121, v121\n\t" \
  "v_fma_f32 v122, v122, s59, v240\n\t" \
  "v_cvt_pk_bf16_f32 v123, 0, v123\n\t" \
  "v_mul_f32_e32 v124, s58, v124\n\t" \
  "v_cvt_pk_bf16_f32 v125, 0, v125\n\t" \
  "s_add_u32 s68, s68, 1\n\t" \
  "v_mfma_f32_32x32x16_bf16 v[144:159], v[232:235], v[24:27], v[144:159]\n\t" \
  "ds_read_b128 v[232:235], v249 offset:26624\n\t" \
  "v_exp_f32_e32 v122, v122\n\t" \
  "v_fma_f32 v123, v123, s59, v240\n\t" \
  "v_cvt_pk_bf16_f32 v124, 0, v124\n\t" \
  "v_mul_f32_e32 v125, s58, v125\n\t" \
  "v_cvt_pk_bf16_f32 v126, 0, v126\n\t" \
  "v_cvt_pk_bf16_f32 v179, v118, v119\n\t" \
  "v_mfma_f32_32x32x16_bf16 v[144:159], v[236:239], v[28:31], v[144:159]\n\t" \
  "ds_read_b128 v[236:239], v249 offset:27648\n\t" \
  "v_exp_f32_e32 v123, v123\n\t" \
  "v_fma_f32 v124, v124, s59, v240\n\t" \
  "v_cvt_pk_bf16_f32 v125, 0, v125\n\t" \
  "v_mul_f32_e32 v126, s58, v126\n\t" \
  "v_cvt_pk_bf16_f32 v127, 0, v127\n\t" \
  "v_cvt_pk_bf16_f32 v180, v120, v121\n\t" \
  "s_waitcnt lgkmcnt(4)\n\t" \
  "v_mfma_f32_32x32x16_bf16 v[144:159], v[208:211], v[32:35], v[144:159]\n\t" \
  "ds_read_b128 v[208:211], v249 offset:28672\n\t" \
  "v_exp_f32_e32 v124, v124\n\t" \
  "v_fma_f32 v125, v125, s59, v240\n\t" \
  "v_cvt_pk_bf16_f32 v126, 0, v126\n\t" \
  "v_mul_f32_e32 v127, s58, v127\n\t" \
  "v_cvt_pk_bf16_f32 v128, 0, v128\n\t" \
  "v_mfma_f32_32x32x16_bf16 v[144:159], v[212:215], v[36:39], v[144:159]\n\t" \
  "ds_read_b128 v[212:215], v249 offset:29696\n\t" \
  "v_exp_f32_e32 v125, v125\n\t" \
  "v_fma_f32 v126, v126, s59, v240\n\t" \
  "v_cvt_pk_bf16_f32 v127, 0, v127\n\t" \
  "v_mul_f32_e32 v128, s58, v128\n\t" \
  "v_cvt_pk_bf16_f32 v129, 0, v129\n\t" \
  "v_cvt_pk_bf16_f32 v181, v122, v123\n\t" \
  "v_mfma_f32_32x32x16_bf16 v[144:159], v[216:219], v[40:43], v[144:159]\n\t" \
  "ds_read_b128 v[216:219], v249 offset:30720\n\t" \
  "v_exp_f32_e32 v126, v126\n\t" \
  "v_fma_f32 v127, v127, s59, v240\n\t" \
  "v_cvt_pk_bf16_f32 v128, 0, v128\n\t" \
  "v_mul_f32_e32 v129, s58, v129\n\t" \
  "v_cvt_pk_bf16_f32 v130, 0, v130\n\t" \
  "v_cvt_pk_bf16_f32 v182, v124, v125\n\t" \
  "v_mfma_f32_32x32x16_bf16 v[144:159], v[220:223], v[44:47], v[144:159]\n\t" \
  "ds_read_b128 v[220:223], v249 offset:31744\n\t" \
  "v_exp_f32_e32 v127, v127\n\t" \
  "v_fma_f32 v128, v128, s59, v240\n\t" \
  "v_cvt_pk_bf16_f32 v129, 0, v129\n\t" \
  "v_mul_f32_e32 v130, s58, v130\n\t" \
  "v_cvt_pk_bf16_f32 v131, 0, v131\n\t" \
  "s_waitcnt lgkmcnt(4)\n\t" \
  "v_mfma_f32_32x32x16_bf16 v[160:175], v[224:227], v[16:19], 0\n\t" \
  "ds_read_b128 v[224:227], v250 offset:16384\n\t" \
  "v_exp_f32_e32 v128, v128\n\t" \
  "v_fma_f32 v129, v129, s59, v240\n\t" \
  "v_cvt_pk_bf16_f32 v130, 0, v130\n\t" \
  "v_mul_f32_e32 v131, s58, v131\n\t" \
  "v_cvt_pk_bf16_f32 v132, 0, v132\n\t" \
  "v_cvt_pk_bf16_f32 v183, v126, v127\n\t" \
  "v_mfma_f32_32x32x16_bf16 v[160:175], v[228:231], v[20:23], v[160:175]\n\t" \
  "ds_read_b128 v[228:231], v250 offset:17408\n\t" \
  "v_exp_f32_e32 v129, v129\n\t" \
  "v_fma_f32 v130, v130, s59, v240\n\t" \
  "v_cvt_pk_bf16_f32 v131, 0, v131\n\t" \
  "v_mul_f32_e32 v132, s58, v132\n\t" \
  "v_cvt_pk_bf16_f32 v133, 0, v133\n\t" \
  "s_cmp_eq_u32 s68, s52\n\t" \
  "s_cbranch_scc1 LB_MASK10%=\n\t" \
  "LB_MASKB10%=:\n\t" \
  "v_mfma_f32_32x32x16_bf16 v[160:175], v[232:235], v[24:27], v[160:175]\n\t" \
  "ds_read_b128 v[232:235], v250 offset:18432\n\t" \
  "v_exp_f32_e32 v130, v130\n\t" \
  "v_fma_f32 v131, v131, s59, v240\n\t" \
  "v_cvt_pk_bf16_f32 v132, 0, v132\n\t" \
  "v_mul_f32_e32 v133, s58, v133\n\t" \
  "v_cvt_pk_bf16_f32 v134, 0, v134\n\t" \
  "v_cvt_pk_bf16_f32 v184, v128, v129\n\t" \
  "v_max_f32_e32 v241, v144, v145\n\t" \
  "v_mfma_f32_32x32x16_bf16 v[160:175], v[236:239], v[28:31], v[160:175]\n\t" \
  "ds_read_b128 v[236:239], v250 offset:19456\n\t" \
  "v_exp_f32_e32 v131, v131\n\t" \
  "v_fma_f32 v132, v132, s59, v240\n\t" \
  "v_cvt_pk_bf16_f32 v133, 0, v133\n\t" \
  "v_mul_f32_e32 v134, s58, v134\n\t" \
  "v_cvt_pk_bf16_f32 v135, 0, v135\n\t" \
  "v_max3_f32 v241, v241, v146, v147\n\t" \
  "s_waitcnt lgkmcnt(4)\n\t" \
  "v_mfma_f32_32x32x16_bf16 v[160:175], v[208:211], v[32:35], v[160:175]\n\t" \
  "ds_read_b128 v[208:211], v250 offset:20480\n\t" \
  "v_exp_f32_e32 v132, v132\n\t" \
  "v_fma_f32 v133, v133, s59, v240\n\t" \
  "v_cvt_pk_bf16_f32 v134, 0, v134\n\t" \
  "v_mul_f32_e32 v135, s58, v135\n\t" \
  "v_cvt_pk_bf16_f32 v136, 0, v136\n\t" \
  "v_max3_f32 v241, v241, v148, v149\n\t" \
  "v_mfma_f32_32x32x16_bf16 v[160:175], v[212:215], v[36:39], v[160:175]\n\t" \
  "ds_read_b128 v[212:215], v250 offset:21504\n\t" \
  "v_exp_f32_e32 v133, v133\n\t" \
  "v_fma_f32 v134, v134, s59, v240\n\t" \
  "v_cvt_pk_bf16_f32 v135, 0, v135\n\t" \
  "v_mul_f32_e32 v136, s58, v136\n\t" \
  "v_cvt_pk_bf16_f32 v137, 0, v137\n\t" \
  "v_cvt_pk_bf16_f32 v185, v130, v131\n\t" \
  "v_max3_f32 v241, v241, v150, v151\n\t" \
  "v_mfma_f32_32x32x16_bf16 v[160:175], v[216:219], v[40:43], v[160:175]\n\t" \
  "ds_read_b128 v[216:219], v250 offset:22528\n\t" \
  "v_exp_f32_e32 v134, v134\n\t" \
  "v_fma_f32 v135, v135, s59, v240\n\t" \
  "v_cvt_pk_bf16_f32 v136, 0, v136\n\t" \
  "v_mul_f32_e32 v137, s58, v137\n\t" \
  "v_cvt_pk_bf16_f32 v138, 0, v138\n\t" \
  "v_cvt_pk_bf16_f32 v186, v132, v133\n\t" \
  "v_max3_f32 v241, v241, v152, v153\n\t" \
  "v_mfma_f32_32x32x16_bf16 v[160:175], v[220:223], v[44:47], v[160:175]\n\t" \
  "ds_read_b128 v[220:223], v250 offset:23552\n\t" \
  "v_exp_f32_e32 v135, v135\n\t" \
  "v_fma_f32 v136, v136, s59, v240\n\t" \
  "v_cvt_pk_bf16_f32 v137, 0, v137\n\t" \
  "v_mul_f32_e32 v138, s58, v138\n\t" \
  "v_cvt_pk_bf16_f32 v139, 0, v139\n\t" \
  "v_max3_f32 v241, v241, v154, v155\n\t" \
  "s_waitcnt lgkmcnt(4)\n\t" \
  "v_mfma_f32_32x32x16_bf16 v[48:63], v[224:227], v[176:179], v[48:63]\n\t" \
  "ds_read_b128 v[224:227], v250 offset:24576\n\t" \
  "v_exp_f32_e32 v136, v136\n\t" \
  "v_fma_f32 v137, v137, s59, v240\n\t" \
  "v_cvt_pk_bf16_f32 v138, 0, v138\n\t" \
  "v_mul_f32_e32 v139, s58, v139\n\t" \
  "v_cvt_pk_bf16_f32 v140, 0, v140\n\t" \
  "v_cvt_pk_bf16_f32 v187, v134, v135\n\t" \
  "v_max3_f32 v241, v241, v156, v157\n\t" \
  "v_mfma_f32_32x32x16_bf16 v[64:79], v[228:231], v[176:179], v[64:79]\n\t" \
  "ds_read_b128 v[228:231], v250 offset:25600\n\t" \
  "v_exp_f32_e32 v137, v137\n\t" \
  "v_fma_f32 v138, v138, s59, v240\n\t" \
  "v_cvt_pk_bf16_f32 v139, 0, v139\n\t" \
  "v_mfma_f32_32x32x16_bf16 v[192:207], v[242:245], v[184:187], v[192:207]\n\t" \
  "v_mul_f32_e32 v140, s58, v140\n\t" \
  "v_cvt_pk_bf16_f32 v141, 0, v141\n\t" \
  "s_cmp_eq_u32 s68, s52\n\t" \
  "s_cbranch_scc1 LB_MASK11%=\n\t" \
  "LB_MASKB11%=:\n\t" \
  "v_max3_f32 v241, v241, v158, v159\n\t" \
  "v_mfma_f32_32x32x16_bf16 v[80:95], v[232:235], v[176:179], v[80:95]\n\t" \
  "ds_read_b128 v[232:235], v250 offset:26624\n\t" \
  "v_exp_f32_e32 v138, v138\n\t" \
  "v_fma_f32 v139, v139, s59, v240\n\t" \
  "v_cvt_pk_bf16_f32 v140, 0, v140\n\t" \
  "v_mul_f32_e32 v141, s58, v141\n\t" \
  "v_cvt_pk_bf16_f32 v142, 0, v142\n\t" \
  "v_max3_f32 v241, v241, v160, v161\n\t" \
  "v_max3_f32 v241, v241, v162, v163\n\t" \
  "v_mfma_f32_32x32x16_bf16 v[96:111], v[236:239], v[176:179], v[96:111]\n\t" \
  "ds_read_b128 v[236:239], v250 offset:27648\n\t" \
  "v_exp_f32_e32 v139, v139\n\t" \
  "v_fma_f32 v140, v140, s59, v240\n\t" \
  "v_cvt_pk_bf16_f32 v141, 0, v141\n\t" \
  "v_mfma_f32_32x32x16_bf16 v[192:207], v[242:245], v[176:179], v[192:207]\n\t" \
  "v_mul_f32_e32 v142, s58, v142\n\t" \
  "v_cvt_pk_bf16_f32 v143, 0, v143\n\t" \
  "v_max3_f32 v241, v241, v164, v165\n\t" \
  "v_max3_f32 v241, v241, v166, v167\n\t" \
  "s_waitcnt lgkmcnt(4)\n\t" \
  "v_mfma_f32_32x32x16_bf16 v[48:63], v[208:211], v[180:183], v[48:63]\n\t" \
  "ds_read_b128 v[208:211], v250 offset:28672\n\t" \
  "v_exp_f32_e32 v140, v140\n\t" \
  "v_fma_f32 v141, v141, s59, v240\n\t" \
  "v_cvt_pk_bf16_f32 v142, 0, v142\n\t" \
  "v_mul_f32_e32 v143, s58, v143\n\t" \
  "v_cvt_pk_bf16_f32 v144, 0, v144\n\t" \
  "v_max3_f32 v241, v241, v168, v169\n\t" \
  "v_max3_f32 v241, v241, v170, v171\n\t" \
  "v_mfma_f32_32x32x16_bf16 v[64:79], v[212:215], v[180:183], v[64:79]\n\t" \
  "ds_read_b128 v[212:215], v250 offset:29696\n\t" \
  "v_exp_f32_e32 v141, v141\n\t" \
  "v_fma_f32 v142, v142, s59, v240\n\t" \
  "v_cvt_pk_bf16_f32 v143, 0, v143\n\t" \
  "v_mul_f32_e32 v144, s58, v144\n\t" \
  "v_cvt_pk_bf16_f32 v145, 0, v145\n\t" \
  "v_max3_f32 v241, v241, v172, v173\n\t" \
  "v_max3_f32 v241, v241, v174, v175\n\t" \
  "v_mov_b32_e32 v246, v241\n\t" \
  "v_mfma_f32_32x32x16_bf16 v[80:95], v[216:219], v[180:183], v[80:95]\n\t" \
  "ds_read_b128 v[216:219], v250 offset:30720\n\t" \
  "v_exp_f32_e32 v142, v142\n\t" \
  "v_fma_f32 v143, v143, s59, v240\n\t" \
  "v_cvt_pk_bf16_f32 v144, 0, v144\n\t" \
  "v_mfma_f32_32x32x16_bf16 v[192:207], v[242:245], v[180:183], v[192:207]\n\t" \
  "v_mul_f32_e32 v145, s58, v145\n\t" \
  "v_cvt_pk_bf16_f32 v146, 0, v146\n\t" \
  "v_permlane32_swap_b32_e32 v241, v246\n\t" \
  "s_nop 0\n\t" \
  "v_max_f32_e32 v241, v241, v246\n\t" \
  "v_fma_f32 v241, v241, s60, v240\n\t" \
  "v_cmp_lt_f32_e32 vcc, 0x41c00000, v241\n\t" \
  "v_mfma_f32_32x32x16_bf16 v[96:111], v[220:223], v[180:183], v[96:111]\n\t" \
  "ds_read_b128 v[220:223], v250 offset:31744\n\t" \
  "s_cbranch_vccnz LB_RARE1%=\n\t" \
  "LB_RAREB1%=:\n\t" \
  "v_fma_f32 v144, v144, s59, v240\n\t" \
  "v_exp_f32_e32 v143, v143\n\t" \
  "v_cvt_pk_bf16_f32 v145, 0, v145\n\t" \
  "v_mul_f32_e32 v146, s58, v146\n\t" \
  "v_cvt_pk_bf16_f32 v147, 0, v147\n\t" \
  "s_waitcnt lgkmcnt(4)\n\t" \
  "v_mfma_f32_32x32x16_bf16 v[48:63], v[224:227], v[184:187], v[48:63]\n\t" \
  "v_exp_f32_e32 v144, v144\n\t" \
  "v_fma_f32 v145, v145, s59, v240\n\t" \
  "v_cvt_pk_bf16_f32 v146, 0, v146\n\t" \
  "v_mul_f32_e32 v147, s58, v147\n\t" \
  "v_cvt_pk_bf16_f32 v148, 0, v148\n\t" \
  "v_cvt_pk_bf16_f32 v188, v136, v137\n\t" \
  "v_mfma_f32_32x32x16_bf16 v[64:79], v[228:231], v[184:187], v[64:79]\n\t" \
  "v_exp_f32_e32 v145, v145\n\t" \
  "v_fma_f32 v146, v146, s59, v240\n\t" \
  "v_cvt_pk_bf16_f32 v147, 0, v147\n\t" \
  "v_mul_f32_e32 v148, s58, v148\n\t" \
  "v_cvt_pk_bf16_f32 v149, 0, v149\n\t" \
  "v_cvt_pk_bf16_f32 v189, v138, v139\n\t" \
  "v_xor_b32_e32 v249, 0x8000, v249\n\t" \
  "v_mfma_f32_32x32x16_bf16 v[80:95], v[232:235], v[184:187], v[80:95]\n\t" \
  "v_exp_f32_e32 v146, v146\n\t" \
  "v_fma_f32 v147, v147, s59, v240\n\t" \
  "v_cvt_pk_bf16_f32 v148, 0, v148\n\t" \
  "v_mul_f32_e32 v149, s58, v149\n\t" \
  "v_cvt_pk_bf16_f32 v150, 0, v150\n\t" \
  "v_cvt_pk_bf16_f32 v190, v140, v141\n\t" \
  "v_xor_b32_e32 v250, 0x8000, v250\n\t" \
  "v_mfma_f32_32x32x16_bf16 v[96:111], v[236:239], v[184:187], v[96:111]\n\t" \
  "v_exp_f32_e32 v147, v147\n\t" \
  "v_fma_f32 v148, v148, s59, v240\n\t" \
  "v_cvt_pk_bf16_f32 v149, 0, v149\n\t" \
  "v_mul_f32_e32 v150, s58, v150\n\t" \
  "v_cvt_pk_bf16_f32 v151, 0, v151\n\t" \
  "v_cvt_pk_bf16_f32 v191, v142, v143\n\t" \
  "s_waitcnt vmcnt(0) lgkmcnt(0)\n\t" \
  "s_barrier\n\t" \
  "s_add_u32 s71, s71, 1\n\t" \
  "s_cmp_eq_u32 s68, s52\n\t" \
  "s_cbranch_scc0 LB_STEP0%=\n\t" \
  "s_branch LB_DRAIN1%=\n\t" \
  "LB_DRAIN0%=:\n\t" \
  "s_waitcnt lgkmcnt(0)\n\t" \
  "ds_read_b128 v[224:227], v250 offset:16384\n\t" \
  "ds_read_b128 v[228:231], v250 offset:17408\n\t" \
  "ds_read_b128 v[232:235], v250 offset:18432\n\t" \
  "ds_read_b128 v[236:239], v250 offset:19456\n\t" \
  "v_mfma_f32_32x32x16_bf16 v[48:63], v[208:211], v[188:191], v[48:63]\n\t" \
  "ds_read_b128 v[208:211], v250 offset:20480\n\t" \
  "v_cvt_pk_bf16_f32 v176, v112, v113\n\t" \
  "v_cvt_pk_bf16_f32 v177, v114, v115\n\t" \
  "v_exp_f32_e32 v116, v116\n\t" \
  "v_fma_f32 v117, v117, s59, v240\n\t" \
  "v_cvt_pk_bf16_f32 v118, 0, v118\n\t" \
  "v_mul_f32_e32 v119, s58, v119\n\t" \
  "v_cvt_pk_bf16_f32 v120, 0, v120\n\t" \
  "v_exp_f32_e32 v117, v117\n\t" \
  "v_fma_f32 v118, v118, s59, v240\n\t" \
  "v_cvt_pk_bf16_f32 v119, 0, v119\n\t" \
  "v_mul_f32_e32 v120, s58, v120\n\t" \
  "v_cvt_pk_bf16_f32 v121, 0, v121\n\t" \
  "v_mfma_f32_32x32x16_bf16 v[64:79], v[212:215], v[188:191], v[64:79]\n\t" \
  "ds_read_b128 v[212:215], v250 offset:21504\n\t" \
  "v_cvt_pk_bf16_f32 v178, v116, v117\n\t" \
  "v_exp_f32_e32 v118, v118\n\t" \
  "v_fma_f32 v119, v119, s59, v240\n\t" \
  "v_cvt_pk_bf16_f32 v120, 0, v120\n\t" \
  "v_mul_f32_e32 v121, s58, v121\n\t" \
  "v_cvt_pk_bf16_f32 v122, 0, v122\n\t" \
  "v_exp_f32_e32 v119, v119\n\t" \
  "v_fma_f32 v120, v120, s59, v240\n\t" \
  "v_cvt_pk_bf16_f32 v121, 0, v121\n\t" \
  "v_mul_f32_e32 v122, s58, v122\n\t" \
  "v_cvt_pk_bf16_f32 v123, 0, v123\n\t" \
  "v_mfma_f32_32x32x16_bf16 v[80:95], v[216:219], v[188:191], v[80:95]\n\t" \
  "ds_read_b128 v[216:219], v250 offset:22528\n\t" \
  "v_cvt_pk_bf16_f32 v179, v118, v119\n\t" \
  "v_exp_f32_e32 v120, v120\n\t" \
  "v_fma_f32 v121, v121, s59, v240\n\t" \
  "v_cvt_pk_bf16_f32 v122, 0, v122\n\t" \
  "v_mul_f32_e32 v123, s58, v123\n\t" \
  "v_cvt_pk_bf16_f32 v124, 0, v124\n\t" \
  "v_exp_f32_e32 v121, v121\n\t" \
  "v_fma_f32 v122, v122, s59, v240\n\t" \
  "v_cvt_pk_bf16_f32 v123, 0, v123\n\t" \
  "v_mul_f32_e32 v124, s58, v124\n\t" \
  "v_cvt_pk_bf16_f32 v125, 0, v125\n\t" \
  "v_mfma_f32_32x32x16_bf16 v[96:111], v[220:223], v[188:191], v[96:111]\n\t" \
  "ds_read_b128 v[220:223], v250 offset:23552\n\t" \
  "v_cvt_pk_bf16_f32 v180, v120, v121\n\t" \
  "v_exp_f32_e32 v122, v122\n\t" \
  "v_fma_f32 v123, v123, s59, v240\n\t" \
  "v_cvt_pk_bf16_f32 v124, 0, v124\n\t" \
  "v_mul_f32_e32 v125, s58, v125\n\t" \
  "v_cvt_pk_bf16_f32 v126, 0, v126\n\t" \
  "v_exp_f32_e32 v123, v123\n\t" \
  "v_fma_f32 v124, v124, s59, v240\n\t" \
  "v_cvt_pk_bf16_f32 v125, 0, v125\n\t" \
  "v_mul_f32_e32 v126, s58, v126\n\t" \
  "v_cvt_pk_bf16_f32 v127, 0, v127\n\t" \
  "v_mfma_f32_32x32x16_bf16 v[192:207], v[242:245], v[188:191], v[192:207]\n\t" \
  "s_cmp_lg_u32 s69, 0\n\t" \
  "s_cbranch_scc1 LB_ROD0%=\n\t" \
  "LB_ROBD0%=:\n\t" \
  "s_nop 0\n\t" \
  "s_waitcnt lgkmcnt(7)\n\t" \
  "v_mfma_f32_32x32x16_bf16 v[48:63], v[224:227], v[176:179], v[48:63]\n\t" \
  "ds_read_b128 v[224:227], v250 offset:24576\n\t" \
  "v_cvt_pk_bf16_f32 v181, v122, v123\n\t" \
  "v_exp_f32_e32 v124, v124\n\t" \
  "v_fma_f32 v125, v125, s59, v240\n\t" \
  "v_cvt_pk_bf16_f32 v126, 0, v126\n\t" \
  "v_mul_f32_e32 v127, s58, v127\n\t" \
  "v_cvt_pk_bf16_f32 v128, 0, v128\n\t" \
  "v_exp_f32_e32 v125, v125\n\t" \
  "v_fma_f32 v126, v126, s59, v240\n\t" \
  "v_cvt_pk_bf16_f32 v127, 0, v127\n\t" \
  "v_mul_f32_e32 v128, s58, v128\n\t" \
  "v_cvt_pk_bf16_f32 v129, 0, v129\n\t" \
  "s_waitcnt lgkmcnt(7)\n\t" \
  "v_mfma_f32_32x32x16_bf16 v[64:79], v[228:231], v[176:179], v[64:79]\n\t" \
  "ds_read_b128 v[228:231], v250 offset:25600\n\t" \
  "v_cvt_pk_bf16_f32 v182, v124, v125\n\t" \
  "v_exp_f32_e32 v126, v126\n\t" \
  "v_fma_f32 v127, v127, s59, v240\n\t" \
  "v_cvt_pk_bf16_f32 v128, 0, v128\n\t" \
  "v_mul_f32_e32 v129, s58, v129\n\t" \
  "v_cvt_pk_bf16_f32 v130, 0, v130\n\t" \
  "v_exp_f32_e32 v127, v127\n\t" \
  "v_fma_f32 v128, v128, s59, v240\n\t" \
  "v_cvt_pk_bf16_f32 v129, 0, v129\n\t" \
  "v_mul_f32_e32 v130, s58, v130\n\t" \
  "v_cvt_pk_bf16_f32 v131, 0, v131\n\t" \
  "s_waitcnt lgkmcnt(7)\n\t" \
  "v_mfma_f32_32x32x16_bf16 v[80:95], v[232:235], v[176:179], v[80:95]\n\t" \
  "ds_read_b128 v[232:235], v250 offset:26624\n\t" \
  "v_cvt_pk_bf16_f32 v183, v126, v127\n\t" \
  "v_exp_f32_e32 v128, v128\n\t" \
  "v_fma_f32 v129, v129, s59, v240\n\t" \
  "v_cvt_pk_bf16_f32 v130, 0, v130\n\t" \
  "v_mul_f32_e32 v131, s58, v131\n\t" \
  "v_cvt_pk_bf16_f32 v132, 0, v132\n\t" \
  "v_exp_f32_e32 v129, v129\n\t" \
  "v_fma_f32 v130, v130, s59, v240\n\t" \
  "v_cvt_pk_bf16_f32 v131, 0, v131\n\t" \
  "v_mul_f32_e32 v132, s58, v132\n\t" \
  "v_cvt_pk_bf16_f32 v133, 0, v133\n\t" \
  "s_waitcnt lgkmcnt(7)\n\t" \
  "v_mfma_f32_32x32x16_bf16 v[96:111], v[236:239], v[176:179], v[96:111]\n\t" \
  "ds_read_b128 v[236:239], v250 offset:27648\n\t" \
  "v_mfma_f32_32x32x16_bf16 v[192:207], v[242:245], v[176:179], v[192:207]\n\t" \
  "v_cvt_pk_bf16_f32 v184, v128, v129\n\t" \
  "v_exp_f32_e32 v130, v130\n\t" \
  "v_fma_f32 v131, v131, s59, v240\n\t" \
  "v_cvt_pk_bf16_f32 v132, 0, v132\n\t" \
  "v_mul_f32_e32 v133, s58, v133\n\t" \
  "v_cvt_pk_bf16_f32 v134, 0, v134\n\t" \
  "v_exp_f32_e32 v131, v131\n\t" \
  "v_fma_f32 v132, v132, s59, v240\n\t" \
  "v_cvt_pk_bf16_f32 v133, 0, v133\n\t" \
  "v_mul_f32_e32 v134, s58, v134\n\t" \
  "v_cvt_pk_bf16_f32 v135, 0, v135\n\t" \
  "s_waitcnt lgkmcnt(7)\n\t" \
  "v_mfma_f32_32x32x16_bf16 v[48:63], v[208:211], v[180:183], v[48:63]\n\t" \
  "ds_read_b128 v[208:211], v250 offset:28672\n\t" \
  "v_cvt_pk_bf16_f32 v185, v130, v131\n\t" \
  "v_exp_f32_e32 v132, v132\n\t" \
  "v_fma_f32 v133, v133, s59, v240\n\t" \
  "v_cvt_pk_bf16_f32 v134, 0, v134\n\t" \
  "v_mul_f32_e32 v135, s58, v135\n\t" \
  "v_cvt_pk_bf16_f32 v136, 0, v136\n\t" \
  "v_exp_f32_e32 v133, v133\n\t" \
  "v_fma_f32 v134, v134, s59, v240\n\t" \
  "v_cvt_pk_bf16_f32 v135, 0, v135\n\t" \
  "v_mul_f32_e32 v136, s58, v136\n\t" \
  "v_cvt_pk_bf16_f32 v137, 0, v137\n\t" \
  "s_waitcnt lgkmcnt(7)\n\t" \
  "v_mfma_f32_32x32x16_bf16 v[64:79], v[212:215], v[180:183], v[64:79]\n\t" \
  "ds_read_b128 v[212:215], v250 offset:29696\n\t" \
  "v_cvt_pk_bf16_f32 v186, v132, v133\n\t" \
  "v_exp_f32_e32 v134, v134\n\t" \
  "v_fma_f32 v135, v135, s59, v240\n\t" \
  "v_cvt_pk_bf16_f32 v136, 0, v136\n\t" \
  "v_mul_f32_e32 v137, s58, v137\n\t" \
  "v_cvt_pk_bf16_f32 v138, 0, v138\n\t" \
  "v_exp_f32_e32 v135, v135\n\t" \
  "v_fma_f32 v136, v136, s59, v240\n\t" \
  "v_cvt_pk_bf16_f32 v137, 0, v137\n\t" \
  "v_mul_f32_e32 v138, s58, v138\n\t" \
  "v_cvt_pk_bf16_f32 v139, 0, v139\n\t" \
  "s_waitcnt lgkmcnt(7)\n\t" \
  "v_mfma_f32_32x32x16_bf16 v[80:95], v[216:219], v[180:183], v[80:95]\n\t" \
  "ds_read_b128 v[216:219], v250 offset:30720\n\t" \
  "v_cvt_pk_bf16_f32 v187, v134, v135\n\t" \
  "v_exp_f32_e32 v136, v136\n\t" \
  "v_fma_f32 v137, v137, s59, v240\n\t" \
  "v_cvt_pk_bf16_f32 v138, 0, v138\n\t" \
  "v_mul_f32_e32 v139, s58, v139\n\t" \
  "v_cvt_pk_bf16_f32 v140, 0, v140\n\t" \
  "v_exp_f32_e32 v137, v137\n\t" \
  "v_fma_f32 v138, v138, s59, v240\n\t" \
  "v_cvt_pk_bf16_f32 v139, 0, v139\n\t" \
  "v_mul_f32_e32 v140, s58, v140\n\t" \
  "v_cvt_pk_bf16_f32 v141, 0, v141\n\t" \
  "s_waitcnt lgkmcnt(7)\n\t" \
  "v_mfma_f32_32x32x16_bf16 v[96:111], v[220:223], v[180:183], v[96:111]\n\t" \
  "ds_read_b128 v[220:223], v250 offset:31744\n\t" \
  "v_mfma_f32_32x32x16_bf16 v[192:207], v[242:245], v[180:183], v[192:207]\n\t" \
  "v_cvt_pk_bf16_f32 v188, v136, v137\n\t" \
  "v_exp_f32_e32 v138, v138\n\t" \
  "v_fma_f32 v139, v139, s59, v240\n\t" \
  "v_cvt_pk_bf16_f32 v140, 0, v140\n\t" \
  "v_mul_f32_e32 v141, s58, v141\n\t" \
  "v_cvt_pk_bf16_f32 v142, 0, v142\n\t" \
  "v_exp_f32_e32 v139, v139\n\t" \
  "v_fma_f32 v140, v140, s59, v240\n\t" \
  "v_cvt_pk_bf16_f32 v141, 0, v141\n\t" \
  "v_mul_f32_e32 v142, s58, v142\n\t" \
  "v_cvt_pk_bf16_f32 v143, 0, v143\n\t" \
  "s_waitcnt lgkmcnt(7)\n\t" \
  "v_mfma_f32_32x32x16_bf16 v[48:63], v[224:227], v[184:187], v[48:63]\n\t" \
  "v_cvt_pk_bf16_f32 v189, v138, v139\n\t" \
  "v_exp_f32_e32 v140, v140\n\t" \
  "v_fma_f32 v141, v141, s59, v240\n\t" \
  "v_cvt_pk_bf16_f32 v142, 0, v142\n\t" \
  "v_mul_f32_e32 v143, s58, v143\n\t" \
  "v_exp_f32_e32 v141, v141\n\t" \
  "v_fma_f32 v142, v142, s59, v240\n\t" \
  "v_cvt_pk_bf16_f32 v143, 0, v143\n\t" \
  "s_waitcnt lgkmcnt(6)\n\t" \
  "v_mfma_f32_32x32x16_bf16 v[64:79], v[228:231], v[184:187], v[64:79]\n\t" \
  "v_cvt_pk_bf16_f32 v190, v140, v141\n\t" \
  "v_exp_f32_e32 v142, v142\n\t" \
  "v_fma_f32 v143, v143, s59, v240\n\t" \
  "v_exp_f32_e32 v143, v143\n\t" \
  "s_waitcnt lgkmcnt(5)\n\t" \
  "v_mfma_f32_32x32x16_bf16 v[80:95], v[232:235], v[184:187], v[80:95]\n\t" \
  "v_cvt_pk_bf16_f32 v191, v142, v143\n\t" \
  "s_waitcnt lgkmcnt(4)\n\t" \
  "v_mfma_f32_32x32x16_bf16 v[96:111], v[236:239], v[184:187], v[96:111]\n\t" \
  "v_mfma_f32_32x32x16_bf16 v[192:207], v[242:245], v[184:187], v[192:207]\n\t" \
  "s_waitcnt lgkmcnt(3)\n\t" \
  "v_mfma_f32_32x32x16_bf16 v[48:63], v[208:211], v[188:191], v[48:63]\n\t" \
  "s_waitcnt lgkmcnt(2)\n\t" \
  "v_mfma_f32_32x32x16_bf16 v[64:79], v[212:215], v[188:191], v[64:79]\n\t" \
  "s_waitcnt lgkmcnt(1)\n\t" \
  "v_mfma_f32_32x32x16_bf16 v[80:95], v[216:219], v[188:191], v[80:95]\n\t" \
  "s_waitcnt lgkmcnt(0)\n\t" \
  "v_mfma_f32_32x32x16_bf16 v[96:111], v[220:223], v[188:191], v[96:111]\n\t" \
  "v_mfma_f32_32x32x16_bf16 v[192:207], v[242:245], v[188:191], v[192:207]\n\t" \
  "s_branch L_EPI%=\n\t" \
  "LB_DRAIN1%=:\n\t" \
  "s_waitcnt lgkmcnt(0)\n\t" \
  "ds_read_b128 v[224:227], v250 offset:0\n\t" \
  "ds_read_b128 v[228:231], v250 offset:1024\n\t" \
  "ds_read_b128 v[232:235], v250 offset:2048\n\t" \
  "ds_read_b128 v[236:239], v250 offset:3072\n\t" \
  "s_add_u32 m0, s80, 0\n\t" \
  "s_add_u32 s64, s64, 0x8000\n\t" \
  "s_addc_u32 s65, s65, 0\n\t" \
  "global_load_lds_dwordx4 %[lin], s[64:65] offset:0 sc1\n\t" \
  "global_load_lds_dwordx4 %[lin], s[64:65] offset:1024 sc1\n\t" \
  "global_load_lds_dwordx4 %[lin], s[64:65] offset:2048 sc1\n\t" \
  "global_load_lds_dwordx4 %[lin], s[64:65] offset:3072 sc1\n\t" \
  "s_add_u32 m0, s80, 4096\n\t" \
  "s_nop 0\n\t" \
  "global_load_lds_dwordx4 v252, s[64:65] offset:0 sc1\n\t" \
  "global_load_lds_dwordx4 v252, s[64:65] offset:1024 sc1\n\t" \
  "global_load_lds_dwordx4 v252, s[64:65] offset:2048 sc1\n\t" \
  "global_load_lds_dwordx4 v252, s[64:65] offset:3072 sc1\n\t" \
  "s_add_u32 m0, s80, 65536\n\t" \
  "s_add_u32 s66, s66, 0x8000\n\t" \
  "s_addc_u32 s67, s67, 0\n\t" \
  "global_load_lds_dwordx4 %[lin], s[66:67] offset:0 sc1\n\t" \
  "global_load_lds_dwordx4 %[lin], s[66:67] offset:1024 sc1\n\t" \
  "global_load_lds_dwordx4 %[lin], s[66:67] offset:2048 sc1\n\t" \
  "global_load_lds_dwordx4 %[lin], s[66:67] offset:3072 sc1\n\t" \
  "s_add_u32 m0, s80, 69632\n\t" \
  "s_nop 0\n\t" \
  "global_load_lds_dwordx4 v252, s[66:67] offset:0 sc1\n\t" \
  "global_load_lds_dwordx4 v252, s[66:67] offset:1024 sc1\n\t" \
  "global_load_lds_dwordx4 v252, s[66:67] offset:2048 sc1\n\t" \
  "global_load_lds_dwordx4 v252, s[66:67] offset:3072 sc1\n\t" \
  "s_xor_b32 s80, s80, 0x8000\n\t" \
  "v_mfma_f32_32x32x16_bf16 v[48:63], v[208:211], v[188:191], v[48:63]\n\t" \
  "ds_read_b128 v[208:211], v250 offset:4096\n\t" \
  "v_cvt_pk_bf16_f32 v176, v144, v145\n\t" \
  "v_cvt_pk_bf16_f32 v177, v146, v147\n\t" \
  "v_exp_f32_e32 v148, v148\n\t" \
  "v_fma_f32 v149, v149, s59, v240\n\t" \
  "v_cvt_pk_bf16_f32 v150, 0, v150\n\t" \
  "v_mul_f32_e32 v151, s58, v151\n\t" \
  "v_cvt_pk_bf16_f32 v152, 0, v152\n\t" \
  "v_exp_f32_e32 v149, v149\n\t" \
  "v_fma_f32 v150, v150, s59, v240\n\t" \
  "v_cvt_pk_bf16_f32 v151, 0, v151\n\t" \
  "v_mul_f32_e32 v152, s58, v152\n\t" \
  "v_cvt_pk_bf16_f32 v153, 0, v153\n\t" \
  "v_mfma_f32_32x32x16_bf16 v[64:79], v[212:215], v[188:191], v[64:79]\n\t" \
  "ds_read_b128 v[212:215], v250 offset:5120\n\t" \
  "v_cvt_pk_bf16_f32 v178, v148, v149\n\t" \
  "v_exp_f32_e32 v150, v150\n\t" \
  "v_fma_f32 v151, v151, s59, v240\n\t" \
  "v_cvt_pk_bf16_f32 v152, 0, v152\n\t" \
  "v_mul_f32_e32 v153, s58, v153\n\t" \
  "v_cvt_pk_bf16_f32 v154, 0, v154\n\t" \
  "v_exp_f32_e32 v151, v151\n\t" \
  "v_fma_f32 v152, v152, s59, v240\n\t" \
  "v_cvt_pk_bf16_f32 v153, 0, v153\n\t" \
  "v_mul_f32_e32 v154, s58, v154\n\t" \
  "v_cvt_pk_bf16_f32 v155, 0, v155\n\t" \
  "v_mfma_f32_32x32x16_bf16 v[80:95], v[216:219], v[188:191], v[80:95]\n\t" \
  "ds_read_b128 v[216:219], v250 offset:6144\n\t" \
  "v_cvt_pk_bf16_f32 v179, v150, v151\n\t" \
  "v_exp_f32_e32 v152, v152\n\t" \
  "v_fma_f32 v153, v153, s59, v240\n\t" \
  "v_cvt_pk_bf16_f32 v154, 0, v154\n\t" \
  "v_mul_f32_e32 v155, s58, v155\n\t" \
  "v_cvt_pk_bf16_f32 v156, 0, v156\n\t" \
  "v_exp_f32_e32 v153, v153\n\t" \
  "v_fma_f32 v154, v154, s59, v240\n\t" \
  "v_cvt_pk_bf16_f32 v155, 0, v155\n\t" \
  "v_mul_f32_e32 v156, s58, v156\n\t" \
  "v_cvt_pk_bf16_f32 v157, 0, v157\n\t" \
  "v_mfma_f32_32x32x16_bf16 v[96:111], v[220:223], v[188:191], v[96:111]\n\t" \
  "ds_read_b128 v[220:223], v250 offset:7168\n\t" \
  "v_cvt_pk_bf16_f32 v180, v152, v153\n\t" \
  "v_exp_f32_e32 v154, v154\n\t" \
  "v_fma_f32 v155, v155, s59, v240\n\t" \
  "v_cvt_pk_bf16_f32 v156, 0, v156\n\t" \
  "v_mul_f32_e32 v157, s58, v157\n\t" \
  "v_cvt_pk_bf16_f32 v158, 0, v158\n\t" \
  "v_exp_f32_e32 v155, v155\n\t" \
  "v_fma_f32 v156, v156, s59, v240\n\t" \
  "v_cvt_pk_bf16_f32 v157, 0, v157\n\t" \
  "v_mul_f32_e32 v158, s58, v158\n\t" \
  "v_cvt_pk_bf16_f32 v159, 0, v159\n\t" \
  "v_mfma_f32_32x32x16_bf16 v[192:207], v[242:245], v[188:191], v[192:207]\n\t" \
  "s_cmp_lg_u32 s69, 0\n\t" \
  "s_cbranch_scc1 LB_ROD1%=\n\t" \
  "LB_ROBD1%=:\n\t" \
  "s_nop 0\n\t" \
  "s_waitcnt lgkmcnt(7)\n\t" \
  "v_mfma_f32_32x32x16_bf16 v[48:63], v[224:227], v[176:179], v[48:63]\n\t" \
  "ds_read_b128 v[224:227], v250 offset:8192\n\t" \
  "v_cvt_pk_bf16_f32 v181, v154, v155\n\t" \
  "v_exp_f32_e32 v156, v156\n\t" \
  "v_fma_f32 v157, v157, s59, v240\n\t" \
  "v_cvt_pk_bf16_f32 v158, 0, v158\n\t" \
  "v_mul_f32_e32 v159, s58, v159\n\t" \
  "v_cvt_pk_bf16_f32 v160, 0, v160\n\t" \
  "v_exp_f32_e32 v157, v157\n\t" \
  "v_fma_f32 v158, v158, s59, v240\n\t" \
  "v_cvt_pk_bf16_f32 v159, 0, v159\n\t" \
  "v_mul_f32_e32 v160, s58, v160\n\t" \
  "v_cvt_pk_bf16_f32 v161, 0, v161\n\t" \
  "s_waitcnt lgkmcnt(7)\n\t" \
  "v_mfma_f32_32x32x16_bf16 v[64:79], v[228:231], v[176:179], v[64:79]\n\t" \
  "ds_read_b128 v[228:231], v250 offset:9216\n\t" \
  "v_cvt_pk_bf16_f32 v182, v156, v157\n\t" \
  "v_exp_f32_e32 v158, v158\n\t" \
  "v_fma_f32 v159, v159, s59, v240\n\t" \
  "v_cvt_pk_bf16_f32 v160, 0, v160\n\t" \
  "v_mul_f32_e32 v161, s58, v161\n\t" \
  "v_cvt_pk_bf16_f32 v162, 0, v162\n\t" \
  "v_exp_f32_e32 v159, v159\n\t" \
  "v_fma_f32 v160, v160, s59, v240\n\t" \
  "v_cvt_pk_bf16_f32 v161, 0, v161\n\t" \
  "v_mul_f32_e32 v162, s58, v162\n\t" \
  "v_cvt_pk_bf16_f32 v163, 0, v163\n\t" \
  "s_waitcnt lgkmcnt(7)\n\t" \
  "v_mfma_f32_32x32x16_bf16 v[80:95], v[232:235], v[176:179], v[80:95]\n\t" \
  "ds_read_b128 v[232:235], v250 offset:10240\n\t" \
  "v_cvt_pk_bf16_f32 v183, v158, v159\n\t" \
  "v_exp_f32_e32 v160, v160\n\t" \
  "v_fma_f32 v161, v161, s59, v240\n\t" \
  "v_cvt_pk_bf16_f32 v162, 0, v162\n\t" \
  "v_mul_f32_e32 v163, s58, v163\n\t" \
  "v_cvt_pk_bf16_f32 v164, 0, v164\n\t" \
  "v_exp_f32_e32 v161, v161\n\t" \
  "v_fma_f32 v162, v162, s59, v240\n\t" \
  "v_cvt_pk_bf16_f32 v163, 0, v163\n\t" \
  "v_mul_f32_e32 v164, s58, v164\n\t" \
  "v_cvt_pk_bf16_f32 v165, 0, v165\n\t" \
  "s_waitcnt lgkmcnt(7)\n\t" \
  "v_mfma_f32_32x32x16_bf16 v[96:111], v[236:239], v[176:179], v[96:111]\n\t" \
  "ds_read_b128 v[236:239], v250 offset:11264\n\t" \
  "v_mfma_f32_32x32x16_bf16 v[192:207], v[242:245], v[176:179], v[192:207]\n\t" \
  "v_cvt_pk_bf16_f32 v184, v160, v161\n\t" \
  "v_exp_f32_e32 v162, v162\n\t" \
  "v_fma_f32 v163, v163, s59, v240\n\t" \
  "v_cvt_pk_bf16_f32 v164, 0, v164\n\t" \
  "v_mul_f32_e32 v165, s58, v165\n\t" \
  "v_cvt_pk_bf16_f32 v166, 0, v166\n\t" \
  "v_exp_f32_e32 v163, v163\n\t" \
  "v_fma_f32 v164, v164, s59, v240\n\t" \
  "v_cvt_pk_bf16_f32 v165, 0, v165\n\t" \
  "v_mul_f32_e32 v166, s58, v166\n\t" \
  "v_cvt_pk_bf16_f32 v167, 0, v167\n\t" \
  "s_waitcnt lgkmcnt(7)\n\t" \
  "v_mfma_f32_32x32x16_bf16 v[48:63], v[208:211], v[180:183], v[48:63]\n\t" \
  "ds_read_b128 v[208:211], v250 offset:12288\n\t" \
  "v_cvt_pk_bf16_f32 v185, v162, v163\n\t" \
  "v_exp_f32_e32 v164, v164\n\t" \
  "v_fma_f32 v165, v165, s59, v240\n\t" \
  "v_cvt_pk_bf16_f32 v166, 0, v166\n\t" \
  "v_mul_f32_e32 v167, s58, v167\n\t" \
  "v_cvt_pk_bf16_f32 v168, 0, v168\n\t" \
  "v_exp_f32_e32 v165, v165\n\t" \
  "v_fma_f32 v166, v166, s59, v240\n\t" \
  "v_cvt_pk_bf16_f32 v167, 0, v167\n\t" \
  "v_mul_f32_e32 v168, s58, v168\n\t" \
  "v_cvt_pk_bf16_f32 v169, 0, v169\n\t" \
  "s_waitcnt lgkmcnt(7)\n\t" \
  "v_mfma_f32_32x32x16_bf16 v[64:79], v[212:215], v[180:183], v[64:79]\n\t" \
  "ds_read_b128 v[212:215], v250 offset:13312\n\t" \
  "v_cvt_pk_bf16_f32 v186, v164, v165\n\t" \
  "v_exp_f32_e32 v166, v166\n\t" \
  "v_fma_f32 v167, v167, s59, v240\n\t" \
  "v_cvt_pk_bf16_f32 v168, 0, v168\n\t" \
  "v_mul_f32_e32 v169, s58, v169\n\t" \
  "v_cvt_pk_bf16_f32 v170, 0, v170\n\t" \
  "v_exp_f32_e32 v167, v167\n\t" \
  "v_fma_f32 v168, v168, s59, v240\n\t" \
  "v_cvt_pk_bf16_f32 v169, 0, v169\n\t" \
  "v_mul_f32_e32 v170, s58, v170\n\t" \
  "v_cvt_pk_bf16_f32 v171, 0, v171\n\t" \
  "s_waitcnt lgkmcnt(7)\n\t" \
  "v_mfma_f32_32x32x16_bf16 v[80:95], v[216:219], v[180:183], v[80:95]\n\t" \
  "ds_read_b128 v[216:219], v250 offset:14336\n\t" \
  "v_cvt_pk_bf16_f32 v187, v166, v167\n\t" \
  "v_exp_f32_e32 v168, v168\n\t" \
  "v_fma_f32 v169, v169, s59, v240\n\t" \
  "v_cvt_pk_bf16_f32 v170, 0, v170\n\t" \
  "v_mul_f32_e32 v171, s58, v171\n\t" \
  "v_cvt_pk_bf16_f32 v172, 0, v172\n\t" \
  "v_exp_f32_e32 v169, v169\n\t" \
  "v_fma_f32 v170, v170, s59, v240\n\t" \
  "v_cvt_pk_bf16_f32 v171, 0, v171\n\t" \
  "v_mul_f32_e32 v172, s58, v172\n\t" \
  "v_cvt_pk_bf16_f32 v173, 0, v173\n\t" \
  "s_waitcnt lgkmcnt(7)\n\t" \
  "v_mfma_f32_32x32x16_bf16 v[96:111], v[220:223], v[180:183], v[96:111]\n\t" \
  "ds_read_b128 v[220:223], v250 offset:15360\n\t" \
  "v_mfma_f32_32x32x16_bf16 v[192:207], v[242:245], v[180:183], v[192:207]\n\t" \
  "v_cvt_pk_bf16_f32 v188, v168, v169\n\t" \
  "v_exp_f32_e32 v170, v170\n\t" \
  "v_fma_f32 v171, v171, s59, v240\n\t" \
  "v_cvt_pk_bf16_f32 v172, 0, v172\n\t" \
  "v_mul_f32_e32 v173, s58, v173\n\t" \
  "v_cvt_pk_bf16_f32 v174, 0, v174\n\t" \
  "v_exp_f32_e32 v171, v171\n\t" \
  "v_fma_f32 v172, v172, s59, v240\n\t" \
  "v_cvt_pk_bf16_f32 v173, 0, v173\n\t" \
  "v_mul_f32_e32 v174, s58, v174\n\t" \
  "v_cvt_pk_bf16_f32 v175, 0, v175\n\t" \
  "s_waitcnt lgkmcnt(7)\n\t" \
  "v_mfma_f32_32x32x16_bf16 v[48:63], v[224:227], v[184:187], v[48:63]\n\t" \
  "v_cvt_pk_bf16_f32 v189, v170, v171\n\t" \
  "v_exp_f32_e32 v172, v172\n\t" \
  "v_fma_f32 v173, v173, s59, v240\n\t" \
  "v_cvt_pk_bf16_f32 v174, 0, v174\n\t" \
  "v_mul_f32_e32 v175, s58, v175\n\t" \
  "v_exp_f32_e32 v173, v173\n\t" \
  "v_fma_f32 v174, v174, s59, v240\n\t" \
  "v_cvt_pk_bf16_f32 v175, 0, v175\n\t" \
  "s_waitcnt lgkmcnt(6)\n\t" \
  "v_mfma_f32_32x32x16_bf16 v[64:79], v[228:231], v[184:187], v[64:79]\n\t" \
  "v_cvt_pk_bf16_f32 v190, v172, v173\n\t" \
  "v_exp_f32_e32 v174, v174\n\t" \
  "v_fma_f32 v175, v175, s59, v240\n\t" \
  "v_exp_f32_e32 v175, v175\n\t" \
  "s_waitcnt lgkmcnt(5)\n\t" \
  "v_mfma_f32_32x32x16_bf16 v[80:95], v[232:235], v[184:187], v[80:95]\n\t" \
  "v_cvt_pk_bf16_f32 v191, v174, v175\n\t" \
  "s_waitcnt lgkmcnt(4)\n\t" \
  "v_mfma_f32_32x32x16_bf16 v[96:111], v[236:239], v[184:187], v[96:111]\n\t" \
  "v_mfma_f32_32x32x16_bf16 v[192:207], v[242:245], v[184:187], v[192:207]\n\t" \
  "s_waitcnt lgkmcnt(3)\n\t" \
  "v_mfma_f32_32x32x16_bf16 v[48:63], v[208:211], v[188:191], v[48:63]\n\t" \
  "s_waitcnt lgkmcnt(2)\n\t" \
  "v_mfma_f32_32x32x16_bf16 v[64:79], v[212:215], v[188:191], v[64:79]\n\t" \
  "s_waitcnt lgkmcnt(1)\n\t" \
  "v_mfma_f32_32x32x16_bf16 v[80:95], v[216:219], v[188:191], v[80:95]\n\t" \
  "s_waitcnt lgkmcnt(0)\n\t" \
  "v_mfma_f32_32x32x16_bf16 v[96:111], v[220:223], v[188:191], v[96:111]\n\t" \
  "v_mfma_f32_32x32x16_bf16 v[192:207], v[242:245], v[188:191], v[192:207]\n\t" \
  "s_branch L_EPI%=\n\t" \
  "L_EPI%=:\n\t" \
  "s_cmp_ge_u32 s71, s51\n\t" \
  "s_cbranch_scc1 L_EPI2%=\n\t" \
  "s_waitcnt vmcnt(0)\n\t" \
  "s_barrier\n\t" \
  "s_add_u32 s71, s71, 1\n\t" \
  "s_cmp_ge_u32 s71, s51\n\t" \
  "s_cbranch_scc1 L_EPI2%=\n\t" \
  "s_add_u32 m0, s80, 0\n\t" \
  "s_add_u32 s64, s64, 0x8000\n\t" \
  "s_addc_u32 s65, s65, 0\n\t" \
  "global_load_lds_dwordx4 %[lin], s[64:65] offset:0 sc1\n\t" \
  "global_load_lds_dwordx4 %[lin], s[64:65] offset:1024 sc1\n\t" \
  "global_load_lds_dwordx4 %[lin], s[64:65] offset:2048 sc1\n\t" \
  "global_load_lds_dwordx4 %[lin], s[64:65] offset:3072 sc1\n\t" \
  "s_add_u32 m0, s80, 4096\n\t" \
  "s_nop 0\n\t" \
  "global_load_lds_dwordx4 v252, s[64:65] offset:0 sc1\n\t" \
  "global_load_lds_dwordx4 v252, s[64:65] offset:1024 sc1\n\t" \
  "global_load_lds_dwordx4 v252, s[64:65] offset:2048 sc1\n\t" \
  "global_load_lds_dwordx4 v252, s[64:65] offset:3072 sc1\n\t" \
  "s_add_u32 m0, s80, 65536\n\t" \
  "s_add_u32 s66, s66, 0x8000\n\t" \
  "s_addc_u32 s67, s67, 0\n\t" \
  "global_load_lds_dwordx4 %[lin], s[66:67] offset:0 sc1\n\t" \
  "global_load_lds_dwordx4 %[lin], s[66:67] offset:1024 sc1\n\t" \
  "global_load_lds_dwordx4 %[lin], s[66:67] offset:2048 sc1\n\t" \
  "global_load_lds_dwordx4 %[lin], s[66:67] offset:3072 sc1\n\t" \
  "s_add_u32 m0, s80, 69632\n\t" \
  "s_nop 0\n\t" \
  "global_load_lds_dwordx4 v252, s[66:67] offset:0 sc1\n\t" \
  "global_load_lds_dwordx4 v252, s[66:67] offset:1024 sc1\n\t" \
  "global_load_lds_dwordx4 v252, s[66:67] offset:2048 sc1\n\t" \
  "global_load_lds_dwordx4 v252, s[66:67] offset:3072 sc1\n\t" \
  "s_xor_b32 s80, s80, 0x8000\n\t" \
  "L_EPI2%=:\n\t" \
  "s_nop 11\n\t" \
  "v_rcp_f32_e32 v241, v192\n\t" \
  "v_readlane_b32 s72, %[prm], 12\n\t" \
  "v_readlane_b32 s73, %[prm], 13\n\t" \
  "v_mul_f32_e32 v48, v241, v48\n\t" \
  "v_mul_f32_e32 v49, v241, v49\n\t" \
  "v_mul_f32_e32 v50, v241, v50\n\t" \
  "v_mul_f32_e32 v51, v241, v51\n\t" \
  "v_mul_f32_e32 v52, v241, v52\n\t" \
  "v_mul_f32_e32 v53, v241, v53\n\t" \
  "v_mul_f32_e32 v54, v241, v54\n\t" \
  "v_mul_f32_e32 v55, v241, v55\n\t" \
  "v_mul_f32_e32 v56, v241, v56\n\t" \
  "v_mul_f32_e32 v57, v241, v57\n\t" \
  "v_mul_f32_e32 v58, v241, v58\n\t" \
  "v_mul_f32_e32 v59, v241, v59\n\t" \
  "v_mul_f32_e32 v60, v241, v60\n\t" \
  "v_mul_f32_e32 v61, v241, v61\n\t" \
  "v_mul_f32_e32 v62, v241, v62\n\t" \
  "v_mul_f32_e32 v63, v241, v63\n\t" \
  "v_cvt_pk_bf16_f32 v208, v48, v49\n\t" \
  "v_cvt_pk_bf16_f32 v209, v50, v51\n\t" \
  "v_cvt_pk_bf16_f32 v210, v52, v53\n\t" \
  "v_cvt_pk_bf16_f32 v211, v54, v55\n\t" \
  "global_store_dwordx4 %[lin], v[208:211], s[72:73] offset:0 sc1\n\t" \
  "v_cvt_pk_bf16_f32 v212, v56, v57\n\t" \
  "v_cvt_pk_bf16_f32 v213, v58, v59\n\t" \
  "v_cvt_pk_bf16_f32 v214, v60, v61\n\t" \
  "v_cvt_pk_bf16_f32 v215, v62, v63\n\t" \
  "global_store_dwordx4 %[lin], v[212:215], s[72:73] offset:1024 sc1\n\t" \
  "v_mul_f32_e32 v64, v241, v64\n\t" \
  "v_mul_f32_e32 v65, v241, v65\n\t" \
  "v_mul_f32_e32 v66, v241, v66\n\t" \
  "v_mul_f32_e32 v67, v241, v67\n\t" \
  "v_mul_f32_e32 v68, v241, v68\n\t" \
  "v_mul_f32_e32 v69, v241, v69\n\t" \
  "v_mul_f32_e32 v70, v241, v70\n\t" \
  "v_mul_f32_e32 v71, v241, v71\n\t" \
  "v_mul_f32_e32 v72, v241, v72\n\t" \
  "v_mul_f32_e32 v73, v241, v73\n\t" \
  "v_mul_f32_e32 v74, v241, v74\n\t" \
  "v_mul_f32_e32 v75, v241, v75\n\t" \
  "v_mul_f32_e32 v76, v241, v76\n\t" \
  "v_mul_f32_e32 v77, v241, v77\n\t" \
  "v_mul_f32_e32 v78, v241, v78\n\t" \
  "v_mul_f32_e32 v79, v241, v79\n\t" \
  "v_cvt_pk_bf16_f32 v216, v64, v65\n\t" \
  "v_cvt_pk_bf16_f32 v217, v66, v67\n\t" \
  "v_cvt_pk_bf16_f32 v218, v68, v69\n\t" \
  "v_cvt_pk_bf16_f32 v219, v70, v71\n\t" \
  "global_store_dwordx4 %[lin], v[216:219], s[72:73] offset:2048 sc1\n\t" \
  "v_cvt_pk_bf16_f32 v220, v72, v73\n\t" \
  "v_cvt_pk_bf16_f32 v221, v74, v75\n\t" \
  "v_cvt_pk_bf16_f32 v222, v76, v77\n\t" \
  "v_cvt_pk_bf16_f32 v223, v78, v79\n\t" \
  "global_store_dwordx4 %[lin], v[220:223], s[72:73] offset:3072 sc1\n\t" \
  "v_mul_f32_e32 v80, v241, v80\n\t" \
  "v_mul_f32_e32 v81, v241, v81\n\t" \
  "v_mul_f32_e32 v82, v241, v82\n\t" \
  "v_mul_f32_e32 v83, v241, v83\n\t" \
  "v_mul_f32_e32 v84, v241, v84\n\t" \
  "v_mul_f32_e32 v85, v241, v85\n\t" \
  "v_mul_f32_e32 v86, v241, v86\n\t" \
  "v_mul_f32_e32 v87, v241, v87\n\t" \
  "v_mul_f32_e32 v88, v241, v88\n\t" \
  "v_mul_f32_e32 v89, v241, v89\n\t" \
  "v_mul_f32_e32 v90, v241, v90\n\t" \
  "v_mul_f32_e32 v91, v241, v91\n\t" \
  "v_mul_f32_e32 v92, v241, v92\n\t" \
  "v_mul_f32_e32 v93, v241, v93\n\t" \
  "v_mul_f32_e32 v94, v241, v94\n\t" \
  "v_mul_f32_e32 v95, v241, v95\n\t" \
  "s_add_u32 s72, s72, 4096\n\t" \
  "s_addc_u32 s73, s73, 0\n\t" \
  "v_cvt_pk_bf16_f32 v224, v80, v81\n\t" \
  "v_cvt_pk_bf16_f32 v225, v82, v83\n\t" \
  "v_cvt_pk_bf16_f32 v226, v84, v85\n\t" \
  "v_cvt_pk_bf16_f32 v227, v86, v87\n\t" \
  "global_store_dwordx4 %[lin], v[224:227], s[72:73] offset:0 sc1\n\t" \
  "v_cvt_pk_bf16_f32 v228, v88, v89\n\t" \
  "v_cvt_pk_bf16_f32 v229, v90, v91\n\t" \
  "v_cvt_pk_bf16_f32 v230, v92, v93\n\t" \
  "v_cvt_pk_bf16_f32 v231, v94, v95\n\t" \
  "global_store_dwordx4 %[lin], v[228:231], s[72:73] offset:1024 sc1\n\t" \
  "v_mul_f32_e32 v96, v241, v96\n\t" \
  "v_mul_f32_e32 v97, v241, v97\n\t" \
  "v_mul_f32_e32 v98, v241, v98\n\t" \
  "v_mul_f32_e32 v99, v241, v99\n\t" \
  "v_mul_f32_e32 v100, v241, v100\n\t" \
  "v_mul_f32_e32 v101, v241, v101\n\t" \
  "v_mul_f32_e32 v102, v241, v102\n\t" \
  "v_mul_f32_e32 v103, v241, v103\n\t" \
  "v_mul_f32_e32 v104, v241, v104\n\t" \
  "v_mul_f32_e32 v105, v241, v105\n\t" \
  "v_mul_f32_e32 v106, v241, v106\n\t" \
  "v_mul_f32_e32 v107, v241, v107\n\t" \
  "v_mul_f32_e32 v108, v241, v108\n\t" \
  "v_mul_f32_e32 v109, v241, v109\n\t" \
  "v_mul_f32_e32 v110, v241, v110\n\t" \
  "v_mul_f32_e32 v111, v241, v111\n\t" \
  "v_cvt_pk_bf16_f32 v232, v96, v97\n\t" \
  "v_cvt_pk_bf16_f32 v233, v98, v99\n\t" \
  "v_cvt_pk_bf16_f32 v234, v100, v101\n\t" \
  "v_cvt_pk_bf16_f32 v235, v102, v103\n\t" \
  "global_store_dwordx4 %[lin], v[232:235], s[72:73] offset:2048 sc1\n\t" \
  "v_cvt_pk_bf16_f32 v236, v104, v105\n\t" \
  "v_cvt_pk_bf16_f32 v237, v106, v107\n\t" \
  "v_cvt_pk_bf16_f32 v238, v108, v109\n\t" \
  "v_cvt_pk_bf16_f32 v239, v110, v111\n\t" \
  "global_store_dwordx4 %[lin], v[236:239], s[72:73] offset:3072 sc1\n\t" \
  "s_waitcnt vmcnt(8)\n\t" \
  "s_mov_b32 s70, 999\n\t" \
  "s_cmp_eq_u32 s53, 0\n\t" \
  "s_cbranch_scc1 L_REST%=\n\t" \
  "v_readlane_b32 s72, %[prm], 14\n\t" \
  "v_readlane_b32 s73, %[prm], 15\n\t" \
  "v_mov_b32_e32 v176, -1\n\t" \
  "v_mov_b32_e32 v177, -1\n\t" \
  "v_mov_b32_e32 v178, -1\n\t" \
  "v_mov_b32_e32 v179, -1\n\t" \
  "s_nop 1\n\t" \
  "global_store_dwordx4 %[lin], v[176:179], s[72:73] offset:0 sc1\n\t" \
  "global_store_dwordx4 %[lin], v[176:179], s[72:73] offset:1024 sc1\n\t" \
  "global_store_dwordx4 %[lin], v[176:179], s[72:73] offset:2048 sc1\n\t" \
  "global_store_dwordx4 %[lin], v[176:179], s[72:73] offset:3072 sc1\n\t" \
  "s_add_u32 s72, s72, 4096\n\t" \
  "s_addc_u32 s73, s73, 0\n\t" \
  "global_store_dwordx4 %[lin], v[176:179], s[72:73] offset:0 sc1\n\t" \
  "global_store_dwordx4 %[lin], v[176:179], s[72:73] offset:1024 sc1\n\t" \
  "global_store_dwordx4 %[lin], v[176:179], s[72:73] offset:2048 sc1\n\t" \
  "global_store_dwordx4 %[lin], v[176:179], s[72:73] offset:3072 sc1\n\t" \
  "s_add_u32 s72, s72, 4096\n\t" \
  "s_addc_u32 s73, s73, 0\n\t" \
  "global_store_dwordx4 %[lin], v[176:179], s[72:73] offset:0 sc1\n\t" \
  "global_store_dwordx4 %[lin], v[176:179], s[72:73] offset:1024 sc1\n\t" \
  "v_readlane_b32 s70, %[prm], 20\n\t" \
  "s_nop 0\n\t" \
  "L_REST%=:\n\t" \
  "s_cmp_lt_u32 s71, s70\n\t" \
  "s_cbranch_scc1 L_NORT%=\n\t" \
  "s_mov_b32 s70, 999\n\t" \
  "v_readlane_b32 s72, %[prm], 16\n\t" \
  "v_readlane_b32 s73, %[prm], 17\n\t" \
  "v_readlane_b32 s74, %[prm], 18\n\t" \
  "v_readlane_b32 s75, %[prm], 19\n\t" \
  "v_readlane_b32 s55, %[prm], 21\n\t" \
  "v_readlane_b32 s56, %[prm], 22\n\t" \
  "v_lshrrev_b32_e32 v241, 7, %[lin]\n\t" \
  "v_and_b32_e32 v245, 0x70, %[lin]\n\t" \
  "v_mad_u32_u24 v246, v241, s55, v245\n\t" \
  "v_bfe_u32 v245, %[lin], 5, 2\n\t" \
  "v_and_b32_e32 v247, 16, %[lin]\n\t" \
  "v_lshl_add_u32 v247, v241, 5, v247\n\t" \
  "v_mad_u32_u24 v247, v245, s56, v247\n\t" \
  "s_lshl_b32 s55, s55, 3\n\t" \
  "global_load_dwordx4 v[48:51], v246, s[72:73]\n\t" \
  "s_add_u32 s72, s72, s55\n\t" \
  "s_addc_u32 s73, s73, 0\n\t" \
  "global_load_dwordx4 v[52:55], v246, s[72:73]\n\t" \
  "s_add_u32 s72, s72, s55\n\t" \
  "s_addc_u32 s73, s73, 0\n\t" \
  "global_load_dwordx4 v[56:59], v246, s[72:73]\n\t" \
  "s_add_u32 s72, s72, s55\n\t" \
  "s_addc_u32 s73, s73, 0\n\t" \
  "global_load_dwordx4 v[60:63], v246, s[72:73]\n\t" \
  "s_add_u32 s72, s72, s55\n\t" \
  "s_addc_u32 s73, s73, 0\n\t" \
  "global_load_dwordx4 v[64:67], v246, s[72:73]\n\t" \
  "s_add_u32 s72, s72, s55\n\t" \
  "s_addc_u32 s73, s73, 0\n\t" \
  "global_load_dwordx4 v[68:71], v246, s[72:73]\n\t" \
  "s_add_u32 s72, s72, s55\n\t" \
  "s_addc_u32 s73, s73, 0\n\t" \
  "global_load_dwordx4 v[72:75], v246, s[72:73]\n\t" \
  "s_add_u32 s72, s72, s55\n\t" \
  "s_addc_u32 s73, s73, 0\n\t" \
  "global_load_dwordx4 v[76:79], v246, s[72:73]\n\t" \
  "s_add_u32 s72, s72, s55\n\t" \
  "s_addc_u32 s73, s73, 0\n\t" \
  "global_load_dwordx4 v[80:83], v246, s[72:73]\n\t" \
  "s_add_u32 s72, s72, s55\n\t" \
  "s_addc_u32 s73, s73, 0\n\t" \
  "global_load_dwordx4 v[84:87], v246, s[72:73]\n\t" \
  "s_add_u32 s72, s72, s55\n\t" \
  "s_addc_u32 s73, s73, 0\n\t" \
  "s_waitcnt vmcnt(9)\n\t" \
  "global_store_dwordx4 v247, v[48:51], s[74:75] offset:0\n\t" \
  "s_waitcnt vmcnt(9)\n\t" \
  "global_store_dwordx4 v247, v[52:55], s[74:75] offset:256\n\t" \
  "s_waitcnt vmcnt(9)\n\t" \
  "global_store_dwordx4 v247, v[56:59], s[74:75] offset:512\n\t" \
  "s_waitcnt vmcnt(9)\n\t" \
  "global_store_dwordx4 v247, v[60:63], s[74:75] offset:768\n\t" \
  "s_waitcnt vmcnt(9)\n\t" \
  "global_store_dwordx4 v247, v[64:67], s[74:75] offset:1024\n\t" \
  "s_waitcnt vmcnt(9)\n\t" \
  "global_store_dwordx4 v247, v[68:71], s[74:75] offset:1280\n\t" \
  "s_waitcnt vmcnt(9)\n\t" \
  "global_store_dwordx4 v247, v[72:75], s[74:75] offset:1536\n\t" \
  "s_waitcnt vmcnt(9)\n\t" \
  "global_store_dwordx4 v247, v[76:79], s[74:75] offset:1792\n\t" \
  "s_waitcnt vmcnt(9)\n\t" \
  "global_store_dwordx4 v247, v[80:83], s[74:75] offset:2048\n\t" \
  "s_waitcnt vmcnt(9)\n\t" \
  "global_store_dwordx4 v247, v[84:87], s[74:75] offset:2304\n\t" \
  "L_NORT%=:\n\t" \
  "s_cmp_ge_u32 s71, s51\n\t" \
  "s_cbranch_scc1 L_END%=\n\t" \
  "s_barrier\n\t" \
  "s_add_u32 s71, s71, 1\n\t" \
  "s_cmp_ge_u32 s71, s51\n\t" \
  "s_cbranch_scc1 L_END%=\n\t" \
  "s_add_u32 m0, s80, 0\n\t" \
  "s_add_u32 s64, s64, 0x8000\n\t" \
  "s_addc_u32 s65, s65, 0\n\t" \
  "global_load_lds_dwordx4 %[lin], s[64:65] offset:0 sc1\n\t" \
  "global_load_lds_dwordx4 %[lin], s[64:65] offset:1024 sc1\n\t" \
  "global_load_lds_dwordx4 %[lin], s[64:65] offset:2048 sc1\n\t" \
  "global_load_lds_dwordx4 %[lin], s[64:65] offset:3072 sc1\n\t" \
  "s_add_u32 m0, s80, 4096\n\t" \
  "s_nop 0\n\t" \
  "global_load_lds_dwordx4 v252, s[64:65] offset:0 sc1\n\t" \
  "global_load_lds_dwordx4 v252, s[64:65] offset:1024 sc1\n\t" \
  "global_load_lds_dwordx4 v252, s[64:65] offset:2048 sc1\n\t" \
  "global_load_lds_dwordx4 v252, s[64:65] offset:3072 sc1\n\t" \
  "s_add_u32 m0, s80, 65536\n\t" \
  "s_add_u32 s66, s66, 0x8000\n\t" \
  "s_addc_u32 s67, s67, 0\n\t" \
  "global_load_lds_dwordx4 %[lin], s[66:67] offset:0 sc1\n\t" \
  "global_load_lds_dwordx4 %[lin], s[66:67] offset:1024 sc1\n\t" \
  "global_load_lds_dwordx4 %[lin], s[66:67] offset:2048 sc1\n\t" \
  "global_load_lds_dwordx4 %[lin], s[66:67] offset:3072 sc1\n\t" \
  "s_add_u32 m0, s80, 69632\n\t" \
  "s_nop 0\n\t" \
  "global_load_lds_dwordx4 v252, s[66:67] offset:0 sc1\n\t" \
  "global_load_lds_dwordx4 v252, s[66:67] offset:1024 sc1\n\t" \
  "global_load_lds_dwordx4 v252, s[66:67] offset:2048 sc1\n\t" \
  "global_load_lds_dwordx4 v252, s[66:67] offset:3072 sc1\n\t" \
  "s_xor_b32 s80, s80, 0x8000\n\t" \
  "s_waitcnt vmcnt(0)\n\t" \
  "s_branch L_REST%=\n\t" \
  "LA_PMASK%=:\n\t" \
  "v_cmp_gt_i32_e64 s[72:73], 0, %[dq]\n\t" \
  "v_cmp_gt_i32_e64 s[74:75], 1, %[dq]\n\t" \
  "v_cmp_gt_i32_e64 s[76:77], 2, %[dq]\n\t" \
  "v_cmp_gt_i32_e64 s[78:79], 3, %[dq]\n\t" \
  "v_cndmask_b32_e64 v112, v112, v248, s[72:73]\n\t" \
  "v_cmp_gt_i32_e64 s[72:73], 16, %[dq]\n\t" \
  "v_cndmask_b32_e64 v113, v113, v248, s[74:75]\n\t" \
  "v_cmp_gt_i32_e64 s[74:75], 17, %[dq]\n\t" \
  "v_cndmask_b32_e64 v114, v114, v248, s[76:77]\n\t" \
  "v_cmp_gt_i32_e64 s[76:77], 18, %[dq]\n\t" \
  "v_cndmask_b32_e64 v115, v115, v248, s[78:79]\n\t" \
  "v_cmp_gt_i32_e64 s[78:79], 19, %[dq]\n\t" \
  "v_cndmask_b32_e64 v116, v116, v248, s[72:73]\n\t" \
  "v_cmp_gt_i32_e64 s[72:73], 8, %[dq]\n\t" \
  "v_cndmask_b32_e64 v117, v117, v248, s[74:75]\n\t" \
  "v_cmp_gt_i32_e64 s[74:75], 9, %[dq]\n\t" \
  "v_cndmask_b32_e64 v118, v118, v248, s[76:77]\n\t" \
  "v_cmp_gt_i32_e64 s[76:77], 10, %[dq]\n\t" \
  "v_cndmask_b32_e64 v119, v119, v248, s[78:79]\n\t" \
  "v_cmp_gt_i32_e64 s[78:79], 11, %[dq]\n\t" \
  "v_cndmask_b32_e64 v120, v120, v248, s[72:73]\n\t" \
  "v_cmp_gt_i32_e64 s[72:73], 24, %[dq]\n\t" \
  "v_cndmask_b32_e64 v121, v121, v248, s[74:75]\n\t" \
  "v_cmp_gt_i32_e64 s[74:75], 25, %[dq]\n\t" \
  "v_cndmask_b32_e64 v122, v122, v248, s[76:77]\n\t" \
  "v_cmp_gt_i32_e64 s[76:77], 26, %[dq]\n\t" \
  "v_cndmask_b32_e64 v123, v123, v248, s[78:79]\n\t" \
  "v_cmp_gt_i32_e64 s[78:79], 27, %[dq]\n\t" \
  "v_cndmask_b32_e64 v124, v124, v248, s[72:73]\n\t" \
  "v_cndmask_b32_e64 v125, v125, v248, s[74:75]\n\t" \
  "v_cndmask_b32_e64 v126, v126, v248, s[76:77]\n\t" \
  "v_cndmask_b32_e64 v127, v127, v248, s[78:79]\n\t" \
  "v_cmp_gt_i32_e64 s[72:73], 32, %[dq]\n\t" \
  "v_cmp_gt_i32_e64 s[74:75], 33, %[dq]\n\t" \
  "v_cmp_gt_i32_e64 s[76:77], 34, %[dq]\n\t" \
  "v_cmp_gt_i32_e64 s[78:79], 35, %[dq]\n\t" \
  "v_cndmask_b32_e64 v128, v128, v248, s[72:73]\n\t" \
  "v_cmp_gt_i32_e64 s[72:73], 48, %[dq]\n\t" \
  "v_cndmask_b32_e64 v129, v129, v248, s[74:75]\n\t" \
  "v_cmp_gt_i32_e64 s[74:75], 49, %[dq]\n\t" \
  "v_cndmask_b32_e64 v130, v130, v248, s[76:77]\n\t" \
  "v_cmp_gt_i32_e64 s[76:77], 50, %[dq]\n\t" \
  "v_cndmask_b32_e64 v131, v131, v248, s[78:79]\n\t" \
  "v_cmp_gt_i32_e64 s[78:79], 51, %[dq]\n\t" \
  "v_cndmask_b32_e64 v132, v132, v248, s[72:73]\n\t" \
  "v_cmp_gt_i32_e64 s[72:73], 40, %[dq]\n\t" \
  "v_cndmask_b32_e64 v133, v133, v248, s[74:75]\n\t" \
  "v_cmp_gt_i32_e64 s[74:75], 41, %[dq]\n\t" \
  "v_cndmask_b32_e64 v134, v134, v248, s[76:77]\n\t" \
  "v_cmp_gt_i32_e64 s[76:77], 42, %[dq]\n\t" \
  "v_cndmask_b32_e64 v135, v135, v248, s[78:79]\n\t" \
  "v_cmp_gt_i32_e64 s[78:79], 43, %[dq]\n\t" \
  "v_cndmask_b32_e64 v136, v136, v248, s[72:73]\n\t" \
  "v_cmp_gt_i32_e64 s[72:73], 56, %[dq]\n\t" \
  "v_cndmask_b32_e64 v137, v137, v248, s[74:75]\n\t" \
  "v_cmp_gt_i32_e64 s[74:75], 57, %[dq]\n\t" \
  "v_cndmask_b32_e64 v138, v138, v248, s[76:77]\n\t" \
  "v_cmp_gt_i32_e64 s[76:77], 58, %[dq]\n\t" \
  "v_cndmask_b32_e64 v139, v139, v248, s[78:79]\n\t" \
  "v_cmp_gt_i32_e64 s[78:79], 59, %[dq]\n\t" \
  "v_cndmask_b32_e64 v140, v140, v248, s[72:73]\n\t" \
  "v_cndmask_b32_e64 v141, v141, v248, s[74:75]\n\t" \
  "v_cndmask_b32_e64 v142, v142, v248, s[76:77]\n\t" \
  "v_cndmask_b32_e64 v143, v143, v248, s[78:79]\n\t" \
  "s_nop 0\n\t" \
  "s_branch LA_PMASKB%=\n\t" \
  "LA_ROS0%=:\n\t" \
  "s_nop 11\n\t" \
  "v_mul_f32_e32 v48, v247, v48\n\t" \
  "v_mul_f32_e32 v49, v247, v49\n\t" \
  "v_mul_f32_e32 v50, v247, v50\n\t" \
  "v_mul_f32_e32 v51, v247, v51\n\t" \
  "v_mul_f32_e32 v52, v247, v52\n\t" \
  "v_mul_f32_e32 v53, v247, v53\n\t" \
  "v_mul_f32_e32 v54, v247, v54\n\t" \
  "v_mul_f32_e32 v55, v247, v55\n\t" \
  "v_mul_f32_e32 v56, v247, v56\n\t" \
  "v_mul_f32_e32 v57, v247, v57\n\t" \
  "v_mul_f32_e32 v58, v247, v58\n\t" \
  "v_mul_f32_e32 v59, v247, v59\n\t" \
  "v_mul_f32_e32 v60, v247, v60\n\t" \
  "v_mul_f32_e32 v61, v247, v61\n\t" \
  "v_mul_f32_e32 v62, v247, v62\n\t" \
  "v_mul_f32_e32 v63, v247, v63\n\t" \
  "v_mul_f32_e32 v64, v247, v64\n\t" \
  "v_mul_f32_e32 v65, v247, v65\n\t" \
  "v_mul_f32_e32 v66, v247, v66\n\t" \
  "v_mul_f32_e32 v67, v247, v67\n\t" \
  "v_mul_f32_e32 v68, v247, v68\n\t" \
  "v_mul_f32_e32 v69, v247, v69\n\t" \
  "v_mul_f32_e32 v70, v247, v70\n\t" \
  "v_mul_f32_e32 v71, v247, v71\n\t" \
  "v_mul_f32_e32 v72, v247, v72\n\t" \
  "v_mul_f32_e32 v73, v247, v73\n\t" \
  "v_mul_f32_e32 v74, v247, v74\n\t" \
  "v_mul_f32_e32 v75, v247, v75\n\t" \
  "v_mul_f32_e32 v76, v247, v76\n\t" \
  "v_mul_f32_e32 v77, v247, v77\n\t" \
  "v_mul_f32_e32 v78, v247, v78\n\t" \
  "v_mul_f32_e32 v79, v247, v79\n\t" \
  "v_mul_f32_e32 v80, v247, v80\n\t" \
  "v_mul_f32_e32 v81, v247, v81\n\t" \
  "v_mul_f32_e32 v82, v247, v82\n\t" \
  "v_mul_f32_e32 v83, v247, v83\n\t" \
  "v_mul_f32_e32 v84, v247, v84\n\t" \
  "v_mul_f32_e32 v85, v247, v85\n\t" \
  "v_mul_f32_e32 v86, v247, v86\n\t" \
  "v_mul_f32_e32 v87, v247, v87\n\t" \
  "v_mul_f32_e32 v88, v247, v88\n\t" \
  "v_mul_f32_e32 v89, v247, v89\n\t" \
  "v_mul_f32_e32 v90, v247, v90\n\t" \
  "v_mul_f32_e32 v91, v247, v91\n\t" \
  "v_mul_f32_e32 v92, v247, v92\n\t" \
  "v_mul_f32_e32 v93, v247, v93\n\t" \
  "v_mul_f32_e32 v94, v247, v94\n\t" \
  "v_mul_f32_e32 v95, v247, v95\n\t" \
  "v_mul_f32_e32 v96, v247, v96\n\t" \
  "v_mul_f32_e32 v97, v247, v97\n\t" \
  "v_mul_f32_e32 v98, v247, v98\n\t" \
  "v_mul_f32_e32 v99, v247, v99\n\t" \
  "v_mul_f32_e32 v100, v247, v100\n\t" \
  "v_mul_f32_e32 v101, v247, v101\n\t" \
  "v_mul_f32_e32 v102, v247, v102\n\t" \
  "v_mul_f32_e32 v103, v247, v103\n\t" \
  "v_mul_f32_e32 v104, v247, v104\n\t" \
  "v_mul_f32_e32 v105, v247, v105\n\t" \
  "v_mul_f32_e32 v106, v247, v106\n\t" \
  "v_mul_f32_e32 v107, v247, v107\n\t" \
  "v_mul_f32_e32 v108, v247, v108\n\t" \
  "v_mul_f32_e32 v109, v247, v109\n\t" \
  "v_mul_f32_e32 v110, v247, v110\n\t" \
  "v_mul_f32_e32 v111, v247, v111\n\t" \
  "v_mul_f32_e32 v192, v247, v192\n\t" \
  "s_mov_b32 s69, 0\n\t" \
  "s_nop 1\n\t" \
  "s_branch LA_ROBS0%=\n\t" \
  "LA_MASK00%=:\n\t" \
  "s_nop 3\n\t" \
  "v_cmp_gt_i32_e64 s[72:73], 0, %[dq]\n\t" \
  "v_cmp_gt_i32_e64 s[74:75], 1, %[dq]\n\t" \
  "v_cmp_gt_i32_e64 s[76:77], 2, %[dq]\n\t" \
  "v_cmp_gt_i32_e64 s[78:79], 3, %[dq]\n\t" \
  "v_cndmask_b32_e64 v112, v112, v248, s[72:73]\n\t" \
  "v_cmp_gt_i32_e64 s[72:73], 16, %[dq]\n\t" \
  "v_cndmask_b32_e64 v113, v113, v248, s[74:75]\n\t" \
  "v_cmp_gt_i32_e64 s[74:75], 17, %[dq]\n\t" \
  "v_cndmask_b32_e64 v114, v114, v248, s[76:77]\n\t" \
  "v_cmp_gt_i32_e64 s[76:77], 18, %[dq]\n\t" \
  "v_cndmask_b32_e64 v115, v115, v248, s[78:79]\n\t" \
  "v_cmp_gt_i32_e64 s[78:79], 19, %[dq]\n\t" \
  "v_cndmask_b32_e64 v116, v116, v248, s[72:73]\n\t" \
  "v_cmp_gt_i32_e64 s[72:73], 8, %[dq]\n\t" \
  "v_cndmask_b32_e64 v117, v117, v248, s[74:75]\n\t" \
  "v_cmp_gt_i32_e64 s[74:75], 9, %[dq]\n\t" \
  "v_cndmask_b32_e64 v118, v118, v248, s[76:77]\n\t" \
  "v_cmp_gt_i32_e64 s[76:77], 10, %[dq]\n\t" \
  "v_cndmask_b32_e64 v119, v119, v248, s[78:79]\n\t" \
  "v_cmp_gt_i32_e64 s[78:79], 11, %[dq]\n\t" \
  "v_cndmask_b32_e64 v120, v120, v248, s[72:73]\n\t" \
  "v_cmp_gt_i32_e64 s[72:73], 24, %[dq]\n\t" \
  "v_cndmask_b32_e64 v121, v121, v248, s[74:75]\n\t" \
  "v_cmp_gt_i32_e64 s[74:75], 25, %[dq]\n\t" \
  "v_cndmask_b32_e64 v122, v122, v248, s[76:77]\n\t" \
  "v_cmp_gt_i32_e64 s[76:77], 26, %[dq]\n\t" \
  "v_cndmask_b32_e64 v123, v123, v248, s[78:79]\n\t" \
  "v_cmp_gt_i32_e64 s[78:79], 27, %[dq]\n\t" \
  "v_cndmask_b32_e64 v124, v124, v248, s[72:73]\n\t" \
  "v_cndmask_b32_e64 v125, v125, v248, s[74:75]\n\t" \
  "v_cndmask_b32_e64 v126, v126, v248, s[76:77]\n\t" \
  "v_cndmask_b32_e64 v127, v127, v248, s[78:79]\n\t" \
  "s_nop 0\n\t" \
  "s_branch LA_MASKB00%=\n\t" \
  "LA_MASK01%=:\n\t" \
  "s_nop 3\n\t" \
  "v_cmp_gt_i32_e64 s[72:73], 32, %[dq]\n\t" \
  "v_cmp_gt_i32_e64 s[74:75], 33, %[dq]\n\t" \
  "v_cmp_gt_i32_e64 s[76:77], 34, %[dq]\n\t" \
  "v_cmp_gt_i32_e64 s[78:79], 35, %[dq]\n\t" \
  "v_cndmask_b32_e64 v128, v128, v248, s[72:73]\n\t" \
  "v_cmp_gt_i32_e64 s[72:73], 48, %[dq]\n\t" \
  "v_cndmask_b32_e64 v129, v129, v248, s[74:75]\n\t" \
  "v_cmp_gt_i32_e64 s[74:75], 49, %[dq]\n\t" \
  "v_cndmask_b32_e64 v130, v130, v248, s[76:77]\n\t" \
  "v_cmp_gt_i32_e64 s[76:77], 50, %[dq]\n\t" \
  "v_cndmask_b32_e64 v131, v131, v248, s[78:79]\n\t" \
  "v_cmp_gt_i32_e64 s[78:79], 51, %[dq]\n\t" \
  "v_cndmask_b32_e64 v132, v132, v248, s[72:73]\n\t" \
  "v_cmp_gt_i32_e64 s[72:73], 40, %[dq]\n\t" \
  "v_cndmask_b32_e64 v133, v133, v248, s[74:75]\n\t" \
  "v_cmp_gt_i32_e64 s[74:75], 41, %[dq]\n\t" \
  "v_cndmask_b32_e64 v134, v134, v248, s[76:77]\n\t" \
  "v_cmp_gt_i32_e64 s[76:77], 42, %[dq]\n\t" \
  "v_cndmask_b32_e64 v135, v135, v248, s[78:79]\n\t" \
  "v_cmp_gt_i32_e64 s[78:79], 43, %[dq]\n\t" \
  "v_cndmask_b32_e64 v136, v136, v248, s[72:73]\n\t" \
  "v_cmp_gt_i32_e64 s[72:73], 56, %[dq]\n\t" \
  "v_cndmask_b32_e64 v137, v137, v248, s[74:75]\n\t" \
  "v_cmp_gt_i32_e64 s[74:75], 57, %[dq]\n\t" \
  "v_cndmask_b32_e64 v138, v138, v248, s[76:77]\n\t" \
  "v_cmp_gt_i32_e64 s[76:77], 58, %[dq]\n\t" \
  "v_cndmask_b32_e64 v139, v139, v248, s[78:79]\n\t" \
  "v_cmp_gt_i32_e64 s[78:79], 59, %[dq]\n\t" \
  "v_cndmask_b32_e64 v140, v140, v248, s[72:73]\n\t" \
  "v_cndmask_b32_e64 v141, v141, v248, s[74:75]\n\t" \
  "v_cndmask_b32_e64 v142, v142, v248, s[76:77]\n\t" \
  "v_cndmask_b32_e64 v143, v143, v248, s[78:79]\n\t" \
  "s_nop 0\n\t" \
  "s_branch LA_MASKB01%=\n\t" \
  "LA_RARE0%=:\n\t" \
  "v_max_f32_e32 v246, 0, v241\n\t" \
  "v_sub_f32_e32 v240, v240, v246\n\t" \
  "v_exp_f32_e64 v247, -v246\n\t" \
  "s_mov_b32 s69, 1\n\t" \
  "s_nop 1\n\t" \
  "s_branch LA_RAREB0%=\n\t" \
  "L_WM%=:\n\t" \
  "s_add_u32 m0, s54, 24\n\t" \
  "v_min_u32_e32 v254, 0x130, %[lin]\n\t" \
  "v_readlane_b32 s56, %[prm], m0\n\t" \
  "s_add_u32 m0, s54, 25\n\t" \
  "v_lshlrev_b32_e32 v254, 3, v254\n\t" \
  "v_readlane_b32 s57, %[prm], m0\n\t" \
  "s_add_u32 s54, s54, 2\n\t" \
  "s_add_u32 s70, s70, 1\n\t" \
  "s_cmp_ge_u32 s54, 10\n\t" \
  "s_cselect_b32 s70, 99, s70\n\t" \
  "s_nop 1\n\t" \
  "global_load_dword v253, v254, s[56:57]\n\t" \
  "s_branch L_WMB%=\n\t" \
  "LA_ROS1%=:\n\t" \
  "s_nop 11\n\t" \
  "v_mul_f32_e32 v48, v247, v48\n\t" \
  "v_mul_f32_e32 v49, v247, v49\n\t" \
  "v_mul_f32_e32 v50, v247, v50\n\t" \
  "v_mul_f32_e32 v51, v247, v51\n\t" \
  "v_mul_f32_e32 v52, v247, v52\n\t" \
  "v_mul_f32_e32 v53, v247, v53\n\t" \
  "v_mul_f32_e32 v54, v247, v54\n\t" \
  "v_mul_f32_e32 v55, v247, v55\n\t" \
  "v_mul_f32_e32 v56, v247, v56\n\t" \
  "v_mul_f32_e32 v57, v247, v57\n\t" \
  "v_mul_f32_e32 v58, v247, v58\n\t" \
  "v_mul_f32_e32 v59, v247, v59\n\t" \
  "v_mul_f32_e32 v60, v247, v60\n\t" \
  "v_mul_f32_e32 v61, v247, v61\n\t" \
  "v_mul_f32_e32 v62, v247, v62\n\t" \
  "v_mul_f32_e32 v63, v247, v63\n\t" \
  "v_mul_f32_e32 v64, v247, v64\n\t" \
  "v_mul_f32_e32 v65, v247, v65\n\t" \
  "v_mul_f32_e32 v66, v247, v66\n\t" \
  "v_mul_f32_e32 v67, v247, v67\n\t" \
  "v_mul_f32_e32 v68, v247, v68\n\t" \
  "v_mul_f32_e32 v69, v247, v69\n\t" \
  "v_mul_f32_e32 v70, v247, v70\n\t" \
  "v_mul_f32_e32 v71, v247, v71\n\t" \
  "v_mul_f32_e32 v72, v247, v72\n\t" \
  "v_mul_f32_e32 v73, v247, v73\n\t" \
  "v_mul_f32_e32 v74, v247, v74\n\t" \
  "v_mul_f32_e32 v75, v247, v75\n\t" \
  "v_mul_f32_e32 v76, v247, v76\n\t" \
  "v_mul_f32_e32 v77, v247, v77\n\t" \
  "v_mul_f32_e32 v78, v247, v78\n\t" \
  "v_mul_f32_e32 v79, v247, v79\n\t" \
  "v_mul_f32_e32 v80, v247, v80\n\t" \
  "v_mul_f32_e32 v81, v247, v81\n\t" \
  "v_mul_f32_e32 v82, v247, v82\n\t" \
  "v_mul_f32_e32 v83, v247, v83\n\t" \
  "v_mul_f32_e32 v84, v247, v84\n\t" \
  "v_mul_f32_e32 v85, v247, v85\n\t" \
  "v_mul_f32_e32 v86, v247, v86\n\t" \
  "v_mul_f32_e32 v87, v247, v87\n\t" \
  "v_mul_f32_e32 v88, v247, v88\n\t" \
  "v_mul_f32_e32 v89, v247, v89\n\t" \
  "v_mul_f32_e32 v90, v247, v90\n\t" \
  "v_mul_f32_e32 v91, v247, v91\n\t" \
  "v_mul_f32_e32 v92, v247, v92\n\t" \
  "v_mul_f32_e32 v93, v247, v93\n\t" \
  "v_mul_f32_e32 v94, v247, v94\n\t" \
  "v_mul_f32_e32 v95, v247, v95\n\t" \
  "v_mul_f32_e32 v96, v247, v96\n\t" \
  "v_mul_f32_e32 v97, v247, v97\n\t" \
  "v_mul_f32_e32 v98, v247, v98\n\t" \
  "v_mul_f32_e32 v99, v247, v99\n\t" \
  "v_mul_f32_e32 v100, v247, v100\n\t" \
  "v_mul_f32_e32 v101, v247, v101\n\t" \
  "v_mul_f32_e32 v102, v247, v102\n\t" \
  "v_mul_f32_e32 v103, v247, v103\n\t" \
  "v_mul_f32_e32 v104, v247, v104\n\t" \
  "v_mul_f32_e32 v105, v247, v105\n\t" \
  "v_mul_f32_e32 v106, v247, v106\n\t" \
  "v_mul_f32_e32 v107, v247, v107\n\t" \
  "v_mul_f32_e32 v108, v247, v108\n\t" \
  "v_mul_f32_e32 v109, v247, v109\n\t" \
  "v_mul_f32_e32 v110, v247, v110\n\t" \
  "v_mul_f32_e32 v111, v247, v111\n\t" \
  "v_mul_f32_e32 v192, v247, v192\n\t" \
  "s_mov_b32 s69, 0\n\t" \
  "s_nop 1\n\t" \
  "s_branch LA_ROBS1%=\n\t" \
  "LA_MASK10%=:\n\t" \
  "s_nop 3\n\t" \
  "v_cmp_gt_i32_e64 s[72:73], 0, %[dq]\n\t" \
  "v_cmp_gt_i32_e64 s[74:75], 1, %[dq]\n\t" \
  "v_cmp_gt_i32_e64 s[76:77], 2, %[dq]\n\t" \
  "v_cmp_gt_i32_e64 s[78:79], 3, %[dq]\n\t" \
  "v_cndmask_b32_e64 v144, v144, v248, s[72:73]\n\t" \
  "v_cmp_gt_i32_e64 s[72:73], 16, %[dq]\n\t" \
  "v_cndmask_b32_e64 v145, v145, v248, s[74:75]\n\t" \
  "v_cmp_gt_i32_e64 s[74:75], 17, %[dq]\n\t" \
  "v_cndmask_b32_e64 v146, v146, v248, s[76:77]\n\t" \
  "v_cmp_gt_i32_e64 s[76:77], 18, %[dq]\n\t" \
  "v_cndmask_b32_e64 v147, v147, v248, s[78:79]\n\t" \
  "v_cmp_gt_i32_e64 s[78:79], 19, %[dq]\n\t" \
  "v_cndmask_b32_e64 v148, v148, v248, s[72:73]\n\t" \
  "v_cmp_gt_i32_e64 s[72:73], 8, %[dq]\n\t" \
  "v_cndmask_b32_e64 v149, v149, v248, s[74:75]\n\t" \
  "v_cmp_gt_i32_e64 s[74:75], 9, %[dq]\n\t" \
  "v_cndmask_b32_e64 v150, v150, v248, s[76:77]\n\t" \
  "v_cmp_gt_i32_e64 s[76:77], 10, %[dq]\n\t" \
  "v_cndmask_b32_e64 v151, v151, v248, s[78:79]\n\t" \
  "v_cmp_gt_i32_e64 s[78:79], 11, %[dq]\n\t" \
  "v_cndmask_b32_e64 v152, v152, v248, s[72:73]\n\t" \
  "v_cmp_gt_i32_e64 s[72:73], 24, %[dq]\n\t" \
  "v_cndmask_b32_e64 v153, v153, v248, s[74:75]\n\t" \
  "v_cmp_gt_i32_e64 s[74:75], 25, %[dq]\n\t" \
  "v_cndmask_b32_e64 v154, v154, v248, s[76:77]\n\t" \
  "v_cmp_gt_i32_e64 s[76:77], 26, %[dq]\n\t" \
  "v_cndmask_b32_e64 v155, v155, v248, s[78:79]\n\t" \
  "v_cmp_gt_i32_e64 s[78:79], 27, %[dq]\n\t" \
  "v_cndmask_b32_e64 v156, v156, v248, s[72:73]\n\t" \
  "v_cndmask_b32_e64 v157, v157, v248, s[74:75]\n\t" \
  "v_cndmask_b32_e64 v158, v158, v248, s[76:77]\n\t" \
  "v_cndmask_b32_e64 v159, v159, v248, s[78:79]\n\t" \
  "s_nop 0\n\t" \
  "s_branch LA_MASKB10%=\n\t" \
  "LA_MASK11%=:\n\t" \
  "s_nop 3\n\t" \
  "v_cmp_gt_i32_e64 s[72:73], 32, %[dq]\n\t" \
  "v_cmp_gt_i32_e64 s[74:75], 33, %[dq]\n\t" \
  "v_cmp_gt_i32_e64 s[76:77], 34, %[dq]\n\t" \
  "v_cmp_gt_i32_e64 s[78:79], 35, %[dq]\n\t" \
  "v_cndmask_b32_e64 v160, v160, v248, s[72:73]\n\t" \
  "v_cmp_gt_i32_e64 s[72:73], 48, %[dq]\n\t" \
  "v_cndmask_b32_e64 v161, v161, v248, s[74:75]\n\t" \
  "v_cmp_gt_i32_e64 s[74:75], 49, %[dq]\n\t" \
  "v_cndmask_b32_e64 v162, v162, v248, s[76:77]\n\t" \
  "v_cmp_gt_i32_e64 s[76:77], 50, %[dq]\n\t" \
  "v_cndmask_b32_e64 v163, v163, v248, s[78:79]\n\t" \
  "v_cmp_gt_i32_e64 s[78:79], 51, %[dq]\n\t" \
  "v_cndmask_b32_e64 v164, v164, v248, s[72:73]\n\t" \
  "v_cmp_gt_i32_e64 s[72:73], 40, %[dq]\n\t" \
  "v_cndmask_b32_e64 v165, v165, v248, s[74:75]\n\t" \
  "v_cmp_gt_i32_e64 s[74:75], 41, %[dq]\n\t" \
  "v_cndmask_b32_e64 v166, v166, v248, s[76:77]\n\t" \
  "v_cmp_gt_i32_e64 s[76:77], 42, %[dq]\n\t" \
  "v_cndmask_b32_e64 v167, v167, v248, s[78:79]\n\t" \
  "v_cmp_gt_i32_e64 s[78:79], 43, %[dq]\n\t" \
  "v_cndmask_b32_e64 v168, v168, v248, s[72:73]\n\t" \
  "v_cmp_gt_i32_e64 s[72:73], 56, %[dq]\n\t" \
  "v_cndmask_b32_e64 v169, v169, v248, s[74:75]\n\t" \
  "v_cmp_gt_i32_e64 s[74:75], 57, %[dq]\n\t" \
  "v_cndmask_b32_e64 v170, v170, v248, s[76:77]\n\t" \
  "v_cmp_gt_i32_e64 s[76:77], 58, %[dq]\n\t" \
  "v_cndmask_b32_e64 v171, v171, v248, s[78:79]\n\t" \
  "v_cmp_gt_i32_e64 s[78:79], 59, %[dq]\n\t" \
  "v_cndmask_b32_e64 v172, v172, v248, s[72:73]\n\t" \
  "v_cndmask_b32_e64 v173, v173, v248, s[74:75]\n\t" \
  "v_cndmask_b32_e64 v174, v174, v248, s[76:77]\n\t" \
  "v_cndmask_b32_e64 v175, v175, v248, s[78:79]\n\t" \
  "s_nop 0\n\t" \
  "s_branch LA_MASKB11%=\n\t" \
  "LA_RARE1%=:\n\t" \
  "v_max_f32_e32 v246, 0, v241\n\t" \
  "v_sub_f32_e32 v240, v240, v246\n\t" \
  "v_exp_f32_e64 v247, -v246\n\t" \
  "s_mov_b32 s69, 1\n\t" \
  "s_nop 1\n\t" \
  "s_branch LA_RAREB1%=\n\t" \
  "LA_ROD0%=:\n\t" \
  "s_nop 11\n\t" \
  "v_mul_f32_e32 v48, v247, v48\n\t" \
  "v_mul_f32_e32 v49, v247, v49\n\t" \
  "v_mul_f32_e32 v50, v247, v50\n\t" \
  "v_mul_f32_e32 v51, v247, v51\n\t" \
  "v_mul_f32_e32 v52, v247, v52\n\t" \
  "v_mul_f32_e32 v53, v247, v53\n\t" \
  "v_mul_f32_e32 v54, v247, v54\n\t" \
  "v_mul_f32_e32 v55, v247, v55\n\t" \
  "v_mul_f32_e32 v56, v247, v56\n\t" \
  "v_mul_f32_e32 v57, v247, v57\n\t" \
  "v_mul_f32_e32 v58, v247, v58\n\t" \
  "v_mul_f32_e32 v59, v247, v59\n\t" \
  "v_mul_f32_e32 v60, v247, v60\n\t" \
  "v_mul_f32_e32 v61, v247, v61\n\t" \
  "v_mul_f32_e32 v62, v247, v62\n\t" \
  "v_mul_f32_e32 v63, v247, v63\n\t" \
  "v_mul_f32_e32 v64, v247, v64\n\t" \
  "v_mul_f32_e32 v65, v247, v65\n\t" \
  "v_mul_f32_e32 v66, v247, v66\n\t" \
  "v_mul_f32_e32 v67, v247, v67\n\t" \
  "v_mul_f32_e32 v68, v247, v68\n\t" \
  "v_mul_f32_e32 v69, v247, v69\n\t" \
  "v_mul_f32_e32 v70, v247, v70\n\t" \
  "v_mul_f32_e32 v71, v247, v71\n\t" \
  "v_mul_f32_e32 v72, v247, v72\n\t" \
  "v_mul_f32_e32 v73, v247, v73\n\t" \
  "v_mul_f32_e32 v74, v247, v74\n\t" \
  "v_mul_f32_e32 v75, v247, v75\n\t" \
  "v_mul_f32_e32 v76, v247, v76\n\t" \
  "v_mul_f32_e32 v77, v247, v77\n\t" \
  "v_mul_f32_e32 v78, v247, v78\n\t" \
  "v_mul_f32_e32 v79, v247, v79\n\t" \
  "v_mul_f32_e32 v80, v247, v80\n\t" \
  "v_mul_f32_e32 v81, v247, v81\n\t" \
  "v_mul_f32_e32 v82, v247, v82\n\t" \
  "v_mul_f32_e32 v83, v247, v83\n\t" \
  "v_mul_f32_e32 v84, v247, v84\n\t" \
  "v_mul_f32_e32 v85, v247, v85\n\t" \
  "v_mul_f32_e32 v86, v247, v86\n\t" \
  "v_mul_f32_e32 v87, v247, v87\n\t" \
  "v_mul_f32_e32 v88, v247, v88\n\t" \
  "v_mul_f32_e32 v89, v247, v89\n\t" \
  "v_mul_f32_e32 v90, v247, v90\n\t" \
  "v_mul_f32_e32 v91, v247, v91\n\t" \
  "v_mul_f32_e32 v92, v247, v92\n\t" \
  "v_mul_f32_e32 v93, v247, v93\n\t" \
  "v_mul_f32_e32 v94, v247, v94\n\t" \
  "v_mul_f32_e32 v95, v247, v95\n\t" \
  "v_mul_f32_e32 v96, v247, v96\n\t" \
  "v_mul_f32_e32 v97, v247, v97\n\t" \
  "v_mul_f32_e32 v98, v247, v98\n\t" \
  "v_mul_f32_e32 v99, v247, v99\n\t" \
  "v_mul_f32_e32 v100, v247, v100\n\t" \
  "v_mul_f32_e32 v101, v247, v101\n\t" \
  "v_mul_f32_e32 v102, v247, v102\n\t" \
  "v_mul_f32_e32 v103, v247, v103\n\t" \
  "v_mul_f32_e32 v104, v247, v104\n\t" \
  "v_mul_f32_e32 v105, v247, v105\n\t" \
  "v_mul_f32_e32 v106, v247, v106\n\t" \
  "v_mul_f32_e32 v107, v247, v107\n\t" \
  "v_mul_f32_e32 v108, v247, v108\n\t" \
  "v_mul_f32_e32 v109, v247, v109\n\t" \
  "v_mul_f32_e32 v110, v247, v110\n\t" \
  "v_mul_f32_e32 v111, v247, v111\n\t" \
  "v_mul_f32_e32 v192, v247, v192\n\t" \
  "s_mov_b32 s69, 0\n\t" \
  "s_nop 1\n\t" \
  "s_branch LA_ROBD0%=\n\t" \
  "LA_ROD1%=:\n\t" \
  "s_nop 11\n\t" \
  "v_mul_f32_e32 v48, v247, v48\n\t" \
  "v_mul_f32_e32 v49, v247, v49\n\t" \
  "v_mul_f32_e32 v50, v247, v50\n\t" \
  "v_mul_f32_e32 v51, v247, v51\n\t" \
  "v_mul_f32_e32 v52, v247, v52\n\t" \
  "v_mul_f32_e32 v53, v247, v53\n\t" \
  "v_mul_f32_e32 v54, v247, v54\n\t" \
  "v_mul_f32_e32 v55, v247, v55\n\t" \
  "v_mul_f32_e32 v56, v247, v56\n\t" \
  "v_mul_f32_e32 v57, v247, v57\n\t" \
  "v_mul_f32_e32 v58, v247, v58\n\t" \
  "v_mul_f32_e32 v59, v247, v59\n\t" \
  "v_mul_f32_e32 v60, v247, v60\n\t" \
  "v_mul_f32_e32 v61, v247, v61\n\t" \
  "v_mul_f32_e32 v62, v247, v62\n\t" \
  "v_mul_f32_e32 v63, v247, v63\n\t" \
  "v_mul_f32_e32 v64, v247, v64\n\t" \
  "v_mul_f32_e32 v65, v247, v65\n\t" \
  "v_mul_f32_e32 v66, v247, v66\n\t" \
  "v_mul_f32_e32 v67, v247, v67\n\t" \
  "v_mul_f32_e32 v68, v247, v68\n\t" \
  "v_mul_f32_e32 v69, v247, v69\n\t" \
  "v_mul_f32_e32 v70, v247, v70\n\t" \
  "v_mul_f32_e32 v71, v247, v71\n\t" \
  "v_mul_f32_e32 v72, v247, v72\n\t" \
  "v_mul_f32_e32 v73, v247, v73\n\t" \
  "v_mul_f32_e32 v74, v247, v74\n\t" \
  "v_mul_f32_e32 v75, v247, v75\n\t" \
  "v_mul_f32_e32 v76, v247, v76\n\t" \
  "v_mul_f32_e32 v77, v247, v77\n\t" \
  "v_mul_f32_e32 v78, v247, v78\n\t" \
  "v_mul_f32_e32 v79, v247, v79\n\t" \
  "v_mul_f32_e32 v80, v247, v80\n\t" \
  "v_mul_f32_e32 v81, v247, v81\n\t" \
  "v_mul_f32_e32 v82, v247, v82\n\t" \
  "v_mul_f32_e32 v83, v247, v83\n\t" \
  "v_mul_f32_e32 v84, v247, v84\n\t" \
  "v_mul_f32_e32 v85, v247, v85\n\t" \
  "v_mul_f32_e32 v86, v247, v86\n\t" \
  "v_mul_f32_e32 v87, v247, v87\n\t" \
  "v_mul_f32_e32 v88, v247, v88\n\t" \
  "v_mul_f32_e32 v89, v247, v89\n\t" \
  "v_mul_f32_e32 v90, v247, v90\n\t" \
  "v_mul_f32_e32 v91, v247, v91\n\t" \
  "v_mul_f32_e32 v92, v247, v92\n\t" \
  "v_mul_f32_e32 v93, v247, v93\n\t" \
  "v_mul_f32_e32 v94, v247, v94\n\t" \
  "v_mul_f32_e32 v95, v247, v95\n\t" \
  "v_mul_f32_e32 v96, v247, v96\n\t" \
  "v_mul_f32_e32 v97, v247, v97\n\t" \
  "v_mul_f32_e32 v98, v247, v98\n\t" \
  "v_mul_f32_e32 v99, v247, v99\n\t" \
  "v_mul_f32_e32 v100, v247, v100\n\t" \
  "v_mul_f32_e32 v101, v247, v101\n\t" \
  "v_mul_f32_e32 v102, v247, v102\n\t" \
  "v_mul_f32_e32 v103, v247, v103\n\t" \
  "v_mul_f32_e32 v104, v247, v104\n\t" \
  "v_mul_f32_e32 v105, v247, v105\n\t" \
  "v_mul_f32_e32 v106, v247, v106\n\t" \
  "v_mul_f32_e32 v107, v247, v107\n\t" \
  "v_mul_f32_e32 v108, v247, v108\n\t" \
  "v_mul_f32_e32 v109, v247, v109\n\t" \
  "v_mul_f32_e32 v110, v247, v110\n\t" \
  "v_mul_f32_e32 v111, v247, v111\n\t" \
  "v_mul_f32_e32 v192, v247, v192\n\t" \
  "s_mov_b32 s69, 0\n\t" \
  "s_nop 1\n\t" \
  "s_branch LA_ROBD1%=\n\t" \
  "LB_PMASK%=:\n\t" \
  "v_cmp_gt_i32_e64 s[72:73], 0, %[dq]\n\t" \
  "v_cmp_gt_i32_e64 s[74:75], 1, %[dq]\n\t" \
  "v_cmp_gt_i32_e64 s[76:77], 2, %[dq]\n\t" \
  "v_cmp_gt_i32_e64 s[78:79], 3, %[dq]\n\t" \
  "v_cndmask_b32_e64 v112, v112, v248, s[72:73]\n\t" \
  "v_cmp_gt_i32_e64 s[72:73], 16, %[dq]\n\t" \
  "v_cndmask_b32_e64 v113, v113, v248, s[74:75]\n\t" \
  "v_cmp_gt_i32_e64 s[74:75], 17, %[dq]\n\t" \
  "v_cndmask_b32_e64 v114, v114, v248, s[76:77]\n\t" \
  "v_cmp_gt_i32_e64 s[76:77], 18, %[dq]\n\t" \
  "v_cndmask_b32_e64 v115, v115, v248, s[78:79]\n\t" \
  "v_cmp_gt_i32_e64 s[78:79], 19, %[dq]\n\t" \
  "v_cndmask_b32_e64 v116, v116, v248, s[72:73]\n\t" \
  "v_cmp_gt_i32_e64 s[72:73], 8, %[dq]\n\t" \
  "v_cndmask_b32_e64 v117, v117, v248, s[74:75]\n\t" \
  "v_cmp_gt_i32_e64 s[74:75], 9, %[dq]\n\t" \
  "v_cndmask_b32_e64 v118, v118, v248, s[76:77]\n\t" \
  "v_cmp_gt_i32_e64 s[76:77], 10, %[dq]\n\t" \
  "v_cndmask_b32_e64 v119, v119, v248, s[78:79]\n\t" \
  "v_cmp_gt_i32_e64 s[78:79], 11, %[dq]\n\t" \
  "v_cndmask_b32_e64 v120, v120, v248, s[72:73]\n\t" \
  "v_cmp_gt_i32_e64 s[72:73], 24, %[dq]\n\t" \
  "v_cndmask_b32_e64 v121, v121, v248, s[74:75]\n\t" \
  "v_cmp_gt_i32_e64 s[74:75], 25, %[dq]\n\t" \
  "v_cndmask_b32_e64 v122, v122, v248, s[76:77]\n\t" \
  "v_cmp_gt_i32_e64 s[76:77], 26, %[dq]\n\t" \
  "v_cndmask_b32_e64 v123, v123, v248, s[78:79]\n\t" \
  "v_cmp_gt_i32_e64 s[78:79], 27, %[dq]\n\t" \
  "v_cndmask_b32_e64 v124, v124, v248, s[72:73]\n\t" \
  "v_cndmask_b32_e64 v125, v125, v248, s[74:75]\n\t" \
  "v_cndmask_b32_e64 v126, v126, v248, s[76:77]\n\t" \
  "v_cndmask_b32_e64 v127, v127, v248, s[78:79]\n\t" \
  "v_cmp_gt_i32_e64 s[72:73], 32, %[dq]\n\t" \
  "v_cmp_gt_i32_e64 s[74:75], 33, %[dq]\n\t" \
  "v_cmp_gt_i32_e64 s[76:77], 34, %[dq]\n\t" \
  "v_cmp_gt_i32_e64 s[78:79], 35, %[dq]\n\t" \
  "v_cndmask_b32_e64 v128, v128, v248, s[72:73]\n\t" \
  "v_cmp_gt_i32_e64 s[72:73], 48, %[dq]\n\t" \
  "v_cndmask_b32_e64 v129, v129, v248, s[74:75]\n\t" \
  "v_cmp_gt_i32_e64 s[74:75], 49, %[dq]\n\t" \
  "v_cndmask_b32_e64 v130, v130, v248, s[76:77]\n\t" \
  "v_cmp_gt_i32_e64 s[76:77], 50, %[dq]\n\t" \
  "v_cndmask_b32_e64 v131, v131, v248, s[78:79]\n\t" \
  "v_cmp_gt_i32_e64 s[78:79], 51, %[dq]\n\t" \
  "v_cndmask_b32_e64 v132, v132, v248, s[72:73]\n\t" \
  "v_cmp_gt_i32_e64 s[72:73], 40, %[dq]\n\t" \
  "v_cndmask_b32_e64 v133, v133, v248, s[74:75]\n\t" \
  "v_cmp_gt_i32_e64 s[74:75], 41, %[dq]\n\t" \
  "v_cndmask_b32_e64 v134, v134, v248, s[76:77]\n\t" \
  "v_cmp_gt_i32_e64 s[76:77], 42, %[dq]\n\t" \
  "v_cndmask_b32_e64 v135, v135, v248, s[78:79]\n\t" \
  "v_cmp_gt_i32_e64 s[78:79], 43, %[dq]\n\t" \
  "v_cndmask_b32_e64 v136, v136, v248, s[72:73]\n\t" \
  "v_cmp_gt_i32_e64 s[72:73], 56, %[dq]\n\t" \
  "v_cndmask_b32_e64 v137, v137, v248, s[74:75]\n\t" \
  "v_cmp_gt_i32_e64 s[74:75], 57, %[dq]\n\t" \
  "v_cndmask_b32_e64 v138, v138, v248, s[76:77]\n\t" \
  "v_cmp_gt_i32_e64 s[76:77], 58, %[dq]\n\t" \
  "v_cndmask_b32_e64 v139, v139, v248, s[78:79]\n\t" \
  "v_cmp_gt_i32_e64 s[78:79], 59, %[dq]\n\t" \
  "v_cndmask_b32_e64 v140, v140, v248, s[72:73]\n\t" \
  "v_cndmask_b32_e64 v141, v141, v248, s[74:75]\n\t" \
  "v_cndmask_b32_e64 v142, v142, v248, s[76:77]\n\t" \
  "v_cndmask_b32_e64 v143, v143, v248, s[78:79]\n\t" \
  "s_nop 0\n\t" \
  "s_branch LB_PMASKB%=\n\t" \
  "LB_ROS0%=:\n\t" \
  "s_nop 11\n\t" \
  "v_mul_f32_e32 v48, v247, v48\n\t" \
  "v_mul_f32_e32 v49, v247, v49\n\t" \
  "v_mul_f32_e32 v50, v247, v50\n\t" \
  "v_mul_f32_e32 v51, v247, v51\n\t" \
  "v_mul_f32_e32 v52, v247, v52\n\t" \
  "v_mul_f32_e32 v53, v247, v53\n\t" \
  "v_mul_f32_e32 v54, v247, v54\n\t" \
  "v_mul_f32_e32 v55, v247, v55\n\t" \
  "v_mul_f32_e32 v56, v247, v56\n\t" \
  "v_mul_f32_e32 v57, v247, v57\n\t" \
  "v_mul_f32_e32 v58, v247, v58\n\t" \
  "v_mul_f32_e32 v59, v247, v59\n\t" \
  "v_mul_f32_e32 v60, v247, v60\n\t" \
  "v_mul_f32_e32 v61, v247, v61\n\t" \
  "v_mul_f32_e32 v62, v247, v62\n\t" \
  "v_mul_f32_e32 v63, v247, v63\n\t" \
  "v_mul_f32_e32 v64, v247, v64\n\t" \
  "v_mul_f32_e32 v65, v247, v65\n\t" \
  "v_mul_f32_e32 v66, v247, v66\n\t" \
  "v_mul_f32_e32 v67, v247, v67\n\t" \
  "v_mul_f32_e32 v68, v247, v68\n\t" \
  "v_mul_f32_e32 v69, v247, v69\n\t" \
  "v_mul_f32_e32 v70, v247, v70\n\t" \
  "v_mul_f32_e32 v71, v247, v71\n\t" \
  "v_mul_f32_e32 v72, v247, v72\n\t" \
  "v_mul_f32_e32 v73, v247, v73\n\t" \
  "v_mul_f32_e32 v74, v247, v74\n\t" \
  "v_mul_f32_e32 v75, v247, v75\n\t" \
  "v_mul_f32_e32 v76, v247, v76\n\t" \
  "v_mul_f32_e32 v77, v247, v77\n\t" \
  "v_mul_f32_e32 v78, v247, v78\n\t" \
  "v_mul_f32_e32 v79, v247, v79\n\t" \
  "v_mul_f32_e32 v80, v247, v80\n\t" \
  "v_mul_f32_e32 v81, v247, v81\n\t" \
  "v_mul_f32_e32 v82, v247, v82\n\t" \
  "v_mul_f32_e32 v83, v247, v83\n\t" \
  "v_mul_f32_e32 v84, v247, v84\n\t" \
  "v_mul_f32_e32 v85, v247, v85\n\t" \
  "v_mul_f32_e32 v86, v247, v86\n\t" \
  "v_mul_f32_e32 v87, v247, v87\n\t" \
  "v_mul_f32_e32 v88, v247, v88\n\t" \
  "v_mul_f32_e32 v89, v247, v89\n\t" \
  "v_mul_f32_e32 v90, v247, v90\n\t" \
  "v_mul_f32_e32 v91, v247, v91\n\t" \
  "v_mul_f32_e32 v92, v247, v92\n\t" \
  "v_mul_f32_e32 v93, v247, v93\n\t" \
  "v_mul_f32_e32 v94, v247, v94\n\t" \
  "v_mul_f32_e32 v95, v247, v95\n\t" \
  "v_mul_f32_e32 v96, v247, v96\n\t" \
  "v_mul_f32_e32 v97, v247, v97\n\t" \
  "v_mul_f32_e32 v98, v247, v98\n\t" \
  "v_mul_f32_e32 v99, v247, v99\n\t" \
  "v_mul_f32_e32 v100, v247, v100\n\t" \
  "v_mul_f32_e32 v101, v247, v101\n\t" \
  "v_mul_f32_e32 v102, v247, v102\n\t" \
  "v_mul_f32_e32 v103, v247, v103\n\t" \
  "v_mul_f32_e32 v104, v247, v104\n\t" \
  "v_mul_f32_e32 v105, v247, v105\n\t" \
  "v_mul_f32_e32 v106, v247, v106\n\t" \
  "v_mul_f32_e32 v107, v247, v107\n\t" \
  "v_mul_f32_e32 v108, v247, v108\n\t" \
  "v_mul_f32_e32 v109, v247, v109\n\t" \
  "v_mul_f32_e32 v110, v247, v110\n\t" \
  "v_mul_f32_e32 v111, v247, v111\n\t" \
  "v_mul_f32_e32 v192, v247, v192\n\t" \
  "s_mov_b32 s69, 0\n\t" \
  "s_nop 1\n\t" \
  "s_branch LB_ROBS0%=\n\t" \
  "LB_MASK00%=:\n\t" \
  "s_nop 3\n\t" \
  "v_cmp_gt_i32_e64 s[72:73], 0, %[dq]\n\t" \
  "v_cmp_gt_i32_e64 s[74:75], 1, %[dq]\n\t" \
  "v_cmp_gt_i32_e64 s[76:77], 2, %[dq]\n\t" \
  "v_cmp_gt_i32_e64 s[78:79], 3, %[dq]\n\t" \
  "v_cndmask_b32_e64 v112, v112, v248, s[72:73]\n\t" \
  "v_cmp_gt_i32_e64 s[72:73], 16, %[dq]\n\t" \
  "v_cndmask_b32_e64 v113, v113, v248, s[74:75]\n\t" \
  "v_cmp_gt_i32_e64 s[74:75], 17, %[dq]\n\t" \
  "v_cndmask_b32_e64 v114, v114, v248, s[76:77]\n\t" \
  "v_cmp_gt_i32_e64 s[76:77], 18, %[dq]\n\t" \
  "v_cndmask_b32_e64 v115, v115, v248, s[78:79]\n\t" \
  "v_cmp_gt_i32_e64 s[78:79], 19, %[dq]\n\t" \
  "v_cndmask_b32_e64 v116, v116, v248, s[72:73]\n\t" \
  "v_cmp_gt_i32_e64 s[72:73], 8, %[dq]\n\t" \
  "v_cndmask_b32_e64 v117, v117, v248, s[74:75]\n\t" \
  "v_cmp_gt_i32_e64 s[74:75], 9, %[dq]\n\t" \
  "v_cndmask_b32_e64 v118, v118, v248, s[76:77]\n\t" \
  "v_cmp_gt_i32_e64 s[76:77], 10, %[dq]\n\t" \
  "v_cndmask_b32_e64 v119, v119, v248, s[78:79]\n\t" \
  "v_cmp_gt_i32_e64 s[78:79], 11, %[dq]\n\t" \
  "v_cndmask_b32_e64 v120, v120, v248, s[72:73]\n\t" \
  "v_cmp_gt_i32_e64 s[72:73], 24, %[dq]\n\t" \
  "v_cndmask_b32_e64 v121, v121, v248, s[74:75]\n\t" \
  "v_cmp_gt_i32_e64 s[74:75], 25, %[dq]\n\t" \
  "v_cndmask_b32_e64 v122, v122, v248, s[76:77]\n\t" \
  "v_cmp_gt_i32_e64 s[76:77], 26, %[dq]\n\t" \
  "v_cndmask_b32_e64 v123, v123, v248, s[78:79]\n\t" \
  "v_cmp_gt_i32_e64 s[78:79], 27, %[dq]\n\t" \
  "v_cndmask_b32_e64 v124, v124, v248, s[72:73]\n\t" \
  "v_cndmask_b32_e64 v125, v125, v248, s[74:75]\n\t" \
  "v_cndmask_b32_e64 v126, v126, v248, s[76:77]\n\t" \
  "v_cndmask_b32_e64 v127, v127, v248, s[78:79]\n\t" \
  "s_nop 0\n\t" \
  "s_branch LB_MASKB00%=\n\t" \
  "LB_MASK01%=:\n\t" \
  "s_nop 3\n\t" \
  "v_cmp_gt_i32_e64 s[72:73], 32, %[dq]\n\t" \
  "v_cmp_gt_i32_e64 s[74:75], 33, %[dq]\n\t" \
  "v_cmp_gt_i32_e64 s[76:77], 34, %[dq]\n\t" \
  "v_cmp_gt_i32_e64 s[78:79], 35, %[dq]\n\t" \
  "v_cndmask_b32_e64 v128, v128, v248, s[72:73]\n\t" \
  "v_cmp_gt_i32_e64 s[72:73], 48, %[dq]\n\t" \
  "v_cndmask_b32_e64 v129, v129, v248, s[74:75]\n\t" \
  "v_cmp_gt_i32_e64 s[74:75], 49, %[dq]\n\t" \
  "v_cndmask_b32_e64 v130, v130, v248, s[76:77]\n\t" \
  "v_cmp_gt_i32_e64 s[76:77], 50, %[dq]\n\t" \
  "v_cndmask_b32_e64 v131, v131, v248, s[78:79]\n\t" \
  "v_cmp_gt_i32_e64 s[78:79], 51, %[dq]\n\t" \
  "v_cndmask_b32_e64 v132, v132, v248, s[72:73]\n\t" \
  "v_cmp_gt_i32_e64 s[72:73], 40, %[dq]\n\t" \
  "v_cndmask_b32_e64 v133, v133, v248, s[74:75]\n\t" \
  "v_cmp_gt_i32_e64 s[74:75], 41, %[dq]\n\t" \
  "v_cndmask_b32_e64 v134, v134, v248, s[76:77]\n\t" \
  "v_cmp_gt_i32_e64 s[76:77], 42, %[dq]\n\t" \
  "v_cndmask_b32_e64 v135, v135, v248, s[78:79]\n\t" \
  "v_cmp_gt_i32_e64 s[78:79], 43, %[dq]\n\t" \
  "v_cndmask_b32_e64 v136, v136, v248, s[72:73]\n\t" \
  "v_cmp_gt_i32_e64 s[72:73], 56, %[dq]\n\t" \
  "v_cndmask_b32_e64 v137, v137, v248, s[74:75]\n\t" \
  "v_cmp_gt_i32_e64 s[74:75], 57, %[dq]\n\t" \
  "v_cndmask_b32_e64 v138, v138, v248, s[76:77]\n\t" \
  "v_cmp_gt_i32_e64 s[76:77], 58, %[dq]\n\t" \
  "v_cndmask_b32_e64 v139, v139, v248, s[78:79]\n\t" \
  "v_cmp_gt_i32_e64 s[78:79], 59, %[dq]\n\t" \
  "v_cndmask_b32_e64 v140, v140, v248, s[72:73]\n\t" \
  "v_cndmask_b32_e64 v141, v141, v248, s[74:75]\n\t" \
  "v_cndmask_b32_e64 v142, v142, v248, s[76:77]\n\t" \
  "v_cndmask_b32_e64 v143, v143, v248, s[78:79]\n\t" \
  "s_nop 0\n\t" \
  "s_branch LB_MASKB01%=\n\t" \
  "LB_RARE0%=:\n\t" \
  "v_max_f32_e32 v246, 0, v241\n\t" \
  "v_sub_f32_e32 v240, v240, v246\n\t" \
  "v_exp_f32_e64 v247, -v246\n\t" \
  "s_mov_b32 s69, 1\n\t" \
  "s_nop 1\n\t" \
  "s_branch LB_RAREB0%=\n\t" \
  "LB_ROS1%=:\n\t" \
  "s_nop 11\n\t" \
  "v_mul_f32_e32 v48, v247, v48\n\t" \
  "v_mul_f32_e32 v49, v247, v49\n\t" \
  "v_mul_f32_e32 v50, v247, v50\n\t" \
  "v_mul_f32_e32 v51, v247, v51\n\t" \
  "v_mul_f32_e32 v52, v247, v52\n\t" \
  "v_mul_f32_e32 v53, v247, v53\n\t" \
  "v_mul_f32_e32 v54, v247, v54\n\t" \
  "v_mul_f32_e32 v55, v247, v55\n\t" \
  "v_mul_f32_e32 v56, v247, v56\n\t" \
  "v_mul_f32_e32 v57, v247, v57\n\t" \
  "v_mul_f32_e32 v58, v247, v58\n\t" \
  "v_mul_f32_e32 v59, v247, v59\n\t" \
  "v_mul_f32_e32 v60, v247, v60\n\t" \
  "v_mul_f32_e32 v61, v247, v61\n\t" \
  "v_mul_f32_e32 v62, v247, v62\n\t" \
  "v_mul_f32_e32 v63, v247, v63\n\t" \
  "v_mul_f32_e32 v64, v247, v64\n\t" \
  "v_mul_f32_e32 v65, v247, v65\n\t" \
  "v_mul_f32_e32 v66, v247, v66\n\t" \
  "v_mul_f32_e32 v67, v247, v67\n\t" \
  "v_mul_f32_e32 v68, v247, v68\n\t" \
  "v_mul_f32_e32 v69, v247, v69\n\t" \
  "v_mul_f32_e32 v70, v247, v70\n\t" \
  "v_mul_f32_e32 v71, v247, v71\n\t" \
  "v_mul_f32_e32 v72, v247, v72\n\t" \
  "v_mul_f32_e32 v73, v247, v73\n\t" \
  "v_mul_f32_e32 v74, v247, v74\n\t" \
  "v_mul_f32_e32 v75, v247, v75\n\t" \
  "v_mul_f32_e32 v76, v247, v76\n\t" \
  "v_mul_f32_e32 v77, v247, v77\n\t" \
  "v_mul_f32_e32 v78, v247, v78\n\t" \
  "v_mul_f32_e32 v79, v247, v79\n\t" \
  "v_mul_f32_e32 v80, v247, v80\n\t" \
  "v_mul_f32_e32 v81, v247, v81\n\t" \
  "v_mul_f32_e32 v82, v247, v82\n\t" \
  "v_mul_f32_e32 v83, v247, v83\n\t" \
  "v_mul_f32_e32 v84, v247, v84\n\t" \
  "v_mul_f32_e32 v85, v247, v85\n\t" \
  "v_mul_f32_e32 v86, v247, v86\n\t" \
  "v_mul_f32_e32 v87, v247, v87\n\t" \
  "v_mul_f32_e32 v88, v247, v88\n\t" \
  "v_mul_f32_e32 v89, v247, v89\n\t" \
  "v_mul_f32_e32 v90, v247, v90\n\t" \
  "v_mul_f32_e32 v91, v247, v91\n\t" \
  "v_mul_f32_e32 v92, v247, v92\n\t" \
  "v_mul_f32_e32 v93, v247, v93\n\t" \
  "v_mul_f32_e32 v94, v247, v94\n\t" \
  "v_mul_f32_e32 v95, v247, v95\n\t" \
  "v_mul_f32_e32 v96, v247, v96\n\t" \
  "v_mul_f32_e32 v97, v247, v97\n\t" \
  "v_mul_f32_e32 v98, v247, v98\n\t" \
  "v_mul_f32_e32 v99, v247, v99\n\t" \
  "v_mul_f32_e32 v100, v247, v100\n\t" \
  "v_mul_f32_e32 v101, v247, v101\n\t" \
  "v_mul_f32_e32 v102, v247, v102\n\t" \
  "v_mul_f32_e32 v103, v247, v103\n\t" \
  "v_mul_f32_e32 v104, v247, v104\n\t" \
  "v_mul_f32_e32 v105, v247, v105\n\t" \
  "v_mul_f32_e32 v106, v247, v106\n\t" \
  "v_mul_f32_e32 v107, v247, v107\n\t" \
  "v_mul_f32_e32 v108, v247, v108\n\t" \
  "v_mul_f32_e32 v109, v247, v109\n\t" \
  "v_mul_f32_e32 v110, v247, v110\n\t" \
  "v_mul_f32_e32 v111, v247, v111\n\t" \
  "v_mul_f32_e32 v192, v247, v192\n\t" \
  "s_mov_b32 s69, 0\n\t" \
  "s_nop 1\n\t" \
  "s_branch LB_ROBS1%=\n\t" \
  "LB_MASK10%=:\n\t" \
  "s_nop 3\n\t" \
  "v_cmp_gt_i32_e64 s[72:73], 0, %[dq]\n\t" \
  "v_cmp_gt_i32_e64 s[74:75], 1, %[dq]\n\t" \
  "v_cmp_gt_i32_e64 s[76:77], 2, %[dq]\n\t" \
  "v_cmp_gt_i32_e64 s[78:79], 3, %[dq]\n\t" \
  "v_cndmask_b32_e64 v144, v144, v248, s[72:73]\n\t" \
  "v_cmp_gt_i32_e64 s[72:73], 16, %[dq]\n\t" \
  "v_cndmask_b32_e64 v145, v145, v248, s[74:75]\n\t" \
  "v_cmp_gt_i32_e64 s[74:75], 17, %[dq]\n\t" \
  "v_cndmask_b32_e64 v146, v146, v248, s[76:77]\n\t" \
  "v_cmp_gt_i32_e64 s[76:77], 18, %[dq]\n\t" \
  "v_cndmask_b32_e64 v147, v147, v248, s[78:79]\n\t" \
  "v_cmp_gt_i32_e64 s[78:79], 19, %[dq]\n\t" \
  "v_cndmask_b32_e64 v148, v148, v248, s[72:73]\n\t" \
  "v_cmp_gt_i32_e64 s[72:73], 8, %[dq]\n\t" \
  "v_cndmask_b32_e64 v149, v149, v248, s[74:75]\n\t" \
  "v_cmp_gt_i32_e64 s[74:75], 9, %[dq]\n\t" \
  "v_cndmask_b32_e64 v150, v150, v248, s[76:77]\n\t" \
  "v_cmp_gt_i32_e64 s[76:77], 10, %[dq]\n\t" \
  "v_cndmask_b32_e64 v151, v151, v248, s[78:79]\n\t" \
  "v_cmp_gt_i32_e64 s[78:79], 11, %[dq]\n\t" \
  "v_cndmask_b32_e64 v152, v152, v248, s[72:73]\n\t" \
  "v_cmp_gt_i32_e64 s[72:73], 24, %[dq]\n\t" \
  "v_cndmask_b32_e64 v153, v153, v248, s[74:75]\n\t" \
  "v_cmp_gt_i32_e64 s[74:75], 25, %[dq]\n\t" \
  "v_cndmask_b32_e64 v154, v154, v248, s[76:77]\n\t" \
  "v_cmp_gt_i32_e64 s[76:77], 26, %[dq]\n\t" \
  "v_cndmask_b32_e64 v155, v155, v248, s[78:79]\n\t" \
  "v_cmp_gt_i32_e64 s[78:79], 27, %[dq]\n\t" \
  "v_cndmask_b32_e64 v156, v156, v248, s[72:73]\n\t" \
  "v_cndmask_b32_e64 v157, v157, v248, s[74:75]\n\t" \
  "v_cndmask_b32_e64 v158, v158, v248, s[76:77]\n\t" \
  "v_cndmask_b32_e64 v159, v159, v248, s[78:79]\n\t" \
  "s_nop 0\n\t" \
  "s_branch LB_MASKB10%=\n\t" \
  "LB_MASK11%=:\n\t" \
  "s_nop 3\n\t" \
  "v_cmp_gt_i32_e64 s[72:73], 32, %[dq]\n\t" \
  "v_cmp_gt_i32_e64 s[74:75], 33, %[dq]\n\t" \
  "v_cmp_gt_i32_e64 s[76:77], 34, %[dq]\n\t" \
  "v_cmp_gt_i32_e64 s[78:79], 35, %[dq]\n\t" \
  "v_cndmask_b32_e64 v160, v160, v248, s[72:73]\n\t" \
  "v_cmp_gt_i32_e64 s[72:73], 48, %[dq]\n\t" \
  "v_cndmask_b32_e64 v161, v161, v248, s[74:75]\n\t" \
  "v_cmp_gt_i32_e64 s[74:75], 49, %[dq]\n\t" \
  "v_cndmask_b32_e64 v162, v162, v248, s[76:77]\n\t" \
  "v_cmp_gt_i32_e64 s[76:77], 50, %[dq]\n\t" \
  "v_cndmask_b32_e64 v163, v163, v248, s[78:79]\n\t" \
  "v_cmp_gt_i32_e64 s[78:79], 51, %[dq]\n\t" \
  "v_cndmask_b32_e64 v164, v164, v248, s[72:73]\n\t" \
  "v_cmp_gt_i32_e64 s[72:73], 40, %[dq]\n\t" \
  "v_cndmask_b32_e64 v165, v165, v248, s[74:75]\n\t" \
  "v_cmp_gt_i32_e64 s[74:75], 41, %[dq]\n\t" \
  "v_cndmask_b32_e64 v166, v166, v248, s[76:77]\n\t" \
  "v_cmp_gt_i32_e64 s[76:77], 42, %[dq]\n\t" \
  "v_cndmask_b32_e64 v167, v167, v248, s[78:79]\n\t" \
  "v_cmp_gt_i32_e64 s[78:79], 43, %[dq]\n\t" \
  "v_cndmask_b32_e64 v168, v168, v248, s[72:73]\n\t" \
  "v_cmp_gt_i32_e64 s[72:73], 56, %[dq]\n\t" \
  "v_cndmask_b32_e64 v169, v169, v248, s[74:75]\n\t" \
  "v_cmp_gt_i32_e64 s[74:75], 57, %[dq]\n\t" \
  "v_cndmask_b32_e64 v170, v170, v248, s[76:77]\n\t" \
  "v_cmp_gt_i32_e64 s[76:77], 58, %[dq]\n\t" \
  "v_cndmask_b32_e64 v171, v171, v248, s[78:79]\n\t" \
  "v_cmp_gt_i32_e64 s[78:79], 59, %[dq]\n\t" \
  "v_cndmask_b32_e64 v172, v172, v248, s[72:73]\n\t" \
  "v_cndmask_b32_e64 v173, v173, v248, s[74:75]\n\t" \
  "v_cndmask_b32_e64 v174, v174, v248, s[76:77]\n\t" \
  "v_cndmask_b32_e64 v175, v175, v248, s[78:79]\n\t" \
  "s_nop 0\n\t" \
  "s_branch LB_MASKB11%=\n\t" \
  "LB_RARE1%=:\n\t" \
  "v_max_f32_e32 v246, 0, v241\n\t" \
  "v_sub_f32_e32 v240, v240, v246\n\t" \
  "v_exp_f32_e64 v247, -v246\n\t" \
  "s_mov_b32 s69, 1\n\t" \
  "s_nop 1\n\t" \
  "s_branch LB_RAREB1%=\n\t" \
  "LB_ROD0%=:\n\t" \
  "s_nop 11\n\t" \
  "v_mul_f32_e32 v48, v247, v48\n\t" \
  "v_mul_f32_e32 v49, v247, v49\n\t" \
  "v_mul_f32_e32 v50, v247, v50\n\t" \
  "v_mul_f32_e32 v51, v247, v51\n\t" \
  "v_mul_f32_e32 v52, v247, v52\n\t" \
  "v_mul_f32_e32 v53, v247, v53\n\t" \
  "v_mul_f32_e32 v54, v247, v54\n\t" \
  "v_mul_f32_e32 v55, v247, v55\n\t" \
  "v_mul_f32_e32 v56, v247, v56\n\t" \
  "v_mul_f32_e32 v57, v247, v57\n\t" \
  "v_mul_f32_e32 v58, v247, v58\n\t" \
  "v_mul_f32_e32 v59, v247, v59\n\t" \
  "v_mul_f32_e32 v60, v247, v60\n\t" \
  "v_mul_f32_e32 v61, v247, v61\n\t" \
  "v_mul_f32_e32 v62, v247, v62\n\t" \
  "v_mul_f32_e32 v63, v247, v63\n\t" \
  "v_mul_f32_e32 v64, v247, v64\n\t" \
  "v_mul_f32_e32 v65, v247, v65\n\t" \
  "v_mul_f32_e32 v66, v247, v66\n\t" \
  "v_mul_f32_e32 v67, v247, v67\n\t" \
  "v_mul_f32_e32 v68, v247, v68\n\t" \
  "v_mul_f32_e32 v69, v247, v69\n\t" \
  "v_mul_f32_e32 v70, v247, v70\n\t" \
  "v_mul_f32_e32 v71, v247, v71\n\t" \
  "v_mul_f32_e32 v72, v247, v72\n\t" \
  "v_mul_f32_e32 v73, v247, v73\n\t" \
  "v_mul_f32_e32 v74, v247, v74\n\t" \
  "v_mul_f32_e32 v75, v247, v75\n\t" \
  "v_mul_f32_e32 v76, v247, v76\n\t" \
  "v_mul_f32_e32 v77, v247, v77\n\t" \
  "v_mul_f32_e32 v78, v247, v78\n\t" \
  "v_mul_f32_e32 v79, v247, v79\n\t" \
  "v_mul_f32_e32 v80, v247, v80\n\t" \
  "v_mul_f32_e32 v81, v247, v81\n\t" \
  "v_mul_f32_e32 v82, v247, v82\n\t" \
  "v_mul_f32_e32 v83, v247, v83\n\t" \
  "v_mul_f32_e32 v84, v247, v84\n\t" \
  "v_mul_f32_e32 v85, v247, v85\n\t" \
  "v_mul_f32_e32 v86, v247, v86\n\t" \
  "v_mul_f32_e32 v87, v247, v87\n\t" \
  "v_mul_f32_e32 v88, v247, v88\n\t" \
  "v_mul_f32_e32 v89, v247, v89\n\t" \
  "v_mul_f32_e32 v90, v247, v90\n\t" \
  "v_mul_f32_e32 v91, v247, v91\n\t" \
  "v_mul_f32_e32 v92, v247, v92\n\t" \
  "v_mul_f32_e32 v93, v247, v93\n\t" \
  "v_mul_f32_e32 v94, v247, v94\n\t" \
  "v_mul_f32_e32 v95, v247, v95\n\t" \
  "v_mul_f32_e32 v96, v247, v96\n\t" \
  "v_mul_f32_e32 v97, v247, v97\n\t" \
  "v_mul_f32_e32 v98, v247, v98\n\t" \
  "v_mul_f32_e32 v99, v247, v99\n\t" \
  "v_mul_f32_e32 v100, v247, v100\n\t" \
  "v_mul_f32_e32 v101, v247, v101\n\t" \
  "v_mul_f32_e32 v102, v247, v102\n\t" \
  "v_mul_f32_e32 v103, v247, v103\n\t" \
  "v_mul_f32_e32 v104, v247, v104\n\t" \
  "v_mul_f32_e32 v105, v247, v105\n\t" \
  "v_mul_f32_e32 v106, v247, v106\n\t" \
  "v_mul_f32_e32 v107, v247, v107\n\t" \
  "v_mul_f32_e32 v108, v247, v108\n\t" \
  "v_mul_f32_e32 v109, v247, v109\n\t" \
  "v_mul_f32_e32 v110, v247, v110\n\t" \
  "v_mul_f32_e32 v111, v247, v111\n\t" \
  "v_mul_f32_e32 v192, v247, v192\n\t" \
  "s_mov_b32 s69, 0\n\t" \
  "s_nop 1\n\t" \
  "s_branch LB_ROBD0%=\n\t" \
  "LB_ROD1%=:\n\t" \
  "s_nop 11\n\t" \
  "v_mul_f32_e32 v48, v247, v48\n\t" \
  "v_mul_f32_e32 v49, v247, v49\n\t" \
  "v_mul_f32_e32 v50, v247, v50\n\t" \
  "v_mul_f32_e32 v51, v247, v51\n\t" \
  "v_mul_f32_e32 v52, v247, v52\n\t" \
  "v_mul_f32_e32 v53, v247, v53\n\t" \
  "v_mul_f32_e32 v54, v247, v54\n\t" \
  "v_mul_f32_e32 v55, v247, v55\n\t" \
  "v_mul_f32_e32 v56, v247, v56\n\t" \
  "v_mul_f32_e32 v57, v247, v57\n\t" \
  "v_mul_f32_e32 v58, v247, v58\n\t" \
  "v_mul_f32_e32 v59, v247, v59\n\t" \
  "v_mul_f32_e32 v60, v247, v60\n\t" \
  "v_mul_f32_e32 v61, v247, v61\n\t" \
  "v_mul_f32_e32 v62, v247, v62\n\t" \
  "v_mul_f32_e32 v63, v247, v63\n\t" \
  "v_mul_f32_e32 v64, v247, v64\n\t" \
  "v_mul_f32_e32 v65, v247, v65\n\t" \
  "v_mul_f32_e32 v66, v247, v66\n\t" \
  "v_mul_f32_e32 v67, v247, v67\n\t" \
  "v_mul_f32_e32 v68, v247, v68\n\t" \
  "v_mul_f32_e32 v69, v247, v69\n\t" \
  "v_mul_f32_e32 v70, v247, v70\n\t" \
  "v_mul_f32_e32 v71, v247, v71\n\t" \
  "v_mul_f32_e32 v72, v247, v72\n\t" \
  "v_mul_f32_e32 v73, v247, v73\n\t" \
  "v_mul_f32_e32 v74, v247, v74\n\t" \
  "v_mul_f32_e32 v75, v247, v75\n\t" \
  "v_mul_f32_e32 v76, v247, v76\n\t" \
  "v_mul_f32_e32 v77, v247, v77\n\t" \
  "v_mul_f32_e32 v78, v247, v78\n\t" \
  "v_mul_f32_e32 v79, v247, v79\n\t" \
  "v_mul_f32_e32 v80, v247, v80\n\t" \
  "v_mul_f32_e32 v81, v247, v81\n\t" \
  "v_mul_f32_e32 v82, v247, v82\n\t" \
  "v_mul_f32_e32 v83, v247, v83\n\t" \
  "v_mul_f32_e32 v84, v247, v84\n\t" \
  "v_mul_f32_e32 v85, v247, v85\n\t" \
  "v_mul_f32_e32 v86, v247, v86\n\t" \
  "v_mul_f32_e32 v87, v247, v87\n\t" \
  "v_mul_f32_e32 v88, v247, v88\n\t" \
  "v_mul_f32_e32 v89, v247, v89\n\t" \
  "v_mul_f32_e32 v90, v247, v90\n\t" \
  "v_mul_f32_e32 v91, v247, v91\n\t" \
  "v_mul_f32_e32 v92, v247, v92\n\t" \
  "v_mul_f32_e32 v93, v247, v93\n\t" \
  "v_mul_f32_e32 v94, v247, v94\n\t" \
  "v_mul_f32_e32 v95, v247, v95\n\t" \
  "v_mul_f32_e32 v96, v247, v96\n\t" \
  "v_mul_f32_e32 v97, v247, v97\n\t" \
  "v_mul_f32_e32 v98, v247, v98\n\t" \
  "v_mul_f32_e32 v99, v247, v99\n\t" \
  "v_mul_f32_e32 v100, v247, v100\n\t" \
  "v_mul_f32_e32 v101, v247, v101\n\t" \
  "v_mul_f32_e32 v102, v247, v102\n\t" \
  "v_mul_f32_e32 v103, v247, v103\n\t" \
  "v_mul_f32_e32 v104, v247, v104\n\t" \
  "v_mul_f32_e32 v105, v247, v105\n\t" \
  "v_mul_f32_e32 v106, v247, v106\n\t" \
  "v_mul_f32_e32 v107, v247, v107\n\t" \
  "v_mul_f32_e32 v108, v247, v108\n\t" \
  "v_mul_f32_e32 v109, v247, v109\n\t" \
  "v_mul_f32_e32 v110, v247, v110\n\t" \
  "v_mul_f32_e32 v111, v247, v111\n\t" \
  "v_mul_f32_e32 v192, v247, v192\n\t" \
  "s_mov_b32 s69, 0\n\t" \
  "s_nop 1\n\t" \
  "s_branch LB_ROBD1%=\n\t" \
  "L_END%=:\n\t" \
  "s_setprio 0\n\t"
#define ATTNM_CLOB "v48", "v49", "v50", "v51", "v52", "v53", "v54", "v55", "v56", "v57", "v58", "v59", "v60", "v61", "v62", "v63", "v64", "v65", "v66", "v67", "v68", "v69", "v70", "v71", "v72", "v73", "v74", "v75", "v76", "v77", "v78", "v79", "v80", "v81", "v82", "v83", "v84", "v85", "v86", "v87", "v88", "v89", "v90", "v91", "v92", "v93", "v94", "v95", "v96", "v97", "v98", "v99", "v100", "v101", "v102", "v103", "v104", "v105", "v106", "v107", "v108", "v109", "v110", "v111", "v112", "v113", "v114", "v115", "v116", "v117", "v118", "v119", "v120", "v121", "v122", "v123", "v124", "v125", "v126", "v127", "v128", "v129", "v130", "v131", "v132", "v133", "v134", "v135", "v136", "v137", "v138", "v139", "v140", "v141", "v142", "v143", "v144", "v145", "v146", "v147", "v148", "v149", "v150", "v151", "v152", "v153", "v154", "v155", "v156", "v157", "v158", "v159", "v160", "v161", "v162", "v163", "v164", "v165", "v166", "v167", "v168", "v169", "v170", "v171", "v172", "v173", "v174", "v175", "v176", "v177", "v178", "v179", "v180", "v181", "v182", "v183", "v184", "v185", "v186", "v187", "v188", "v189", "v190", "v191", "v192", "v193", "v194", "v195", "v196", "v197", "v198", "v199", "v200", "v201", "v202", "v203", "v204", "v205", "v206", "v207", "v208", "v209", "v210", "v211", "v212", "v213", "v214", "v215", "v216", "v217", "v218", "v219", "v220", "v221", "v222", "v223", "v224", "v225", "v226", "v227", "v228", "v229", "v230", "v231", "v232", "v233", "v234", "v235", "v236", "v237", "v238", "v239", "v240", "v241", "v242", "v243", "v244", "v245", "v246", "v247", "v248", "v249", "v250", "v251", "v252", "v253", "v254", "v255", "s48", "s49", "s50", "s51", "s52", "s53", "s54", "s55", "s56", "s57", "s58", "s59", "s60", "s61", "s62", "s63", "s64", "s65", "s66", "s67", "s68", "s69", "s70", "s71", "s72", "s73", "s74", "s75", "s76", "s77", "s78", "s79", "s80", "vcc", "scc", "memory"

#define FRONT_PRE \
  "v_add_u32_e32 v248, 7168, %[va]\n\t" \
  "v_add_u32_e32 v249, 14336, %[va]\n\t" \
  "v_add_u32_e32 v250, 21504, %[va]\n\t" \
  "v_add_u32_e32 v251, 31744, %[vb]\n\t" \
  "s_add_u32 m0, %[db], 0\n\t" \
  "s_mov_b64 s[66:67], %[gb0]\n\t" \
  "global_load_lds_dwordx4 %[vb], s[66:67] offset:0 nt\n\t" \
  "global_load_lds_dwordx4 v251, s[66:67] offset:1024 nt\n\t" \
  "s_add_u32 m0, %[da], 0\n\t" \
  "s_mov_b64 s[64:65], %[ga]\n\t" \
  "global_load_lds_dwordx4 %[va], s[64:65] offset:0\n\t" \
  "global_load_lds_dwordx4 v248, s[64:65] offset:1024\n\t" \
  "global_load_lds_dwordx4 v249, s[64:65] offset:2048\n\t" \
  "global_load_lds_dwordx4 v250, s[64:65] offset:3072\n\t" \
  "s_add_u32 m0, %[db], 49152\n\t" \
  "s_add_u32 s66, s66, 524288\n\t" \
  "s_addc_u32 s67, s67, 0\n\t" \
  "global_load_lds_dwordx4 %[vb], s[66:67] offset:0 nt\n\t" \
  "global_load_lds_dwordx4 v251, s[66:67] offset:1024 nt\n\t" \
  "s_add_u32 m0, %[da], 49152\n\t" \
  "s_add_u32 s64, s64, 128\n\t" \
  "s_addc_u32 s65, s65, 0\n\t" \
  "global_load_lds_dwordx4 %[va], s[64:65] offset:0\n\t" \
  "global_load_lds_dwordx4 v248, s[64:65] offset:1024\n\t" \
  "global_load_lds_dwordx4 v249, s[64:65] offset:2048\n\t" \
  "global_load_lds_dwordx4 v250, s[64:65] offset:3072\n\t"
#define FRONT_ASM \
  "v_add_u32_e32 v248, 7168, %[va]\n\t" \
  "v_add_u32_e32 v249, 14336, %[va]\n\t" \
  "v_add_u32_e32 v250, 21504, %[va]\n\t" \
  "v_add_u32_e32 v251, 31744, %[vb]\n\t" \
  "v_add_u32_e32 v252, 31744, %[wv0]\n\t" \
  "v_add_u32_e32 v253, 0x10000, %[wrd]\n\t" \
  "v_add_u32_e32 v246, 0x20000, %[wrd]\n\t" \
  "v_mov_b32_e32 v240, 0\n\t" \
  "v_mov_b32_e32 v241, 1\n\t" \
  "s_mov_b64 s[64:65], %[ga]\n\t" \
  "s_mov_b64 s[66:67], %[gb0]\n\t" \
  "s_add_u32 s64, s64, 128\n\t" \
  "s_addc_u32 s65, s65, 0\n\t" \
  "s_add_u32 s66, s66, 524288\n\t" \
  "s_addc_u32 s67, s67, 0\n\t" \
  "s_waitcnt vmcnt(8)\n\t" \
  "s_barrier\n\t" \
  "s_cmp_ge_u32 %[wid], 4\n\t" \
  "s_cbranch_scc1 L_CLS_T%=\n\t" \
  "ds_read_b64_tr_b16 v[192:193], %[br0] offset:0\n\t" \
  "ds_read_b64_tr_b16 v[194:195], %[br0] offset:1024\n\t" \
  "ds_read_b128 v[176:179], %[ar0] offset:0\n\t" \
  "s_add_u32 m0, %[db], 98304\n\t" \
  "s_add_u32 s66, s66, 524288\n\t" \
  "s_addc_u32 s67, s67, 0\n\t" \
  "global_load_lds_dwordx4 %[vb], s[66:67] offset:0 nt\n\t" \
  "ds_read_b64_tr_b16 v[196:197], %[br1] offset:0\n\t" \
  "global_load_lds_dwordx4 v251, s[66:67] offset:1024 nt\n\t" \
  "ds_read_b64_tr_b16 v[198:199], %[br1] offset:1024\n\t" \
  "ds_read_b64_tr_b16 v[200:201], %[br0] offset:256\n\t" \
  "s_add_u32 m0, %[da], 98304\n\t" \
  "s_add_u32 s64, s64, 128\n\t" \
  "s_addc_u32 s65, s65, 0\n\t" \
  "global_load_lds_dwordx4 %[va], s[64:65] offset:0\n\t" \
  "ds_read_b64_tr_b16 v[202:203], %[br0] offset:1280\n\t" \
  "global_load_lds_dwordx4 v248, s[64:65] offset:1024\n\t" \
  "ds_read_b64_tr_b16 v[204:205], %[br1] offset:256\n\t" \
  "ds_read_b64_tr_b16 v[206:207], %[br1] offset:1280\n\t" \
  "global_load_lds_dwordx4 v249, s[64:65] offset:2048\n\t" \
  "ds_read_b128 v[180:183], %[ar0] offset:2048\n\t" \
  "global_load_lds_dwordx4 v250, s[64:65] offset:3072\n\t" \
  "ds_read_b128 v[184:187], %[ar0] offset:4096\n\t" \
  "ds_read_b128 v[188:191], %[ar0] offset:6144\n\t" \
  "s_add_u32 m0, %[db], 106496\n\t" \
  "s_add_u32 s68, s66, 262144\n\t" \
  "s_addc_u32 s69, s67, 0\n\t" \
  "global_load_lds_dwordx4 %[vb], s[68:69] offset:0 nt\n\t" \
  "global_load_lds_dwordx4 v251, s[68:69] offset:1024 nt\n\t" \
  "s_add_u32 m0, %[da], 114688\n\t" \
  "s_add_u32 s68, s64, 131072\n\t" \
  "s_addc_u32 s69, s65, 0\n\t" \
  "global_load_lds_dwordx4 %[va], s[68:69] offset:0\n\t" \
  "global_load_lds_dwordx4 v248, s[68:69] offset:1024\n\t" \
  "s_waitcnt lgkmcnt(0)\n\t" \
  "v_mfma_f32_16x16x32_bf16 v[48:51], v[192:195], v[176:179], 0\n\t" \
  "ds_read_b64_tr_b16 v[224:225], %[br0] offset:8192\n\t" \
  "global_load_lds_dwordx4 v249, s[68:69] offset:2048\n\t" \
  "v_mfma_f32_16x16x32_bf16 v[52:55], v[196:199], v[176:179], 0\n\t" \
  "ds_read_b64_tr_b16 v[226:227], %[br0] offset:9216\n\t" \
  "global_load_lds_dwordx4 v250, s[68:69] offset:3072\n\t" \
  "v_mfma_f32_16x16x32_bf16 v[56:59], v[200:203], v[176:179], 0\n\t" \
  "ds_read_b128 v[208:211], %[ar1] offset:0\n\t" \
  "v_mfma_f32_16x16x32_bf16 v[60:63], v[204:207], v[176:179], 0\n\t" \
  "ds_read_b64_tr_b16 v[228:229], %[br1] offset:8192\n\t" \
  "v_mfma_f32_16x16x32_bf16 v[64:67], v[192:195], v[180:183], 0\n\t" \
  "ds_read_b64_tr_b16 v[230:231], %[br1] offset:9216\n\t" \
  "v_mfma_f32_16x16x32_bf16 v[68:71], v[196:199], v[180:183], 0\n\t" \
  "ds_read_b64_tr_b16 v[232:233], %[br0] offset:8448\n\t" \
  "v_mfma_f32_16x16x32_bf16 v[72:75], v[200:203], v[180:183], 0\n\t" \
  "ds_read_b64_tr_b16 v[234:235], %[br0] offset:9472\n\t" \
  "v_mfma_f32_16x16x32_bf16 v[76:79], v[204:207], v[180:183], 0\n\t" \
  "ds_read_b64_tr_b16 v[236:237], %[br1] offset:8448\n\t" \
  "v_mfma_f32_16x16x32_bf16 v[80:83], v[192:195], v[184:187], 0\n\t" \
  "ds_read_b64_tr_b16 v[238:239], %[br1] offset:9472\n\t" \
  "v_mfma_f32_16x16x32_bf16 v[84:87], v[196:199], v[184:187], 0\n\t" \
  "ds_read_b128 v[212:215], %[ar1] offset:2048\n\t" \
  "v_mfma_f32_16x16x32_bf16 v[88:91], v[200:203], v[184:187], 0\n\t" \
  "ds_read_b128 v[216:219], %[ar1] offset:4096\n\t" \
  "v_mfma_f32_16x16x32_bf16 v[92:95], v[204:207], v[184:187], 0\n\t" \
  "ds_read_b128 v[220:223], %[ar1] offset:6144\n\t" \
  "v_mfma_f32_16x16x32_bf16 v[96:99], v[192:195], v[188:191], 0\n\t" \
  "v_mfma_f32_16x16x32_bf16 v[100:103], v[196:199], v[188:191], 0\n\t" \
  "v_mfma_f32_16x16x32_bf16 v[104:107], v[200:203], v[188:191], 0\n\t" \
  "v_mfma_f32_16x16x32_bf16 v[108:111], v[204:207], v[188:191], 0\n\t" \
  "s_waitcnt vmcnt(14) lgkmcnt(0)\n\t" \
  "s_barrier\n\t" \
  "v_mfma_f32_16x16x32_bf16 v[48:51], v[224:227], v[208:211], v[48:51]\n\t" \
  "v_add_u32_e32 %[br0], 49152, %[br0]\n\t" \
  "v_add_u32_e32 %[br1], 49152, %[br1]\n\t" \
  "v_add_u32_e32 %[ar0], 49152, %[ar0]\n\t" \
  "v_add_u32_e32 %[ar1], 49152, %[ar1]\n\t" \
  "v_mfma_f32_16x16x32_bf16 v[52:55], v[228:231], v[208:211], v[52:55]\n\t" \
  "ds_read_b64_tr_b16 v[192:193], %[br0] offset:0\n\t" \
  "ds_read_b64_tr_b16 v[194:195], %[br0] offset:1024\n\t" \
  "s_add_u32 m0, %[db], 0\n\t" \
  "s_add_u32 s66, s66, 524288\n\t" \
  "s_addc_u32 s67, s67, 0\n\t" \
  "global_load_lds_dwordx4 %[vb], s[66:67] offset:0 nt\n\t" \
  "v_mfma_f32_16x16x32_bf16 v[56:59], v[232:235], v[208:211], v[56:59]\n\t" \
  "ds_read_b128 v[176:179], %[ar0] offset:0\n\t" \
  "global_load_lds_dwordx4 v251, s[66:67] offset:1024 nt\n\t" \
  "v_mfma_f32_16x16x32_bf16 v[60:63], v[236:239], v[208:211], v[60:63]\n\t" \
  "ds_read_b64_tr_b16 v[196:197], %[br1] offset:0\n\t" \
  "v_mfma_f32_16x16x32_bf16 v[64:67], v[224:227], v[212:215], v[64:67]\n\t" \
  "ds_read_b64_tr_b16 v[198:199], %[br1] offset:1024\n\t" \
  "s_add_u32 m0, %[da], 0\n\t" \
  "s_add_u32 s64, s64, 128\n\t" \
  "s_addc_u32 s65, s65, 0\n\t" \
  "global_load_lds_dwordx4 %[va], s[64:65] offset:0\n\t" \
  "v_mfma_f32_16x16x32_bf16 v[68:71], v[228:231], v[212:215], v[68:71]\n\t" \
  "ds_read_b64_tr_b16 v[200:201], %[br0] offset:256\n\t" \
  "global_load_lds_dwordx4 v248, s[64:65] offset:1024\n\t" \
  "v_mfma_f32_16x16x32_bf16 v[72:75], v[232:235], v[212:215], v[72:75]\n\t" \
  "ds_read_b64_tr_b16 v[202:203], %[br0] offset:1280\n\t" \
  "v_mfma_f32_16x16x32_bf16 v[76:79], v[236:239], v[212:215], v[76:79]\n\t" \
  "ds_read_b64_tr_b16 v[204:205], %[br1] offset:256\n\t" \
  "global_load_lds_dwordx4 v249, s[64:65] offset:2048\n\t" \
  "v_mfma_f32_16x16x32_bf16 v[80:83], v[224:227], v[216:219], v[80:83]\n\t" \
  "ds_read_b64_tr_b16 v[206:207], %[br1] offset:1280\n\t" \
  "global_load_lds_dwordx4 v250, s[64:65] offset:3072\n\t" \
  "v_mfma_f32_16x16x32_bf16 v[84:87], v[228:231], v[216:219], v[84:87]\n\t" \
  "ds_read_b128 v[180:183], %[ar0] offset:2048\n\t" \
  "v_mfma_f32_16x16x32_bf16 v[88:91], v[232:235], v[216:219], v[88:91]\n\t" \
  "ds_read_b128 v[184:187], %[ar0] offset:4096\n\t" \
  "s_add_u32 m0, %[db], 8192\n\t" \
  "s_add_u32 s68, s66, 262144\n\t" \
  "s_addc_u32 s69, s67, 0\n\t" \
  "global_load_lds_dwordx4 %[vb], s[68:69] offset:0 nt\n\t" \
  "v_mfma_f32_16x16x32_bf16 v[92:95], v[236:239], v[216:219], v[92:95]\n\t" \
  "ds_read_b128 v[188:191], %[ar0] offset:6144\n\t" \
  "global_load_lds_dwordx4 v251, s[68:69] offset:1024 nt\n\t" \
  "v_mfma_f32_16x16x32_bf16 v[96:99], v[224:227], v[220:223], v[96:99]\n\t" \
  "v_mfma_f32_16x16x32_bf16 v[100:103], v[228:231], v[220:223], v[100:103]\n\t" \
  "s_add_u32 m0, %[da], 16384\n\t" \
  "s_add_u32 s68, s64, 131072\n\t" \
  "s_addc_u32 s69, s65, 0\n\t" \
  "global_load_lds_dwordx4 %[va], s[68:69] offset:0\n\t" \
  "v_mfma_f32_16x16x32_bf16 v[104:107], v[232:235], v[220:223], v[104:107]\n\t" \
  "global_load_lds_dwordx4 v248, s[68:69] offset:1024\n\t" \
  "v_mfma_f32_16x16x32_bf16 v[108:111], v[236:239], v[220:223], v[108:111]\n\t" \
  "s_waitcnt lgkmcnt(0)\n\t" \
  "v_mfma_f32_16x16x32_bf16 v[48:51], v[192:195], v[176:179], v[48:51]\n\t" \
  "ds_read_b64_tr_b16 v[224:225], %[br0] offset:8192\n\t" \
  "global_load_lds_dwordx4 v249, s[68:69] offset:2048\n\t" \
  "v_mfma_f32_16x16x32_bf16 v[52:55], v[196:199], v[176:179], v[52:55]\n\t" \
  "ds_read_b64_tr_b16 v[226:227], %[br0] offset:9216\n\t" \
  "global_load_lds_dwordx4 v250, s[68:69] offset:3072\n\t" \
  "v_mfma_f32_16x16x32_bf16 v[56:59], v[200:203], v[176:179], v[56:59]\n\t" \
  "ds_read_b128 v[208:211], %[ar1] offset:0\n\t" \
  "v_mfma_f32_16x16x32_bf16 v[60:63], v[204:207], v[176:179], v[60:63]\n\t" \
  "ds_read_b64_tr_b16 v[228:229], %[br1] offset:8192\n\t" \
  "v_mfma_f32_16x16x32_bf16 v[64:67], v[192:195], v[180:183], v[64:67]\n\t" \
  "ds_read_b64_tr_b16 v[230:231], %[br1] offset:9216\n\t" \
  "v_mfma_f32_16x16x32_bf16 v[68:71], v[196:199], v[180:183], v[68:71]\n\t" \
  "ds_read_b64_tr_b16 v[232:233], %[br0] offset:8448\n\t" \
  "v_mfma_f32_16x16x32_bf16 v[72:75], v[200:203], v[180:183], v[72:75]\n\t" \
  "ds_read_b64_tr_b16 v[234:235], %[br0] offset:9472\n\t" \
  "v_mfma_f32_16x16x32_bf16 v[76:79], v[204:207], v[180:183], v[76:79]\n\t" \
  "ds_read_b64_tr_b16 v[236:237], %[br1] offset:8448\n\t" \
  "v_mfma_f32_16x16x32_bf16 v[80:83], v[192:195], v[184:187], v[80:83]\n\t" \
  "ds_read_b64_tr_b16 v[238:239], %[br1] offset:9472\n\t" \
  "v_mfma_f32_16x16x32_bf16 v[84:87], v[196:199], v[184:187], v[84:87]\n\t" \
  "ds_read_b128 v[212:215], %[ar1] offset:2048\n\t" \
  "v_mfma_f32_16x16x32_bf16 v[88:91], v[200:203], v[184:187], v[88:91]\n\t" \
  "ds_read_b128 v[216:219], %[ar1] offset:4096\n\t" \
  "v_mfma_f32_16x16x32_bf16 v[92:95], v[204:207], v[184:187], v[92:95]\n\t" \
  "ds_read_b128 v[220:223], %[ar1] offset:6144\n\t" \
  "v_mfma_f32_16x16x32_bf16 v[96:99], v[192:195], v[188:191], v[96:99]\n\t" \
  "v_mfma_f32_16x16x32_bf16 v[100:103], v[196:199], v[188:191], v[100:103]\n\t" \
  "v_mfma_f32_16x16x32_bf16 v[104:107], v[200:203], v[188:191], v[104:107]\n\t" \
  "v_mfma_f32_16x16x32_bf16 v[108:111], v[204:207], v[188:191], v[108:111]\n\t" \
  "s_waitcnt vmcnt(12) lgkmcnt(0)\n\t" \
  "s_barrier\n\t" \
  "v_mfma_f32_16x16x32_bf16 v[48:51], v[224:227], v[208:211], v[48:51]\n\t" \
  "v_add_u32_e32 %[br0], 49152, %[br0]\n\t" \
  "v_add_u32_e32 %[br1], 49152, %[br1]\n\t" \
  "v_add_u32_e32 %[ar0], 49152, %[ar0]\n\t" \
  "v_add_u32_e32 %[ar1], 49152, %[ar1]\n\t" \
  "v_mfma_f32_16x16x32_bf16 v[52:55], v[228:231], v[208:211], v[52:55]\n\t" \
  "ds_read_b64_tr_b16 v[192:193], %[br0] offset:0\n\t" \
  "ds_read_b64_tr_b16 v[194:195], %[br0] offset:1024\n\t" \
  "s_add_u32 m0, %[db], 49152\n\t" \
  "s_add_u32 s66, s66, 524288\n\t" \
  "s_addc_u32 s67, s67, 0\n\t" \
  "global_load_lds_dwordx4 %[vb], s[66:67] offset:0 nt\n\t" \
  "v_mfma_f32_16x16x32_bf16 v[56:59], v[232:235], v[208:211], v[56:59]\n\t" \
  "ds_read_b128 v[176:179], %[ar0] offset:0\n\t" \
  "global_load_lds_dwordx4 v251, s[66:67] offset:1024 nt\n\t" \
  "v_mfma_f32_16x16x32_bf16 v[60:63], v[236:239], v[208:211], v[60:63]\n\t" \
  "ds_read_b64_tr_b16 v[196:197], %[br1] offset:0\n\t" \
  "v_mfma_f32_16x16x32_bf16 v[64:67], v[224:227], v[212:215], v[64:67]\n\t" \
  "ds_read_b64_tr_b16 v[198:199], %[br1] offset:1024\n\t" \
  "s_add_u32 m0, %[da], 49152\n\t" \
  "s_add_u32 s64, s64, 128\n\t" \
  "s_addc_u32 s65, s65, 0\n\t" \
  "global_load_lds_dwordx4 %[va], s[64:65] offset:0\n\t" \
  "v_mfma_f32_16x16x32_bf16 v[68:71], v[228:231], v[212:215], v[68:71]\n\t" \
  "ds_read_b64_tr_b16 v[200:201], %[br0] offset:256\n\t" \
  "global_load_lds_dwordx4 v248, s[64:65] offset:1024\n\t" \
  "v_mfma_f32_16x16x32_bf16 v[72:75], v[232:235], v[212:215], v[72:75]\n\t" \
  "ds_read_b64_tr_b16 v[202:203], %[br0] offset:1280\n\t" \
  "v_mfma_f32_16x16x32_bf16 v[76:79], v[236:239], v[212:215], v[76:79]\n\t" \
  "ds_read_b64_tr_b16 v[204:205], %[br1] offset:256\n\t" \
  "global_load_lds_dwordx4 v249, s[64:65] offset:2048\n\t" \
  "v_mfma_f32_16x16x32_bf16 v[80:83], v[224:227], v[216:219], v[80:83]\n\t" \
  "ds_read_b64_tr_b16 v[206:207], %[br1] offset:1280\n\t" \
  "global_load_lds_dwordx4 v250, s[64:65] offset:3072\n\t" \
  "v_mfma_f32_16x16x32_bf16 v[84:87], v[228:231], v[216:219], v[84:87]\n\t" \
  "ds_read_b128 v[180:183], %[ar0] offset:2048\n\t" \
  "v_mfma_f32_16x16x32_bf16 v[88:91], v[232:235], v[216:219], v[88:91]\n\t" \
  "ds_read_b128 v[184:187], %[ar0] offset:4096\n\t" \
  "s_add_u32 m0, %[db], 57344\n\t" \
  "s_add_u32 s68, s66, 262144\n\t" \
  "s_addc_u32 s69, s67, 0\n\t" \
  "global_load_lds_dwordx4 %[vb], s[68:69] offset:0 nt\n\t" \
  "v_mfma_f32_16x16x32_bf16 v[92:95], v[236:239], v[216:219], v[92:95]\n\t" \
  "ds_read_b128 v[188:191], %[ar0] offset:6144\n\t" \
  "global_load_lds_dwordx4 v251, s[68:69] offset:1024 nt\n\t" \
  "v_mfma_f32_16x16x32_bf16 v[96:99], v[224:227], v[220:223], v[96:99]\n\t" \
  "v_mfma_f32_16x16x32_bf16 v[100:103], v[228:231], v[220:223], v[100:103]\n\t" \
  "s_add_u32 m0, %[da], 65536\n\t" \
  "s_add_u32 s68, s64, 131072\n\t" \
  "s_addc_u32 s69, s65, 0\n\t" \
  "global_load_lds_dwordx4 %[va], s[68:69] offset:0\n\t" \
  "v_mfma_f32_16x16x32_bf16 v[104:107], v[232:235], v[220:223], v[104:107]\n\t" \
  "global_load_lds_dwordx4 v248, s[68:69] offset:1024\n\t" \
  "v_mfma_f32_16x16x32_bf16 v[108:111], v[236:239], v[220:223], v[108:111]\n\t" \
  "s_waitcnt lgkmcnt(0)\n\t" \
  "v_mfma_f32_16x16x32_bf16 v[48:51], v[192:195], v[176:179], v[48:51]\n\t" \
  "ds_read_b64_tr_b16 v[224:225], %[br0] offset:8192\n\t" \
  "global_load_lds_dwordx4 v249, s[68:69] offset:2048\n\t" \
  "v_mfma_f32_16x16x32_bf16 v[52:55], v[196:199], v[176:179], v[52:55]\n\t" \
  "ds_read_b64_tr_b16 v[226:227], %[br0] offset:9216\n\t" \
  "global_load_lds_dwordx4 v250, s[68:69] offset:3072\n\t" \
  "v_mfma_f32_16x16x32_bf16 v[56:59], v[200:203], v[176:179], v[56:59]\n\t" \
  "ds_read_b128 v[208:211], %[ar1] offset:0\n\t" \
  "v_mfma_f32_16x16x32_bf16 v[60:63], v[204:207], v[176:179], v[60:63]\n\t" \
  "ds_read_b64_tr_b16 v[228:229], %[br1] offset:8192\n\t" \
  "v_mfma_f32_16x16x32_bf16 v[64:67], v[192:195], v[180:183], v[64:67]\n\t" \
  "ds_read_b64_tr_b16 v[230:231], %[br1] offset:9216\n\t" \
  "v_mfma_f32_16x16x32_bf16 v[68:71], v[196:199], v[180:183], v[68:71]\n\t" \
  "ds_read_b64_tr_b16 v[232:233], %[br0] offset:8448\n\t" \
  "v_mfma_f32_16x16x32_bf16 v[72:75], v[200:203], v[180:183], v[72:75]\n\t" \
  "ds_read_b64_tr_b16 v[234:235], %[br0] offset:9472\n\t" \
  "v_mfma_f32_16x16x32_bf16 v[76:79], v[204:207], v[180:183], v[76:79]\n\t" \
  "ds_read_b64_tr_b16 v[236:237], %[br1] offset:8448\n\t" \
  "v_mfma_f32_16x16x32_bf16 v[80:83], v[192:195], v[184:187], v[80:83]\n\t" \
  "ds_read_b64_tr_b16 v[238:239], %[br1] offset:9472\n\t" \
  "s_mov_b64 exec, 1\n\t" \
  "global_atomic_add v240, v241, %[hcp]\n\t" \
  "s_mov_b64 exec, -1\n\t" \
  "v_mfma_f32_16x16x32_bf16 v[84:87], v[196:199], v[184:187], v[84:87]\n\t" \
  "ds_read_b128 v[212:215], %[ar1] offset:2048\n\t" \
  "v_mfma_f32_16x16x32_bf16 v[88:91], v[200:203], v[184:187], v[88:91]\n\t" \
  "ds_read_b128 v[216:219], %[ar1] offset:4096\n\t" \
  "v_mfma_f32_16x16x32_bf16 v[92:95], v[204:207], v[184:187], v[92:95]\n\t" \
  "ds_read_b128 v[220:223], %[ar1] offset:6144\n\t" \
  "v_mfma_f32_16x16x32_bf16 v[96:99], v[192:195], v[188:191], v[96:99]\n\t" \
  "v_mfma_f32_16x16x32_bf16 v[100:103], v[196:199], v[188:191], v[100:103]\n\t" \
  "v_mfma_f32_16x16x32_bf16 v[104:107], v[200:203], v[188:191], v[104:107]\n\t" \
  "v_mfma_f32_16x16x32_bf16 v[108:111], v[204:207], v[188:191], v[108:111]\n\t" \
  "s_waitcnt vmcnt(13) lgkmcnt(0)\n\t" \
  "s_barrier\n\t" \
  "v_mfma_f32_16x16x32_bf16 v[48:51], v[224:227], v[208:211], v[48:51]\n\t" \
  "v_add_u32_e32 %[br0], -98304, %[br0]\n\t" \
  "v_add_u32_e32 %[br1], -98304, %[br1]\n\t" \
  "v_add_u32_e32 %[ar0], -98304, %[ar0]\n\t" \
  "v_add_u32_e32 %[ar1], -98304, %[ar1]\n\t" \
  "v_mfma_f32_16x16x32_bf16 v[52:55], v[228:231], v[208:211], v[52:55]\n\t" \
  "ds_read_b64_tr_b16 v[192:193], %[br0] offset:0\n\t" \
  "ds_read_b64_tr_b16 v[194:195], %[br0] offset:1024\n\t" \
  "s_add_u32 m0, %[db], 98304\n\t" \
  "s_add_u32 s66, s66, 524288\n\t" \
  "s_addc_u32 s67, s67, 0\n\t" \
  "global_load_lds_dwordx4 %[vb], s[66:67] offset:0 nt\n\t" \
  "v_mfma_f32_16x16x32_bf16 v[56:59], v[232:235], v[208:211], v[56:59]\n\t" \
  "ds_read_b128 v[176:179], %[ar0] offset:0\n\t" \
  "global_load_lds_dwordx4 v251, s[66:67] offset:1024 nt\n\t" \
  "v_mfma_f32_16x16x32_bf16 v[60:63], v[236:239], v[208:211], v[60:63]\n\t" \
  "ds_read_b64_tr_b16 v[196:197], %[br1] offset:0\n\t" \
  "v_mfma_f32_16x16x32_bf16 v[64:67], v[224:227], v[212:215], v[64:67]\n\t" \
  "ds_read_b64_tr_b16 v[198:199], %[br1] offset:1024\n\t" \
  "s_add_u32 m0, %[da], 98304\n\t" \
  "s_add_u32 s64, s64, 128\n\t" \
  "s_addc_u32 s65, s65, 0\n\t" \
  "global_load_lds_dwordx4 %[va], s[64:65] offset:0\n\t" \
  "v_mfma_f32_16x16x32_bf16 v[68:71], v[228:231], v[212:215], v[68:71]\n\t" \
  "ds_read_b64_tr_b16 v[200:201], %[br0] offset:256\n\t" \
  "global_load_lds_dwordx4 v248, s[64:65] offset:1024\n\t" \
  "v_mfma_f32_16x16x32_bf16 v[72:75], v[232:235], v[212:215], v[72:75]\n\t" \
  "ds_read_b64_tr_b16 v[202:203], %[br0] offset:1280\n\t" \
  "v_mfma_f32_16x16x32_bf16 v[76:79], v[236:239], v[212:215], v[76:79]\n\t" \
  "ds_read_b64_tr_b16 v[204:205], %[br1] offset:256\n\t" \
  "global_load_lds_dwordx4 v249, s[64:65] offset:2048\n\t" \
  "v_mfma_f32_16x16x32_bf16 v[80:83], v[224:227], v[216:219], v[80:83]\n\t" \
  "ds_read_b64_tr_b16 v[206:207], %[br1] offset:1280\n\t" \
  "global_load_lds_dwordx4 v250, s[64:65] offset:3072\n\t" \
  "v_mfma_f32_16x16x32_bf16 v[84:87], v[228:231], v[216:219], v[84:87]\n\t" \
  "ds_read_b128 v[180:183], %[ar0] offset:2048\n\t" \
  "v_mfma_f32_16x16x32_bf16 v[88:91], v[232:235], v[216:219], v[88:91]\n\t" \
  "ds_read_b128 v[184:187], %[ar0] offset:4096\n\t" \
  "s_add_u32 m0, %[db], 106496\n\t" \
  "s_add_u32 s68, s66, 262144\n\t" \
  "s_addc_u32 s69, s67, 0\n\t" \
  "global_load_lds_dwordx4 %[vb], s[68:69] offset:0 nt\n\t" \
  "v_mfma_f32_16x16x32_bf16 v[92:95], v[236:239], v[216:219], v[92:95]\n\t" \
  "ds_read_b128 v[188:191], %[ar0] offset:6144\n\t" \
  "global_load_lds_dwordx4 v251, s[68:69] offset:1024 nt\n\t" \
  "v_mfma_f32_16x16x32_bf16 v[96:99], v[224:227], v[220:223], v[96:99]\n\t" \
  "v_mfma_f32_16x16x32_bf16 v[100:103], v[228:231], v[220:223], v[100:103]\n\t" \
  "s_add_u32 m0, %[da], 114688\n\t" \
  "s_add_u32 s68, s64, 131072\n\t" \
  "s_addc_u32 s69, s65, 0\n\t" \
  "global_load_lds_dwordx4 %[va], s[68:69] offset:0\n\t" \
  "v_mfma_f32_16x16x32_bf16 v[104:107], v[232:235], v[220:223], v[104:107]\n\t" \
  "global_load_lds_dwordx4 v248, s[68:69] offset:1024\n\t" \
  "v_mfma_f32_16x16x32_bf16 v[108:111], v[236:239], v[220:223], v[108:111]\n\t" \
  "s_waitcnt lgkmcnt(0)\n\t" \
  "v_mfma_f32_16x16x32_bf16 v[48:51], v[192:195], v[176:179], v[48:51]\n\t" \
  "ds_read_b64_tr_b16 v[224:225], %[br0] offset:8192\n\t" \
  "global_load_lds_dwordx4 v249, s[68:69] offset:2048\n\t" \
  "v_mfma_f32_16x16x32_bf16 v[52:55], v[196:199], v[176:179], v[52:55]\n\t" \
  "ds_read_b64_tr_b16 v[226:227], %[br0] offset:9216\n\t" \
  "global_load_lds_dwordx4 v250, s[68:69] offset:3072\n\t" \
  "v_mfma_f32_16x16x32_bf16 v[56:59], v[200:203], v[176:179], v[56:59]\n\t" \
  "ds_read_b128 v[208:211], %[ar1] offset:0\n\t" \
  "v_mfma_f32_16x16x32_bf16 v[60:63], v[204:207], v[176:179], v[60:63]\n\t" \
  "ds_read_b64_tr_b16 v[228:229], %[br1] offset:8192\n\t" \
  "v_mfma_f32_16x16x32_bf16 v[64:67], v[192:195], v[180:183], v[64:67]\n\t" \
  "ds_read_b64_tr_b16 v[230:231], %[br1] offset:9216\n\t" \
  "v_mfma_f32_16x16x32_bf16 v[68:71], v[196:199], v[180:183], v[68:71]\n\t" \
  "ds_read_b64_tr_b16 v[232:233], %[br0] offset:8448\n\t" \
  "v_mfma_f32_16x16x32_bf16 v[72:75], v[200:203], v[180:183], v[72:75]\n\t" \
  "ds_read_b64_tr_b16 v[234:235], %[br0] offset:9472\n\t" \
  "v_mfma_f32_16x16x32_bf16 v[76:79], v[204:207], v[180:183], v[76:79]\n\t" \
  "ds_read_b64_tr_b16 v[236:237], %[br1] offset:8448\n\t" \
  "v_mfma_f32_16x16x32_bf16 v[80:83], v[192:195], v[184:187], v[80:83]\n\t" \
  "ds_read_b64_tr_b16 v[238:239], %[br1] offset:9472\n\t" \
  "v_mfma_f32_16x16x32_bf16 v[84:87], v[196:199], v[184:187], v[84:87]\n\t" \
  "ds_read_b128 v[212:215], %[ar1] offset:2048\n\t" \
  "v_mfma_f32_16x16x32_bf16 v[88:91], v[200:203], v[184:187], v[88:91]\n\t" \
  "ds_read_b128 v[216:219], %[ar1] offset:4096\n\t" \
  "v_mfma_f32_16x16x32_bf16 v[92:95], v[204:207], v[184:187], v[92:95]\n\t" \
  "ds_read_b128 v[220:223], %[ar1] offset:6144\n\t" \
  "v_mfma_f32_16x16x32_bf16 v[96:99], v[192:195], v[188:191], v[96:99]\n\t" \
  "v_mfma_f32_16x16x32_bf16 v[100:103], v[196:199], v[188:191], v[100:103]\n\t" \
  "v_mfma_f32_16x16x32_bf16 v[104:107], v[200:203], v[188:191], v[104:107]\n\t" \
  "v_mfma_f32_16x16x32_bf16 v[108:111], v[204:207], v[188:191], v[108:111]\n\t" \
  "s_waitcnt vmcnt(13) lgkmcnt(0)\n\t" \
  "s_barrier\n\t" \
  "v_mfma_f32_16x16x32_bf16 v[48:51], v[224:227], v[208:211], v[48:51]\n\t" \
  "v_add_u32_e32 %[br0], 49152, %[br0]\n\t" \
  "v_add_u32_e32 %[br1], 49152, %[br1]\n\t" \
  "v_add_u32_e32 %[ar0], 49152, %[ar0]\n\t" \
  "v_add_u32_e32 %[ar1], 49152, %[ar1]\n\t" \
  "v_mfma_f32_16x16x32_bf16 v[52:55], v[228:231], v[208:211], v[52:55]\n\t" \
  "ds_read_b64_tr_b16 v[192:193], %[br0] offset:0\n\t" \
  "ds_read_b64_tr_b16 v[194:195], %[br0] offset:1024\n\t" \
  "s_add_u32 m0, %[db], 0\n\t" \
  "s_add_u32 s66, s66, 524288\n\t" \
  "s_addc_u32 s67, s67, 0\n\t" \
  "global_load_lds_dwordx4 %[vb], s[66:67] offset:0 nt\n\t" \
  "v_mfma_f32_16x16x32_bf16 v[56:59], v[232:235], v[208:211], v[56:59]\n\t" \
  "ds_read_b128 v[176:179], %[ar0] offset:0\n\t" \
  "global_load_lds_dwordx4 v251, s[66:67] offset:1024 nt\n\t" \
  "v_mfma_f32_16x16x32_bf16 v[60:63], v[236:239], v[208:211], v[60:63]\n\t" \
  "ds_read_b64_tr_b16 v[196:197], %[br1] offset:0\n\t" \
  "v_mfma_f32_16x16x32_bf16 v[64:67], v[224:227], v[212:215], v[64:67]\n\t" \
  "ds_read_b64_tr_b16 v[198:199], %[br1] offset:1024\n\t" \
  "s_add_u32 m0, %[da], 0\n\t" \
  "s_add_u32 s64, s64, 128\n\t" \
  "s_addc_u32 s65, s65, 0\n\t" \
  "global_load_lds_dwordx4 %[va], s[64:65] offset:0\n\t" \
  "v_mfma_f32_16x16x32_bf16 v[68:71], v[228:231], v[212:215], v[68:71]\n\t" \
  "ds_read_b64_tr_b16 v[200:201], %[br0] offset:256\n\t" \
  "global_load_lds_dwordx4 v248, s[64:65] offset:1024\n\t" \
  "v_mfma_f32_16x16x32_bf16 v[72:75], v[232:235], v[212:215], v[72:75]\n\t" \
  "ds_read_b64_tr_b16 v[202:203], %[br0] offset:1280\n\t" \
  "v_mfma_f32_16x16x32_bf16 v[76:79], v[236:239], v[212:215], v[76:79]\n\t" \
  "ds_read_b64_tr_b16 v[204:205], %[br1] offset:256\n\t" \
  "global_load_lds_dwordx4 v249, s[64:65] offset:2048\n\t" \
  "v_mfma_f32_16x16x32_bf16 v[80:83], v[224:227], v[216:219], v[80:83]\n\t" \
  "ds_read_b64_tr_b16 v[206:207], %[br1] offset:1280\n\t" \
  "global_load_lds_dwordx4 v250, s[64:65] offset:3072\n\t" \
  "v_mfma_f32_16x16x32_bf16 v[84:87], v[228:231], v[216:219], v[84:87]\n\t" \
  "ds_read_b128 v[180:183], %[ar0] offset:2048\n\t" \
  "v_mfma_f32_16x16x32_bf16 v[88:91], v[232:235], v[216:219], v[88:91]\n\t" \
  "ds_read_b128 v[184:187], %[ar0] offset:4096\n\t" \
  "s_add_u32 m0, %[db], 8192\n\t" \
  "s_add_u32 s68, s66, 262144\n\t" \
  "s_addc_u32 s69, s67, 0\n\t" \
  "global_load_lds_dwordx4 %[vb], s[68:69] offset:0 nt\n\t" \
  "v_mfma_f32_16x16x32_bf16 v[92:95], v[236:239], v[216:219], v[92:95]\n\t" \
  "ds_read_b128 v[188:191], %[ar0] offset:6144\n\t" \
  "global_load_lds_dwordx4 v251, s[68:69] offset:1024 nt\n\t" \
  "v_mfma_f32_16x16x32_bf16 v[96:99], v[224:227], v[220:223], v[96:99]\n\t" \
  "v_mfma_f32_16x16x32_bf16 v[100:103], v[228:231], v[220:223], v[100:103]\n\t" \
  "s_add_u32 m0, %[da], 16384\n\t" \
  "s_add_u32 s68, s64, 131072\n\t" \
  "s_addc_u32 s69, s65, 0\n\t" \
  "global_load_lds_dwordx4 %[va], s[68:69] offset:0\n\t" \
  "v_mfma_f32_16x16x32_bf16 v[104:107], v[232:235], v[220:223], v[104:107]\n\t" \
  "global_load_lds_dwordx4 v248, s[68:69] offset:1024\n\t" \
  "v_mfma_f32_16x16x32_bf16 v[108:111], v[236:239], v[220:223], v[108:111]\n\t" \
  "s_waitcnt lgkmcnt(0)\n\t" \
  "v_mfma_f32_16x16x32_bf16 v[48:51], v[192:195], v[176:179], v[48:51]\n\t" \
  "ds_read_b64_tr_b16 v[224:225], %[br0] offset:8192\n\t" \
  "global_load_lds_dwordx4 v249, s[68:69] offset:2048\n\t" \
  "v_mfma_f32_16x16x32_bf16 v[52:55], v[196:199], v[176:179], v[52:55]\n\t" \
  "ds_read_b64_tr_b16 v[226:227], %[br0] offset:9216\n\t" \
  "global_load_lds_dwordx4 v250, s[68:69] offset:3072\n\t" \
  "v_mfma_f32_16x16x32_bf16 v[56:59], v[200:203], v[176:179], v[56:59]\n\t" \
  "ds_read_b128 v[208:211], %[ar1] offset:0\n\t" \
  "v_mfma_f32_16x16x32_bf16 v[60:63], v[204:207], v[176:179], v[60:63]\n\t" \
  "ds_read_b64_tr_b16 v[228:229], %[br1] offset:8192\n\t" \
  "v_mfma_f32_16x16x32_bf16 v[64:67], v[192:195], v[180:183], v[64:67]\n\t" \
  "ds_read_b64_tr_b16 v[230:231], %[br1] offset:9216\n\t" \
  "v_mfma_f32_16x16x32_bf16 v[68:71], v[196:199], v[180:183], v[68:71]\n\t" \
  "ds_read_b64_tr_b16 v[232:233], %[br0] offset:8448\n\t" \
  "v_mfma_f32_16x16x32_bf16 v[72:75], v[200:203], v[180:183], v[72:75]\n\t" \
  "ds_read_b64_tr_b16 v[234:235], %[br0] offset:9472\n\t" \
  "v_mfma_f32_16x16x32_bf16 v[76:79], v[204:207], v[180:183], v[76:79]\n\t" \
  "ds_read_b64_tr_b16 v[236:237], %[br1] offset:8448\n\t" \
  "v_mfma_f32_16x16x32_bf16 v[80:83], v[192:195], v[184:187], v[80:83]\n\t" \
  "ds_read_b64_tr_b16 v[238:239], %[br1] offset:9472\n\t" \
  "v_mfma_f32_16x16x32_bf16 v[84:87], v[196:199], v[184:187], v[84:87]\n\t" \
  "ds_read_b128 v[212:215], %[ar1] offset:2048\n\t" \
  "v_mfma_f32_16x16x32_bf16 v[88:91], v[200:203], v[184:187], v[88:91]\n\t" \
  "ds_read_b128 v[216:219], %[ar1] offset:4096\n\t" \
  "v_mfma_f32_16x16x32_bf16 v[92:95], v[204:207], v[184:187], v[92:95]\n\t" \
  "ds_read_b128 v[220:223], %[ar1] offset:6144\n\t" \
  "v_mfma_f32_16x16x32_bf16 v[96:99], v[192:195], v[188:191], v[96:99]\n\t" \
  "v_mfma_f32_16x16x32_bf16 v[100:103], v[196:199], v[188:191], v[100:103]\n\t" \
  "v_mfma_f32_16x16x32_bf16 v[104:107], v[200:203], v[188:191], v[104:107]\n\t" \
  "v_mfma_f32_16x16x32_bf16 v[108:111], v[204:207], v[188:191], v[108:111]\n\t" \
  "s_waitcnt vmcnt(12) lgkmcnt(0)\n\t" \
  "s_barrier\n\t" \
  "v_mfma_f32_16x16x32_bf16 v[48:51], v[224:227], v[208:211], v[48:51]\n\t" \
  "v_add_u32_e32 %[br0], 49152, %[br0]\n\t" \
  "v_add_u32_e32 %[br1], 49152, %[br1]\n\t" \
  "v_add_u32_e32 %[ar0], 49152, %[ar0]\n\t" \
  "v_add_u32_e32 %[ar1], 49152, %[ar1]\n\t" \
  "v_mfma_f32_16x16x32_bf16 v[52:55], v[228:231], v[208:211], v[52:55]\n\t" \
  "ds_read_b64_tr_b16 v[192:193], %[br0] offset:0\n\t" \
  "ds_read_b64_tr_b16 v[194:195], %[br0] offset:1024\n\t" \
  "s_add_u32 m0, %[db], 49152\n\t" \
  "s_add_u32 s66, s66, 524288\n\t" \
  "s_addc_u32 s67, s67, 0\n\t" \
  "global_load_lds_dwordx4 %[vb], s[66:67] offset:0 nt\n\t" \
  "v_mfma_f32_16x16x32_bf16 v[56:59], v[232:235], v[208:211], v[56:59]\n\t" \
  "ds_read_b128 v[176:179], %[ar0] offset:0\n\t" \
  "global_load_lds_dwordx4 v251, s[66:67] offset:1024 nt\n\t" \
  "v_mfma_f32_16x16x32_bf16 v[60:63], v[236:239], v[208:211], v[60:63]\n\t" \
  "ds_read_b64_tr_b16 v[196:197], %[br1] offset:0\n\t" \
  "v_mfma_f32_16x16x32_bf16 v[64:67], v[224:227], v[212:215], v[64:67]\n\t" \
  "ds_read_b64_tr_b16 v[198:199], %[br1] offset:1024\n\t" \
  "s_add_u32 m0, %[da], 49152\n\t" \
  "s_add_u32 s64, s64, 128\n\t" \
  "s_addc_u32 s65, s65, 0\n\t" \
  "global_load_lds_dwordx4 %[va], s[64:65] offset:0\n\t" \
  "v_mfma_f32_16x16x32_bf16 v[68:71], v[228:231], v[212:215], v[68:71]\n\t" \
  "ds_read_b64_tr_b16 v[200:201], %[br0] offset:256\n\t" \
  "global_load_lds_dwordx4 v248, s[64:65] offset:1024\n\t" \
  "v_mfma_f32_16x16x32_bf16 v[72:75], v[232:235], v[212:215], v[72:75]\n\t" \
  "ds_read_b64_tr_b16 v[202:203], %[br0] offset:1280\n\t" \
  "v_mfma_f32_16x16x32_bf16 v[76:79], v[236:239], v[212:215], v[76:79]\n\t" \
  "ds_read_b64_tr_b16 v[204:205], %[br1] offset:256\n\t" \
  "global_load_lds_dwordx4 v249, s[64:65] offset:2048\n\t" \
  "v_mfma_f32_16x16x32_bf16 v[80:83], v[224:227], v[216:219], v[80:83]\n\t" \
  "ds_read_b64_tr_b16 v[206:207], %[br1] offset:1280\n\t" \
  "global_load_lds_dwordx4 v250, s[64:65] offset:3072\n\t" \
  "v_mfma_f32_16x16x32_bf16 v[84:87], v[228:231], v[216:219], v[84:87]\n\t" \
  "ds_read_b128 v[180:183], %[ar0] offset:2048\n\t" \
  "v_mfma_f32_16x16x32_bf16 v[88:91], v[232:235], v[216:219], v[88:91]\n\t" \
  "ds_read_b128 v[184:187], %[ar0] offset:4096\n\t" \
  "s_add_u32 m0, %[db], 57344\n\t" \
  "s_add_u32 s68, s66, 262144\n\t" \
  "s_addc_u32 s69, s67, 0\n\t" \
  "global_load_lds_dwordx4 %[vb], s[68:69] offset:0 nt\n\t" \
  "v_mfma_f32_16x16x32_bf16 v[92:95], v[236:239], v[216:219], v[92:95]\n\t" \
  "ds_read_b128 v[188:191], %[ar0] offset:6144\n\t" \
  "global_load_lds_dwordx4 v251, s[68:69] offset:1024 nt\n\t" \
  "v_mfma_f32_16x16x32_bf16 v[96:99], v[224:227], v[220:223], v[96:99]\n\t" \
  "v_mfma_f32_16x16x32_bf16 v[100:103], v[228:231], v[220:223], v[100:103]\n\t" \
  "s_add_u32 m0, %[da], 65536\n\t" \
  "s_add_u32 s68, s64, 131072\n\t" \
  "s_addc_u32 s69, s65, 0\n\t" \
  "global_load_lds_dwordx4 %[va], s[68:69] offset:0\n\t" \
  "v_mfma_f32_16x16x32_bf16 v[104:107], v[232:235], v[220:223], v[104:107]\n\t" \
  "global_load_lds_dwordx4 v248, s[68:69] offset:1024\n\t" \
  "v_mfma_f32_16x16x32_bf16 v[108:111], v[236:239], v[220:223], v[108:111]\n\t" \
  "s_waitcnt lgkmcnt(0)\n\t" \
  "v_mfma_f32_16x16x32_bf16 v[48:51], v[192:195], v[176:179], v[48:51]\n\t" \
  "ds_read_b64_tr_b16 v[224:225], %[br0] offset:8192\n\t" \
  "global_load_lds_dwordx4 v249, s[68:69] offset:2048\n\t" \
  "v_mfma_f32_16x16x32_bf16 v[52:55], v[196:199], v[176:179], v[52:55]\n\t" \
  "ds_read_b64_tr_b16 v[226:227], %[br0] offset:9216\n\t" \
  "global_load_lds_dwordx4 v250, s[68:69] offset:3072\n\t" \
  "v_mfma_f32_16x16x32_bf16 v[56:59], v[200:203], v[176:179], v[56:59]\n\t" \
  "ds_read_b128 v[208:211], %[ar1] offset:0\n\t" \
  "v_mfma_f32_16x16x32_bf16 v[60:63], v[204:207], v[176:179], v[60:63]\n\t" \
  "ds_read_b64_tr_b16 v[228:229], %[br1] offset:8192\n\t" \
  "v_mfma_f32_16x16x32_bf16 v[64:67], v[192:195], v[180:183], v[64:67]\n\t" \
  "ds_read_b64_tr_b16 v[230:231], %[br1] offset:9216\n\t" \
  "v_mfma_f32_16x16x32_bf16 v[68:71], v[196:199], v[180:183], v[68:71]\n\t" \
  "ds_read_b64_tr_b16 v[232:233], %[br0] offset:8448\n\t" \
  "v_mfma_f32_16x16x32_bf16 v[72:75], v[200:203], v[180:183], v[72:75]\n\t" \
  "ds_read_b64_tr_b16 v[234:235], %[br0] offset:9472\n\t" \
  "v_mfma_f32_16x16x32_bf16 v[76:79], v[204:207], v[180:183], v[76:79]\n\t" \
  "ds_read_b64_tr_b16 v[236:237], %[br1] offset:8448\n\t" \
  "v_mfma_f32_16x16x32_bf16 v[80:83], v[192:195], v[184:187], v[80:83]\n\t" \
  "ds_read_b64_tr_b16 v[238:239], %[br1] offset:9472\n\t" \
  "v_mfma_f32_16x16x32_bf16 v[84:87], v[196:199], v[184:187], v[84:87]\n\t" \
  "ds_read_b128 v[212:215], %[ar1] offset:2048\n\t" \
  "v_mfma_f32_16x16x32_bf16 v[88:91], v[200:203], v[184:187], v[88:91]\n\t" \
  "ds_read_b128 v[216:219], %[ar1] offset:4096\n\t" \
  "v_mfma_f32_16x16x32_bf16 v[92:95], v[204:207], v[184:187], v[92:95]\n\t" \
  "ds_read_b128 v[220:223], %[ar1] offset:6144\n\t" \
  "v_mfma_f32_16x16x32_bf16 v[96:99], v[192:195], v[188:191], v[96:99]\n\t" \
  "v_mfma_f32_16x16x32_bf16 v[100:103], v[196:199], v[188:191], v[100:103]\n\t" \
  "v_mfma_f32_16x16x32_bf16 v[104:107], v[200:203], v[188:191], v[104:107]\n\t" \
  "v_mfma_f32_16x16x32_bf16 v[108:111], v[204:207], v[188:191], v[108:111]\n\t" \
  "s_waitcnt vmcnt(12) lgkmcnt(0)\n\t" \
  "s_barrier\n\t" \
  "v_mfma_f32_16x16x32_bf16 v[48:51], v[224:227], v[208:211], v[48:51]\n\t" \
  "v_add_u32_e32 %[br0], -98304, %[br0]\n\t" \
  "v_add_u32_e32 %[br1], -98304, %[br1]\n\t" \
  "v_add_u32_e32 %[ar0], -98304, %[ar0]\n\t" \
  "v_add_u32_e32 %[ar1], -98304, %[ar1]\n\t" \
  "v_mfma_f32_16x16x32_bf16 v[52:55], v[228:231], v[208:211], v[52:55]\n\t" \
  "ds_read_b64_tr_b16 v[192:193], %[br0] offset:0\n\t" \
  "ds_read_b64_tr_b16 v[194:195], %[br0] offset:1024\n\t" \
  "s_add_u32 m0, %[db], 98304\n\t" \
  "s_mov_b64 s[66:67], %[gb1]\n\t" \
  "global_load_lds_dwordx4 %[vb], s[66:67] offset:0\n\t" \
  "v_mfma_f32_16x16x32_bf16 v[56:59], v[232:235], v[208:211], v[56:59]\n\t" \
  "ds_read_b128 v[176:179], %[ar0] offset:0\n\t" \
  "global_load_lds_dwordx4 v251, s[66:67] offset:1024\n\t" \
  "v_mfma_f32_16x16x32_bf16 v[60:63], v[236:239], v[208:211], v[60:63]\n\t" \
  "ds_read_b64_tr_b16 v[196:197], %[br1] offset:0\n\t" \
  "v_mfma_f32_16x16x32_bf16 v[64:67], v[224:227], v[212:215], v[64:67]\n\t" \
  "ds_read_b64_tr_b16 v[198:199], %[br1] offset:1024\n\t" \
  "s_add_u32 m0, %[da], 98304\n\t" \
  "s_mov_b64 s[64:65], %[ga]\n\t" \
  "global_load_lds_dwordx4 %[va], s[64:65] offset:0\n\t" \
  "v_mfma_f32_16x16x32_bf16 v[68:71], v[228:231], v[212:215], v[68:71]\n\t" \
  "ds_read_b64_tr_b16 v[200:201], %[br0] offset:256\n\t" \
  "global_load_lds_dwordx4 v248, s[64:65] offset:1024\n\t" \
  "v_mfma_f32_16x16x32_bf16 v[72:75], v[232:235], v[212:215], v[72:75]\n\t" \
  "ds_read_b64_tr_b16 v[202:203], %[br0] offset:1280\n\t" \
  "v_mfma_f32_16x16x32_bf16 v[76:79], v[236:239], v[212:215], v[76:79]\n\t" \
  "ds_read_b64_tr_b16 v[204:205], %[br1] offset:256\n\t" \
  "global_load_lds_dwordx4 v249, s[64:65] offset:2048\n\t" \
  "v_mfma_f32_16x16x32_bf16 v[80:83], v[224:227], v[216:219], v[80:83]\n\t" \
  "ds_read_b64_tr_b16 v[206:207], %[br1] offset:1280\n\t" \
  "global_load_lds_dwordx4 v250, s[64:65] offset:3072\n\t" \
  "v_mfma_f32_16x16x32_bf16 v[84:87], v[228:231], v[216:219], v[84:87]\n\t" \
  "ds_read_b128 v[180:183], %[ar0] offset:2048\n\t" \
  "v_mfma_f32_16x16x32_bf16 v[88:91], v[232:235], v[216:219], v[88:91]\n\t" \
  "ds_read_b128 v[184:187], %[ar0] offset:4096\n\t" \
  "s_add_u32 m0, %[db], 106496\n\t" \
  "s_add_u32 s68, s66, 262144\n\t" \
  "s_addc_u32 s69, s67, 0\n\t" \
  "global_load_lds_dwordx4 %[vb], s[68:69] offset:0\n\t" \
  "v_mfma_f32_16x16x32_bf16 v[92:95], v[236:239], v[216:219], v[92:95]\n\t" \
  "ds_read_b128 v[188:191], %[ar0] offset:6144\n\t" \
  "global_load_lds_dwordx4 v251, s[68:69] offset:1024\n\t" \
  "v_mfma_f32_16x16x32_bf16 v[96:99], v[224:227], v[220:223], v[96:99]\n\t" \
  "v_mfma_f32_16x16x32_bf16 v[100:103], v[228:231], v[220:223], v[100:103]\n\t" \
  "s_add_u32 m0, %[da], 114688\n\t" \
  "s_add_u32 s68, s64, 131072\n\t" \
  "s_addc_u32 s69, s65, 0\n\t" \
  "global_load_lds_dwordx4 %[va], s[68:69] offset:0\n\t" \
  "v_mfma_f32_16x16x32_bf16 v[104:107], v[232:235], v[220:223], v[104:107]\n\t" \
  "global_load_lds_dwordx4 v248, s[68:69] offset:1024\n\t" \
  "v_mfma_f32_16x16x32_bf16 v[108:111], v[236:239], v[220:223], v[108:111]\n\t" \
  "s_waitcnt lgkmcnt(0)\n\t" \
  "v_mfma_f32_16x16x32_bf16 v[48:51], v[192:195], v[176:179], v[48:51]\n\t" \
  "ds_read_b64_tr_b16 v[224:225], %[br0] offset:8192\n\t" \
  "global_load_lds_dwordx4 v249, s[68:69] offset:2048\n\t" \
  "v_mfma_f32_16x16x32_bf16 v[52:55], v[196:199], v[176:179], v[52:55]\n\t" \
  "ds_read_b64_tr_b16 v[226:227], %[br0] offset:9216\n\t" \
  "global_load_lds_dwordx4 v250, s[68:69] offset:3072\n\t" \
  "v_mfma_f32_16x16x32_bf16 v[56:59], v[200:203], v[176:179], v[56:59]\n\t" \
  "ds_read_b128 v[208:211], %[ar1] offset:0\n\t" \
  "v_mfma_f32_16x16x32_bf16 v[60:63], v[204:207], v[176:179], v[60:63]\n\t" \
  "ds_read_b64_tr_b16 v[228:229], %[br1] offset:8192\n\t" \
  "v_mfma_f32_16x16x32_bf16 v[64:67], v[192:195], v[180:183], v[64:67]\n\t" \
  "ds_read_b64_tr_b16 v[230:231], %[br1] offset:9216\n\t" \
  "v_mfma_f32_16x16x32_bf16 v[68:71], v[196:199], v[180:183], v[68:71]\n\t" \
  "ds_read_b64_tr_b16 v[232:233], %[br0] offset:8448\n\t" \
  "v_mfma_f32_16x16x32_bf16 v[72:75], v[200:203], v[180:183], v[72:75]\n\t" \
  "ds_read_b64_tr_b16 v[234:235], %[br0] offset:9472\n\t" \
  "v_mfma_f32_16x16x32_bf16 v[76:79], v[204:207], v[180:183], v[76:79]\n\t" \
  "ds_read_b64_tr_b16 v[236:237], %[br1] offset:8448\n\t" \
  "v_mfma_f32_16x16x32_bf16 v[80:83], v[192:195], v[184:187], v[80:83]\n\t" \
  "ds_read_b64_tr_b16 v[238:239], %[br1] offset:9472\n\t" \
  "v_mfma_f32_16x16x32_bf16 v[84:87], v[196:199], v[184:187], v[84:87]\n\t" \
  "ds_read_b128 v[212:215], %[ar1] offset:2048\n\t" \
  "v_mfma_f32_16x16x32_bf16 v[88:91], v[200:203], v[184:187], v[88:91]\n\t" \
  "ds_read_b128 v[216:219], %[ar1] offset:4096\n\t" \
  "v_mfma_f32_16x16x32_bf16 v[92:95], v[204:207], v[184:187], v[92:95]\n\t" \
  "ds_read_b128 v[220:223], %[ar1] offset:6144\n\t" \
  "v_mfma_f32_16x16x32_bf16 v[96:99], v[192:195], v[188:191], v[96:99]\n\t" \
  "v_mfma_f32_16x16x32_bf16 v[100:103], v[196:199], v[188:191], v[100:103]\n\t" \
  "v_mfma_f32_16x16x32_bf16 v[104:107], v[200:203], v[188:191], v[104:107]\n\t" \
  "v_mfma_f32_16x16x32_bf16 v[108:111], v[204:207], v[188:191], v[108:111]\n\t" \
  "s_waitcnt vmcnt(12) lgkmcnt(0)\n\t" \
  "s_barrier\n\t" \
  "v_mfma_f32_16x16x32_bf16 v[48:51], v[224:227], v[208:211], v[48:51]\n\t" \
  "v_add_u32_e32 %[br0], 49152, %[br0]\n\t" \
  "v_add_u32_e32 %[br1], 49152, %[br1]\n\t" \
  "v_add_u32_e32 %[ar0], 49152, %[ar0]\n\t" \
  "v_add_u32_e32 %[ar1], 49152, %[ar1]\n\t" \
  "v_mfma_f32_16x16x32_bf16 v[52:55], v[228:231], v[208:211], v[52:55]\n\t" \
  "ds_read_b64_tr_b16 v[192:193], %[br0] offset:0\n\t" \
  "ds_read_b64_tr_b16 v[194:195], %[br0] offset:1024\n\t" \
  "s_add_u32 m0, %[db], 0\n\t" \
  "s_add_u32 s66, s66, 524288\n\t" \
  "s_addc_u32 s67, s67, 0\n\t" \
  "global_load_lds_dwordx4 %[vb], s[66:67] offset:0\n\t" \
  "v_mfma_f32_16x16x32_bf16 v[56:59], v[232:235], v[208:211], v[56:59]\n\t" \
  "ds_read_b128 v[176:179], %[ar0] offset:0\n\t" \
  "global_load_lds_dwordx4 v251, s[66:67] offset:1024\n\t" \
  "v_mfma_f32_16x16x32_bf16 v[60:63], v[236:239], v[208:211], v[60:63]\n\t" \
  "ds_read_b64_tr_b16 v[196:197], %[br1] offset:0\n\t" \
  "v_mfma_f32_16x16x32_bf16 v[64:67], v[224:227], v[212:215], v[64:67]\n\t" \
  "ds_read_b64_tr_b16 v[198:199], %[br1] offset:1024\n\t" \
  "s_add_u32 m0, %[da], 0\n\t" \
  "s_add_u32 s64, s64, 128\n\t" \
  "s_addc_u32 s65, s65, 0\n\t" \
  "global_load_lds_dwordx4 %[va], s[64:65] offset:0\n\t" \
  "v_mfma_f32_16x16x32_bf16 v[68:71], v[228:231], v[212:215], v[68:71]\n\t" \
  "ds_read_b64_tr_b16 v[200:201], %[br0] offset:256\n\t" \
  "global_load_lds_dwordx4 v248, s[64:65] offset:1024\n\t" \
  "v_mfma_f32_16x16x32_bf16 v[72:75], v[232:235], v[212:215], v[72:75]\n\t" \
  "ds_read_b64_tr_b16 v[202:203], %[br0] offset:1280\n\t" \
  "v_mfma_f32_16x16x32_bf16 v[76:79], v[236:239], v[212:215], v[76:79]\n\t" \
  "ds_read_b64_tr_b16 v[204:205], %[br1] offset:256\n\t" \
  "global_load_lds_dwordx4 v249, s[64:65] offset:2048\n\t" \
  "v_mfma_f32_16x16x32_bf16 v[80:83], v[224:227], v[216:219], v[80:83]\n\t" \
  "ds_read_b64_tr_b16 v[206:207], %[br1] offset:1280\n\t" \
  "global_load_lds_dwordx4 v250, s[64:65] offset:3072\n\t" \
  "v_mfma_f32_16x16x32_bf16 v[84:87], v[228:231], v[216:219], v[84:87]\n\t" \
  "ds_read_b128 v[180:183], %[ar0] offset:2048\n\t" \
  "v_mfma_f32_16x16x32_bf16 v[88:91], v[232:235], v[216:219], v[88:91]\n\t" \
  "ds_read_b128 v[184:187], %[ar0] offset:4096\n\t" \
  "s_add_u32 m0, %[db], 8192\n\t" \
  "s_add_u32 s68, s66, 262144\n\t" \
  "s_addc_u32 s69, s67, 0\n\t" \
  "global_load_lds_dwordx4 %[vb], s[68:69] offset:0\n\t" \
  "v_mfma_f32_16x16x32_bf16 v[92:95], v[236:239], v[216:219], v[92:95]\n\t" \
  "ds_read_b128 v[188:191], %[ar0] offset:6144\n\t" \
  "global_load_lds_dwordx4 v251, s[68:69] offset:1024\n\t" \
  "v_mfma_f32_16x16x32_bf16 v[96:99], v[224:227], v[220:223], v[96:99]\n\t" \
  "v_mfma_f32_16x16x32_bf16 v[100:103], v[228:231], v[220:223], v[100:103]\n\t" \
  "s_add_u32 m0, %[da], 16384\n\t" \
  "s_add_u32 s68, s64, 131072\n\t" \
  "s_addc_u32 s69, s65, 0\n\t" \
  "global_load_lds_dwordx4 %[va], s[68:69] offset:0\n\t" \
  "v_mfma_f32_16x16x32_bf16 v[104:107], v[232:235], v[220:223], v[104:107]\n\t" \
  "global_load_lds_dwordx4 v248, s[68:69] offset:1024\n\t" \
  "v_mfma_f32_16x16x32_bf16 v[108:111], v[236:239], v[220:223], v[108:111]\n\t" \
  "s_waitcnt lgkmcnt(0)\n\t" \
  "v_mfma_f32_16x16x32_bf16 v[48:51], v[192:195], v[176:179], v[48:51]\n\t" \
  "ds_read_b64_tr_b16 v[224:225], %[br0] offset:8192\n\t" \
  "global_load_lds_dwordx4 v249, s[68:69] offset:2048\n\t" \
  "v_mfma_f32_16x16x32_bf16 v[52:55], v[196:199], v[176:179], v[52:55]\n\t" \
  "ds_read_b64_tr_b16 v[226:227], %[br0] offset:9216\n\t" \
  "global_load_lds_dwordx4 v250, s[68:69] offset:3072\n\t" \
  "v_mfma_f32_16x16x32_bf16 v[56:59], v[200:203], v[176:179], v[56:59]\n\t" \
  "ds_read_b128 v[208:211], %[ar1] offset:0\n\t" \
  "v_mfma_f32_16x16x32_bf16 v[60:63], v[204:207], v[176:179], v[60:63]\n\t" \
  "ds_read_b64_tr_b16 v[228:229], %[br1] offset:8192\n\t" \
  "v_mfma_f32_16x16x32_bf16 v[64:67], v[192:195], v[180:183], v[64:67]\n\t" \
  "ds_read_b64_tr_b16 v[230:231], %[br1] offset:9216\n\t" \
  "v_mfma_f32_16x16x32_bf16 v[68:71], v[196:199], v[180:183], v[68:71]\n\t" \
  "ds_read_b64_tr_b16 v[232:233], %[br0] offset:8448\n\t" \
  "v_mfma_f32_16x16x32_bf16 v[72:75], v[200:203], v[180:183], v[72:75]\n\t" \
  "ds_read_b64_tr_b16 v[234:235], %[br0] offset:9472\n\t" \
  "v_mfma_f32_16x16x32_bf16 v[76:79], v[204:207], v[180:183], v[76:79]\n\t" \
  "ds_read_b64_tr_b16 v[236:237], %[br1] offset:8448\n\t" \
  "v_mfma_f32_16x16x32_bf16 v[80:83], v[192:195], v[184:187], v[80:83]\n\t" \
  "ds_read_b64_tr_b16 v[238:239], %[br1] offset:9472\n\t" \
  "v_mfma_f32_16x16x32_bf16 v[84:87], v[196:199], v[184:187], v[84:87]\n\t" \
  "ds_read_b128 v[212:215], %[ar1] offset:2048\n\t" \
  "v_mfma_f32_16x16x32_bf16 v[88:91], v[200:203], v[184:187], v[88:91]\n\t" \
  "ds_read_b128 v[216:219], %[ar1] offset:4096\n\t" \
  "v_mfma_f32_16x16x32_bf16 v[92:95], v[204:207], v[184:187], v[92:95]\n\t" \
  "ds_read_b128 v[220:223], %[ar1] offset:6144\n\t" \
  "v_mfma_f32_16x16x32_bf16 v[96:99], v[192:195], v[188:191], v[96:99]\n\t" \
  "v_mfma_f32_16x16x32_bf16 v[100:103], v[196:199], v[188:191], v[100:103]\n\t" \
  "v_mfma_f32_16x16x32_bf16 v[104:107], v[200:203], v[188:191], v[104:107]\n\t" \
  "v_mfma_f32_16x16x32_bf16 v[108:111], v[204:207], v[188:191], v[108:111]\n\t" \
  "s_waitcnt vmcnt(12) lgkmcnt(0)\n\t" \
  "s_barrier\n\t" \
  "v_mfma_f32_16x16x32_bf16 v[48:51], v[224:227], v[208:211], v[48:51]\n\t" \
  "v_add_u32_e32 %[br0], 49152, %[br0]\n\t" \
  "v_add_u32_e32 %[br1], 49152, %[br1]\n\t" \
  "v_add_u32_e32 %[ar0], 49152, %[ar0]\n\t" \
  "v_add_u32_e32 %[ar1], 49152, %[ar1]\n\t" \
  "v_mfma_f32_16x16x32_bf16 v[52:55], v[228:231], v[208:211], v[52:55]\n\t" \
  "ds_read_b64_tr_b16 v[192:193], %[br0] offset:0\n\t" \
  "ds_read_b64_tr_b16 v[194:195], %[br0] offset:1024\n\t" \
  "s_add_u32 m0, %[db], 49152\n\t" \
  "s_add_u32 s66, s66, 524288\n\t" \
  "s_addc_u32 s67, s67, 0\n\t" \
  "global_load_lds_dwordx4 %[vb], s[66:67] offset:0\n\t" \
  "v_mfma_f32_16x16x32_bf16 v[56:59], v[232:235], v[208:211], v[56:59]\n\t" \
  "ds_read_b128 v[176:179], %[ar0] offset:0\n\t" \
  "global_load_lds_dwordx4 v251, s[66:67] offset:1024\n\t" \
  "v_mfma_f32_16x16x32_bf16 v[60:63], v[236:239], v[208:211], v[60:63]\n\t" \
  "ds_read_b64_tr_b16 v[196:197], %[br1] offset:0\n\t" \
  "v_mfma_f32_16x16x32_bf16 v[64:67], v[224:227], v[212:215], v[64:67]\n\t" \
  "ds_read_b64_tr_b16 v[198:199], %[br1] offset:1024\n\t" \
  "s_add_u32 m0, %[da], 49152\n\t" \
  "s_add_u32 s64, s64, 128\n\t" \
  "s_addc_u32 s65, s65, 0\n\t" \
  "global_load_lds_dwordx4 %[va], s[64:65] offset:0\n\t" \
  "v_mfma_f32_16x16x32_bf16 v[68:71], v[228:231], v[212:215], v[68:71]\n\t" \
  "ds_read_b64_tr_b16 v[200:201], %[br0] offset:256\n\t" \
  "global_load_lds_dwordx4 v248, s[64:65] offset:1024\n\t" \
  "v_mfma_f32_16x16x32_bf16 v[72:75], v[232:235], v[212:215], v[72:75]\n\t" \
  "ds_read_b64_tr_b16 v[202:203], %[br0] offset:1280\n\t" \
  "v_mfma_f32_16x16x32_bf16 v[76:79], v[236:239], v[212:215], v[76:79]\n\t" \
  "ds_read_b64_tr_b16 v[204:205], %[br1] offset:256\n\t" \
  "global_load_lds_dwordx4 v249, s[64:65] offset:2048\n\t" \
  "v_mfma_f32_16x16x32_bf16 v[80:83], v[224:227], v[216:219], v[80:83]\n\t" \
  "ds_read_b64_tr_b16 v[206:207], %[br1] offset:1280\n\t" \
  "global_load_lds_dwordx4 v250, s[64:65] offset:3072\n\t" \
  "v_mfma_f32_16x16x32_bf16 v[84:87], v[228:231], v[216:219], v[84:87]\n\t" \
  "ds_read_b128 v[180:183], %[ar0] offset:2048\n\t" \
  "v_mfma_f32_16x16x32_bf16 v[88:91], v[232:235], v[216:219], v[88:91]\n\t" \
  "ds_read_b128 v[184:187], %[ar0] offset:4096\n\t" \
  "s_add_u32 m0, %[db], 57344\n\t" \
  "s_add_u32 s68, s66, 262144\n\t" \
  "s_addc_u32 s69, s67, 0\n\t" \
  "global_load_lds_dwordx4 %[vb], s[68:69] offset:0\n\t" \
  "v_mfma_f32_16x16x32_bf16 v[92:95], v[236:239], v[216:219], v[92:95]\n\t" \
  "ds_read_b128 v[188:191], %[ar0] offset:6144\n\t" \
  "global_load_lds_dwordx4 v251, s[68:69] offset:1024\n\t" \
  "v_mfma_f32_16x16x32_bf16 v[96:99], v[224:227], v[220:223], v[96:99]\n\t" \
  "v_mfma_f32_16x16x32_bf16 v[100:103], v[228:231], v[220:223], v[100:103]\n\t" \
  "s_add_u32 m0, %[da], 65536\n\t" \
  "s_add_u32 s68, s64, 131072\n\t" \
  "s_addc_u32 s69, s65, 0\n\t" \
  "global_load_lds_dwordx4 %[va], s[68:69] offset:0\n\t" \
  "v_mfma_f32_16x16x32_bf16 v[104:107], v[232:235], v[220:223], v[104:107]\n\t" \
  "global_load_lds_dwordx4 v248, s[68:69] offset:1024\n\t" \
  "v_mfma_f32_16x16x32_bf16 v[108:111], v[236:239], v[220:223], v[108:111]\n\t" \
  "s_waitcnt lgkmcnt(0)\n\t" \
  "v_mfma_f32_16x16x32_bf16 v[112:115], v[176:179], v[192:195], 0\n\t" \
  "ds_read_b64_tr_b16 v[224:225], %[br0] offset:8192\n\t" \
  "global_load_lds_dwordx4 v249, s[68:69] offset:2048\n\t" \
  "v_mfma_f32_16x16x32_bf16 v[116:119], v[176:179], v[196:199], 0\n\t" \
  "ds_read_b64_tr_b16 v[226:227], %[br0] offset:9216\n\t" \
  "global_load_lds_dwordx4 v250, s[68:69] offset:3072\n\t" \
  "v_mfma_f32_16x16x32_bf16 v[120:123], v[176:179], v[200:203], 0\n\t" \
  "ds_read_b128 v[208:211], %[ar1] offset:0\n\t" \
  "v_cvt_pk_bf16_f32 v16, v48, v49\n\t" \
  "v_cvt_pk_bf16_f32 v17, v50, v51\n\t" \
  "v_cvt_pk_bf16_f32 v18, v52, v53\n\t" \
  "v_mfma_f32_16x16x32_bf16 v[124:127], v[176:179], v[204:207], 0\n\t" \
  "ds_read_b64_tr_b16 v[228:229], %[br1] offset:8192\n\t" \
  "v_cvt_pk_bf16_f32 v19, v54, v55\n\t" \
  "v_cvt_pk_bf16_f32 v20, v56, v57\n\t" \
  "v_mfma_f32_16x16x32_bf16 v[128:131], v[180:183], v[192:195], 0\n\t" \
  "ds_read_b64_tr_b16 v[230:231], %[br1] offset:9216\n\t" \
  "v_cvt_pk_bf16_f32 v21, v58, v59\n\t" \
  "v_cvt_pk_bf16_f32 v22, v60, v61\n\t" \
  "v_mfma_f32_16x16x32_bf16 v[132:135], v[180:183], v[196:199], 0\n\t" \
  "ds_read_b64_tr_b16 v[232:233], %[br0] offset:8448\n\t" \
  "v_cvt_pk_bf16_f32 v23, v62, v63\n\t" \
  "v_cvt_pk_bf16_f32 v24, v64, v65\n\t" \
  "v_cvt_pk_bf16_f32 v25, v66, v67\n\t" \
  "v_mfma_f32_16x16x32_bf16 v[136:139], v[180:183], v[200:203], 0\n\t" \
  "ds_read_b64_tr_b16 v[234:235], %[br0] offset:9472\n\t" \
  "v_cvt_pk_bf16_f32 v26, v68, v69\n\t" \
  "v_cvt_pk_bf16_f32 v27, v70, v71\n\t" \
  "v_mfma_f32_16x16x32_bf16 v[140:143], v[180:183], v[204:207], 0\n\t" \
  "ds_read_b64_tr_b16 v[236:237], %[br1] offset:8448\n\t" \
  "v_cvt_pk_bf16_f32 v28, v72, v73\n\t" \
  "v_cvt_pk_bf16_f32 v29, v74, v75\n\t" \
  "v_mfma_f32_16x16x32_bf16 v[144:147], v[184:187], v[192:195], 0\n\t" \
  "ds_read_b64_tr_b16 v[238:239], %[br1] offset:9472\n\t" \
  "v_cvt_pk_bf16_f32 v30, v76, v77\n\t" \
  "v_cvt_pk_bf16_f32 v31, v78, v79\n\t" \
  "v_mfma_f32_16x16x32_bf16 v[148:151], v[184:187], v[196:199], 0\n\t" \
  "ds_read_b128 v[212:215], %[ar1] offset:2048\n\t" \
  "v_cvt_pk_bf16_f32 v32, v80, v81\n\t" \
  "v_cvt_pk_bf16_f32 v33, v82, v83\n\t" \
  "v_cvt_pk_bf16_f32 v34, v84, v85\n\t" \
  "v_mfma_f32_16x16x32_bf16 v[152:155], v[184:187], v[200:203], 0\n\t" \
  "ds_read_b128 v[216:219], %[ar1] offset:4096\n\t" \
  "v_cvt_pk_bf16_f32 v35, v86, v87\n\t" \
  "v_cvt_pk_bf16_f32 v36, v88, v89\n\t" \
  "v_mfma_f32_16x16x32_bf16 v[156:159], v[184:187], v[204:207], 0\n\t" \
  "ds_read_b128 v[220:223], %[ar1] offset:6144\n\t" \
  "v_cvt_pk_bf16_f32 v37, v90, v91\n\t" \
  "v_cvt_pk_bf16_f32 v38, v92, v93\n\t" \
  "v_mfma_f32_16x16x32_bf16 v[160:163], v[188:191], v[192:195], 0\n\t" \
  "v_cvt_pk_bf16_f32 v39, v94, v95\n\t" \
  "v_cvt_pk_bf16_f32 v40, v96, v97\n\t" \
  "v_cvt_pk_bf16_f32 v41, v98, v99\n\t" \
  "v_mfma_f32_16x16x32_bf16 v[164:167], v[188:191], v[196:199], 0\n\t" \
  "v_cvt_pk_bf16_f32 v42, v100, v101\n\t" \
  "v_cvt_pk_bf16_f32 v43, v102, v103\n\t" \
  "v_mfma_f32_16x16x32_bf16 v[168:171], v[188:191], v[200:203], 0\n\t" \
  "v_cvt_pk_bf16_f32 v44, v104, v105\n\t" \
  "v_cvt_pk_bf16_f32 v45, v106, v107\n\t" \
  "v_mfma_f32_16x16x32_bf16 v[172:175], v[188:191], v[204:207], 0\n\t" \
  "v_cvt_pk_bf16_f32 v46, v108, v109\n\t" \
  "v_cvt_pk_bf16_f32 v47, v110, v111\n\t" \
  "s_waitcnt vmcnt(12) lgkmcnt(0)\n\t" \
  "s_barrier\n\t" \
  "v_mfma_f32_16x16x32_bf16 v[112:115], v[208:211], v[224:227], v[112:115]\n\t" \
  "v_add_u32_e32 %[br0], -98304, %[br0]\n\t" \
  "v_add_u32_e32 %[br1], -98304, %[br1]\n\t" \
  "v_add_u32_e32 %[ar0], -98304, %[ar0]\n\t" \
  "v_add_u32_e32 %[ar1], -98304, %[ar1]\n\t" \
  "v_mfma_f32_16x16x32_bf16 v[116:119], v[208:211], v[228:231], v[116:119]\n\t" \
  "ds_read_b64_tr_b16 v[192:193], %[br0] offset:0\n\t" \
  "ds_read_b64_tr_b16 v[194:195], %[br0] offset:1024\n\t" \
  "s_add_u32 m0, %[db], 98304\n\t" \
  "s_add_u32 s66, s66, 524288\n\t" \
  "s_addc_u32 s67, s67, 0\n\t" \
  "global_load_lds_dwordx4 %[vb], s[66:67] offset:0\n\t" \
  "v_mfma_f32_16x16x32_bf16 v[120:123], v[208:211], v[232:235], v[120:123]\n\t" \
  "ds_read_b128 v[176:179], %[ar0] offset:0\n\t" \
  "global_load_lds_dwordx4 v251, s[66:67] offset:1024\n\t" \
  "v_mfma_f32_16x16x32_bf16 v[124:127], v[208:211], v[236:239], v[124:127]\n\t" \
  "ds_read_b64_tr_b16 v[196:197], %[br1] offset:0\n\t" \
  "v_mfma_f32_16x16x32_bf16 v[128:131], v[212:215], v[224:227], v[128:131]\n\t" \
  "ds_read_b64_tr_b16 v[198:199], %[br1] offset:1024\n\t" \
  "s_add_u32 m0, %[da], 98304\n\t" \
  "s_add_u32 s64, s64, 128\n\t" \
  "s_addc_u32 s65, s65, 0\n\t" \
  "global_load_lds_dwordx4 %[va], s[64:65] offset:0\n\t" \
  "v_mfma_f32_16x16x32_bf16 v[132:135], v[212:215], v[228:231], v[132:135]\n\t" \
  "ds_read_b64_tr_b16 v[200:201], %[br0] offset:256\n\t" \
  "global_load_lds_dwordx4 v248, s[64:65] offset:1024\n\t" \
  "v_mfma_f32_16x16x32_bf16 v[136:139], v[212:215], v[232:235], v[136:139]\n\t" \
  "ds_read_b64_tr_b16 v[202:203], %[br0] offset:1280\n\t" \
  "v_mfma_f32_16x16x32_bf16 v[140:143], v[212:215], v[236:239], v[140:143]\n\t" \
  "ds_read_b64_tr_b16 v[204:205], %[br1] offset:256\n\t" \
  "global_load_lds_dwordx4 v249, s[64:65] offset:2048\n\t" \
  "v_mfma_f32_16x16x32_bf16 v[144:147], v[216:219], v[224:227], v[144:147]\n\t" \
  "ds_read_b64_tr_b16 v[206:207], %[br1] offset:1280\n\t" \
  "global_load_lds_dwordx4 v250, s[64:65] offset:3072\n\t" \
  "v_mfma_f32_16x16x32_bf16 v[148:151], v[216:219], v[228:231], v[148:151]\n\t" \
  "ds_read_b128 v[180:183], %[ar0] offset:2048\n\t" \
  "v_mfma_f32_16x16x32_bf16 v[152:155], v[216:219], v[232:235], v[152:155]\n\t" \
  "ds_read_b128 v[184:187], %[ar0] offset:4096\n\t" \
  "s_add_u32 m0, %[db], 106496\n\t" \
  "s_add_u32 s68, s66, 262144\n\t" \
  "s_addc_u32 s69, s67, 0\n\t" \
  "global_load_lds_dwordx4 %[vb], s[68:69] offset:0\n\t" \
  "v_mfma_f32_16x16x32_bf16 v[156:159], v[216:219], v[236:239], v[156:159]\n\t" \
  "ds_read_b128 v[188:191], %[ar0] offset:6144\n\t" \
  "global_load_lds_dwordx4 v251, s[68:69] offset:1024\n\t" \
  "v_mfma_f32_16x16x32_bf16 v[160:163], v[220:223], v[224:227], v[160:163]\n\t" \
  "v_mfma_f32_16x16x32_bf16 v[164:167], v[220:223], v[228:231], v[164:167]\n\t" \
  "s_add_u32 m0, %[da], 114688\n\t" \
  "s_add_u32 s68, s64, 131072\n\t" \
  "s_addc_u32 s69, s65, 0\n\t" \
  "global_load_lds_dwordx4 %[va], s[68:69] offset:0\n\t" \
  "v_mfma_f32_16x16x32_bf16 v[168:171], v[220:223], v[232:235], v[168:171]\n\t" \
  "s_mov_b64 s[70:71], %[kd]\n\t" \
  "global_store_dwordx4 %[kst], v[16:19], s[70:71] offset:0 sc1\n\t" \
  "global_load_lds_dwordx4 v248, s[68:69] offset:1024\n\t" \
  "v_mfma_f32_16x16x32_bf16 v[172:175], v[220:223], v[236:239], v[172:175]\n\t" \
  "s_waitcnt lgkmcnt(0)\n\t" \
  "v_mfma_f32_16x16x32_bf16 v[112:115], v[176:179], v[192:195], v[112:115]\n\t" \
  "ds_read_b64_tr_b16 v[224:225], %[br0] offset:8192\n\t" \
  "global_load_lds_dwordx4 v249, s[68:69] offset:2048\n\t" \
  "v_mfma_f32_16x16x32_bf16 v[116:119], v[176:179], v[196:199], v[116:119]\n\t" \
  "ds_read_b64_tr_b16 v[226:227], %[br0] offset:9216\n\t" \
  "global_load_lds_dwordx4 v250, s[68:69] offset:3072\n\t" \
  "v_mfma_f32_16x16x32_bf16 v[120:123], v[176:179], v[200:203], v[120:123]\n\t" \
  "ds_read_b128 v[208:211], %[ar1] offset:0\n\t" \
  "v_mfma_f32_16x16x32_bf16 v[124:127], v[176:179], v[204:207], v[124:127]\n\t" \
  "ds_read_b64_tr_b16 v[228:229], %[br1] offset:8192\n\t" \
  "v_mfma_f32_16x16x32_bf16 v[128:131], v[180:183], v[192:195], v[128:131]\n\t" \
  "ds_read_b64_tr_b16 v[230:231], %[br1] offset:9216\n\t" \
  "v_mfma_f32_16x16x32_bf16 v[132:135], v[180:183], v[196:199], v[132:135]\n\t" \
  "ds_read_b64_tr_b16 v[232:233], %[br0] offset:8448\n\t" \
  "v_mfma_f32_16x16x32_bf16 v[136:139], v[180:183], v[200:203], v[136:139]\n\t" \
  "ds_read_b64_tr_b16 v[234:235], %[br0] offset:9472\n\t" \
  "v_mfma_f32_16x16x32_bf16 v[140:143], v[180:183], v[204:207], v[140:143]\n\t" \
  "ds_read_b64_tr_b16 v[236:237], %[br1] offset:8448\n\t" \
  "v_mfma_f32_16x16x32_bf16 v[144:147], v[184:187], v[192:195], v[144:147]\n\t" \
  "ds_read_b64_tr_b16 v[238:239], %[br1] offset:9472\n\t" \
  "v_mfma_f32_16x16x32_bf16 v[148:151], v[184:187], v[196:199], v[148:151]\n\t" \
  "ds_read_b128 v[212:215], %[ar1] offset:2048\n\t" \
  "v_mfma_f32_16x16x32_bf16 v[152:155], v[184:187], v[200:203], v[152:155]\n\t" \
  "ds_read_b128 v[216:219], %[ar1] offset:4096\n\t" \
  "v_mfma_f32_16x16x32_bf16 v[156:159], v[184:187], v[204:207], v[156:159]\n\t" \
  "ds_read_b128 v[220:223], %[ar1] offset:6144\n\t" \
  "v_mfma_f32_16x16x32_bf16 v[160:163], v[188:191], v[192:195], v[160:163]\n\t" \
  "v_mfma_f32_16x16x32_bf16 v[164:167], v[188:191], v[196:199], v[164:167]\n\t" \
  "v_mfma_f32_16x16x32_bf16 v[168:171], v[188:191], v[200:203], v[168:171]\n\t" \
  "v_mfma_f32_16x16x32_bf16 v[172:175], v[188:191], v[204:207], v[172:175]\n\t" \
  "s_waitcnt vmcnt(13) lgkmcnt(0)\n\t" \
  "s_barrier\n\t" \
  "s_branch L_JOIN%=\n\t" \
  "L_CLS_T%=:\n\t" \
  "ds_read_b64_tr_b16 v[192:193], %[br0] offset:0\n\t" \
  "ds_read_b64_tr_b16 v[194:195], %[br0] offset:1024\n\t" \
  "ds_read_b128 v[176:179], %[ar0] offset:0\n\t" \
  "ds_read_b64_tr_b16 v[196:197], %[br1] offset:0\n\t" \
  "ds_read_b64_tr_b16 v[198:199], %[br1] offset:1024\n\t" \
  "ds_read_b64_tr_b16 v[200:201], %[br0] offset:256\n\t" \
  "ds_read_b64_tr_b16 v[202:203], %[br0] offset:1280\n\t" \
  "ds_read_b64_tr_b16 v[204:205], %[br1] offset:256\n\t" \
  "ds_read_b64_tr_b16 v[206:207], %[br1] offset:1280\n\t" \
  "ds_read_b128 v[180:183], %[ar0] offset:2048\n\t" \
  "ds_read_b128 v[184:187], %[ar0] offset:4096\n\t" \
  "ds_read_b128 v[188:191], %[ar0] offset:6144\n\t" \
  "s_waitcnt lgkmcnt(0)\n\t" \
  "v_mfma_f32_16x16x32_bf16 v[48:51], v[192:195], v[176:179], 0\n\t" \
  "ds_read_b64_tr_b16 v[224:225], %[br0] offset:8192\n\t" \
  "v_mfma_f32_16x16x32_bf16 v[52:55], v[196:199], v[176:179], 0\n\t" \
  "ds_read_b64_tr_b16 v[226:227], %[br0] offset:9216\n\t" \
  "v_mfma_f32_16x16x32_bf16 v[56:59], v[200:203], v[176:179], 0\n\t" \
  "ds_read_b128 v[208:211], %[ar1] offset:0\n\t" \
  "v_mfma_f32_16x16x32_bf16 v[60:63], v[204:207], v[176:179], 0\n\t" \
  "ds_read_b64_tr_b16 v[228:229], %[br1] offset:8192\n\t" \
  "v_mfma_f32_16x16x32_bf16 v[64:67], v[192:195], v[180:183], 0\n\t" \
  "ds_read_b64_tr_b16 v[230:231], %[br1] offset:9216\n\t" \
  "v_mfma_f32_16x16x32_bf16 v[68:71], v[196:199], v[180:183], 0\n\t" \
  "ds_read_b64_tr_b16 v[232:233], %[br0] offset:8448\n\t" \
  "v_mfma_f32_16x16x32_bf16 v[72:75], v[200:203], v[180:183], 0\n\t" \
  "ds_read_b64_tr_b16 v[234:235], %[br0] offset:9472\n\t" \
  "v_mfma_f32_16x16x32_bf16 v[76:79], v[204:207], v[180:183], 0\n\t" \
  "ds_read_b64_tr_b16 v[236:237], %[br1] offset:8448\n\t" \
  "v_mfma_f32_16x16x32_bf16 v[80:83], v[192:195], v[184:187], 0\n\t" \
  "ds_read_b64_tr_b16 v[238:239], %[br1] offset:9472\n\t" \
  "v_mfma_f32_16x16x32_bf16 v[84:87], v[196:199], v[184:187], 0\n\t" \
  "ds_read_b128 v[212:215], %[ar1] offset:2048\n\t" \
  "v_mfma_f32_16x16x32_bf16 v[88:91], v[200:203], v[184:187], 0\n\t" \
  "ds_read_b128 v[216:219], %[ar1] offset:4096\n\t" \
  "v_mfma_f32_16x16x32_bf16 v[92:95], v[204:207], v[184:187], 0\n\t" \
  "ds_read_b128 v[220:223], %[ar1] offset:6144\n\t" \
  "v_mfma_f32_16x16x32_bf16 v[96:99], v[192:195], v[188:191], 0\n\t" \
  "v_mfma_f32_16x16x32_bf16 v[100:103], v[196:199], v[188:191], 0\n\t" \
  "v_mfma_f32_16x16x32_bf16 v[104:107], v[200:203], v[188:191], 0\n\t" \
  "v_mfma_f32_16x16x32_bf16 v[108:111], v[204:207], v[188:191], 0\n\t" \
  "s_waitcnt vmcnt(2) lgkmcnt(0)\n\t" \
  "s_barrier\n\t" \
  "v_mfma_f32_16x16x32_bf16 v[48:51], v[224:227], v[208:211], v[48:51]\n\t" \
  "v_add_u32_e32 %[br0], 49152, %[br0]\n\t" \
  "v_add_u32_e32 %[br1], 49152, %[br1]\n\t" \
  "v_add_u32_e32 %[ar0], 49152, %[ar0]\n\t" \
  "v_add_u32_e32 %[ar1], 49152, %[ar1]\n\t" \
  "v_mfma_f32_16x16x32_bf16 v[52:55], v[228:231], v[208:211], v[52:55]\n\t" \
  "ds_read_b64_tr_b16 v[192:193], %[br0] offset:0\n\t" \
  "ds_read_b64_tr_b16 v[194:195], %[br0] offset:1024\n\t" \
  "v_mfma_f32_16x16x32_bf16 v[56:59], v[232:235], v[208:211], v[56:59]\n\t" \
  "ds_read_b128 v[176:179], %[ar0] offset:0\n\t" \
  "v_mfma_f32_16x16x32_bf16 v[60:63], v[236:239], v[208:211], v[60:63]\n\t" \
  "ds_read_b64_tr_b16 v[196:197], %[br1] offset:0\n\t" \
  "v_mfma_f32_16x16x32_bf16 v[64:67], v[224:227], v[212:215], v[64:67]\n\t" \
  "ds_read_b64_tr_b16 v[198:199], %[br1] offset:1024\n\t" \
  "v_mfma_f32_16x16x32_bf16 v[68:71], v[228:231], v[212:215], v[68:71]\n\t" \
  "ds_read_b64_tr_b16 v[200:201], %[br0] offset:256\n\t" \
  "v_mfma_f32_16x16x32_bf16 v[72:75], v[232:235], v[212:215], v[72:75]\n\t" \
  "ds_read_b64_tr_b16 v[202:203], %[br0] offset:1280\n\t" \
  "v_mfma_f32_16x16x32_bf16 v[76:79], v[236:239], v[212:215], v[76:79]\n\t" \
  "ds_read_b64_tr_b16 v[204:205], %[br1] offset:256\n\t" \
  "v_mfma_f32_16x16x32_bf16 v[80:83], v[224:227], v[216:219], v[80:83]\n\t" \
  "ds_read_b64_tr_b16 v[206:207], %[br1] offset:1280\n\t" \
  "v_mfma_f32_16x16x32_bf16 v[84:87], v[228:231], v[216:219], v[84:87]\n\t" \
  "ds_read_b128 v[180:183], %[ar0] offset:2048\n\t" \
  "v_mfma_f32_16x16x32_bf16 v[88:91], v[232:235], v[216:219], v[88:91]\n\t" \
  "ds_read_b128 v[184:187], %[ar0] offset:4096\n\t" \
  "v_mfma_f32_16x16x32_bf16 v[92:95], v[236:239], v[216:219], v[92:95]\n\t" \
  "ds_read_b128 v[188:191], %[ar0] offset:6144\n\t" \
  "v_mfma_f32_16x16x32_bf16 v[96:99], v[224:227], v[220:223], v[96:99]\n\t" \
  "v_mfma_f32_16x16x32_bf16 v[100:103], v[228:231], v[220:223], v[100:103]\n\t" \
  "v_mfma_f32_16x16x32_bf16 v[104:107], v[232:235], v[220:223], v[104:107]\n\t" \
  "v_mfma_f32_16x16x32_bf16 v[108:111], v[236:239], v[220:223], v[108:111]\n\t" \
  "s_waitcnt lgkmcnt(0)\n\t" \
  "v_mfma_f32_16x16x32_bf16 v[48:51], v[192:195], v[176:179], v[48:51]\n\t" \
  "ds_read_b64_tr_b16 v[224:225], %[br0] offset:8192\n\t" \
  "v_mfma_f32_16x16x32_bf16 v[52:55], v[196:199], v[176:179], v[52:55]\n\t" \
  "ds_read_b64_tr_b16 v[226:227], %[br0] offset:9216\n\t" \
  "v_mfma_f32_16x16x32_bf16 v[56:59], v[200:203], v[176:179], v[56:59]\n\t" \
  "ds_read_b128 v[208:211], %[ar1] offset:0\n\t" \
  "v_mfma_f32_16x16x32_bf16 v[60:63], v[204:207], v[176:179], v[60:63]\n\t" \
  "ds_read_b64_tr_b16 v[228:229], %[br1] offset:8192\n\t" \
  "v_mfma_f32_16x16x32_bf16 v[64:67], v[192:195], v[180:183], v[64:67]\n\t" \
  "ds_read_b64_tr_b16 v[230:231], %[br1] offset:9216\n\t" \
  "v_mfma_f32_16x16x32_bf16 v[68:71], v[196:199], v[180:183], v[68:71]\n\t" \
  "ds_read_b64_tr_b16 v[232:233], %[br0] offset:8448\n\t" \
  "v_mfma_f32_16x16x32_bf16 v[72:75], v[200:203], v[180:183], v[72:75]\n\t" \
  "ds_read_b64_tr_b16 v[234:235], %[br0] offset:9472\n\t" \
  "v_mfma_f32_16x16x32_bf16 v[76:79], v[204:207], v[180:183], v[76:79]\n\t" \
  "ds_read_b64_tr_b16 v[236:237], %[br1] offset:8448\n\t" \
  "v_mfma_f32_16x16x32_bf16 v[80:83], v[192:195], v[184:187], v[80:83]\n\t" \
  "ds_read_b64_tr_b16 v[238:239], %[br1] offset:9472\n\t" \
  "v_mfma_f32_16x16x32_bf16 v[84:87], v[196:199], v[184:187], v[84:87]\n\t" \
  "ds_read_b128 v[212:215], %[ar1] offset:2048\n\t" \
  "v_mfma_f32_16x16x32_bf16 v[88:91], v[200:203], v[184:187], v[88:91]\n\t" \
  "ds_read_b128 v[216:219], %[ar1] offset:4096\n\t" \
  "v_mfma_f32_16x16x32_bf16 v[92:95], v[204:207], v[184:187], v[92:95]\n\t" \
  "ds_read_b128 v[220:223], %[ar1] offset:6144\n\t" \
  "v_mfma_f32_16x16x32_bf16 v[96:99], v[192:195], v[188:191], v[96:99]\n\t" \
  "v_mfma_f32_16x16x32_bf16 v[100:103], v[196:199], v[188:191], v[100:103]\n\t" \
  "v_mfma_f32_16x16x32_bf16 v[104:107], v[200:203], v[188:191], v[104:107]\n\t" \
  "v_mfma_f32_16x16x32_bf16 v[108:111], v[204:207], v[188:191], v[108:111]\n\t" \
  "s_waitcnt lgkmcnt(0)\n\t" \
  "s_barrier\n\t" \
  "v_mfma_f32_16x16x32_bf16 v[48:51], v[224:227], v[208:211], v[48:51]\n\t" \
  "v_add_u32_e32 %[br0], 49152, %[br0]\n\t" \
  "v_add_u32_e32 %[br1], 49152, %[br1]\n\t" \
  "v_add_u32_e32 %[ar0], 49152, %[ar0]\n\t" \
  "v_add_u32_e32 %[ar1], 49152, %[ar1]\n\t" \
  "v_mfma_f32_16x16x32_bf16 v[52:55], v[228:231], v[208:211], v[52:55]\n\t" \
  "ds_read_b64_tr_b16 v[192:193], %[br0] offset:0\n\t" \
  "ds_read_b64_tr_b16 v[194:195], %[br0] offset:1024\n\t" \
  "v_mfma_f32_16x16x32_bf16 v[56:59], v[232:235], v[208:211], v[56:59]\n\t" \
  "ds_read_b128 v[176:179], %[ar0] offset:0\n\t" \
  "v_mfma_f32_16x16x32_bf16 v[60:63], v[236:239], v[208:211], v[60:63]\n\t" \
  "ds_read_b64_tr_b16 v[196:197], %[br1] offset:0\n\t" \
  "v_mfma_f32_16x16x32_bf16 v[64:67], v[224:227], v[212:215], v[64:67]\n\t" \
  "ds_read_b64_tr_b16 v[198:199], %[br1] offset:1024\n\t" \
  "v_mfma_f32_16x16x32_bf16 v[68:71], v[228:231], v[212:215], v[68:71]\n\t" \
  "ds_read_b64_tr_b16 v[200:201], %[br0] offset:256\n\t" \
  "v_mfma_f32_16x16x32_bf16 v[72:75], v[232:235], v[212:215], v[72:75]\n\t" \
  "ds_read_b64_tr_b16 v[202:203], %[br0] offset:1280\n\t" \
  "v_mfma_f32_16x16x32_bf16 v[76:79], v[236:239], v[212:215], v[76:79]\n\t" \
  "ds_read_b64_tr_b16 v[204:205], %[br1] offset:256\n\t" \
  "v_mfma_f32_16x16x32_bf16 v[80:83], v[224:227], v[216:219], v[80:83]\n\t" \
  "ds_read_b64_tr_b16 v[206:207], %[br1] offset:1280\n\t" \
  "v_mfma_f32_16x16x32_bf16 v[84:87], v[228:231], v[216:219], v[84:87]\n\t" \
  "ds_read_b128 v[180:183], %[ar0] offset:2048\n\t" \
  "v_mfma_f32_16x16x32_bf16 v[88:91], v[232:235], v[216:219], v[88:91]\n\t" \
  "ds_read_b128 v[184:187], %[ar0] offset:4096\n\t" \
  "v_mfma_f32_16x16x32_bf16 v[92:95], v[236:239], v[216:219], v[92:95]\n\t" \
  "ds_read_b128 v[188:191], %[ar0] offset:6144\n\t" \
  "v_mfma_f32_16x16x32_bf16 v[96:99], v[224:227], v[220:223], v[96:99]\n\t" \
  "v_mfma_f32_16x16x32_bf16 v[100:103], v[228:231], v[220:223], v[100:103]\n\t" \
  "v_mfma_f32_16x16x32_bf16 v[104:107], v[232:235], v[220:223], v[104:107]\n\t" \
  "v_mfma_f32_16x16x32_bf16 v[108:111], v[236:239], v[220:223], v[108:111]\n\t" \
  "s_waitcnt lgkmcnt(0)\n\t" \
  "v_mfma_f32_16x16x32_bf16 v[48:51], v[192:195], v[176:179], v[48:51]\n\t" \
  "ds_read_b64_tr_b16 v[224:225], %[br0] offset:8192\n\t" \
  "v_mfma_f32_16x16x32_bf16 v[52:55], v[196:199], v[176:179], v[52:55]\n\t" \
  "ds_read_b64_tr_b16 v[226:227], %[br0] offset:9216\n\t" \
  "v_mfma_f32_16x16x32_bf16 v[56:59], v[200:203], v[176:179], v[56:59]\n\t" \
  "ds_read_b128 v[208:211], %[ar1] offset:0\n\t" \
  "v_mfma_f32_16x16x32_bf16 v[60:63], v[204:207], v[176:179], v[60:63]\n\t" \
  "ds_read_b64_tr_b16 v[228:229], %[br1] offset:8192\n\t" \
  "v_mfma_f32_16x16x32_bf16 v[64:67], v[192:195], v[180:183], v[64:67]\n\t" \
  "ds_read_b64_tr_b16 v[230:231], %[br1] offset:9216\n\t" \
  "v_mfma_f32_16x16x32_bf16 v[68:71], v[196:199], v[180:183], v[68:71]\n\t" \
  "ds_read_b64_tr_b16 v[232:233], %[br0] offset:8448\n\t" \
  "v_mfma_f32_16x16x32_bf16 v[72:75], v[200:203], v[180:183], v[72:75]\n\t" \
  "ds_read_b64_tr_b16 v[234:235], %[br0] offset:9472\n\t" \
  "v_mfma_f32_16x16x32_bf16 v[76:79], v[204:207], v[180:183], v[76:79]\n\t" \
  "ds_read_b64_tr_b16 v[236:237], %[br1] offset:8448\n\t" \
  "v_mfma_f32_16x16x32_bf16 v[80:83], v[192:195], v[184:187], v[80:83]\n\t" \
  "ds_read_b64_tr_b16 v[238:239], %[br1] offset:9472\n\t" \
  "s_waitcnt vmcnt(0)\n\t" \
  "s_mov_b64 exec, 1\n\t" \
  "global_atomic_add v240, v241, %[hcp]\n\t" \
  "s_mov_b64 exec, -1\n\t" \
  "v_mfma_f32_16x16x32_bf16 v[84:87], v[196:199], v[184:187], v[84:87]\n\t" \
  "ds_read_b128 v[212:215], %[ar1] offset:2048\n\t" \
  "v_mfma_f32_16x16x32_bf16 v[88:91], v[200:203], v[184:187], v[88:91]\n\t" \
  "ds_read_b128 v[216:219], %[ar1] offset:4096\n\t" \
  "v_mfma_f32_16x16x32_bf16 v[92:95], v[204:207], v[184:187], v[92:95]\n\t" \
  "ds_read_b128 v[220:223], %[ar1] offset:6144\n\t" \
  "v_mfma_f32_16x16x32_bf16 v[96:99], v[192:195], v[188:191], v[96:99]\n\t" \
  "v_mfma_f32_16x16x32_bf16 v[100:103], v[196:199], v[188:191], v[100:103]\n\t" \
  "v_mfma_f32_16x16x32_bf16 v[104:107], v[200:203], v[188:191], v[104:107]\n\t" \
  "v_mfma_f32_16x16x32_bf16 v[108:111], v[204:207], v[188:191], v[108:111]\n\t" \
  "s_waitcnt lgkmcnt(0)\n\t" \
  "s_barrier\n\t" \
  "v_mfma_f32_16x16x32_bf16 v[48:51], v[224:227], v[208:211], v[48:51]\n\t" \
  "v_add_u32_e32 %[br0], -98304, %[br0]\n\t" \
  "v_add_u32_e32 %[br1], -98304, %[br1]\n\t" \
  "v_add_u32_e32 %[ar0], -98304, %[ar0]\n\t" \
  "v_add_u32_e32 %[ar1], -98304, %[ar1]\n\t" \
  "v_mfma_f32_16x16x32_bf16 v[52:55], v[228:231], v[208:211], v[52:55]\n\t" \
  "ds_read_b64_tr_b16 v[192:193], %[br0] offset:0\n\t" \
  "ds_read_b64_tr_b16 v[194:195], %[br0] offset:1024\n\t" \
  "v_mfma_f32_16x16x32_bf16 v[56:59], v[232:235], v[208:211], v[56:59]\n\t" \
  "ds_read_b128 v[176:179], %[ar0] offset:0\n\t" \
  "global_load_dword v243, %[wmv], %[wm0]\n\t" \
  "v_mfma_f32_16x16x32_bf16 v[60:63], v[236:239], v[208:211], v[60:63]\n\t" \
  "ds_read_b64_tr_b16 v[196:197], %[br1] offset:0\n\t" \
  "v_mfma_f32_16x16x32_bf16 v[64:67], v[224:227], v[212:215], v[64:67]\n\t" \
  "ds_read_b64_tr_b16 v[198:199], %[br1] offset:1024\n\t" \
  "v_mfma_f32_16x16x32_bf16 v[68:71], v[228:231], v[212:215], v[68:71]\n\t" \
  "ds_read_b64_tr_b16 v[200:201], %[br0] offset:256\n\t" \
  "v_mfma_f32_16x16x32_bf16 v[72:75], v[232:235], v[212:215], v[72:75]\n\t" \
  "ds_read_b64_tr_b16 v[202:203], %[br0] offset:1280\n\t" \
  "v_mfma_f32_16x16x32_bf16 v[76:79], v[236:239], v[212:215], v[76:79]\n\t" \
  "ds_read_b64_tr_b16 v[204:205], %[br1] offset:256\n\t" \
  "v_mfma_f32_16x16x32_bf16 v[80:83], v[224:227], v[216:219], v[80:83]\n\t" \
  "ds_read_b64_tr_b16 v[206:207], %[br1] offset:1280\n\t" \
  "v_mfma_f32_16x16x32_bf16 v[84:87], v[228:231], v[216:219], v[84:87]\n\t" \
  "ds_read_b128 v[180:183], %[ar0] offset:2048\n\t" \
  "v_mfma_f32_16x16x32_bf16 v[88:91], v[232:235], v[216:219], v[88:91]\n\t" \
  "ds_read_b128 v[184:187], %[ar0] offset:4096\n\t" \
  "v_mfma_f32_16x16x32_bf16 v[92:95], v[236:239], v[216:219], v[92:95]\n\t" \
  "ds_read_b128 v[188:191], %[ar0] offset:6144\n\t" \
  "v_mfma_f32_16x16x32_bf16 v[96:99], v[224:227], v[220:223], v[96:99]\n\t" \
  "v_mfma_f32_16x16x32_bf16 v[100:103], v[228:231], v[220:223], v[100:103]\n\t" \
  "v_mfma_f32_16x16x32_bf16 v[104:107], v[232:235], v[220:223], v[104:107]\n\t" \
  "v_mfma_f32_16x16x32_bf16 v[108:111], v[236:239], v[220:223], v[108:111]\n\t" \
  "s_waitcnt lgkmcnt(0)\n\t" \
  "v_mfma_f32_16x16x32_bf16 v[48:51], v[192:195], v[176:179], v[48:51]\n\t" \
  "ds_read_b64_tr_b16 v[224:225], %[br0] offset:8192\n\t" \
  "v_mfma_f32_16x16x32_bf16 v[52:55], v[196:199], v[176:179], v[52:55]\n\t" \
  "ds_read_b64_tr_b16 v[226:227], %[br0] offset:9216\n\t" \
  "v_mfma_f32_16x16x32_bf16 v[56:59], v[200:203], v[176:179], v[56:59]\n\t" \
  "ds_read_b128 v[208:211], %[ar1] offset:0\n\t" \
  "v_mfma_f32_16x16x32_bf16 v[60:63], v[204:207], v[176:179], v[60:63]\n\t" \
  "ds_read_b64_tr_b16 v[228:229], %[br1] offset:8192\n\t" \
  "v_mfma_f32_16x16x32_bf16 v[64:67], v[192:195], v[180:183], v[64:67]\n\t" \
  "ds_read_b64_tr_b16 v[230:231], %[br1] offset:9216\n\t" \
  "v_mfma_f32_16x16x32_bf16 v[68:71], v[196:199], v[180:183], v[68:71]\n\t" \
  "ds_read_b64_tr_b16 v[232:233], %[br0] offset:8448\n\t" \
  "v_mfma_f32_16x16x32_bf16 v[72:75], v[200:203], v[180:183], v[72:75]\n\t" \
  "ds_read_b64_tr_b16 v[234:235], %[br0] offset:9472\n\t" \
  "v_mfma_f32_16x16x32_bf16 v[76:79], v[204:207], v[180:183], v[76:79]\n\t" \
  "ds_read_b64_tr_b16 v[236:237], %[br1] offset:8448\n\t" \
  "v_mfma_f32_16x16x32_bf16 v[80:83], v[192:195], v[184:187], v[80:83]\n\t" \
  "ds_read_b64_tr_b16 v[238:239], %[br1] offset:9472\n\t" \
  "v_mfma_f32_16x16x32_bf16 v[84:87], v[196:199], v[184:187], v[84:87]\n\t" \
  "ds_read_b128 v[212:215], %[ar1] offset:2048\n\t" \
  "v_mfma_f32_16x16x32_bf16 v[88:91], v[200:203], v[184:187], v[88:91]\n\t" \
  "ds_read_b128 v[216:219], %[ar1] offset:4096\n\t" \
  "v_mfma_f32_16x16x32_bf16 v[92:95], v[204:207], v[184:187], v[92:95]\n\t" \
  "ds_read_b128 v[220:223], %[ar1] offset:6144\n\t" \
  "v_mfma_f32_16x16x32_bf16 v[96:99], v[192:195], v[188:191], v[96:99]\n\t" \
  "v_mfma_f32_16x16x32_bf16 v[100:103], v[196:199], v[188:191], v[100:103]\n\t" \
  "v_mfma_f32_16x16x32_bf16 v[104:107], v[200:203], v[188:191], v[104:107]\n\t" \
  "v_mfma_f32_16x16x32_bf16 v[108:111], v[204:207], v[188:191], v[108:111]\n\t" \
  "s_waitcnt lgkmcnt(0)\n\t" \
  "s_barrier\n\t" \
  "v_mfma_f32_16x16x32_bf16 v[48:51], v[224:227], v[208:211], v[48:51]\n\t" \
  "v_add_u32_e32 %[br0], 49152, %[br0]\n\t" \
  "v_add_u32_e32 %[br1], 49152, %[br1]\n\t" \
  "v_add_u32_e32 %[ar0], 49152, %[ar0]\n\t" \
  "v_add_u32_e32 %[ar1], 49152, %[ar1]\n\t" \
  "v_mfma_f32_16x16x32_bf16 v[52:55], v[228:231], v[208:211], v[52:55]\n\t" \
  "ds_read_b64_tr_b16 v[192:193], %[br0] offset:0\n\t" \
  "ds_read_b64_tr_b16 v[194:195], %[br0] offset:1024\n\t" \
  "v_mfma_f32_16x16x32_bf16 v[56:59], v[232:235], v[208:211], v[56:59]\n\t" \
  "ds_read_b128 v[176:179], %[ar0] offset:0\n\t" \
  "v_mfma_f32_16x16x32_bf16 v[60:63], v[236:239], v[208:211], v[60:63]\n\t" \
  "ds_read_b64_tr_b16 v[196:197], %[br1] offset:0\n\t" \
  "v_mfma_f32_16x16x32_bf16 v[64:67], v[224:227], v[212:215], v[64:67]\n\t" \
  "ds_read_b64_tr_b16 v[198:199], %[br1] offset:1024\n\t" \
  "v_mfma_f32_16x16x32_bf16 v[68:71], v[228:231], v[212:215], v[68:71]\n\t" \
  "ds_read_b64_tr_b16 v[200:201], %[br0] offset:256\n\t" \
  "v_mfma_f32_16x16x32_bf16 v[72:75], v[232:235], v[212:215], v[72:75]\n\t" \
  "ds_read_b64_tr_b16 v[202:203], %[br0] offset:1280\n\t" \
  "v_mfma_f32_16x16x32_bf16 v[76:79], v[236:239], v[212:215], v[76:79]\n\t" \
  "ds_read_b64_tr_b16 v[204:205], %[br1] offset:256\n\t" \
  "v_mfma_f32_16x16x32_bf16 v[80:83], v[224:227], v[216:219], v[80:83]\n\t" \
  "ds_read_b64_tr_b16 v[206:207], %[br1] offset:1280\n\t" \
  "v_mfma_f32_16x16x32_bf16 v[84:87], v[228:231], v[216:219], v[84:87]\n\t" \
  "ds_read_b128 v[180:183], %[ar0] offset:2048\n\t" \
  "v_mfma_f32_16x16x32_bf16 v[88:91], v[232:235], v[216:219], v[88:91]\n\t" \
  "ds_read_b128 v[184:187], %[ar0] offset:4096\n\t" \
  "v_mfma_f32_16x16x32_bf16 v[92:95], v[236:239], v[216:219], v[92:95]\n\t" \
  "ds_read_b128 v[188:191], %[ar0] offset:6144\n\t" \
  "v_mfma_f32_16x16x32_bf16 v[96:99], v[224:227], v[220:223], v[96:99]\n\t" \
  "v_mfma_f32_16x16x32_bf16 v[100:103], v[228:231], v[220:223], v[100:103]\n\t" \
  "v_mfma_f32_16x16x32_bf16 v[104:107], v[232:235], v[220:223], v[104:107]\n\t" \
  "v_mfma_f32_16x16x32_bf16 v[108:111], v[236:239], v[220:223], v[108:111]\n\t" \
  "s_waitcnt lgkmcnt(0)\n\t" \
  "v_mfma_f32_16x16x32_bf16 v[48:51], v[192:195], v[176:179], v[48:51]\n\t" \
  "ds_read_b64_tr_b16 v[224:225], %[br0] offset:8192\n\t" \
  "v_mfma_f32_16x16x32_bf16 v[52:55], v[196:199], v[176:179], v[52:55]\n\t" \
  "ds_read_b64_tr_b16 v[226:227], %[br0] offset:9216\n\t" \
  "v_mfma_f32_16x16x32_bf16 v[56:59], v[200:203], v[176:179], v[56:59]\n\t" \
  "ds_read_b128 v[208:211], %[ar1] offset:0\n\t" \
  "v_mfma_f32_16x16x32_bf16 v[60:63], v[204:207], v[176:179], v[60:63]\n\t" \
  "ds_read_b64_tr_b16 v[228:229], %[br1] offset:8192\n\t" \
  "v_mfma_f32_16x16x32_bf16 v[64:67], v[192:195], v[180:183], v[64:67]\n\t" \
  "ds_read_b64_tr_b16 v[230:231], %[br1] offset:9216\n\t" \
  "v_mfma_f32_16x16x32_bf16 v[68:71], v[196:199], v[180:183], v[68:71]\n\t" \
  "ds_read_b64_tr_b16 v[232:233], %[br0] offset:8448\n\t" \
  "v_mfma_f32_16x16x32_bf16 v[72:75], v[200:203], v[180:183], v[72:75]\n\t" \
  "ds_read_b64_tr_b16 v[234:235], %[br0] offset:9472\n\t" \
  "v_mfma_f32_16x16x32_bf16 v[76:79], v[204:207], v[180:183], v[76:79]\n\t" \
  "ds_read_b64_tr_b16 v[236:237], %[br1] offset:8448\n\t" \
  "v_mfma_f32_16x16x32_bf16 v[80:83], v[192:195], v[184:187], v[80:83]\n\t" \
  "ds_read_b64_tr_b16 v[238:239], %[br1] offset:9472\n\t" \
  "v_mfma_f32_16x16x32_bf16 v[84:87], v[196:199], v[184:187], v[84:87]\n\t" \
  "ds_read_b128 v[212:215], %[ar1] offset:2048\n\t" \
  "v_mfma_f32_16x16x32_bf16 v[88:91], v[200:203], v[184:187], v[88:91]\n\t" \
  "ds_read_b128 v[216:219], %[ar1] offset:4096\n\t" \
  "v_mfma_f32_16x16x32_bf16 v[92:95], v[204:207], v[184:187], v[92:95]\n\t" \
  "ds_read_b128 v[220:223], %[ar1] offset:6144\n\t" \
  "v_mfma_f32_16x16x32_bf16 v[96:99], v[192:195], v[188:191], v[96:99]\n\t" \
  "v_mfma_f32_16x16x32_bf16 v[100:103], v[196:199], v[188:191], v[100:103]\n\t" \
  "v_mfma_f32_16x16x32_bf16 v[104:107], v[200:203], v[188:191], v[104:107]\n\t" \
  "v_mfma_f32_16x16x32_bf16 v[108:111], v[204:207], v[188:191], v[108:111]\n\t" \
  "s_waitcnt lgkmcnt(0)\n\t" \
  "s_barrier\n\t" \
  "v_mfma_f32_16x16x32_bf16 v[48:51], v[224:227], v[208:211], v[48:51]\n\t" \
  "v_add_u32_e32 %[br0], 49152, %[br0]\n\t" \
  "v_add_u32_e32 %[br1], 49152, %[br1]\n\t" \
  "v_add_u32_e32 %[ar0], 49152, %[ar0]\n\t" \
  "v_add_u32_e32 %[ar1], 49152, %[ar1]\n\t" \
  "v_mfma_f32_16x16x32_bf16 v[52:55], v[228:231], v[208:211], v[52:55]\n\t" \
  "ds_read_b64_tr_b16 v[192:193], %[br0] offset:0\n\t" \
  "ds_read_b64_tr_b16 v[194:195], %[br0] offset:1024\n\t" \
  "v_mfma_f32_16x16x32_bf16 v[56:59], v[232:235], v[208:211], v[56:59]\n\t" \
  "ds_read_b128 v[176:179], %[ar0] offset:0\n\t" \
  "v_mfma_f32_16x16x32_bf16 v[60:63], v[236:239], v[208:211], v[60:63]\n\t" \
  "ds_read_b64_tr_b16 v[196:197], %[br1] offset:0\n\t" \
  "v_mfma_f32_16x16x32_bf16 v[64:67], v[224:227], v[212:215], v[64:67]\n\t" \
  "ds_read_b64_tr_b16 v[198:199], %[br1] offset:1024\n\t" \
  "v_mfma_f32_16x16x32_bf16 v[68:71], v[228:231], v[212:215], v[68:71]\n\t" \
  "ds_read_b64_tr_b16 v[200:201], %[br0] offset:256\n\t" \
  "v_mfma_f32_16x16x32_bf16 v[72:75], v[232:235], v[212:215], v[72:75]\n\t" \
  "ds_read_b64_tr_b16 v[202:203], %[br0] offset:1280\n\t" \
  "v_mfma_f32_16x16x32_bf16 v[76:79], v[236:239], v[212:215], v[76:79]\n\t" \
  "ds_read_b64_tr_b16 v[204:205], %[br1] offset:256\n\t" \
  "v_mfma_f32_16x16x32_bf16 v[80:83], v[224:227], v[216:219], v[80:83]\n\t" \
  "ds_read_b64_tr_b16 v[206:207], %[br1] offset:1280\n\t" \
  "v_mfma_f32_16x16x32_bf16 v[84:87], v[228:231], v[216:219], v[84:87]\n\t" \
  "ds_read_b128 v[180:183], %[ar0] offset:2048\n\t" \
  "v_mfma_f32_16x16x32_bf16 v[88:91], v[232:235], v[216:219], v[88:91]\n\t" \
  "ds_read_b128 v[184:187], %[ar0] offset:4096\n\t" \
  "v_mfma_f32_16x16x32_bf16 v[92:95], v[236:239], v[216:219], v[92:95]\n\t" \
  "ds_read_b128 v[188:191], %[ar0] offset:6144\n\t" \
  "v_mfma_f32_16x16x32_bf16 v[96:99], v[224:227], v[220:223], v[96:99]\n\t" \
  "v_mfma_f32_16x16x32_bf16 v[100:103], v[228:231], v[220:223], v[100:103]\n\t" \
  "v_mfma_f32_16x16x32_bf16 v[104:107], v[232:235], v[220:223], v[104:107]\n\t" \
  "v_mfma_f32_16x16x32_bf16 v[108:111], v[236:239], v[220:223], v[108:111]\n\t" \
  "s_waitcnt lgkmcnt(0)\n\t" \
  "v_mfma_f32_16x16x32_bf16 v[48:51], v[192:195], v[176:179], v[48:51]\n\t" \
  "ds_read_b64_tr_b16 v[224:225], %[br0] offset:8192\n\t" \
  "v_mfma_f32_16x16x32_bf16 v[52:55], v[196:199], v[176:179], v[52:55]\n\t" \
  "ds_read_b64_tr_b16 v[226:227], %[br0] offset:9216\n\t" \
  "v_mfma_f32_16x16x32_bf16 v[56:59], v[200:203], v[176:179], v[56:59]\n\t" \
  "ds_read_b128 v[208:211], %[ar1] offset:0\n\t" \
  "v_mfma_f32_16x16x32_bf16 v[60:63], v[204:207], v[176:179], v[60:63]\n\t" \
  "ds_read_b64_tr_b16 v[228:229], %[br1] offset:8192\n\t" \
  "v_mfma_f32_16x16x32_bf16 v[64:67], v[192:195], v[180:183], v[64:67]\n\t" \
  "ds_read_b64_tr_b16 v[230:231], %[br1] offset:9216\n\t" \
  "v_mfma_f32_16x16x32_bf16 v[68:71], v[196:199], v[180:183], v[68:71]\n\t" \
  "ds_read_b64_tr_b16 v[232:233], %[br0] offset:8448\n\t" \
  "v_mfma_f32_16x16x32_bf16 v[72:75], v[200:203], v[180:183], v[72:75]\n\t" \
  "ds_read_b64_tr_b16 v[234:235], %[br0] offset:9472\n\t" \
  "v_mfma_f32_16x16x32_bf16 v[76:79], v[204:207], v[180:183], v[76:79]\n\t" \
  "ds_read_b64_tr_b16 v[236:237], %[br1] offset:8448\n\t" \
  "v_mfma_f32_16x16x32_bf16 v[80:83], v[192:195], v[184:187], v[80:83]\n\t" \
  "ds_read_b64_tr_b16 v[238:239], %[br1] offset:9472\n\t" \
  "v_mfma_f32_16x16x32_bf16 v[84:87], v[196:199], v[184:187], v[84:87]\n\t" \
  "ds_read_b128 v[212:215], %[ar1] offset:2048\n\t" \
  "v_mfma_f32_16x16x32_bf16 v[88:91], v[200:203], v[184:187], v[88:91]\n\t" \
  "ds_read_b128 v[216:219], %[ar1] offset:4096\n\t" \
  "v_mfma_f32_16x16x32_bf16 v[92:95], v[204:207], v[184:187], v[92:95]\n\t" \
  "ds_read_b128 v[220:223], %[ar1] offset:6144\n\t" \
  "v_mfma_f32_16x16x32_bf16 v[96:99], v[192:195], v[188:191], v[96:99]\n\t" \
  "v_mfma_f32_16x16x32_bf16 v[100:103], v[196:199], v[188:191], v[100:103]\n\t" \
  "v_mfma_f32_16x16x32_bf16 v[104:107], v[200:203], v[188:191], v[104:107]\n\t" \
  "v_mfma_f32_16x16x32_bf16 v[108:111], v[204:207], v[188:191], v[108:111]\n\t" \
  "s_waitcnt lgkmcnt(0)\n\t" \
  "s_barrier\n\t" \
  "v_mfma_f32_16x16x32_bf16 v[48:51], v[224:227], v[208:211], v[48:51]\n\t" \
  "v_add_u32_e32 %[br0], -98304, %[br0]\n\t" \
  "v_add_u32_e32 %[br1], -98304, %[br1]\n\t" \
  "v_add_u32_e32 %[ar0], -98304, %[ar0]\n\t" \
  "v_add_u32_e32 %[ar1], -98304, %[ar1]\n\t" \
  "v_mfma_f32_16x16x32_bf16 v[52:55], v[228:231], v[208:211], v[52:55]\n\t" \
  "ds_read_b64_tr_b16 v[192:193], %[br0] offset:0\n\t" \
  "ds_read_b64_tr_b16 v[194:195], %[br0] offset:1024\n\t" \
  "s_mov_b64 s[66:67], %[gb1]\n\t" \
  "s_mov_b64 s[64:65], %[ga]\n\t" \
  "v_mfma_f32_16x16x32_bf16 v[56:59], v[232:235], v[208:211], v[56:59]\n\t" \
  "ds_read_b128 v[176:179], %[ar0] offset:0\n\t" \
  "v_mfma_f32_16x16x32_bf16 v[60:63], v[236:239], v[208:211], v[60:63]\n\t" \
  "ds_read_b64_tr_b16 v[196:197], %[br1] offset:0\n\t" \
  "v_mfma_f32_16x16x32_bf16 v[64:67], v[224:227], v[212:215], v[64:67]\n\t" \
  "ds_read_b64_tr_b16 v[198:199], %[br1] offset:1024\n\t" \
  "v_mfma_f32_16x16x32_bf16 v[68:71], v[228:231], v[212:215], v[68:71]\n\t" \
  "ds_read_b64_tr_b16 v[200:201], %[br0] offset:256\n\t" \
  "v_mfma_f32_16x16x32_bf16 v[72:75], v[232:235], v[212:215], v[72:75]\n\t" \
  "ds_read_b64_tr_b16 v[202:203], %[br0] offset:1280\n\t" \
  "v_mfma_f32_16x16x32_bf16 v[76:79], v[236:239], v[212:215], v[76:79]\n\t" \
  "ds_read_b64_tr_b16 v[204:205], %[br1] offset:256\n\t" \
  "v_mfma_f32_16x16x32_bf16 v[80:83], v[224:227], v[216:219], v[80:83]\n\t" \
  "ds_read_b64_tr_b16 v[206:207], %[br1] offset:1280\n\t" \
  "v_mfma_f32_16x16x32_bf16 v[84:87], v[228:231], v[216:219], v[84:87]\n\t" \
  "ds_read_b128 v[180:183], %[ar0] offset:2048\n\t" \
  "v_mfma_f32_16x16x32_bf16 v[88:91], v[232:235], v[216:219], v[88:91]\n\t" \
  "ds_read_b128 v[184:187], %[ar0] offset:4096\n\t" \
  "v_mfma_f32_16x16x32_bf16 v[92:95], v[236:239], v[216:219], v[92:95]\n\t" \
  "ds_read_b128 v[188:191], %[ar0] offset:6144\n\t" \
  "v_mfma_f32_16x16x32_bf16 v[96:99], v[224:227], v[220:223], v[96:99]\n\t" \
  "v_mfma_f32_16x16x32_bf16 v[100:103], v[228:231], v[220:223], v[100:103]\n\t" \
  "v_mfma_f32_16x16x32_bf16 v[104:107], v[232:235], v[220:223], v[104:107]\n\t" \
  "v_mfma_f32_16x16x32_bf16 v[108:111], v[236:239], v[220:223], v[108:111]\n\t" \
  "s_waitcnt lgkmcnt(0)\n\t" \
  "v_mfma_f32_16x16x32_bf16 v[48:51], v[192:195], v[176:179], v[48:51]\n\t" \
  "ds_read_b64_tr_b16 v[224:225], %[br0] offset:8192\n\t" \
  "v_mfma_f32_16x16x32_bf16 v[52:55], v[196:199], v[176:179], v[52:55]\n\t" \
  "ds_read_b64_tr_b16 v[226:227], %[br0] offset:9216\n\t" \
  "v_mfma_f32_16x16x32_bf16 v[56:59], v[200:203], v[176:179], v[56:59]\n\t" \
  "ds_read_b128 v[208:211], %[ar1] offset:0\n\t" \
  "v_mfma_f32_16x16x32_bf16 v[60:63], v[204:207], v[176:179], v[60:63]\n\t" \
  "ds_read_b64_tr_b16 v[228:229], %[br1] offset:8192\n\t" \
  "v_mfma_f32_16x16x32_bf16 v[64:67], v[192:195], v[180:183], v[64:67]\n\t" \
  "ds_read_b64_tr_b16 v[230:231], %[br1] offset:9216\n\t" \
  "v_mfma_f32_16x16x32_bf16 v[68:71], v[196:199], v[180:183], v[68:71]\n\t" \
  "ds_read_b64_tr_b16 v[232:233], %[br0] offset:8448\n\t" \
  "v_mfma_f32_16x16x32_bf16 v[72:75], v[200:203], v[180:183], v[72:75]\n\t" \
  "ds_read_b64_tr_b16 v[234:235], %[br0] offset:9472\n\t" \
  "v_mfma_f32_16x16x32_bf16 v[76:79], v[204:207], v[180:183], v[76:79]\n\t" \
  "ds_read_b64_tr_b16 v[236:237], %[br1] offset:8448\n\t" \
  "v_mfma_f32_16x16x32_bf16 v[80:83], v[192:195], v[184:187], v[80:83]\n\t" \
  "ds_read_b64_tr_b16 v[238:239], %[br1] offset:9472\n\t" \
  "v_mfma_f32_16x16x32_bf16 v[84:87], v[196:199], v[184:187], v[84:87]\n\t" \
  "ds_read_b128 v[212:215], %[ar1] offset:2048\n\t" \
  "v_mfma_f32_16x16x32_bf16 v[88:91], v[200:203], v[184:187], v[88:91]\n\t" \
  "ds_read_b128 v[216:219], %[ar1] offset:4096\n\t" \
  "v_mfma_f32_16x16x32_bf16 v[92:95], v[204:207], v[184:187], v[92:95]\n\t" \
  "ds_read_b128 v[220:223], %[ar1] offset:6144\n\t" \
  "v_mfma_f32_16x16x32_bf16 v[96:99], v[192:195], v[188:191], v[96:99]\n\t" \
  "v_mfma_f32_16x16x32_bf16 v[100:103], v[196:199], v[188:191], v[100:103]\n\t" \
  "v_mfma_f32_16x16x32_bf16 v[104:107], v[200:203], v[188:191], v[104:107]\n\t" \
  "v_mfma_f32_16x16x32_bf16 v[108:111], v[204:207], v[188:191], v[108:111]\n\t" \
  "s_waitcnt lgkmcnt(0)\n\t" \
  "s_barrier\n\t" \
  "v_mfma_f32_16x16x32_bf16 v[48:51], v[224:227], v[208:211], v[48:51]\n\t" \
  "v_add_u32_e32 %[br0], 49152, %[br0]\n\t" \
  "v_add_u32_e32 %[br1], 49152, %[br1]\n\t" \
  "v_add_u32_e32 %[ar0], 49152, %[ar0]\n\t" \
  "v_add_u32_e32 %[ar1], 49152, %[ar1]\n\t" \
  "v_mfma_f32_16x16x32_bf16 v[52:55], v[228:231], v[208:211], v[52:55]\n\t" \
  "ds_read_b64_tr_b16 v[192:193], %[br0] offset:0\n\t" \
  "ds_read_b64_tr_b16 v[194:195], %[br0] offset:1024\n\t" \
  "s_add_u32 s66, s66, 524288\n\t" \
  "s_addc_u32 s67, s67, 0\n\t" \
  "s_add_u32 s64, s64, 128\n\t" \
  "s_addc_u32 s65, s65, 0\n\t" \
  "v_mfma_f32_16x16x32_bf16 v[56:59], v[232:235], v[208:211], v[56:59]\n\t" \
  "ds_read_b128 v[176:179], %[ar0] offset:0\n\t" \
  "v_mfma_f32_16x16x32_bf16 v[60:63], v[236:239], v[208:211], v[60:63]\n\t" \
  "ds_read_b64_tr_b16 v[196:197], %[br1] offset:0\n\t" \
  "v_mfma_f32_16x16x32_bf16 v[64:67], v[224:227], v[212:215], v[64:67]\n\t" \
  "ds_read_b64_tr_b16 v[198:199], %[br1] offset:1024\n\t" \
  "v_mfma_f32_16x16x32_bf16 v[68:71], v[228:231], v[212:215], v[68:71]\n\t" \
  "ds_read_b64_tr_b16 v[200:201], %[br0] offset:256\n\t" \
  "v_mfma_f32_16x16x32_bf16 v[72:75], v[232:235], v[212:215], v[72:75]\n\t" \
  "ds_read_b64_tr_b16 v[202:203], %[br0] offset:1280\n\t" \
  "global_load_dword v244, %[wmv2], %[wm1]\n\t" \
  "v_mfma_f32_16x16x32_bf16 v[76:79], v[236:239], v[212:215], v[76:79]\n\t" \
  "ds_read_b64_tr_b16 v[204:205], %[br1] offset:256\n\t" \
  "v_mfma_f32_16x16x32_bf16 v[80:83], v[224:227], v[216:219], v[80:83]\n\t" \
  "ds_read_b64_tr_b16 v[206:207], %[br1] offset:1280\n\t" \
  "v_mfma_f32_16x16x32_bf16 v[84:87], v[228:231], v[216:219], v[84:87]\n\t" \
  "ds_read_b128 v[180:183], %[ar0] offset:2048\n\t" \
  "v_mfma_f32_16x16x32_bf16 v[88:91], v[232:235], v[216:219], v[88:91]\n\t" \
  "ds_read_b128 v[184:187], %[ar0] offset:4096\n\t" \
  "v_mfma_f32_16x16x32_bf16 v[92:95], v[236:239], v[216:219], v[92:95]\n\t" \
  "ds_read_b128 v[188:191], %[ar0] offset:6144\n\t" \
  "v_mfma_f32_16x16x32_bf16 v[96:99], v[224:227], v[220:223], v[96:99]\n\t" \
  "v_mfma_f32_16x16x32_bf16 v[100:103], v[228:231], v[220:223], v[100:103]\n\t" \
  "v_mfma_f32_16x16x32_bf16 v[104:107], v[232:235], v[220:223], v[104:107]\n\t" \
  "v_mfma_f32_16x16x32_bf16 v[108:111], v[236:239], v[220:223], v[108:111]\n\t" \
  "s_waitcnt lgkmcnt(0)\n\t" \
  "v_mfma_f32_16x16x32_bf16 v[48:51], v[192:195], v[176:179], v[48:51]\n\t" \
  "ds_read_b64_tr_b16 v[224:225], %[br0] offset:8192\n\t" \
  "v_mfma_f32_16x16x32_bf16 v[52:55], v[196:199], v[176:179], v[52:55]\n\t" \
  "ds_read_b64_tr_b16 v[226:227], %[br0] offset:9216\n\t" \
  "v_mfma_f32_16x16x32_bf16 v[56:59], v[200:203], v[176:179], v[56:59]\n\t" \
  "ds_read_b128 v[208:211], %[ar1] offset:0\n\t" \
  "v_mfma_f32_16x16x32_bf16 v[60:63], v[204:207], v[176:179], v[60:63]\n\t" \
  "ds_read_b64_tr_b16 v[228:229], %[br1] offset:8192\n\t" \
  "v_mfma_f32_16x16x32_bf16 v[64:67], v[192:195], v[180:183], v[64:67]\n\t" \
  "ds_read_b64_tr_b16 v[230:231], %[br1] offset:9216\n\t" \
  "v_mfma_f32_16x16x32_bf16 v[68:71], v[196:199], v[180:183], v[68:71]\n\t" \
  "ds_read_b64_tr_b16 v[232:233], %[br0] offset:8448\n\t" \
  "v_mfma_f32_16x16x32_bf16 v[72:75], v[200:203], v[180:183], v[72:75]\n\t" \
  "ds_read_b64_tr_b16 v[234:235], %[br0] offset:9472\n\t" \
  "v_mfma_f32_16x16x32_bf16 v[76:79], v[204:207], v[180:183], v[76:79]\n\t" \
  "ds_read_b64_tr_b16 v[236:237], %[br1] offset:8448\n\t" \
  "v_mfma_f32_16x16x32_bf16 v[80:83], v[192:195], v[184:187], v[80:83]\n\t" \
  "ds_read_b64_tr_b16 v[238:239], %[br1] offset:9472\n\t" \
  "v_mfma_f32_16x16x32_bf16 v[84:87], v[196:199], v[184:187], v[84:87]\n\t" \
  "ds_read_b128 v[212:215], %[ar1] offset:2048\n\t" \
  "v_mfma_f32_16x16x32_bf16 v[88:91], v[200:203], v[184:187], v[88:91]\n\t" \
  "ds_read_b128 v[216:219], %[ar1] offset:4096\n\t" \
  "v_mfma_f32_16x16x32_bf16 v[92:95], v[204:207], v[184:187], v[92:95]\n\t" \
  "ds_read_b128 v[220:223], %[ar1] offset:6144\n\t" \
  "v_mfma_f32_16x16x32_bf16 v[96:99], v[192:195], v[188:191], v[96:99]\n\t" \
  "v_mfma_f32_16x16x32_bf16 v[100:103], v[196:199], v[188:191], v[100:103]\n\t" \
  "v_mfma_f32_16x16x32_bf16 v[104:107], v[200:203], v[188:191], v[104:107]\n\t" \
  "v_mfma_f32_16x16x32_bf16 v[108:111], v[204:207], v[188:191], v[108:111]\n\t" \
  "s_waitcnt lgkmcnt(0)\n\t" \
  "s_barrier\n\t" \
  "v_mfma_f32_16x16x32_bf16 v[48:51], v[224:227], v[208:211], v[48:51]\n\t" \
  "v_add_u32_e32 %[br0], 49152, %[br0]\n\t" \
  "v_add_u32_e32 %[br1], 49152, %[br1]\n\t" \
  "v_add_u32_e32 %[ar0], 49152, %[ar0]\n\t" \
  "v_add_u32_e32 %[ar1], 49152, %[ar1]\n\t" \
  "v_mfma_f32_16x16x32_bf16 v[52:55], v[228:231], v[208:211], v[52:55]\n\t" \
  "ds_read_b64_tr_b16 v[192:193], %[br0] offset:0\n\t" \
  "ds_read_b64_tr_b16 v[194:195], %[br0] offset:1024\n\t" \
  "s_add_u32 s66, s66, 524288\n\t" \
  "s_addc_u32 s67, s67, 0\n\t" \
  "s_add_u32 s64, s64, 128\n\t" \
  "s_addc_u32 s65, s65, 0\n\t" \
  "v_mfma_f32_16x16x32_bf16 v[56:59], v[232:235], v[208:211], v[56:59]\n\t" \
  "ds_read_b128 v[176:179], %[ar0] offset:0\n\t" \
  "v_mfma_f32_16x16x32_bf16 v[60:63], v[236:239], v[208:211], v[60:63]\n\t" \
  "ds_read_b64_tr_b16 v[196:197], %[br1] offset:0\n\t" \
  "v_mfma_f32_16x16x32_bf16 v[64:67], v[224:227], v[212:215], v[64:67]\n\t" \
  "ds_read_b64_tr_b16 v[198:199], %[br1] offset:1024\n\t" \
  "v_mfma_f32_16x16x32_bf16 v[68:71], v[228:231], v[212:215], v[68:71]\n\t" \
  "ds_read_b64_tr_b16 v[200:201], %[br0] offset:256\n\t" \
  "v_mfma_f32_16x16x32_bf16 v[72:75], v[232:235], v[212:215], v[72:75]\n\t" \
  "ds_read_b64_tr_b16 v[202:203], %[br0] offset:1280\n\t" \
  "v_mfma_f32_16x16x32_bf16 v[76:79], v[236:239], v[212:215], v[76:79]\n\t" \
  "ds_read_b64_tr_b16 v[204:205], %[br1] offset:256\n\t" \
  "v_mfma_f32_16x16x32_bf16 v[80:83], v[224:227], v[216:219], v[80:83]\n\t" \
  "ds_read_b64_tr_b16 v[206:207], %[br1] offset:1280\n\t" \
  "v_mfma_f32_16x16x32_bf16 v[84:87], v[228:231], v[216:219], v[84:87]\n\t" \
  "ds_read_b128 v[180:183], %[ar0] offset:2048\n\t" \
  "v_mfma_f32_16x16x32_bf16 v[88:91], v[232:235], v[216:219], v[88:91]\n\t" \
  "ds_read_b128 v[184:187], %[ar0] offset:4096\n\t" \
  "v_mfma_f32_16x16x32_bf16 v[92:95], v[236:239], v[216:219], v[92:95]\n\t" \
  "ds_read_b128 v[188:191], %[ar0] offset:6144\n\t" \
  "v_mfma_f32_16x16x32_bf16 v[96:99], v[224:227], v[220:223], v[96:99]\n\t" \
  "v_mfma_f32_16x16x32_bf16 v[100:103], v[228:231], v[220:223], v[100:103]\n\t" \
  "v_mfma_f32_16x16x32_bf16 v[104:107], v[232:235], v[220:223], v[104:107]\n\t" \
  "v_mfma_f32_16x16x32_bf16 v[108:111], v[236:239], v[220:223], v[108:111]\n\t" \
  "s_waitcnt lgkmcnt(0)\n\t" \
  "v_mfma_f32_16x16x32_bf16 v[112:115], v[176:179], v[192:195], 0\n\t" \
  "ds_read_b64_tr_b16 v[224:225], %[br0] offset:8192\n\t" \
  "v_mfma_f32_16x16x32_bf16 v[116:119], v[176:179], v[196:199], 0\n\t" \
  "ds_read_b64_tr_b16 v[226:227], %[br0] offset:9216\n\t" \
  "v_mfma_f32_16x16x32_bf16 v[120:123], v[176:179], v[200:203], 0\n\t" \
  "ds_read_b128 v[208:211], %[ar1] offset:0\n\t" \
  "v_cvt_pk_bf16_f32 v16, v48, v49\n\t" \
  "v_cvt_pk_bf16_f32 v17, v50, v51\n\t" \
  "v_cvt_pk_bf16_f32 v18, v52, v53\n\t" \
  "v_mfma_f32_16x16x32_bf16 v[124:127], v[176:179], v[204:207], 0\n\t" \
  "ds_read_b64_tr_b16 v[228:229], %[br1] offset:8192\n\t" \
  "v_cvt_pk_bf16_f32 v19, v54, v55\n\t" \
  "v_cvt_pk_bf16_f32 v20, v56, v57\n\t" \
  "v_mfma_f32_16x16x32_bf16 v[128:131], v[180:183], v[192:195], 0\n\t" \
  "ds_read_b64_tr_b16 v[230:231], %[br1] offset:9216\n\t" \
  "v_cvt_pk_bf16_f32 v21, v58, v59\n\t" \
  "v_cvt_pk_bf16_f32 v22, v60, v61\n\t" \
  "v_mfma_f32_16x16x32_bf16 v[132:135], v[180:183], v[196:199], 0\n\t" \
  "ds_read_b64_tr_b16 v[232:233], %[br0] offset:8448\n\t" \
  "v_cvt_pk_bf16_f32 v23, v62, v63\n\t" \
  "v_cvt_pk_bf16_f32 v24, v64, v65\n\t" \
  "v_cvt_pk_bf16_f32 v25, v66, v67\n\t" \
  "v_mfma_f32_16x16x32_bf16 v[136:139], v[180:183], v[200:203], 0\n\t" \
  "ds_read_b64_tr_b16 v[234:235], %[br0] offset:9472\n\t" \
  "v_cvt_pk_bf16_f32 v26, v68, v69\n\t" \
  "v_cvt_pk_bf16_f32 v27, v70, v71\n\t" \
  "v_mfma_f32_16x16x32_bf16 v[140:143], v[180:183], v[204:207], 0\n\t" \
  "ds_read_b64_tr_b16 v[236:237], %[br1] offset:8448\n\t" \
  "v_cvt_pk_bf16_f32 v28, v72, v73\n\t" \
  "v_cvt_pk_bf16_f32 v29, v74, v75\n\t" \
  "v_mfma_f32_16x16x32_bf16 v[144:147], v[184:187], v[192:195], 0\n\t" \
  "ds_read_b64_tr_b16 v[238:239], %[br1] offset:9472\n\t" \
  "v_cvt_pk_bf16_f32 v30, v76, v77\n\t" \
  "v_cvt_pk_bf16_f32 v31, v78, v79\n\t" \
  "v_mfma_f32_16x16x32_bf16 v[148:151], v[184:187], v[196:199], 0\n\t" \
  "ds_read_b128 v[212:215], %[ar1] offset:2048\n\t" \
  "v_cvt_pk_bf16_f32 v32, v80, v81\n\t" \
  "v_cvt_pk_bf16_f32 v33, v82, v83\n\t" \
  "v_cvt_pk_bf16_f32 v34, v84, v85\n\t" \
  "v_mfma_f32_16x16x32_bf16 v[152:155], v[184:187], v[200:203], 0\n\t" \
  "ds_read_b128 v[216:219], %[ar1] offset:4096\n\t" \
  "v_cvt_pk_bf16_f32 v35, v86, v87\n\t" \
  "v_cvt_pk_bf16_f32 v36, v88, v89\n\t" \
  "v_mfma_f32_16x16x32_bf16 v[156:159], v[184:187], v[204:207], 0\n\t" \
  "ds_read_b128 v[220:223], %[ar1] offset:6144\n\t" \
  "v_cvt_pk_bf16_f32 v37, v90, v91\n\t" \
  "v_cvt_pk_bf16_f32 v38, v92, v93\n\t" \
  "v_mfma_f32_16x16x32_bf16 v[160:163], v[188:191], v[192:195], 0\n\t" \
  "v_cvt_pk_bf16_f32 v39, v94, v95\n\t" \
  "v_cvt_pk_bf16_f32 v40, v96, v97\n\t" \
  "v_cvt_pk_bf16_f32 v41, v98, v99\n\t" \
  "v_mfma_f32_16x16x32_bf16 v[164:167], v[188:191], v[196:199], 0\n\t" \
  "v_cvt_pk_bf16_f32 v42, v100, v101\n\t" \
  "v_cvt_pk_bf16_f32 v43, v102, v103\n\t" \
  "v_mfma_f32_16x16x32_bf16 v[168:171], v[188:191], v[200:203], 0\n\t" \
  "v_cvt_pk_bf16_f32 v44, v104, v105\n\t" \
  "v_cvt_pk_bf16_f32 v45, v106, v107\n\t" \
  "v_mfma_f32_16x16x32_bf16 v[172:175], v[188:191], v[204:207], 0\n\t" \
  "v_cvt_pk_bf16_f32 v46, v108, v109\n\t" \
  "v_cvt_pk_bf16_f32 v47, v110, v111\n\t" \
  "s_waitcnt lgkmcnt(0)\n\t" \
  "s_barrier\n\t" \
  "v_mfma_f32_16x16x32_bf16 v[112:115], v[208:211], v[224:227], v[112:115]\n\t" \
  "v_add_u32_e32 %[br0], -98304, %[br0]\n\t" \
  "v_add_u32_e32 %[br1], -98304, %[br1]\n\t" \
  "v_add_u32_e32 %[ar0], -98304, %[ar0]\n\t" \
  "v_add_u32_e32 %[ar1], -98304, %[ar1]\n\t" \
  "v_mfma_f32_16x16x32_bf16 v[116:119], v[208:211], v[228:231], v[116:119]\n\t" \
  "ds_read_b64_tr_b16 v[192:193], %[br0] offset:0\n\t" \
  "ds_read_b64_tr_b16 v[194:195], %[br0] offset:1024\n\t" \
  "s_add_u32 s66, s66, 524288\n\t" \
  "s_addc_u32 s67, s67, 0\n\t" \
  "s_add_u32 s64, s64, 128\n\t" \
  "s_addc_u32 s65, s65, 0\n\t" \
  "v_mfma_f32_16x16x32_bf16 v[120:123], v[208:211], v[232:235], v[120:123]\n\t" \
  "ds_read_b128 v[176:179], %[ar0] offset:0\n\t" \
  "v_mfma_f32_16x16x32_bf16 v[124:127], v[208:211], v[236:239], v[124:127]\n\t" \
  "ds_read_b64_tr_b16 v[196:197], %[br1] offset:0\n\t" \
  "v_mfma_f32_16x16x32_bf16 v[128:131], v[212:215], v[224:227], v[128:131]\n\t" \
  "ds_read_b64_tr_b16 v[198:199], %[br1] offset:1024\n\t" \
  "v_mfma_f32_16x16x32_bf16 v[132:135], v[212:215], v[228:231], v[132:135]\n\t" \
  "ds_read_b64_tr_b16 v[200:201], %[br0] offset:256\n\t" \
  "v_mfma_f32_16x16x32_bf16 v[136:139], v[212:215], v[232:235], v[136:139]\n\t" \
  "ds_read_b64_tr_b16 v[202:203], %[br0] offset:1280\n\t" \
  "v_mfma_f32_16x16x32_bf16 v[140:143], v[212:215], v[236:239], v[140:143]\n\t" \
  "ds_read_b64_tr_b16 v[204:205], %[br1] offset:256\n\t" \
  "v_mfma_f32_16x16x32_bf16 v[144:147], v[216:219], v[224:227], v[144:147]\n\t" \
  "ds_read_b64_tr_b16 v[206:207], %[br1] offset:1280\n\t" \
  "v_mfma_f32_16x16x32_bf16 v[148:151], v[216:219], v[228:231], v[148:151]\n\t" \
  "ds_read_b128 v[180:183], %[ar0] offset:2048\n\t" \
  "v_mfma_f32_16x16x32_bf16 v[152:155], v[216:219], v[232:235], v[152:155]\n\t" \
  "ds_read_b128 v[184:187], %[ar0] offset:4096\n\t" \
  "v_mfma_f32_16x16x32_bf16 v[156:159], v[216:219], v[236:239], v[156:159]\n\t" \
  "ds_read_b128 v[188:191], %[ar0] offset:6144\n\t" \
  "v_mfma_f32_16x16x32_bf16 v[160:163], v[220:223], v[224:227], v[160:163]\n\t" \
  "v_mfma_f32_16x16x32_bf16 v[164:167], v[220:223], v[228:231], v[164:167]\n\t" \
  "v_mfma_f32_16x16x32_bf16 v[168:171], v[220:223], v[232:235], v[168:171]\n\t" \
  "s_mov_b64 s[70:71], %[kd]\n\t" \
  "global_store_dwordx4 %[kst], v[16:19], s[70:71] offset:0 sc1\n\t" \
  "v_mfma_f32_16x16x32_bf16 v[172:175], v[220:223], v[236:239], v[172:175]\n\t" \
  "s_waitcnt lgkmcnt(0)\n\t" \
  "v_mfma_f32_16x16x32_bf16 v[112:115], v[176:179], v[192:195], v[112:115]\n\t" \
  "ds_read_b64_tr_b16 v[224:225], %[br0] offset:8192\n\t" \
  "v_mfma_f32_16x16x32_bf16 v[116:119], v[176:179], v[196:199], v[116:119]\n\t" \
  "ds_read_b64_tr_b16 v[226:227], %[br0] offset:9216\n\t" \
  "v_mfma_f32_16x16x32_bf16 v[120:123], v[176:179], v[200:203], v[120:123]\n\t" \
  "ds_read_b128 v[208:211], %[ar1] offset:0\n\t" \
  "v_mfma_f32_16x16x32_bf16 v[124:127], v[176:179], v[204:207], v[124:127]\n\t" \
  "ds_read_b64_tr_b16 v[228:229], %[br1] offset:8192\n\t" \
  "v_mfma_f32_16x16x32_bf16 v[128:131], v[180:183], v[192:195], v[128:131]\n\t" \
  "ds_read_b64_tr_b16 v[230:231], %[br1] offset:9216\n\t" \
  "v_mfma_f32_16x16x32_bf16 v[132:135], v[180:183], v[196:199], v[132:135]\n\t" \
  "ds_read_b64_tr_b16 v[232:233], %[br0] offset:8448\n\t" \
  "v_mfma_f32_16x16x32_bf16 v[136:139], v[180:183], v[200:203], v[136:139]\n\t" \
  "ds_read_b64_tr_b16 v[234:235], %[br0] offset:9472\n\t" \
  "v_mfma_f32_16x16x32_bf16 v[140:143], v[180:183], v[204:207], v[140:143]\n\t" \
  "ds_read_b64_tr_b16 v[236:237], %[br1] offset:8448\n\t" \
  "v_mfma_f32_16x16x32_bf16 v[144:147], v[184:187], v[192:195], v[144:147]\n\t" \
  "ds_read_b64_tr_b16 v[238:239], %[br1] offset:9472\n\t" \
  "v_mfma_f32_16x16x32_bf16 v[148:151], v[184:187], v[196:199], v[148:151]\n\t" \
  "ds_read_b128 v[212:215], %[ar1] offset:2048\n\t" \
  "v_mfma_f32_16x16x32_bf16 v[152:155], v[184:187], v[200:203], v[152:155]\n\t" \
  "ds_read_b128 v[216:219], %[ar1] offset:4096\n\t" \
  "v_mfma_f32_16x16x32_bf16 v[156:159], v[184:187], v[204:207], v[156:159]\n\t" \
  "ds_read_b128 v[220:223], %[ar1] offset:6144\n\t" \
  "v_mfma_f32_16x16x32_bf16 v[160:163], v[188:191], v[192:195], v[160:163]\n\t" \
  "v_mfma_f32_16x16x32_bf16 v[164:167], v[188:191], v[196:199], v[164:167]\n\t" \
  "v_mfma_f32_16x16x32_bf16 v[168:171], v[188:191], v[200:203], v[168:171]\n\t" \
  "v_mfma_f32_16x16x32_bf16 v[172:175], v[188:191], v[204:207], v[172:175]\n\t" \
  "s_waitcnt lgkmcnt(0)\n\t" \
  "s_barrier\n\t" \
  "L_JOIN%=:\n\t" \
  "v_mfma_f32_16x16x32_bf16 v[112:115], v[208:211], v[224:227], v[112:115]\n\t" \
  "v_add_u32_e32 %[br0], 49152, %[br0]\n\t" \
  "v_add_u32_e32 %[br1], 49152, %[br1]\n\t" \
  "v_add_u32_e32 %[ar0], 49152, %[ar0]\n\t" \
  "v_add_u32_e32 %[ar1], 49152, %[ar1]\n\t" \
  "v_mfma_f32_16x16x32_bf16 v[116:119], v[208:211], v[228:231], v[116:119]\n\t" \
  "ds_read_b64_tr_b16 v[192:193], %[br0] offset:0\n\t" \
  "ds_read_b64_tr_b16 v[194:195], %[br0] offset:1024\n\t" \
  "v_mfma_f32_16x16x32_bf16 v[120:123], v[208:211], v[232:235], v[120:123]\n\t" \
  "ds_read_b128 v[176:179], %[ar0] offset:0\n\t" \
  "s_add_u32 m0, %[db], 0\n\t" \
  "s_add_u32 s66, s66, 524288\n\t" \
  "s_addc_u32 s67, s67, 0\n\t" \
  "global_load_lds_dwordx4 %[vb], s[66:67] offset:0\n\t" \
  "v_mfma_f32_16x16x32_bf16 v[124:127], v[208:211], v[236:239], v[124:127]\n\t" \
  "ds_read_b64_tr_b16 v[196:197], %[br1] offset:0\n\t" \
  "v_mfma_f32_16x16x32_bf16 v[128:131], v[212:215], v[224:227], v[128:131]\n\t" \
  "ds_read_b64_tr_b16 v[198:199], %[br1] offset:1024\n\t" \
  "global_load_lds_dwordx4 v251, s[66:67] offset:1024\n\t" \
  "v_mfma_f32_16x16x32_bf16 v[132:135], v[212:215], v[228:231], v[132:135]\n\t" \
  "ds_read_b64_tr_b16 v[200:201], %[br0] offset:256\n\t" \
  "v_mfma_f32_16x16x32_bf16 v[136:139], v[212:215], v[232:235], v[136:139]\n\t" \
  "ds_read_b64_tr_b16 v[202:203], %[br0] offset:1280\n\t" \
  "s_add_u32 m0, %[da], 0\n\t" \
  "s_add_u32 s64, s64, 128\n\t" \
  "s_addc_u32 s65, s65, 0\n\t" \
  "global_load_lds_dwordx4 %[va], s[64:65] offset:0\n\t" \
  "v_mfma_f32_16x16x32_bf16 v[140:143], v[212:215], v[236:239], v[140:143]\n\t" \
  "ds_read_b64_tr_b16 v[204:205], %[br1] offset:256\n\t" \
  "v_mfma_f32_16x16x32_bf16 v[144:147], v[216:219], v[224:227], v[144:147]\n\t" \
  "ds_read_b64_tr_b16 v[206:207], %[br1] offset:1280\n\t" \
  "global_load_lds_dwordx4 v248, s[64:65] offset:1024\n\t" \
  "v_mfma_f32_16x16x32_bf16 v[148:151], v[216:219], v[228:231], v[148:151]\n\t" \
  "ds_read_b128 v[180:183], %[ar0] offset:2048\n\t" \
  "v_mfma_f32_16x16x32_bf16 v[152:155], v[216:219], v[232:235], v[152:155]\n\t" \
  "ds_read_b128 v[184:187], %[ar0] offset:4096\n\t" \
  "global_load_lds_dwordx4 v249, s[64:65] offset:2048\n\t" \
  "v_mfma_f32_16x16x32_bf16 v[156:159], v[216:219], v[236:239], v[156:159]\n\t" \
  "ds_read_b128 v[188:191], %[ar0] offset:6144\n\t" \
  "v_mfma_f32_16x16x32_bf16 v[160:163], v[220:223], v[224:227], v[160:163]\n\t" \
  "global_load_lds_dwordx4 v250, s[64:65] offset:3072\n\t" \
  "v_mfma_f32_16x16x32_bf16 v[164:167], v[220:223], v[228:231], v[164:167]\n\t" \
  "v_mfma_f32_16x16x32_bf16 v[168:171], v[220:223], v[232:235], v[168:171]\n\t" \
  "global_store_dwordx4 %[kst], v[20:23], s[70:71] offset:2048 sc1\n\t" \
  "v_mfma_f32_16x16x32_bf16 v[172:175], v[220:223], v[236:239], v[172:175]\n\t" \
  "s_waitcnt lgkmcnt(0)\n\t" \
  "v_mfma_f32_16x16x32_bf16 v[112:115], v[176:179], v[192:195], v[112:115]\n\t" \
  "ds_read_b64_tr_b16 v[224:225], %[br0] offset:8192\n\t" \
  "v_mfma_f32_16x16x32_bf16 v[116:119], v[176:179], v[196:199], v[116:119]\n\t" \
  "ds_read_b64_tr_b16 v[226:227], %[br0] offset:9216\n\t" \
  "v_mfma_f32_16x16x32_bf16 v[120:123], v[176:179], v[200:203], v[120:123]\n\t" \
  "ds_read_b128 v[208:211], %[ar1] offset:0\n\t" \
  "v_mfma_f32_16x16x32_bf16 v[124:127], v[176:179], v[204:207], v[124:127]\n\t" \
  "ds_read_b64_tr_b16 v[228:229], %[br1] offset:8192\n\t" \
  "v_mfma_f32_16x16x32_bf16 v[128:131], v[180:183], v[192:195], v[128:131]\n\t" \
  "ds_read_b64_tr_b16 v[230:231], %[br1] offset:9216\n\t" \
  "v_mfma_f32_16x16x32_bf16 v[132:135], v[180:183], v[196:199], v[132:135]\n\t" \
  "ds_read_b64_tr_b16 v[232:233], %[br0] offset:8448\n\t" \
  "v_mfma_f32_16x16x32_bf16 v[136:139], v[180:183], v[200:203], v[136:139]\n\t" \
  "ds_read_b64_tr_b16 v[234:235], %[br0] offset:9472\n\t" \
  "v_mfma_f32_16x16x32_bf16 v[140:143], v[180:183], v[204:207], v[140:143]\n\t" \
  "ds_read_b64_tr_b16 v[236:237], %[br1] offset:8448\n\t" \
  "v_mfma_f32_16x16x32_bf16 v[144:147], v[184:187], v[192:195], v[144:147]\n\t" \
  "ds_read_b64_tr_b16 v[238:239], %[br1] offset:9472\n\t" \
  "v_mfma_f32_16x16x32_bf16 v[148:151], v[184:187], v[196:199], v[148:151]\n\t" \
  "ds_read_b128 v[212:215], %[ar1] offset:2048\n\t" \
  "v_mfma_f32_16x16x32_bf16 v[152:155], v[184:187], v[200:203], v[152:155]\n\t" \
  "ds_read_b128 v[216:219], %[ar1] offset:4096\n\t" \
  "v_mfma_f32_16x16x32_bf16 v[156:159], v[184:187], v[204:207], v[156:159]\n\t" \
  "ds_read_b128 v[220:223], %[ar1] offset:6144\n\t" \
  "v_mfma_f32_16x16x32_bf16 v[160:163], v[188:191], v[192:195], v[160:163]\n\t" \
  "v_mfma_f32_16x16x32_bf16 v[164:167], v[188:191], v[196:199], v[164:167]\n\t" \
  "v_mfma_f32_16x16x32_bf16 v[168:171], v[188:191], v[200:203], v[168:171]\n\t" \
  "v_mfma_f32_16x16x32_bf16 v[172:175], v[188:191], v[204:207], v[172:175]\n\t" \
  "s_waitcnt vmcnt(7) lgkmcnt(0)\n\t" \
  "s_barrier\n\t" \
  "v_mfma_f32_16x16x32_bf16 v[112:115], v[208:211], v[224:227], v[112:115]\n\t" \
  "v_add_u32_e32 %[br0], 49152, %[br0]\n\t" \
  "v_add_u32_e32 %[br1], 49152, %[br1]\n\t" \
  "v_add_u32_e32 %[ar0], 49152, %[ar0]\n\t" \
  "v_add_u32_e32 %[ar1], 49152, %[ar1]\n\t" \
  "v_mfma_f32_16x16x32_bf16 v[116:119], v[208:211], v[228:231], v[116:119]\n\t" \
  "ds_read_b64_tr_b16 v[192:193], %[br0] offset:0\n\t" \
  "ds_read_b64_tr_b16 v[194:195], %[br0] offset:1024\n\t" \
  "v_mfma_f32_16x16x32_bf16 v[120:123], v[208:211], v[232:235], v[120:123]\n\t" \
  "ds_read_b128 v[176:179], %[ar0] offset:0\n\t" \
  "s_add_u32 m0, %[db], 49152\n\t" \
  "s_add_u32 s66, s66, 524288\n\t" \
  "s_addc_u32 s67, s67, 0\n\t" \
  "global_load_lds_dwordx4 %[vb], s[66:67] offset:0\n\t" \
  "v_mfma_f32_16x16x32_bf16 v[124:127], v[208:211], v[236:239], v[124:127]\n\t" \
  "ds_read_b64_tr_b16 v[196:197], %[br1] offset:0\n\t" \
  "v_mfma_f32_16x16x32_bf16 v[128:131], v[212:215], v[224:227], v[128:131]\n\t" \
  "ds_read_b64_tr_b16 v[198:199], %[br1] offset:1024\n\t" \
  "global_load_lds_dwordx4 v251, s[66:67] offset:1024\n\t" \
  "v_mfma_f32_16x16x32_bf16 v[132:135], v[212:215], v[228:231], v[132:135]\n\t" \
  "ds_read_b64_tr_b16 v[200:201], %[br0] offset:256\n\t" \
  "v_mfma_f32_16x16x32_bf16 v[136:139], v[212:215], v[232:235], v[136:139]\n\t" \
  "ds_read_b64_tr_b16 v[202:203], %[br0] offset:1280\n\t" \
  "s_add_u32 m0, %[da], 49152\n\t" \
  "s_add_u32 s64, s64, 128\n\t" \
  "s_addc_u32 s65, s65, 0\n\t" \
  "global_load_lds_dwordx4 %[va], s[64:65] offset:0\n\t" \
  "v_mfma_f32_16x16x32_bf16 v[140:143], v[212:215], v[236:239], v[140:143]\n\t" \
  "ds_read_b64_tr_b16 v[204:205], %[br1] offset:256\n\t" \
  "v_mfma_f32_16x16x32_bf16 v[144:147], v[216:219], v[224:227], v[144:147]\n\t" \
  "ds_read_b64_tr_b16 v[206:207], %[br1] offset:1280\n\t" \
  "global_load_lds_dwordx4 v248, s[64:65] offset:1024\n\t" \
  "v_mfma_f32_16x16x32_bf16 v[148:151], v[216:219], v[228:231], v[148:151]\n\t" \
  "ds_read_b128 v[180:183], %[ar0] offset:2048\n\t" \
  "v_mfma_f32_16x16x32_bf16 v[152:155], v[216:219], v[232:235], v[152:155]\n\t" \
  "ds_read_b128 v[184:187], %[ar0] offset:4096\n\t" \
  "global_load_lds_dwordx4 v249, s[64:65] offset:2048\n\t" \
  "v_mfma_f32_16x16x32_bf16 v[156:159], v[216:219], v[236:239], v[156:159]\n\t" \
  "ds_read_b128 v[188:191], %[ar0] offset:6144\n\t" \
  "v_mfma_f32_16x16x32_bf16 v[160:163], v[220:223], v[224:227], v[160:163]\n\t" \
  "global_load_lds_dwordx4 v250, s[64:65] offset:3072\n\t" \
  "v_mfma_f32_16x16x32_bf16 v[164:167], v[220:223], v[228:231], v[164:167]\n\t" \
  "v_mfma_f32_16x16x32_bf16 v[168:171], v[220:223], v[232:235], v[168:171]\n\t" \
  "global_store_dwordx4 %[kst], v[24:27], s[70:71] offset:128 sc1\n\t" \
  "v_mfma_f32_16x16x32_bf16 v[172:175], v[220:223], v[236:239], v[172:175]\n\t" \
  "s_waitcnt lgkmcnt(0)\n\t" \
  "v_mfma_f32_16x16x32_bf16 v[112:115], v[176:179], v[192:195], v[112:115]\n\t" \
  "ds_read_b64_tr_b16 v[224:225], %[br0] offset:8192\n\t" \
  "v_mfma_f32_16x16x32_bf16 v[116:119], v[176:179], v[196:199], v[116:119]\n\t" \
  "ds_read_b64_tr_b16 v[226:227], %[br0] offset:9216\n\t" \
  "v_mfma_f32_16x16x32_bf16 v[120:123], v[176:179], v[200:203], v[120:123]\n\t" \
  "ds_read_b128 v[208:211], %[ar1] offset:0\n\t" \
  "v_mfma_f32_16x16x32_bf16 v[124:127], v[176:179], v[204:207], v[124:127]\n\t" \
  "ds_read_b64_tr_b16 v[228:229], %[br1] offset:8192\n\t" \
  "v_mfma_f32_16x16x32_bf16 v[128:131], v[180:183], v[192:195], v[128:131]\n\t" \
  "ds_read_b64_tr_b16 v[230:231], %[br1] offset:9216\n\t" \
  "global_load_dword v242, v240, %[hcc] sc1\n\t" \
  "v_mfma_f32_16x16x32_bf16 v[132:135], v[180:183], v[196:199], v[132:135]\n\t" \
  "ds_read_b64_tr_b16 v[232:233], %[br0] offset:8448\n\t" \
  "v_mfma_f32_16x16x32_bf16 v[136:139], v[180:183], v[200:203], v[136:139]\n\t" \
  "ds_read_b64_tr_b16 v[234:235], %[br0] offset:9472\n\t" \
  "v_mfma_f32_16x16x32_bf16 v[140:143], v[180:183], v[204:207], v[140:143]\n\t" \
  "ds_read_b64_tr_b16 v[236:237], %[br1] offset:8448\n\t" \
  "v_mfma_f32_16x16x32_bf16 v[144:147], v[184:187], v[192:195], v[144:147]\n\t" \
  "ds_read_b64_tr_b16 v[238:239], %[br1] offset:9472\n\t" \
  "v_mfma_f32_16x16x32_bf16 v[148:151], v[184:187], v[196:199], v[148:151]\n\t" \
  "ds_read_b128 v[212:215], %[ar1] offset:2048\n\t" \
  "v_mfma_f32_16x16x32_bf16 v[152:155], v[184:187], v[200:203], v[152:155]\n\t" \
  "ds_read_b128 v[216:219], %[ar1] offset:4096\n\t" \
  "v_mfma_f32_16x16x32_bf16 v[156:159], v[184:187], v[204:207], v[156:159]\n\t" \
  "ds_read_b128 v[220:223], %[ar1] offset:6144\n\t" \
  "v_mfma_f32_16x16x32_bf16 v[160:163], v[188:191], v[192:195], v[160:163]\n\t" \
  "v_mfma_f32_16x16x32_bf16 v[164:167], v[188:191], v[196:199], v[164:167]\n\t" \
  "v_mfma_f32_16x16x32_bf16 v[168:171], v[188:191], v[200:203], v[168:171]\n\t" \
  "v_mfma_f32_16x16x32_bf16 v[172:175], v[188:191], v[204:207], v[172:175]\n\t" \
  "s_waitcnt vmcnt(9) lgkmcnt(0)\n\t" \
  "s_barrier\n\t" \
  "v_mfma_f32_16x16x32_bf16 v[112:115], v[208:211], v[224:227], v[112:115]\n\t" \
  "v_add_u32_e32 %[br0], -98304, %[br0]\n\t" \
  "v_add_u32_e32 %[br1], -98304, %[br1]\n\t" \
  "v_add_u32_e32 %[ar0], -98304, %[ar0]\n\t" \
  "v_add_u32_e32 %[ar1], -98304, %[ar1]\n\t" \
  "v_mfma_f32_16x16x32_bf16 v[116:119], v[208:211], v[228:231], v[116:119]\n\t" \
  "ds_read_b64_tr_b16 v[192:193], %[br0] offset:0\n\t" \
  "ds_read_b64_tr_b16 v[194:195], %[br0] offset:1024\n\t" \
  "v_mfma_f32_16x16x32_bf16 v[120:123], v[208:211], v[232:235], v[120:123]\n\t" \
  "ds_read_b128 v[176:179], %[ar0] offset:0\n\t" \
  "s_add_u32 m0, %[db], 98304\n\t" \
  "s_add_u32 s66, s66, 524288\n\t" \
  "s_addc_u32 s67, s67, 0\n\t" \
  "global_load_lds_dwordx4 %[vb], s[66:67] offset:0\n\t" \
  "v_mfma_f32_16x16x32_bf16 v[124:127], v[208:211], v[236:239], v[124:127]\n\t" \
  "ds_read_b64_tr_b16 v[196:197], %[br1] offset:0\n\t" \
  "v_mfma_f32_16x16x32_bf16 v[128:131], v[212:215], v[224:227], v[128:131]\n\t" \
  "ds_read_b64_tr_b16 v[198:199], %[br1] offset:1024\n\t" \
  "global_load_lds_dwordx4 v251, s[66:67] offset:1024\n\t" \
  "v_mfma_f32_16x16x32_bf16 v[132:135], v[212:215], v[228:231], v[132:135]\n\t" \
  "ds_read_b64_tr_b16 v[200:201], %[br0] offset:256\n\t" \
  "v_mfma_f32_16x16x32_bf16 v[136:139], v[212:215], v[232:235], v[136:139]\n\t" \
  "ds_read_b64_tr_b16 v[202:203], %[br0] offset:1280\n\t" \
  "s_add_u32 m0, %[da], 98304\n\t" \
  "s_add_u32 s64, s64, 128\n\t" \
  "s_addc_u32 s65, s65, 0\n\t" \
  "global_load_lds_dwordx4 %[va], s[64:65] offset:0\n\t" \
  "v_mfma_f32_16x16x32_bf16 v[140:143], v[212:215], v[236:239], v[140:143]\n\t" \
  "ds_read_b64_tr_b16 v[204:205], %[br1] offset:256\n\t" \
  "v_mfma_f32_16x16x32_bf16 v[144:147], v[216:219], v[224:227], v[144:147]\n\t" \
  "ds_read_b64_tr_b16 v[206:207], %[br1] offset:1280\n\t" \
  "global_load_lds_dwordx4 v248, s[64:65] offset:1024\n\t" \
  "v_mfma_f32_16x16x32_bf16 v[148:151], v[216:219], v[228:231], v[148:151]\n\t" \
  "ds_read_b128 v[180:183], %[ar0] offset:2048\n\t" \
  "v_mfma_f32_16x16x32_bf16 v[152:155], v[216:219], v[232:235], v[152:155]\n\t" \
  "ds_read_b128 v[184:187], %[ar0] offset:4096\n\t" \
  "global_load_lds_dwordx4 v249, s[64:65] offset:2048\n\t" \
  "v_mfma_f32_16x16x32_bf16 v[156:159], v[216:219], v[236:239], v[156:159]\n\t" \
  "ds_read_b128 v[188:191], %[ar0] offset:6144\n\t" \
  "v_mfma_f32_16x16x32_bf16 v[160:163], v[220:223], v[224:227], v[160:163]\n\t" \
  "global_load_lds_dwordx4 v250, s[64:65] offset:3072\n\t" \
  "v_mfma_f32_16x16x32_bf16 v[164:167], v[220:223], v[228:231], v[164:167]\n\t" \
  "v_mfma_f32_16x16x32_bf16 v[168:171], v[220:223], v[232:235], v[168:171]\n\t" \
  "global_store_dwordx4 %[kst], v[28:31], s[70:71] offset:2176 sc1\n\t" \
  "v_mfma_f32_16x16x32_bf16 v[172:175], v[220:223], v[236:239], v[172:175]\n\t" \
  "s_waitcnt lgkmcnt(0)\n\t" \
  "v_mfma_f32_16x16x32_bf16 v[112:115], v[176:179], v[192:195], v[112:115]\n\t" \
  "ds_read_b64_tr_b16 v[224:225], %[br0] offset:8192\n\t" \
  "v_mfma_f32_16x16x32_bf16 v[116:119], v[176:179], v[196:199], v[116:119]\n\t" \
  "ds_read_b64_tr_b16 v[226:227], %[br0] offset:9216\n\t" \
  "v_mfma_f32_16x16x32_bf16 v[120:123], v[176:179], v[200:203], v[120:123]\n\t" \
  "ds_read_b128 v[208:211], %[ar1] offset:0\n\t" \
  "v_mfma_f32_16x16x32_bf16 v[124:127], v[176:179], v[204:207], v[124:127]\n\t" \
  "ds_read_b64_tr_b16 v[228:229], %[br1] offset:8192\n\t" \
  "v_mfma_f32_16x16x32_bf16 v[128:131], v[180:183], v[192:195], v[128:131]\n\t" \
  "ds_read_b64_tr_b16 v[230:231], %[br1] offset:9216\n\t" \
  "v_mfma_f32_16x16x32_bf16 v[132:135], v[180:183], v[196:199], v[132:135]\n\t" \
  "ds_read_b64_tr_b16 v[232:233], %[br0] offset:8448\n\t" \
  "v_mfma_f32_16x16x32_bf16 v[136:139], v[180:183], v[200:203], v[136:139]\n\t" \
  "ds_read_b64_tr_b16 v[234:235], %[br0] offset:9472\n\t" \
  "v_mfma_f32_16x16x32_bf16 v[140:143], v[180:183], v[204:207], v[140:143]\n\t" \
  "ds_read_b64_tr_b16 v[236:237], %[br1] offset:8448\n\t" \
  "v_mfma_f32_16x16x32_bf16 v[144:147], v[184:187], v[192:195], v[144:147]\n\t" \
  "ds_read_b64_tr_b16 v[238:239], %[br1] offset:9472\n\t" \
  "v_mfma_f32_16x16x32_bf16 v[148:151], v[184:187], v[196:199], v[148:151]\n\t" \
  "ds_read_b128 v[212:215], %[ar1] offset:2048\n\t" \
  "v_mfma_f32_16x16x32_bf16 v[152:155], v[184:187], v[200:203], v[152:155]\n\t" \
  "ds_read_b128 v[216:219], %[ar1] offset:4096\n\t" \
  "v_mfma_f32_16x16x32_bf16 v[156:159], v[184:187], v[204:207], v[156:159]\n\t" \
  "ds_read_b128 v[220:223], %[ar1] offset:6144\n\t" \
  "v_mfma_f32_16x16x32_bf16 v[160:163], v[188:191], v[192:195], v[160:163]\n\t" \
  "v_mfma_f32_16x16x32_bf16 v[164:167], v[188:191], v[196:199], v[164:167]\n\t" \
  "v_mfma_f32_16x16x32_bf16 v[168:171], v[188:191], v[200:203], v[168:171]\n\t" \
  "s_add_u32 m0, %[dw], 147456\n\t" \
  "s_mov_b64 s[76:77], %[sw]\n\t" \
  "global_load_lds_dwordx4 %[wv0], s[76:77] offset:0\n\t" \
  "global_load_lds_dwordx4 v252, s[76:77] offset:1024\n\t" \
  "v_mfma_f32_16x16x32_bf16 v[172:175], v[188:191], v[204:207], v[172:175]\n\t" \
  "s_waitcnt vmcnt(11) lgkmcnt(0)\n\t" \
  "s_barrier\n\t" \
  "v_mfma_f32_16x16x32_bf16 v[112:115], v[208:211], v[224:227], v[112:115]\n\t" \
  "v_add_u32_e32 %[br0], 49152, %[br0]\n\t" \
  "v_add_u32_e32 %[br1], 49152, %[br1]\n\t" \
  "v_add_u32_e32 %[ar0], 49152, %[ar0]\n\t" \
  "v_add_u32_e32 %[ar1], 49152, %[ar1]\n\t" \
  "v_mfma_f32_16x16x32_bf16 v[116:119], v[208:211], v[228:231], v[116:119]\n\t" \
  "ds_read_b64_tr_b16 v[192:193], %[br0] offset:0\n\t" \
  "ds_read_b64_tr_b16 v[194:195], %[br0] offset:1024\n\t" \
  "v_mfma_f32_16x16x32_bf16 v[120:123], v[208:211], v[232:235], v[120:123]\n\t" \
  "ds_read_b128 v[176:179], %[ar0] offset:0\n\t" \
  "s_add_u32 m0, %[db], 0\n\t" \
  "s_add_u32 s66, s66, 524288\n\t" \
  "s_addc_u32 s67, s67, 0\n\t" \
  "global_load_lds_dwordx4 %[vb], s[66:67] offset:0\n\t" \
  "v_mfma_f32_16x16x32_bf16 v[124:127], v[208:211], v[236:239], v[124:127]\n\t" \
  "ds_read_b64_tr_b16 v[196:197], %[br1] offset:0\n\t" \
  "v_mfma_f32_16x16x32_bf16 v[128:131], v[212:215], v[224:227], v[128:131]\n\t" \
  "ds_read_b64_tr_b16 v[198:199], %[br1] offset:1024\n\t" \
  "global_load_lds_dwordx4 v251, s[66:67] offset:1024\n\t" \
  "v_mfma_f32_16x16x32_bf16 v[132:135], v[212:215], v[228:231], v[132:135]\n\t" \
  "ds_read_b64_tr_b16 v[200:201], %[br0] offset:256\n\t" \
  "v_mfma_f32_16x16x32_bf16 v[136:139], v[212:215], v[232:235], v[136:139]\n\t" \
  "ds_read_b64_tr_b16 v[202:203], %[br0] offset:1280\n\t" \
  "s_add_u32 m0, %[da], 0\n\t" \
  "s_add_u32 s64, s64, 128\n\t" \
  "s_addc_u32 s65, s65, 0\n\t" \
  "global_load_lds_dwordx4 %[va], s[64:65] offset:0\n\t" \
  "v_mfma_f32_16x16x32_bf16 v[140:143], v[212:215], v[236:239], v[140:143]\n\t" \
  "ds_read_b64_tr_b16 v[204:205], %[br1] offset:256\n\t" \
  "v_mfma_f32_16x16x32_bf16 v[144:147], v[216:219], v[224:227], v[144:147]\n\t" \
  "ds_read_b64_tr_b16 v[206:207], %[br1] offset:1280\n\t" \
  "global_load_lds_dwordx4 v248, s[64:65] offset:1024\n\t" \
  "v_mfma_f32_16x16x32_bf16 v[148:151], v[216:219], v[228:231], v[148:151]\n\t" \
  "ds_read_b128 v[180:183], %[ar0] offset:2048\n\t" \
  "v_mfma_f32_16x16x32_bf16 v[152:155], v[216:219], v[232:235], v[152:155]\n\t" \
  "ds_read_b128 v[184:187], %[ar0] offset:4096\n\t" \
  "global_load_lds_dwordx4 v249, s[64:65] offset:2048\n\t" \
  "v_mfma_f32_16x16x32_bf16 v[156:159], v[216:219], v[236:239], v[156:159]\n\t" \
  "ds_read_b128 v[188:191], %[ar0] offset:6144\n\t" \
  "v_mfma_f32_16x16x32_bf16 v[160:163], v[220:223], v[224:227], v[160:163]\n\t" \
  "global_load_lds_dwordx4 v250, s[64:65] offset:3072\n\t" \
  "v_mfma_f32_16x16x32_bf16 v[164:167], v[220:223], v[228:231], v[164:167]\n\t" \
  "v_mfma_f32_16x16x32_bf16 v[168:171], v[220:223], v[232:235], v[168:171]\n\t" \
  "s_add_u32 s70, s70, 8192\n\t" \
  "s_addc_u32 s71, s71, 0\n\t" \
  "global_store_dwordx4 %[kst], v[32:35], s[70:71] offset:0 sc1\n\t" \
  "v_mfma_f32_16x16x32_bf16 v[172:175], v[220:223], v[236:239], v[172:175]\n\t" \
  "s_waitcnt lgkmcnt(0)\n\t" \
  "v_mfma_f32_16x16x32_bf16 v[112:115], v[176:179], v[192:195], v[112:115]\n\t" \
  "ds_read_b64_tr_b16 v[224:225], %[br0] offset:8192\n\t" \
  "v_mfma_f32_16x16x32_bf16 v[116:119], v[176:179], v[196:199], v[116:119]\n\t" \
  "ds_read_b64_tr_b16 v[226:227], %[br0] offset:9216\n\t" \
  "v_mfma_f32_16x16x32_bf16 v[120:123], v[176:179], v[200:203], v[120:123]\n\t" \
  "ds_read_b128 v[208:211], %[ar1] offset:0\n\t" \
  "v_mfma_f32_16x16x32_bf16 v[124:127], v[176:179], v[204:207], v[124:127]\n\t" \
  "ds_read_b64_tr_b16 v[228:229], %[br1] offset:8192\n\t" \
  "v_mfma_f32_16x16x32_bf16 v[128:131], v[180:183], v[192:195], v[128:131]\n\t" \
  "ds_read_b64_tr_b16 v[230:231], %[br1] offset:9216\n\t" \
  "v_mfma_f32_16x16x32_bf16 v[132:135], v[180:183], v[196:199], v[132:135]\n\t" \
  "ds_read_b64_tr_b16 v[232:233], %[br0] offset:8448\n\t" \
  "v_mfma_f32_16x16x32_bf16 v[136:139], v[180:183], v[200:203], v[136:139]\n\t" \
  "ds_read_b64_tr_b16 v[234:235], %[br0] offset:9472\n\t" \
  "v_mfma_f32_16x16x32_bf16 v[140:143], v[180:183], v[204:207], v[140:143]\n\t" \
  "ds_read_b64_tr_b16 v[236:237], %[br1] offset:8448\n\t" \
  "v_mfma_f32_16x16x32_bf16 v[144:147], v[184:187], v[192:195], v[144:147]\n\t" \
  "ds_read_b64_tr_b16 v[238:239], %[br1] offset:9472\n\t" \
  "v_mfma_f32_16x16x32_bf16 v[148:151], v[184:187], v[196:199], v[148:151]\n\t" \
  "ds_read_b128 v[212:215], %[ar1] offset:2048\n\t" \
  "v_mfma_f32_16x16x32_bf16 v[152:155], v[184:187], v[200:203], v[152:155]\n\t" \
  "ds_read_b128 v[216:219], %[ar1] offset:4096\n\t" \
  "v_mfma_f32_16x16x32_bf16 v[156:159], v[184:187], v[204:207], v[156:159]\n\t" \
  "ds_read_b128 v[220:223], %[ar1] offset:6144\n\t" \
  "v_mfma_f32_16x16x32_bf16 v[160:163], v[188:191], v[192:195], v[160:163]\n\t" \
  "s_waitcnt vmcnt(16)\n\t" \
  "v_readfirstlane_b32 s81, v242\n\t" \
  "s_mov_b32 s80, 0\n\t" \
  "s_cmp_ge_u32 s81, 128\n\t" \
  "s_cbranch_scc1 L_OKH%=\n\t" \
  "L_SPINH%=:\n\t" \
  "s_sleep 2\n\t" \
  "global_load_dword v242, v240, %[hcc] sc1\n\t" \
  "s_waitcnt vmcnt(0)\n\t" \
  "v_readfirstlane_b32 s81, v242\n\t" \
  "s_add_u32 s80, s80, 1\n\t" \
  "s_cmp_ge_u32 s81, 128\n\t" \
  "s_cbranch_scc1 L_OKH%=\n\t" \
  "s_cmp_lt_u32 s80, 0x20000\n\t" \
  "s_cbranch_scc1 L_SPINH%=\n\t" \
  "L_OKH%=:\n\t" \
  "v_mfma_f32_16x16x32_bf16 v[164:167], v[188:191], v[196:199], v[164:167]\n\t" \
  "v_mfma_f32_16x16x32_bf16 v[168:171], v[188:191], v[200:203], v[168:171]\n\t" \
  "v_mfma_f32_16x16x32_bf16 v[172:175], v[188:191], v[204:207], v[172:175]\n\t" \
  "s_waitcnt vmcnt(10) lgkmcnt(0)\n\t" \
  "s_barrier\n\t" \
  "v_mfma_f32_16x16x32_bf16 v[112:115], v[208:211], v[224:227], v[112:115]\n\t" \
  "v_add_u32_e32 %[br0], 49152, %[br0]\n\t" \
  "v_add_u32_e32 %[br1], 49152, %[br1]\n\t" \
  "v_add_u32_e32 %[ar0], 49152, %[ar0]\n\t" \
  "v_add_u32_e32 %[ar1], 49152, %[ar1]\n\t" \
  "v_mfma_f32_16x16x32_bf16 v[116:119], v[208:211], v[228:231], v[116:119]\n\t" \
  "ds_read_b64_tr_b16 v[192:193], %[br0] offset:0\n\t" \
  "ds_read_b64_tr_b16 v[194:195], %[br0] offset:1024\n\t" \
  "v_mfma_f32_16x16x32_bf16 v[120:123], v[208:211], v[232:235], v[120:123]\n\t" \
  "ds_read_b128 v[176:179], %[ar0] offset:0\n\t" \
  "v_mfma_f32_16x16x32_bf16 v[124:127], v[208:211], v[236:239], v[124:127]\n\t" \
  "ds_read_b64_tr_b16 v[196:197], %[br1] offset:0\n\t" \
  "v_mfma_f32_16x16x32_bf16 v[128:131], v[212:215], v[224:227], v[128:131]\n\t" \
  "ds_read_b64_tr_b16 v[198:199], %[br1] offset:1024\n\t" \
  "s_add_u32 m0, %[dw], 49152\n\t" \
  "s_add_u32 s76, s76, 524288\n\t" \
  "s_addc_u32 s77, s77, 0\n\t" \
  "global_load_lds_dwordx4 %[wv0], s[76:77] offset:0\n\t" \
  "global_load_lds_dwordx4 v252, s[76:77] offset:1024\n\t" \
  "v_mfma_f32_16x16x32_bf16 v[132:135], v[212:215], v[228:231], v[132:135]\n\t" \
  "ds_read_b64_tr_b16 v[200:201], %[br0] offset:256\n\t" \
  "v_mfma_f32_16x16x32_bf16 v[136:139], v[212:215], v[232:235], v[136:139]\n\t" \
  "ds_read_b64_tr_b16 v[202:203], %[br0] offset:1280\n\t" \
  "s_mov_b64 s[74:75], %[sx]\n\t" \
  "global_load_dwordx4 v[48:51], %[lin], s[74:75] offset:0 sc1\n\t" \
  "v_mfma_f32_16x16x32_bf16 v[140:143], v[212:215], v[236:239], v[140:143]\n\t" \
  "ds_read_b64_tr_b16 v[204:205], %[br1] offset:256\n\t" \
  "v_mfma_f32_16x16x32_bf16 v[144:147], v[216:219], v[224:227], v[144:147]\n\t" \
  "ds_read_b64_tr_b16 v[206:207], %[br1] offset:1280\n\t" \
  "v_mfma_f32_16x16x32_bf16 v[148:151], v[216:219], v[228:231], v[148:151]\n\t" \
  "ds_read_b128 v[180:183], %[ar0] offset:2048\n\t" \
  "v_mfma_f32_16x16x32_bf16 v[152:155], v[216:219], v[232:235], v[152:155]\n\t" \
  "ds_read_b128 v[184:187], %[ar0] offset:4096\n\t" \
  "global_load_dwordx4 v[52:55], %[lin], s[74:75] offset:1024 sc1\n\t" \
  "v_mfma_f32_16x16x32_bf16 v[156:159], v[216:219], v[236:239], v[156:159]\n\t" \
  "ds_read_b128 v[188:191], %[ar0] offset:6144\n\t" \
  "v_mfma_f32_16x16x32_bf16 v[160:163], v[220:223], v[224:227], v[160:163]\n\t" \
  "v_mfma_f32_16x16x32_bf16 v[164:167], v[220:223], v[228:231], v[164:167]\n\t" \
  "v_mfma_f32_16x16x32_bf16 v[168:171], v[220:223], v[232:235], v[168:171]\n\t" \
  "global_store_dwordx4 %[kst], v[36:39], s[70:71] offset:2048 sc1\n\t" \
  "global_load_dwordx4 v[56:59], %[lin], s[74:75] offset:2048 sc1\n\t" \
  "v_mfma_f32_16x16x32_bf16 v[172:175], v[220:223], v[236:239], v[172:175]\n\t" \
  "s_waitcnt lgkmcnt(0)\n\t" \
  "v_mfma_f32_16x16x32_bf16 v[112:115], v[176:179], v[192:195], v[112:115]\n\t" \
  "ds_read_b64_tr_b16 v[224:225], %[br0] offset:8192\n\t" \
  "v_mfma_f32_16x16x32_bf16 v[116:119], v[176:179], v[196:199], v[116:119]\n\t" \
  "ds_read_b64_tr_b16 v[226:227], %[br0] offset:9216\n\t" \
  "v_mfma_f32_16x16x32_bf16 v[120:123], v[176:179], v[200:203], v[120:123]\n\t" \
  "ds_read_b128 v[208:211], %[ar1] offset:0\n\t" \
  "global_load_dwordx4 v[60:63], %[lin], s[74:75] offset:3072 sc1\n\t" \
  "v_mfma_f32_16x16x32_bf16 v[124:127], v[176:179], v[204:207], v[124:127]\n\t" \
  "ds_read_b64_tr_b16 v[228:229], %[br1] offset:8192\n\t" \
  "v_mfma_f32_16x16x32_bf16 v[128:131], v[180:183], v[192:195], v[128:131]\n\t" \
  "ds_read_b64_tr_b16 v[230:231], %[br1] offset:9216\n\t" \
  "v_mfma_f32_16x16x32_bf16 v[132:135], v[180:183], v[196:199], v[132:135]\n\t" \
  "ds_read_b64_tr_b16 v[232:233], %[br0] offset:8448\n\t" \
  "v_mfma_f32_16x16x32_bf16 v[136:139], v[180:183], v[200:203], v[136:139]\n\t" \
  "ds_read_b64_tr_b16 v[234:235], %[br0] offset:9472\n\t" \
  "s_add_u32 s74, s74, 4096\n\t" \
  "s_addc_u32 s75, s75, 0\n\t" \
  "global_load_dwordx4 v[64:67], %[lin], s[74:75] offset:0 sc1\n\t" \
  "v_mfma_f32_16x16x32_bf16 v[140:143], v[180:183], v[204:207], v[140:143]\n\t" \
  "ds_read_b64_tr_b16 v[236:237], %[br1] offset:8448\n\t" \
  "v_mfma_f32_16x16x32_bf16 v[144:147], v[184:187], v[192:195], v[144:147]\n\t" \
  "ds_read_b64_tr_b16 v[238:239], %[br1] offset:9472\n\t" \
  "v_mfma_f32_16x16x32_bf16 v[148:151], v[184:187], v[196:199], v[148:151]\n\t" \
  "ds_read_b128 v[212:215], %[ar1] offset:2048\n\t" \
  "v_mfma_f32_16x16x32_bf16 v[152:155], v[184:187], v[200:203], v[152:155]\n\t" \
  "ds_read_b128 v[216:219], %[ar1] offset:4096\n\t" \
  "global_load_dwordx4 v[68:71], %[lin], s[74:75] offset:1024 sc1\n\t" \
  "v_mfma_f32_16x16x32_bf16 v[156:159], v[184:187], v[204:207], v[156:159]\n\t" \
  "ds_read_b128 v[220:223], %[ar1] offset:6144\n\t" \
  "v_mfma_f32_16x16x32_bf16 v[160:163], v[188:191], v[192:195], v[160:163]\n\t" \
  "v_mfma_f32_16x16x32_bf16 v[164:167], v[188:191], v[196:199], v[164:167]\n\t" \
  "v_mfma_f32_16x16x32_bf16 v[168:171], v[188:191], v[200:203], v[168:171]\n\t" \
  "v_mfma_f32_16x16x32_bf16 v[172:175], v[188:191], v[204:207], v[172:175]\n\t" \
  "s_waitcnt vmcnt(10) lgkmcnt(0)\n\t" \
  "s_barrier\n\t" \
  "v_mfma_f32_16x16x32_bf16 v[112:115], v[208:211], v[224:227], v[112:115]\n\t" \
  "v_add_u32_e32 %[br0], -98304, %[br0]\n\t" \
  "v_add_u32_e32 %[br1], -98304, %[br1]\n\t" \
  "v_add_u32_e32 %[ar0], -98304, %[ar0]\n\t" \
  "v_add_u32_e32 %[ar1], -98304, %[ar1]\n\t" \
  "v_mfma_f32_16x16x32_bf16 v[116:119], v[208:211], v[228:231], v[116:119]\n\t" \
  "ds_read_b64_tr_b16 v[192:193], %[br0] offset:0\n\t" \
  "ds_read_b64_tr_b16 v[194:195], %[br0] offset:1024\n\t" \
  "v_mfma_f32_16x16x32_bf16 v[120:123], v[208:211], v[232:235], v[120:123]\n\t" \
  "ds_read_b128 v[176:179], %[ar0] offset:0\n\t" \
  "v_mfma_f32_16x16x32_bf16 v[124:127], v[208:211], v[236:239], v[124:127]\n\t" \
  "ds_read_b64_tr_b16 v[196:197], %[br1] offset:0\n\t" \
  "v_mfma_f32_16x16x32_bf16 v[128:131], v[212:215], v[224:227], v[128:131]\n\t" \
  "ds_read_b64_tr_b16 v[198:199], %[br1] offset:1024\n\t" \
  "s_add_u32 m0, %[dw], 114688\n\t" \
  "s_add_u32 s76, s76, 524288\n\t" \
  "s_addc_u32 s77, s77, 0\n\t" \
  "global_load_lds_dwordx4 %[wv0], s[76:77] offset:0\n\t" \
  "global_load_lds_dwordx4 v252, s[76:77] offset:1024\n\t" \
  "v_mfma_f32_16x16x32_bf16 v[132:135], v[212:215], v[228:231], v[132:135]\n\t" \
  "ds_read_b64_tr_b16 v[200:201], %[br0] offset:256\n\t" \
  "v_mfma_f32_16x16x32_bf16 v[136:139], v[212:215], v[232:235], v[136:139]\n\t" \
  "ds_read_b64_tr_b16 v[202:203], %[br0] offset:1280\n\t" \
  "global_load_dwordx4 v[72:75], %[lin], s[74:75] offset:2048 sc1\n\t" \
  "v_mfma_f32_16x16x32_bf16 v[140:143], v[212:215], v[236:239], v[140:143]\n\t" \
  "ds_read_b64_tr_b16 v[204:205], %[br1] offset:256\n\t" \
  "v_mfma_f32_16x16x32_bf16 v[144:147], v[216:219], v[224:227], v[144:147]\n\t" \
  "ds_read_b64_tr_b16 v[206:207], %[br1] offset:1280\n\t" \
  "v_mfma_f32_16x16x32_bf16 v[148:151], v[216:219], v[228:231], v[148:151]\n\t" \
  "ds_read_b128 v[180:183], %[ar0] offset:2048\n\t" \
  "v_mfma_f32_16x16x32_bf16 v[152:155], v[216:219], v[232:235], v[152:155]\n\t" \
  "ds_read_b128 v[184:187], %[ar0] offset:4096\n\t" \
  "global_load_dwordx4 v[76:79], %[lin], s[74:75] offset:3072 sc1\n\t" \
  "v_mfma_f32_16x16x32_bf16 v[156:159], v[216:219], v[236:239], v[156:159]\n\t" \
  "ds_read_b128 v[188:191], %[ar0] offset:6144\n\t" \
  "v_mfma_f32_16x16x32_bf16 v[160:163], v[220:223], v[224:227], v[160:163]\n\t" \
  "v_mfma_f32_16x16x32_bf16 v[164:167], v[220:223], v[228:231], v[164:167]\n\t" \
  "v_mfma_f32_16x16x32_bf16 v[168:171], v[220:223], v[232:235], v[168:171]\n\t" \
  "global_store_dwordx4 %[kst], v[40:43], s[70:71] offset:128 sc1\n\t" \
  "s_add_u32 s74, s74, 4096\n\t" \
  "s_addc_u32 s75, s75, 0\n\t" \
  "global_load_dwordx4 v[80:83], %[lin], s[74:75] offset:0 sc1\n\t" \
  "v_mfma_f32_16x16x32_bf16 v[172:175], v[220:223], v[236:239], v[172:175]\n\t" \
  "s_waitcnt lgkmcnt(0)\n\t" \
  "v_mfma_f32_16x16x32_bf16 v[112:115], v[176:179], v[192:195], v[112:115]\n\t" \
  "ds_read_b64_tr_b16 v[224:225], %[br0] offset:8192\n\t" \
  "v_mfma_f32_16x16x32_bf16 v[116:119], v[176:179], v[196:199], v[116:119]\n\t" \
  "ds_read_b64_tr_b16 v[226:227], %[br0] offset:9216\n\t" \
  "v_mfma_f32_16x16x32_bf16 v[120:123], v[176:179], v[200:203], v[120:123]\n\t" \
  "ds_read_b128 v[208:211], %[ar1] offset:0\n\t" \
  "global_load_dwordx4 v[84:87], %[lin], s[74:75] offset:1024 sc1\n\t" \
  "v_mfma_f32_16x16x32_bf16 v[124:127], v[176:179], v[204:207], v[124:127]\n\t" \
  "ds_read_b64_tr_b16 v[228:229], %[br1] offset:8192\n\t" \
  "v_mfma_f32_16x16x32_bf16 v[128:131], v[180:183], v[192:195], v[128:131]\n\t" \
  "ds_read_b64_tr_b16 v[230:231], %[br1] offset:9216\n\t" \
  "v_mfma_f32_16x16x32_bf16 v[132:135], v[180:183], v[196:199], v[132:135]\n\t" \
  "ds_read_b64_tr_b16 v[232:233], %[br0] offset:8448\n\t" \
  "v_mfma_f32_16x16x32_bf16 v[136:139], v[180:183], v[200:203], v[136:139]\n\t" \
  "ds_read_b64_tr_b16 v[234:235], %[br0] offset:9472\n\t" \
  "global_load_dwordx4 v[88:91], %[lin], s[74:75] offset:2048 sc1\n\t" \
  "v_mfma_f32_16x16x32_bf16 v[140:143], v[180:183], v[204:207], v[140:143]\n\t" \
  "ds_read_b64_tr_b16 v[236:237], %[br1] offset:8448\n\t" \
  "v_mfma_f32_16x16x32_bf16 v[144:147], v[184:187], v[192:195], v[144:147]\n\t" \
  "ds_read_b64_tr_b16 v[238:239], %[br1] offset:9472\n\t" \
  "v_mfma_f32_16x16x32_bf16 v[148:151], v[184:187], v[196:199], v[148:151]\n\t" \
  "ds_read_b128 v[212:215], %[ar1] offset:2048\n\t" \
  "v_mfma_f32_16x16x32_bf16 v[152:155], v[184:187], v[200:203], v[152:155]\n\t" \
  "ds_read_b128 v[216:219], %[ar1] offset:4096\n\t" \
  "global_load_dwordx4 v[92:95], %[lin], s[74:75] offset:3072 sc1\n\t" \
  "v_mfma_f32_16x16x32_bf16 v[156:159], v[184:187], v[204:207], v[156:159]\n\t" \
  "ds_read_b128 v[220:223], %[ar1] offset:6144\n\t" \
  "v_mfma_f32_16x16x32_bf16 v[160:163], v[188:191], v[192:195], v[160:163]\n\t" \
  "v_mfma_f32_16x16x32_bf16 v[164:167], v[188:191], v[196:199], v[164:167]\n\t" \
  "v_mfma_f32_16x16x32_bf16 v[168:171], v[188:191], v[200:203], v[168:171]\n\t" \
  "v_mfma_f32_16x16x32_bf16 v[172:175], v[188:191], v[204:207], v[172:175]\n\t" \
  "s_waitcnt vmcnt(16) lgkmcnt(0)\n\t" \
  "s_barrier\n\t" \
  "v_mfma_f32_16x16x32_bf16 v[112:115], v[208:211], v[224:227], v[112:115]\n\t" \
  "ds_read_b64_tr_b16 v[176:177], v246 offset:16384\n\t" \
  "ds_read_b64_tr_b16 v[178:179], v246 offset:17408\n\t" \
  "v_mfma_f32_16x16x32_bf16 v[116:119], v[208:211], v[228:231], v[116:119]\n\t" \
  "ds_read_b64_tr_b16 v[180:181], v246 offset:16640\n\t" \
  "ds_read_b64_tr_b16 v[182:183], v246 offset:17664\n\t" \
  "v_mfma_f32_16x16x32_bf16 v[120:123], v[208:211], v[232:235], v[120:123]\n\t" \
  "ds_read_b64_tr_b16 v[184:185], v246 offset:16896\n\t" \
  "ds_read_b64_tr_b16 v[186:187], v246 offset:17920\n\t" \
  "s_add_u32 s74, s74, 4096\n\t" \
  "s_addc_u32 s75, s75, 0\n\t" \
  "global_load_dwordx4 v[96:99], %[lin], s[74:75] offset:0 sc1\n\t" \
  "v_mfma_f32_16x16x32_bf16 v[124:127], v[208:211], v[236:239], v[124:127]\n\t" \
  "ds_read_b64_tr_b16 v[188:189], v246 offset:17152\n\t" \
  "ds_read_b64_tr_b16 v[190:191], v246 offset:18176\n\t" \
  "v_mfma_f32_16x16x32_bf16 v[128:131], v[212:215], v[224:227], v[128:131]\n\t" \
  "ds_read_b64_tr_b16 v[192:193], v246 offset:20480\n\t" \
  "ds_read_b64_tr_b16 v[194:195], v246 offset:21504\n\t" \
  "v_mfma_f32_16x16x32_bf16 v[132:135], v[212:215], v[228:231], v[132:135]\n\t" \
  "ds_read_b64_tr_b16 v[196:197], v246 offset:20736\n\t" \
  "ds_read_b64_tr_b16 v[198:199], v246 offset:21760\n\t" \
  "global_load_dwordx4 v[100:103], %[lin], s[74:75] offset:1024 sc1\n\t" \
  "v_mfma_f32_16x16x32_bf16 v[136:139], v[212:215], v[232:235], v[136:139]\n\t" \
  "ds_read_b64_tr_b16 v[200:201], v246 offset:20992\n\t" \
  "ds_read_b64_tr_b16 v[202:203], v246 offset:22016\n\t" \
  "v_mfma_f32_16x16x32_bf16 v[140:143], v[212:215], v[236:239], v[140:143]\n\t" \
  "ds_read_b64_tr_b16 v[204:205], v246 offset:21248\n\t" \
  "ds_read_b64_tr_b16 v[206:207], v246 offset:22272\n\t" \
  "v_mfma_f32_16x16x32_bf16 v[144:147], v[216:219], v[224:227], v[144:147]\n\t" \
  "global_load_dwordx4 v[104:107], %[lin], s[74:75] offset:2048 sc1\n\t" \
  "v_mfma_f32_16x16x32_bf16 v[148:151], v[216:219], v[228:231], v[148:151]\n\t" \
  "global_store_dwordx4 %[kst], v[44:47], s[70:71] offset:2176 sc1\n\t" \
  "v_mfma_f32_16x16x32_bf16 v[152:155], v[216:219], v[232:235], v[152:155]\n\t" \
  "s_add_u32 m0, %[dw], 131072\n\t" \
  "s_add_u32 s76, s76, 524288\n\t" \
  "s_addc_u32 s77, s77, 0\n\t" \
  "global_load_lds_dwordx4 %[wv0], s[76:77] offset:0\n\t" \
  "global_load_lds_dwordx4 v252, s[76:77] offset:1024\n\t" \
  "v_mfma_f32_16x16x32_bf16 v[156:159], v[216:219], v[236:239], v[156:159]\n\t" \
  "global_load_dwordx4 v[108:111], %[lin], s[74:75] offset:3072 sc1\n\t" \
  "v_cvt_pk_bf16_f32 v112, v112, v113\n\t" \
  "v_cvt_pk_bf16_f32 v113, v114, v115\n\t" \
  "v_cvt_pk_bf16_f32 v114, v128, v129\n\t" \
  "v_cvt_pk_bf16_f32 v115, v130, v131\n\t" \
  "v_mfma_f32_16x16x32_bf16 v[160:163], v[220:223], v[224:227], v[160:163]\n\t" \
  "v_cvt_pk_bf16_f32 v116, v116, v117\n\t" \
  "v_cvt_pk_bf16_f32 v117, v118, v119\n\t" \
  "v_cvt_pk_bf16_f32 v118, v132, v133\n\t" \
  "v_mfma_f32_16x16x32_bf16 v[164:167], v[220:223], v[228:231], v[164:167]\n\t" \
  "v_cvt_pk_bf16_f32 v119, v134, v135\n\t" \
  "v_cvt_pk_bf16_f32 v120, v120, v121\n\t" \
  "v_cvt_pk_bf16_f32 v121, v122, v123\n\t" \
  "v_mfma_f32_16x16x32_bf16 v[168:171], v[220:223], v[232:235], v[168:171]\n\t" \
  "v_cvt_pk_bf16_f32 v122, v136, v137\n\t" \
  "v_cvt_pk_bf16_f32 v123, v138, v139\n\t" \
  "v_cvt_pk_bf16_f32 v124, v124, v125\n\t" \
  "v_mfma_f32_16x16x32_bf16 v[172:175], v[220:223], v[236:239], v[172:175]\n\t" \
  "v_cvt_pk_bf16_f32 v125, v126, v127\n\t" \
  "v_cvt_pk_bf16_f32 v126, v140, v141\n\t" \
  "v_cvt_pk_bf16_f32 v127, v142, v143\n\t" \
  "s_waitcnt vmcnt(22) lgkmcnt(12)\n\t" \
  "v_mfma_f32_32x32x16_bf16 v[128:143], v[176:179], v[48:51], 0\n\t" \
  "ds_read_b64_tr_b16 v[176:177], v246 offset:24576\n\t" \
  "ds_read_b64_tr_b16 v[178:179], v246 offset:25600\n\t" \
  "v_mfma_f32_32x32x16_bf16 v[208:223], v[180:183], v[48:51], 0\n\t" \
  "ds_read_b64_tr_b16 v[180:181], v246 offset:24832\n\t" \
  "ds_read_b64_tr_b16 v[182:183], v246 offset:25856\n\t" \
  "v_cvt_pk_bf16_f32 v144, v144, v145\n\t" \
  "v_cvt_pk_bf16_f32 v145, v146, v147\n\t" \
  "v_cvt_pk_bf16_f32 v146, v160, v161\n\t" \
  "v_cvt_pk_bf16_f32 v147, v162, v163\n\t" \
  "v_cvt_pk_bf16_f32 v148, v148, v149\n\t" \
  "v_cvt_pk_bf16_f32 v149, v150, v151\n\t" \
  "v_cvt_pk_bf16_f32 v150, v164, v165\n\t" \
  "v_cvt_pk_bf16_f32 v151, v166, v167\n\t" \
  "s_waitcnt lgkmcnt(12)\n\t" \
  "v_mfma_f32_32x32x16_bf16 v[224:239], v[184:187], v[48:51], 0\n\t" \
  "ds_read_b64_tr_b16 v[184:185], v246 offset:25088\n\t" \
  "ds_read_b64_tr_b16 v[186:187], v246 offset:26112\n\t" \
  "v_cvt_pk_bf16_f32 v152, v152, v153\n\t" \
  "v_cvt_pk_bf16_f32 v153, v154, v155\n\t" \
  "v_cvt_pk_bf16_f32 v154, v168, v169\n\t" \
  "v_cvt_pk_bf16_f32 v155, v170, v171\n\t" \
  "v_cvt_pk_bf16_f32 v156, v156, v157\n\t" \
  "v_cvt_pk_bf16_f32 v157, v158, v159\n\t" \
  "v_cvt_pk_bf16_f32 v158, v172, v173\n\t" \
  "v_cvt_pk_bf16_f32 v159, v174, v175\n\t" \
  "v_mfma_f32_32x32x16_bf16 v[160:175], v[188:191], v[48:51], 0\n\t" \
  "ds_read_b64_tr_b16 v[188:189], v246 offset:25344\n\t" \
  "ds_read_b64_tr_b16 v[190:191], v246 offset:26368\n\t" \
  "s_add_u32 s74, s74, 4096\n\t" \
  "s_addc_u32 s75, s75, 0\n\t" \
  "global_load_dwordx4 v[48:51], %[lin], s[74:75] offset:0 sc1\n\t" \
  "s_add_u32 m0, %[dw], 16384\n\t" \
  "s_add_u32 s76, s76, 524288\n\t" \
  "s_addc_u32 s77, s77, 0\n\t" \
  "global_load_lds_dwordx4 %[wv0], s[76:77] offset:0\n\t" \
  "global_load_lds_dwordx4 v252, s[76:77] offset:1024\n\t" \
  "s_waitcnt vmcnt(24) lgkmcnt(12)\n\t" \
  "v_mfma_f32_32x32x16_bf16 v[128:143], v[192:195], v[52:55], v[128:143]\n\t" \
  "ds_read_b64_tr_b16 v[192:193], v246 offset:28672\n\t" \
  "ds_read_b64_tr_b16 v[194:195], v246 offset:29696\n\t" \
  "v_mfma_f32_32x32x16_bf16 v[208:223], v[196:199], v[52:55], v[208:223]\n\t" \
  "ds_read_b64_tr_b16 v[196:197], v246 offset:28928\n\t" \
  "ds_read_b64_tr_b16 v[198:199], v246 offset:29952\n\t" \
  "s_waitcnt lgkmcnt(12)\n\t" \
  "v_mfma_f32_32x32x16_bf16 v[224:239], v[200:203], v[52:55], v[224:239]\n\t" \
  "ds_read_b64_tr_b16 v[200:201], v246 offset:29184\n\t" \
  "ds_read_b64_tr_b16 v[202:203], v246 offset:30208\n\t" \
  "v_mfma_f32_32x32x16_bf16 v[160:175], v[204:207], v[52:55], v[160:175]\n\t" \
  "ds_read_b64_tr_b16 v[204:205], v246 offset:29440\n\t" \
  "ds_read_b64_tr_b16 v[206:207], v246 offset:30464\n\t" \
  "s_waitcnt vmcnt(22) lgkmcnt(12)\n\t" \
  "v_mfma_f32_32x32x16_bf16 v[128:143], v[176:179], v[56:59], v[128:143]\n\t" \
  "ds_read_b64_tr_b16 v[176:177], %[wrd] offset:49152\n\t" \
  "ds_read_b64_tr_b16 v[178:179], %[wrd] offset:50176\n\t" \
  "global_load_dwordx4 v[52:55], %[lin], s[74:75] offset:1024 sc1\n\t" \
  "v_mfma_f32_32x32x16_bf16 v[208:223], v[180:183], v[56:59], v[208:223]\n\t" \
  "ds_read_b64_tr_b16 v[180:181], %[wrd] offset:49408\n\t" \
  "ds_read_b64_tr_b16 v[182:183], %[wrd] offset:50432\n\t" \
  "s_mov_b64 s[72:73], %[vd]\n\t" \
  "global_store_dwordx4 %[vst], v[112:115], s[72:73] offset:0 sc1\n\t" \
  "s_waitcnt lgkmcnt(12)\n\t" \
  "v_mfma_f32_32x32x16_bf16 v[224:239], v[184:187], v[56:59], v[224:239]\n\t" \
  "ds_read_b64_tr_b16 v[184:185], %[wrd] offset:49664\n\t" \
  "ds_read_b64_tr_b16 v[186:187], %[wrd] offset:50688\n\t" \
  "v_mfma_f32_32x32x16_bf16 v[160:175], v[188:191], v[56:59], v[160:175]\n\t" \
  "ds_read_b64_tr_b16 v[188:189], %[wrd] offset:49920\n\t" \
  "ds_read_b64_tr_b16 v[190:191], %[wrd] offset:50944\n\t" \
  "s_waitcnt vmcnt(23) lgkmcnt(12)\n\t" \
  "v_mfma_f32_32x32x16_bf16 v[128:143], v[192:195], v[60:63], v[128:143]\n\t" \
  "ds_read_b64_tr_b16 v[192:193], %[wrd] offset:53248\n\t" \
  "ds_read_b64_tr_b16 v[194:195], %[wrd] offset:54272\n\t" \
  "v_mfma_f32_32x32x16_bf16 v[208:223], v[196:199], v[60:63], v[208:223]\n\t" \
  "ds_read_b64_tr_b16 v[196:197], %[wrd] offset:53504\n\t" \
  "ds_read_b64_tr_b16 v[198:199], %[wrd] offset:54528\n\t" \
  "global_load_dwordx4 v[56:59], %[lin], s[74:75] offset:2048 sc1\n\t" \
  "s_waitcnt lgkmcnt(12)\n\t" \
  "v_mfma_f32_32x32x16_bf16 v[224:239], v[200:203], v[60:63], v[224:239]\n\t" \
  "ds_read_b64_tr_b16 v[200:201], %[wrd] offset:53760\n\t" \
  "ds_read_b64_tr_b16 v[202:203], %[wrd] offset:54784\n\t" \
  "v_mfma_f32_32x32x16_bf16 v[160:175], v[204:207], v[60:63], v[160:175]\n\t" \
  "ds_read_b64_tr_b16 v[204:205], %[wrd] offset:54016\n\t" \
  "ds_read_b64_tr_b16 v[206:207], %[wrd] offset:55040\n\t" \
  "s_waitcnt vmcnt(20)\n\t" \
  "s_barrier\n\t" \
  "s_waitcnt lgkmcnt(12)\n\t" \
  "v_mfma_f32_32x32x16_bf16 v[128:143], v[176:179], v[64:67], v[128:143]\n\t" \
  "ds_read_b64_tr_b16 v[176:177], %[wrd] offset:57344\n\t" \
  "ds_read_b64_tr_b16 v[178:179], %[wrd] offset:58368\n\t" \
  "v_mfma_f32_32x32x16_bf16 v[208:223], v[180:183], v[64:67], v[208:223]\n\t" \
  "ds_read_b64_tr_b16 v[180:181], %[wrd] offset:57600\n\t" \
  "ds_read_b64_tr_b16 v[182:183], %[wrd] offset:58624\n\t" \
  "s_add_u32 m0, %[dw], 32768\n\t" \
  "s_add_u32 s76, s76, 524288\n\t" \
  "s_addc_u32 s77, s77, 0\n\t" \
  "global_load_lds_dwordx4 %[wv0], s[76:77] offset:0\n\t" \
  "global_load_lds_dwordx4 v252, s[76:77] offset:1024\n\t" \
  "s_waitcnt lgkmcnt(12)\n\t" \
  "v_mfma_f32_32x32x16_bf16 v[224:239], v[184:187], v[64:67], v[224:239]\n\t" \
  "ds_read_b64_tr_b16 v[184:185], %[wrd] offset:57856\n\t" \
  "ds_read_b64_tr_b16 v[186:187], %[wrd] offset:58880\n\t" \
  "global_load_dwordx4 v[60:63], %[lin], s[74:75] offset:3072 sc1\n\t" \
  "v_mfma_f32_32x32x16_bf16 v[160:175], v[188:191], v[64:67], v[160:175]\n\t" \
  "ds_read_b64_tr_b16 v[188:189], %[wrd] offset:58112\n\t" \
  "ds_read_b64_tr_b16 v[190:191], %[wrd] offset:59136\n\t" \
  "s_waitcnt lgkmcnt(12)\n\t" \
  "v_mfma_f32_32x32x16_bf16 v[128:143], v[192:195], v[68:71], v[128:143]\n\t" \
  "ds_read_b64_tr_b16 v[192:193], %[wrd] offset:61440\n\t" \
  "ds_read_b64_tr_b16 v[194:195], %[wrd] offset:62464\n\t" \
  "v_mfma_f32_32x32x16_bf16 v[208:223], v[196:199], v[68:71], v[208:223]\n\t" \
  "ds_read_b64_tr_b16 v[196:197], %[wrd] offset:61696\n\t" \
  "ds_read_b64_tr_b16 v[198:199], %[wrd] offset:62720\n\t" \
  "s_waitcnt lgkmcnt(12)\n\t" \
  "v_mfma_f32_32x32x16_bf16 v[224:239], v[200:203], v[68:71], v[224:239]\n\t" \
  "ds_read_b64_tr_b16 v[200:201], %[wrd] offset:61952\n\t" \
  "ds_read_b64_tr_b16 v[202:203], %[wrd] offset:62976\n\t" \
  "v_mfma_f32_32x32x16_bf16 v[160:175], v[204:207], v[68:71], v[160:175]\n\t" \
  "ds_read_b64_tr_b16 v[204:205], %[wrd] offset:62208\n\t" \
  "ds_read_b64_tr_b16 v[206:207], %[wrd] offset:63232\n\t" \
  "s_waitcnt vmcnt(22) lgkmcnt(12)\n\t" \
  "v_mfma_f32_32x32x16_bf16 v[128:143], v[176:179], v[72:75], v[128:143]\n\t" \
  "ds_read_b64_tr_b16 v[176:177], v253 offset:49152\n\t" \
  "ds_read_b64_tr_b16 v[178:179], v253 offset:50176\n\t" \
  "s_add_u32 s74, s74, 4096\n\t" \
  "s_addc_u32 s75, s75, 0\n\t" \
  "global_load_dwordx4 v[64:67], %[lin], s[74:75] offset:0 sc1\n\t" \
  "v_mfma_f32_32x32x16_bf16 v[208:223], v[180:183], v[72:75], v[208:223]\n\t" \
  "ds_read_b64_tr_b16 v[180:181], v253 offset:49408\n\t" \
  "ds_read_b64_tr_b16 v[182:183], v253 offset:50432\n\t" \
  "global_store_dwordx4 %[vst], v[116:119], s[72:73] offset:256 sc1\n\t" \
  "s_waitcnt lgkmcnt(12)\n\t" \
  "v_mfma_f32_32x32x16_bf16 v[224:239], v[184:187], v[72:75], v[224:239]\n\t" \
  "ds_read_b64_tr_b16 v[184:185], v253 offset:49664\n\t" \
  "ds_read_b64_tr_b16 v[186:187], v253 offset:50688\n\t" \
  "v_mfma_f32_32x32x16_bf16 v[160:175], v[188:191], v[72:75], v[160:175]\n\t" \
  "ds_read_b64_tr_b16 v[188:189], v253 offset:49920\n\t" \
  "ds_read_b64_tr_b16 v[190:191], v253 offset:50944\n\t" \
  "s_waitcnt vmcnt(23) lgkmcnt(12)\n\t" \
  "v_mfma_f32_32x32x16_bf16 v[128:143], v[192:195], v[76:79], v[128:143]\n\t" \
  "ds_read_b64_tr_b16 v[192:193], v253 offset:53248\n\t" \
  "ds_read_b64_tr_b16 v[194:195], v253 offset:54272\n\t" \
  "v_mfma_f32_32x32x16_bf16 v[208:223], v[196:199], v[76:79], v[208:223]\n\t" \
  "ds_read_b64_tr_b16 v[196:197], v253 offset:53504\n\t" \
  "ds_read_b64_tr_b16 v[198:199], v253 offset:54528\n\t" \
  "global_load_dwordx4 v[68:71], %[lin], s[74:75] offset:1024 sc1\n\t" \
  "s_waitcnt lgkmcnt(12)\n\t" \
  "v_mfma_f32_32x32x16_bf16 v[224:239], v[200:203], v[76:79], v[224:239]\n\t" \
  "ds_read_b64_tr_b16 v[200:201], v253 offset:53760\n\t" \
  "ds_read_b64_tr_b16 v[202:203], v253 offset:54784\n\t" \
  "v_mfma_f32_32x32x16_bf16 v[160:175], v[204:207], v[76:79], v[160:175]\n\t" \
  "ds_read_b64_tr_b16 v[204:205], v253 offset:54016\n\t" \
  "ds_read_b64_tr_b16 v[206:207], v253 offset:55040\n\t" \
  "s_waitcnt vmcnt(13)\n\t" \
  "s_barrier\n\t" \
  "s_waitcnt lgkmcnt(12)\n\t" \
  "v_mfma_f32_32x32x16_bf16 v[128:143], v[176:179], v[80:83], v[128:143]\n\t" \
  "ds_read_b64_tr_b16 v[176:177], v253 offset:57344\n\t" \
  "ds_read_b64_tr_b16 v[178:179], v253 offset:58368\n\t" \
  "v_mfma_f32_32x32x16_bf16 v[208:223], v[180:183], v[80:83], v[208:223]\n\t" \
  "ds_read_b64_tr_b16 v[180:181], v253 offset:57600\n\t" \
  "ds_read_b64_tr_b16 v[182:183], v253 offset:58624\n\t" \
  "s_add_u32 m0, %[dw], 81920\n\t" \
  "s_add_u32 s76, s76, 524288\n\t" \
  "s_addc_u32 s77, s77, 0\n\t" \
  "global_load_lds_dwordx4 %[wv0], s[76:77] offset:0\n\t" \
  "global_load_lds_dwordx4 v252, s[76:77] offset:1024\n\t" \
  "s_waitcnt lgkmcnt(12)\n\t" \
  "v_mfma_f32_32x32x16_bf16 v[224:239], v[184:187], v[80:83], v[224:239]\n\t" \
  "ds_read_b64_tr_b16 v[184:185], v253 offset:57856\n\t" \
  "ds_read_b64_tr_b16 v[186:187], v253 offset:58880\n\t" \
  "global_load_dwordx4 v[72:75], %[lin], s[74:75] offset:2048 sc1\n\t" \
  "v_mfma_f32_32x32x16_bf16 v[160:175], v[188:191], v[80:83], v[160:175]\n\t" \
  "ds_read_b64_tr_b16 v[188:189], v253 offset:58112\n\t" \
  "ds_read_b64_tr_b16 v[190:191], v253 offset:59136\n\t" \
  "s_waitcnt lgkmcnt(12)\n\t" \
  "v_mfma_f32_32x32x16_bf16 v[128:143], v[192:195], v[84:87], v[128:143]\n\t" \
  "ds_read_b64_tr_b16 v[192:193], v253 offset:61440\n\t" \
  "ds_read_b64_tr_b16 v[194:195], v253 offset:62464\n\t" \
  "v_mfma_f32_32x32x16_bf16 v[208:223], v[196:199], v[84:87], v[208:223]\n\t" \
  "ds_read_b64_tr_b16 v[196:197], v253 offset:61696\n\t" \
  "ds_read_b64_tr_b16 v[198:199], v253 offset:62720\n\t" \
  "s_waitcnt lgkmcnt(12)\n\t" \
  "v_mfma_f32_32x32x16_bf16 v[224:239], v[200:203], v[84:87], v[224:239]\n\t" \
  "ds_read_b64_tr_b16 v[200:201], v253 offset:61952\n\t" \
  "ds_read_b64_tr_b16 v[202:203], v253 offset:62976\n\t" \
  "v_mfma_f32_32x32x16_bf16 v[160:175], v[204:207], v[84:87], v[160:175]\n\t" \
  "ds_read_b64_tr_b16 v[204:205], v253 offset:62208\n\t" \
  "ds_read_b64_tr_b16 v[206:207], v253 offset:63232\n\t" \
  "global_load_dwordx4 v[76:79], %[lin], s[74:75] offset:3072 sc1\n\t" \
  "s_waitcnt lgkmcnt(12)\n\t" \
  "v_mfma_f32_32x32x16_bf16 v[128:143], v[176:179], v[88:91], v[128:143]\n\t" \
  "ds_read_b64_tr_b16 v[176:177], v246 offset:0\n\t" \
  "ds_read_b64_tr_b16 v[178:179], v246 offset:1024\n\t" \
  "v_mfma_f32_32x32x16_bf16 v[208:223], v[180:183], v[88:91], v[208:223]\n\t" \
  "ds_read_b64_tr_b16 v[180:181], v246 offset:256\n\t" \
  "ds_read_b64_tr_b16 v[182:183], v246 offset:1280\n\t" \
  "global_store_dwordx4 %[vst], v[120:123], s[72:73] offset:1024 sc1\n\t" \
  "s_waitcnt lgkmcnt(12)\n\t" \
  "v_mfma_f32_32x32x16_bf16 v[224:239], v[184:187], v[88:91], v[224:239]\n\t" \
  "ds_read_b64_tr_b16 v[184:185], v246 offset:512\n\t" \
  "ds_read_b64_tr_b16 v[186:187], v246 offset:1536\n\t" \
  "v_mfma_f32_32x32x16_bf16 v[160:175], v[188:191], v[88:91], v[160:175]\n\t" \
  "ds_read_b64_tr_b16 v[188:189], v246 offset:768\n\t" \
  "ds_read_b64_tr_b16 v[190:191], v246 offset:1792\n\t" \
  "s_waitcnt lgkmcnt(12)\n\t" \
  "v_mfma_f32_32x32x16_bf16 v[128:143], v[192:195], v[92:95], v[128:143]\n\t" \
  "ds_read_b64_tr_b16 v[192:193], v246 offset:4096\n\t" \
  "ds_read_b64_tr_b16 v[194:195], v246 offset:5120\n\t" \
  "v_mfma_f32_32x32x16_bf16 v[208:223], v[196:199], v[92:95], v[208:223]\n\t" \
  "ds_read_b64_tr_b16 v[196:197], v246 offset:4352\n\t" \
  "ds_read_b64_tr_b16 v[198:199], v246 offset:5376\n\t" \
  "s_add_u32 s74, s74, 4096\n\t" \
  "s_addc_u32 s75, s75, 0\n\t" \
  "global_load_dwordx4 v[80:83], %[lin], s[74:75] offset:0 sc1\n\t" \
  "s_waitcnt lgkmcnt(12)\n\t" \
  "v_mfma_f32_32x32x16_bf16 v[224:239], v[200:203], v[92:95], v[224:239]\n\t" \
  "ds_read_b64_tr_b16 v[200:201], v246 offset:4608\n\t" \
  "ds_read_b64_tr_b16 v[202:203], v246 offset:5632\n\t" \
  "s_mov_b64 exec, 1\n\t" \
  "global_atomic_add v240, v241, %[cnt]\n\t" \
  "s_mov_b64 exec, -1\n\t" \
  "v_mfma_f32_32x32x16_bf16 v[160:175], v[204:207], v[92:95], v[160:175]\n\t" \
  "ds_read_b64_tr_b16 v[204:205], v246 offset:4864\n\t" \
  "ds_read_b64_tr_b16 v[206:207], v246 offset:5888\n\t" \
  "s_waitcnt vmcnt(16)\n\t" \
  "s_barrier\n\t" \
  "s_waitcnt lgkmcnt(12)\n\t" \
  "v_mfma_f32_32x32x16_bf16 v[128:143], v[176:179], v[96:99], v[128:143]\n\t" \
  "ds_read_b64_tr_b16 v[176:177], v246 offset:8192\n\t" \
  "ds_read_b64_tr_b16 v[178:179], v246 offset:9216\n\t" \
  "v_mfma_f32_32x32x16_bf16 v[208:223], v[180:183], v[96:99], v[208:223]\n\t" \
  "ds_read_b64_tr_b16 v[180:181], v246 offset:8448\n\t" \
  "ds_read_b64_tr_b16 v[182:183], v246 offset:9472\n\t" \
  "s_add_u32 m0, %[dw], 98304\n\t" \
  "s_add_u32 s76, s76, 524288\n\t" \
  "s_addc_u32 s77, s77, 0\n\t" \
  "global_load_lds_dwordx4 %[wv0], s[76:77] offset:0\n\t" \
  "global_load_lds_dwordx4 v252, s[76:77] offset:1024\n\t" \
  "s_waitcnt lgkmcnt(12)\n\t" \
  "v_mfma_f32_32x32x16_bf16 v[224:239], v[184:187], v[96:99], v[224:239]\n\t" \
  "ds_read_b64_tr_b16 v[184:185], v246 offset:8704\n\t" \
  "ds_read_b64_tr_b16 v[186:187], v246 offset:9728\n\t" \
  "global_load_dwordx4 v[84:87], %[lin], s[74:75] offset:1024 sc1\n\t" \
  "v_mfma_f32_32x32x16_bf16 v[160:175], v[188:191], v[96:99], v[160:175]\n\t" \
  "ds_read_b64_tr_b16 v[188:189], v246 offset:8960\n\t" \
  "ds_read_b64_tr_b16 v[190:191], v246 offset:9984\n\t" \
  "s_waitcnt lgkmcnt(12)\n\t" \
  "v_mfma_f32_32x32x16_bf16 v[128:143], v[192:195], v[100:103], v[128:143]\n\t" \
  "ds_read_b64_tr_b16 v[192:193], v246 offset:12288\n\t" \
  "ds_read_b64_tr_b16 v[194:195], v246 offset:13312\n\t" \
  "v_mfma_f32_32x32x16_bf16 v[208:223], v[196:199], v[100:103], v[208:223]\n\t" \
  "ds_read_b64_tr_b16 v[196:197], v246 offset:12544\n\t" \
  "ds_read_b64_tr_b16 v[198:199], v246 offset:13568\n\t" \
  "global_store_dwordx4 %[vst], v[124:127], s[72:73] offset:1280 sc1\n\t" \
  "s_waitcnt lgkmcnt(12)\n\t" \
  "v_mfma_f32_32x32x16_bf16 v[224:239], v[200:203], v[100:103], v[224:239]\n\t" \
  "ds_read_b64_tr_b16 v[200:201], v246 offset:12800\n\t" \
  "ds_read_b64_tr_b16 v[202:203], v246 offset:13824\n\t" \
  "v_mfma_f32_32x32x16_bf16 v[160:175], v[204:207], v[100:103], v[160:175]\n\t" \
  "ds_read_b64_tr_b16 v[204:205], v246 offset:13056\n\t" \
  "ds_read_b64_tr_b16 v[206:207], v246 offset:14080\n\t" \
  "global_load_dwordx4 v[88:91], %[lin], s[74:75] offset:2048 sc1\n\t" \
  "s_waitcnt lgkmcnt(12)\n\t" \
  "v_mfma_f32_32x32x16_bf16 v[128:143], v[176:179], v[104:107], v[128:143]\n\t" \
  "ds_read_b64_tr_b16 v[176:177], %[wrd] offset:16384\n\t" \
  "ds_read_b64_tr_b16 v[178:179], %[wrd] offset:17408\n\t" \
  "v_mfma_f32_32x32x16_bf16 v[208:223], v[180:183], v[104:107], v[208:223]\n\t" \
  "ds_read_b64_tr_b16 v[180:181], %[wrd] offset:16640\n\t" \
  "ds_read_b64_tr_b16 v[182:183], %[wrd] offset:17664\n\t" \
  "s_waitcnt lgkmcnt(12)\n\t" \
  "v_mfma_f32_32x32x16_bf16 v[224:239], v[184:187], v[104:107], v[224:239]\n\t" \
  "ds_read_b64_tr_b16 v[184:185], %[wrd] offset:16896\n\t" \
  "ds_read_b64_tr_b16 v[186:187], %[wrd] offset:17920\n\t" \
  "v_mfma_f32_32x32x16_bf16 v[160:175], v[188:191], v[104:107], v[160:175]\n\t" \
  "ds_read_b64_tr_b16 v[188:189], %[wrd] offset:17152\n\t" \
  "ds_read_b64_tr_b16 v[190:191], %[wrd] offset:18176\n\t" \
  "s_waitcnt lgkmcnt(12)\n\t" \
  "v_mfma_f32_32x32x16_bf16 v[128:143], v[192:195], v[108:111], v[128:143]\n\t" \
  "ds_read_b64_tr_b16 v[192:193], %[wrd] offset:20480\n\t" \
  "ds_read_b64_tr_b16 v[194:195], %[wrd] offset:21504\n\t" \
  "global_load_dwordx4 v[92:95], %[lin], s[74:75] offset:3072 sc1\n\t" \
  "v_mfma_f32_32x32x16_bf16 v[208:223], v[196:199], v[108:111], v[208:223]\n\t" \
  "ds_read_b64_tr_b16 v[196:197], %[wrd] offset:20736\n\t" \
  "ds_read_b64_tr_b16 v[198:199], %[wrd] offset:21760\n\t" \
  "s_add_u32 s72, s72, 8192\n\t" \
  "s_addc_u32 s73, s73, 0\n\t" \
  "global_store_dwordx4 %[vst], v[144:147], s[72:73] offset:0 sc1\n\t" \
  "s_waitcnt lgkmcnt(12)\n\t" \
  "v_mfma_f32_32x32x16_bf16 v[224:239], v[200:203], v[108:111], v[224:239]\n\t" \
  "ds_read_b64_tr_b16 v[200:201], %[wrd] offset:20992\n\t" \
  "ds_read_b64_tr_b16 v[202:203], %[wrd] offset:22016\n\t" \
  "v_mfma_f32_32x32x16_bf16 v[160:175], v[204:207], v[108:111], v[160:175]\n\t" \
  "ds_read_b64_tr_b16 v[204:205], %[wrd] offset:21248\n\t" \
  "ds_read_b64_tr_b16 v[206:207], %[wrd] offset:22272\n\t" \
  "s_waitcnt vmcnt(18)\n\t" \
  "s_barrier\n\t" \
  "s_waitcnt lgkmcnt(12)\n\t" \
  "v_mfma_f32_32x32x16_bf16 v[128:143], v[176:179], v[48:51], v[128:143]\n\t" \
  "ds_read_b64_tr_b16 v[176:177], %[wrd] offset:24576\n\t" \
  "ds_read_b64_tr_b16 v[178:179], %[wrd] offset:25600\n\t" \
  "v_mfma_f32_32x32x16_bf16 v[208:223], v[180:183], v[48:51], v[208:223]\n\t" \
  "ds_read_b64_tr_b16 v[180:181], %[wrd] offset:24832\n\t" \
  "ds_read_b64_tr_b16 v[182:183], %[wrd] offset:25856\n\t" \
  "s_add_u32 m0, %[dw], 147456\n\t" \
  "s_add_u32 s76, s76, 524288\n\t" \
  "s_addc_u32 s77, s77, 0\n\t" \
  "global_load_lds_dwordx4 %[wv0], s[76:77] offset:0\n\t" \
  "global_load_lds_dwordx4 v252, s[76:77] offset:1024\n\t" \
  "s_waitcnt lgkmcnt(12)\n\t" \
  "v_mfma_f32_32x32x16_bf16 v[224:239], v[184:187], v[48:51], v[224:239]\n\t" \
  "ds_read_b64_tr_b16 v[184:185], %[wrd] offset:25088\n\t" \
  "ds_read_b64_tr_b16 v[186:187], %[wrd] offset:26112\n\t" \
  "s_add_u32 s74, s74, 4096\n\t" \
  "s_addc_u32 s75, s75, 0\n\t" \
  "global_load_dwordx4 v[96:99], %[lin], s[74:75] offset:0 sc1\n\t" \
  "v_mfma_f32_32x32x16_bf16 v[160:175], v[188:191], v[48:51], v[160:175]\n\t" \
  "ds_read_b64_tr_b16 v[188:189], %[wrd] offset:25344\n\t" \
  "ds_read_b64_tr_b16 v[190:191], %[wrd] offset:26368\n\t" \
  "s_waitcnt lgkmcnt(12)\n\t" \
  "v_mfma_f32_32x32x16_bf16 v[128:143], v[192:195], v[52:55], v[128:143]\n\t" \
  "ds_read_b64_tr_b16 v[192:193], %[wrd] offset:28672\n\t" \
  "ds_read_b64_tr_b16 v[194:195], %[wrd] offset:29696\n\t" \
  "v_mfma_f32_32x32x16_bf16 v[208:223], v[196:199], v[52:55], v[208:223]\n\t" \
  "ds_read_b64_tr_b16 v[196:197], %[wrd] offset:28928\n\t" \
  "ds_read_b64_tr_b16 v[198:199], %[wrd] offset:29952\n\t" \
  "s_waitcnt lgkmcnt(12)\n\t" \
  "v_mfma_f32_32x32x16_bf16 v[224:239], v[200:203], v[52:55], v[224:239]\n\t" \
  "ds_read_b64_tr_b16 v[200:201], %[wrd] offset:29184\n\t" \
  "ds_read_b64_tr_b16 v[202:203], %[wrd] offset:30208\n\t" \
  "v_mfma_f32_32x32x16_bf16 v[160:175], v[204:207], v[52:55], v[160:175]\n\t" \
  "ds_read_b64_tr_b16 v[204:205], %[wrd] offset:29440\n\t" \
  "ds_read_b64_tr_b16 v[206:207], %[wrd] offset:30464\n\t" \
  "global_load_dwordx4 v[100:103], %[lin], s[74:75] offset:1024 sc1\n\t" \
  "s_waitcnt lgkmcnt(12)\n\t" \
  "v_mfma_f32_32x32x16_bf16 v[128:143], v[176:179], v[56:59], v[128:143]\n\t" \
  "ds_read_b64_tr_b16 v[176:177], %[wrd] offset:32768\n\t" \
  "ds_read_b64_tr_b16 v[178:179], %[wrd] offset:33792\n\t" \
  "v_mfma_f32_32x32x16_bf16 v[208:223], v[180:183], v[56:59], v[208:223]\n\t" \
  "ds_read_b64_tr_b16 v[180:181], %[wrd] offset:33024\n\t" \
  "ds_read_b64_tr_b16 v[182:183], %[wrd] offset:34048\n\t" \
  "global_store_dwordx4 %[vst], v[148:151], s[72:73] offset:256 sc1\n\t" \
  "s_waitcnt lgkmcnt(12)\n\t" \
  "v_mfma_f32_32x32x16_bf16 v[224:239], v[184:187], v[56:59], v[224:239]\n\t" \
  "ds_read_b64_tr_b16 v[184:185], %[wrd] offset:33280\n\t" \
  "ds_read_b64_tr_b16 v[186:187], %[wrd] offset:34304\n\t" \
  "v_mfma_f32_32x32x16_bf16 v[160:175], v[188:191], v[56:59], v[160:175]\n\t" \
  "ds_read_b64_tr_b16 v[188:189], %[wrd] offset:33536\n\t" \
  "ds_read_b64_tr_b16 v[190:191], %[wrd] offset:34560\n\t" \
  "s_waitcnt vmcnt(22) lgkmcnt(12)\n\t" \
  "v_mfma_f32_32x32x16_bf16 v[128:143], v[192:195], v[60:63], v[128:143]\n\t" \
  "ds_read_b64_tr_b16 v[192:193], %[wrd] offset:36864\n\t" \
  "ds_read_b64_tr_b16 v[194:195], %[wrd] offset:37888\n\t" \
  "global_load_dwordx4 v[104:107], %[lin], s[74:75] offset:2048 sc1\n\t" \
  "v_mfma_f32_32x32x16_bf16 v[208:223], v[196:199], v[60:63], v[208:223]\n\t" \
  "ds_read_b64_tr_b16 v[196:197], %[wrd] offset:37120\n\t" \
  "ds_read_b64_tr_b16 v[198:199], %[wrd] offset:38144\n\t" \
  "s_waitcnt lgkmcnt(12)\n\t" \
  "v_mfma_f32_32x32x16_bf16 v[224:239], v[200:203], v[60:63], v[224:239]\n\t" \
  "ds_read_b64_tr_b16 v[200:201], %[wrd] offset:37376\n\t" \
  "ds_read_b64_tr_b16 v[202:203], %[wrd] offset:38400\n\t" \
  "v_mfma_f32_32x32x16_bf16 v[160:175], v[204:207], v[60:63], v[160:175]\n\t" \
  "ds_read_b64_tr_b16 v[204:205], %[wrd] offset:37632\n\t" \
  "ds_read_b64_tr_b16 v[206:207], %[wrd] offset:38656\n\t" \
  "s_waitcnt vmcnt(18)\n\t" \
  "s_barrier\n\t" \
  "s_waitcnt lgkmcnt(12)\n\t" \
  "v_mfma_f32_32x32x16_bf16 v[128:143], v[176:179], v[64:67], v[128:143]\n\t" \
  "ds_read_b64_tr_b16 v[176:177], %[wrd] offset:40960\n\t" \
  "ds_read_b64_tr_b16 v[178:179], %[wrd] offset:41984\n\t" \
  "v_mfma_f32_32x32x16_bf16 v[208:223], v[180:183], v[64:67], v[208:223]\n\t" \
  "ds_read_b64_tr_b16 v[180:181], %[wrd] offset:41216\n\t" \
  "ds_read_b64_tr_b16 v[182:183], %[wrd] offset:42240\n\t" \
  "global_load_dwordx4 v[108:111], %[lin], s[74:75] offset:3072 sc1\n\t" \
  "s_add_u32 m0, %[dw], 49152\n\t" \
  "s_add_u32 s76, s76, 524288\n\t" \
  "s_addc_u32 s77, s77, 0\n\t" \
  "global_load_lds_dwordx4 %[wv0], s[76:77] offset:0\n\t" \
  "global_load_lds_dwordx4 v252, s[76:77] offset:1024\n\t" \
  "s_waitcnt lgkmcnt(12)\n\t" \
  "v_mfma_f32_32x32x16_bf16 v[224:239], v[184:187], v[64:67], v[224:239]\n\t" \
  "ds_read_b64_tr_b16 v[184:185], %[wrd] offset:41472\n\t" \
  "ds_read_b64_tr_b16 v[186:187], %[wrd] offset:42496\n\t" \
  "v_mfma_f32_32x32x16_bf16 v[160:175], v[188:191], v[64:67], v[160:175]\n\t" \
  "ds_read_b64_tr_b16 v[188:189], %[wrd] offset:41728\n\t" \
  "ds_read_b64_tr_b16 v[190:191], %[wrd] offset:42752\n\t" \
  "s_waitcnt lgkmcnt(12)\n\t" \
  "v_mfma_f32_32x32x16_bf16 v[128:143], v[192:195], v[68:71], v[128:143]\n\t" \
  "ds_read_b64_tr_b16 v[192:193], %[wrd] offset:45056\n\t" \
  "ds_read_b64_tr_b16 v[194:195], %[wrd] offset:46080\n\t" \
  "v_mfma_f32_32x32x16_bf16 v[208:223], v[196:199], v[68:71], v[208:223]\n\t" \
  "ds_read_b64_tr_b16 v[196:197], %[wrd] offset:45312\n\t" \
  "ds_read_b64_tr_b16 v[198:199], %[wrd] offset:46336\n\t" \
  "global_store_dwordx4 %[vst], v[152:155], s[72:73] offset:1024 sc1\n\t" \
  "s_waitcnt lgkmcnt(12)\n\t" \
  "v_mfma_f32_32x32x16_bf16 v[224:239], v[200:203], v[68:71], v[224:239]\n\t" \
  "ds_read_b64_tr_b16 v[200:201], %[wrd] offset:45568\n\t" \
  "ds_read_b64_tr_b16 v[202:203], %[wrd] offset:46592\n\t" \
  "v_mfma_f32_32x32x16_bf16 v[160:175], v[204:207], v[68:71], v[160:175]\n\t" \
  "ds_read_b64_tr_b16 v[204:205], %[wrd] offset:45824\n\t" \
  "ds_read_b64_tr_b16 v[206:207], %[wrd] offset:46848\n\t" \
  "s_add_u32 s74, s74, 4096\n\t" \
  "s_addc_u32 s75, s75, 0\n\t" \
  "global_load_dwordx4 v[48:51], %[lin], s[74:75] offset:0 sc1\n\t" \
  "s_waitcnt vmcnt(22) lgkmcnt(12)\n\t" \
  "v_mfma_f32_32x32x16_bf16 v[128:143], v[176:179], v[72:75], v[128:143]\n\t" \
  "ds_read_b64_tr_b16 v[176:177], v253 offset:16384\n\t" \
  "ds_read_b64_tr_b16 v[178:179], v253 offset:17408\n\t" \
  "v_mfma_f32_32x32x16_bf16 v[208:223], v[180:183], v[72:75], v[208:223]\n\t" \
  "ds_read_b64_tr_b16 v[180:181], v253 offset:16640\n\t" \
  "ds_read_b64_tr_b16 v[182:183], v253 offset:17664\n\t" \
  "s_waitcnt lgkmcnt(12)\n\t" \
  "v_mfma_f32_32x32x16_bf16 v[224:239], v[184:187], v[72:75], v[224:239]\n\t" \
  "ds_read_b64_tr_b16 v[184:185], v253 offset:16896\n\t" \
  "ds_read_b64_tr_b16 v[186:187], v253 offset:17920\n\t" \
  "v_mfma_f32_32x32x16_bf16 v[160:175], v[188:191], v[72:75], v[160:175]\n\t" \
  "ds_read_b64_tr_b16 v[188:189], v253 offset:17152\n\t" \
  "ds_read_b64_tr_b16 v[190:191], v253 offset:18176\n\t" \
  "s_waitcnt vmcnt(21) lgkmcnt(12)\n\t" \
  "v_mfma_f32_32x32x16_bf16 v[128:143], v[192:195], v[76:79], v[128:143]\n\t" \
  "ds_read_b64_tr_b16 v[192:193], v253 offset:20480\n\t" \
  "ds_read_b64_tr_b16 v[194:195], v253 offset:21504\n\t" \
  "global_load_dwordx4 v[52:55], %[lin], s[74:75] offset:1024 sc1\n\t" \
  "v_mfma_f32_32x32x16_bf16 v[208:223], v[196:199], v[76:79], v[208:223]\n\t" \
  "ds_read_b64_tr_b16 v[196:197], v253 offset:20736\n\t" \
  "ds_read_b64_tr_b16 v[198:199], v253 offset:21760\n\t" \
  "global_store_dwordx4 %[vst], v[156:159], s[72:73] offset:1280 sc1\n\t" \
  "s_waitcnt lgkmcnt(12)\n\t" \
  "v_mfma_f32_32x32x16_bf16 v[224:239], v[200:203], v[76:79], v[224:239]\n\t" \
  "ds_read_b64_tr_b16 v[200:201], v253 offset:20992\n\t" \
  "ds_read_b64_tr_b16 v[202:203], v253 offset:22016\n\t" \
  "v_mfma_f32_32x32x16_bf16 v[160:175], v[204:207], v[76:79], v[160:175]\n\t" \
  "ds_read_b64_tr_b16 v[204:205], v253 offset:21248\n\t" \
  "ds_read_b64_tr_b16 v[206:207], v253 offset:22272\n\t" \
  "s_waitcnt vmcnt(18)\n\t" \
  "s_barrier\n\t" \
  "s_waitcnt lgkmcnt(12)\n\t" \
  "v_mfma_f32_32x32x16_bf16 v[128:143], v[176:179], v[80:83], v[128:143]\n\t" \
  "ds_read_b64_tr_b16 v[176:177], v253 offset:24576\n\t" \
  "ds_read_b64_tr_b16 v[178:179], v253 offset:25600\n\t" \
  "v_mfma_f32_32x32x16_bf16 v[208:223], v[180:183], v[80:83], v[208:223]\n\t" \
  "ds_read_b64_tr_b16 v[180:181], v253 offset:24832\n\t" \
  "ds_read_b64_tr_b16 v[182:183], v253 offset:25856\n\t" \
  "global_load_dwordx4 v[56:59], %[lin], s[74:75] offset:2048 sc1\n\t" \
  "s_waitcnt lgkmcnt(12)\n\t" \
  "v_mfma_f32_32x32x16_bf16 v[224:239], v[184:187], v[80:83], v[224:239]\n\t" \
  "ds_read_b64_tr_b16 v[184:185], v253 offset:25088\n\t" \
  "ds_read_b64_tr_b16 v[186:187], v253 offset:26112\n\t" \
  "global_load_dword v242, v240, %[cnt] sc1\n\t" \
  "v_mfma_f32_32x32x16_bf16 v[160:175], v[188:191], v[80:83], v[160:175]\n\t" \
  "ds_read_b64_tr_b16 v[188:189], v253 offset:25344\n\t" \
  "ds_read_b64_tr_b16 v[190:191], v253 offset:26368\n\t" \
  "s_waitcnt vmcnt(19) lgkmcnt(12)\n\t" \
  "v_mfma_f32_32x32x16_bf16 v[128:143], v[192:195], v[84:87], v[128:143]\n\t" \
  "ds_read_b64_tr_b16 v[192:193], v253 offset:28672\n\t" \
  "ds_read_b64_tr_b16 v[194:195], v253 offset:29696\n\t" \
  "v_mfma_f32_32x32x16_bf16 v[208:223], v[196:199], v[84:87], v[208:223]\n\t" \
  "ds_read_b64_tr_b16 v[196:197], v253 offset:28928\n\t" \
  "ds_read_b64_tr_b16 v[198:199], v253 offset:29952\n\t" \
  "s_waitcnt lgkmcnt(12)\n\t" \
  "v_mfma_f32_32x32x16_bf16 v[224:239], v[200:203], v[84:87], v[224:239]\n\t" \
  "ds_read_b64_tr_b16 v[200:201], v253 offset:29184\n\t" \
  "ds_read_b64_tr_b16 v[202:203], v253 offset:30208\n\t" \
  "global_load_dwordx4 v[60:63], %[lin], s[74:75] offset:3072 sc1\n\t" \
  "v_mfma_f32_32x32x16_bf16 v[160:175], v[204:207], v[84:87], v[160:175]\n\t" \
  "ds_read_b64_tr_b16 v[204:205], v253 offset:29440\n\t" \
  "ds_read_b64_tr_b16 v[206:207], v253 offset:30464\n\t" \
  "s_waitcnt vmcnt(18) lgkmcnt(12)\n\t" \
  "v_mfma_f32_32x32x16_bf16 v[128:143], v[176:179], v[88:91], v[128:143]\n\t" \
  "ds_read_b64_tr_b16 v[176:177], v253 offset:32768\n\t" \
  "ds_read_b64_tr_b16 v[178:179], v253 offset:33792\n\t" \
  "v_mfma_f32_32x32x16_bf16 v[208:223], v[180:183], v[88:91], v[208:223]\n\t" \
  "ds_read_b64_tr_b16 v[180:181], v253 offset:33024\n\t" \
  "ds_read_b64_tr_b16 v[182:183], v253 offset:34048\n\t" \
  "s_waitcnt lgkmcnt(12)\n\t" \
  "v_mfma_f32_32x32x16_bf16 v[224:239], v[184:187], v[88:91], v[224:239]\n\t" \
  "ds_read_b64_tr_b16 v[184:185], v253 offset:33280\n\t" \
  "ds_read_b64_tr_b16 v[186:187], v253 offset:34304\n\t" \
  "v_mfma_f32_32x32x16_bf16 v[160:175], v[188:191], v[88:91], v[160:175]\n\t" \
  "ds_read_b64_tr_b16 v[188:189], v253 offset:33536\n\t" \
  "ds_read_b64_tr_b16 v[190:191], v253 offset:34560\n\t" \
  "s_waitcnt vmcnt(17) lgkmcnt(12)\n\t" \
  "v_mfma_f32_32x32x16_bf16 v[128:143], v[192:195], v[92:95], v[128:143]\n\t" \
  "ds_read_b64_tr_b16 v[192:193], v253 offset:36864\n\t" \
  "ds_read_b64_tr_b16 v[194:195], v253 offset:37888\n\t" \
  "s_add_u32 s74, s74, 4096\n\t" \
  "s_addc_u32 s75, s75, 0\n\t" \
  "global_load_dwordx4 v[64:67], %[lin], s[74:75] offset:0 sc1\n\t" \
  "v_mfma_f32_32x32x16_bf16 v[208:223], v[196:199], v[92:95], v[208:223]\n\t" \
  "ds_read_b64_tr_b16 v[196:197], v253 offset:37120\n\t" \
  "ds_read_b64_tr_b16 v[198:199], v253 offset:38144\n\t" \
  "s_waitcnt lgkmcnt(12)\n\t" \
  "v_mfma_f32_32x32x16_bf16 v[224:239], v[200:203], v[92:95], v[224:239]\n\t" \
  "ds_read_b64_tr_b16 v[200:201], v253 offset:37376\n\t" \
  "ds_read_b64_tr_b16 v[202:203], v253 offset:38400\n\t" \
  "v_mfma_f32_32x32x16_bf16 v[160:175], v[204:207], v[92:95], v[160:175]\n\t" \
  "ds_read_b64_tr_b16 v[204:205], v253 offset:37632\n\t" \
  "ds_read_b64_tr_b16 v[206:207], v253 offset:38656\n\t" \
  "s_waitcnt vmcnt(15)\n\t" \
  "s_barrier\n\t" \
  "s_waitcnt vmcnt(14) lgkmcnt(12)\n\t" \
  "v_mfma_f32_32x32x16_bf16 v[128:143], v[176:179], v[96:99], v[128:143]\n\t" \
  "ds_read_b64_tr_b16 v[176:177], v253 offset:40960\n\t" \
  "ds_read_b64_tr_b16 v[178:179], v253 offset:41984\n\t" \
  "v_mfma_f32_32x32x16_bf16 v[208:223], v[180:183], v[96:99], v[208:223]\n\t" \
  "ds_read_b64_tr_b16 v[180:181], v253 offset:41216\n\t" \
  "ds_read_b64_tr_b16 v[182:183], v253 offset:42240\n\t" \
  "global_load_dwordx4 v[68:71], %[lin], s[74:75] offset:1024 sc1\n\t" \
  "s_waitcnt lgkmcnt(12)\n\t" \
  "v_mfma_f32_32x32x16_bf16 v[224:239], v[184:187], v[96:99], v[224:239]\n\t" \
  "ds_read_b64_tr_b16 v[184:185], v253 offset:41472\n\t" \
  "ds_read_b64_tr_b16 v[186:187], v253 offset:42496\n\t" \
  "v_mfma_f32_32x32x16_bf16 v[160:175], v[188:191], v[96:99], v[160:175]\n\t" \
  "ds_read_b64_tr_b16 v[188:189], v253 offset:41728\n\t" \
  "ds_read_b64_tr_b16 v[190:191], v253 offset:42752\n\t" \
  "s_waitcnt vmcnt(14) lgkmcnt(12)\n\t" \
  "v_mfma_f32_32x32x16_bf16 v[128:143], v[192:195], v[100:103], v[128:143]\n\t" \
  "ds_read_b64_tr_b16 v[192:193], v253 offset:45056\n\t" \
  "ds_read_b64_tr_b16 v[194:195], v253 offset:46080\n\t" \
  "v_mfma_f32_32x32x16_bf16 v[208:223], v[196:199], v[100:103], v[208:223]\n\t" \
  "ds_read_b64_tr_b16 v[196:197], v253 offset:45312\n\t" \
  "ds_read_b64_tr_b16 v[198:199], v253 offset:46336\n\t" \
  "s_waitcnt lgkmcnt(12)\n\t" \
  "v_mfma_f32_32x32x16_bf16 v[224:239], v[200:203], v[100:103], v[224:239]\n\t" \
  "ds_read_b64_tr_b16 v[200:201], v253 offset:45568\n\t" \
  "ds_read_b64_tr_b16 v[202:203], v253 offset:46592\n\t" \
  "global_load_dwordx4 v[72:75], %[lin], s[74:75] offset:2048 sc1\n\t" \
  "v_mfma_f32_32x32x16_bf16 v[160:175], v[204:207], v[100:103], v[160:175]\n\t" \
  "ds_read_b64_tr_b16 v[204:205], v253 offset:45824\n\t" \
  "ds_read_b64_tr_b16 v[206:207], v253 offset:46848\n\t" \
  "s_waitcnt vmcnt(13) lgkmcnt(12)\n\t" \
  "v_mfma_f32_32x32x16_bf16 v[128:143], v[176:179], v[104:107], v[128:143]\n\t" \
  "ds_read_b64_tr_b16 v[176:177], v246 offset:16384\n\t" \
  "ds_read_b64_tr_b16 v[178:179], v246 offset:17408\n\t" \
  "s_waitcnt vmcnt(6)\n\t" \
  "s_mov_b64 exec, 1\n\t" \
  "global_atomic_add v240, v241, %[cnv]\n\t" \
  "s_mov_b64 exec, -1\n\t" \
  "v_mfma_f32_32x32x16_bf16 v[208:223], v[180:183], v[104:107], v[208:223]\n\t" \
  "ds_read_b64_tr_b16 v[180:181], v246 offset:16640\n\t" \
  "ds_read_b64_tr_b16 v[182:183], v246 offset:17664\n\t" \
  "s_waitcnt lgkmcnt(12)\n\t" \
  "v_mfma_f32_32x32x16_bf16 v[224:239], v[184:187], v[104:107], v[224:239]\n\t" \
  "ds_read_b64_tr_b16 v[184:185], v246 offset:16896\n\t" \
  "ds_read_b64_tr_b16 v[186:187], v246 offset:17920\n\t" \
  "v_mfma_f32_32x32x16_bf16 v[160:175], v[188:191], v[104:107], v[160:175]\n\t" \
  "ds_read_b64_tr_b16 v[188:189], v246 offset:17152\n\t" \
  "ds_read_b64_tr_b16 v[190:191], v246 offset:18176\n\t" \
  "global_load_dwordx4 v[76:79], %[lin], s[74:75] offset:3072 sc1\n\t" \
  "s_waitcnt lgkmcnt(12)\n\t" \
  "v_mfma_f32_32x32x16_bf16 v[128:143], v[192:195], v[108:111], v[128:143]\n\t" \
  "ds_read_b64_tr_b16 v[192:193], v246 offset:20480\n\t" \
  "ds_read_b64_tr_b16 v[194:195], v246 offset:21504\n\t" \
  "v_mfma_f32_32x32x16_bf16 v[208:223], v[196:199], v[108:111], v[208:223]\n\t" \
  "ds_read_b64_tr_b16 v[196:197], v246 offset:20736\n\t" \
  "ds_read_b64_tr_b16 v[198:199], v246 offset:21760\n\t" \
  "s_waitcnt lgkmcnt(12)\n\t" \
  "v_mfma_f32_32x32x16_bf16 v[224:239], v[200:203], v[108:111], v[224:239]\n\t" \
  "ds_read_b64_tr_b16 v[200:201], v246 offset:20992\n\t" \
  "ds_read_b64_tr_b16 v[202:203], v246 offset:22016\n\t" \
  "v_mfma_f32_32x32x16_bf16 v[160:175], v[204:207], v[108:111], v[160:175]\n\t" \
  "ds_read_b64_tr_b16 v[204:205], v246 offset:21248\n\t" \
  "ds_read_b64_tr_b16 v[206:207], v246 offset:22272\n\t" \
  "s_barrier\n\t" \
  "s_waitcnt lgkmcnt(12)\n\t" \
  "v_mfma_f32_32x32x16_bf16 v[128:143], v[176:179], v[48:51], v[128:143]\n\t" \
  "ds_read_b64_tr_b16 v[176:177], v246 offset:24576\n\t" \
  "ds_read_b64_tr_b16 v[178:179], v246 offset:25600\n\t" \
  "v_mfma_f32_32x32x16_bf16 v[208:223], v[180:183], v[48:51], v[208:223]\n\t" \
  "ds_read_b64_tr_b16 v[180:181], v246 offset:24832\n\t" \
  "ds_read_b64_tr_b16 v[182:183], v246 offset:25856\n\t" \
  "s_waitcnt lgkmcnt(12)\n\t" \
  "v_mfma_f32_32x32x16_bf16 v[224:239], v[184:187], v[48:51], v[224:239]\n\t" \
  "ds_read_b64_tr_b16 v[184:185], v246 offset:25088\n\t" \
  "ds_read_b64_tr_b16 v[186:187], v246 offset:26112\n\t" \
  "s_waitcnt vmcnt(6)\n\t" \
  "v_readfirstlane_b32 s81, v242\n\t" \
  "s_mov_b32 s80, 0\n\t" \
  "s_cmp_ge_u32 s81, 64\n\t" \
  "s_cbranch_scc1 L_OKK%=\n\t" \
  "L_SPINK%=:\n\t" \
  "s_sleep 2\n\t" \
  "global_load_dword v242, v240, %[cnt] sc1\n\t" \
  "s_waitcnt vmcnt(0)\n\t" \
  "v_readfirstlane_b32 s81, v242\n\t" \
  "s_add_u32 s80, s80, 1\n\t" \
  "s_cmp_ge_u32 s81, 64\n\t" \
  "s_cbranch_scc1 L_OKK%=\n\t" \
  "s_cmp_lt_u32 s80, 0x20000\n\t" \
  "s_cbranch_scc1 L_SPINK%=\n\t" \
  "L_OKK%=:\n\t" \
  "s_add_u32 m0, %[gbase], 0\n\t" \
  "s_nop 0\n\t" \
  "global_load_lds_dwordx4 %[lin], %[kh] offset:0 sc1\n\t" \
  "global_load_lds_dwordx4 %[lin], %[kh] offset:1024 sc1\n\t" \
  "global_load_lds_dwordx4 %[lin], %[kh] offset:2048 sc1\n\t" \
  "global_load_lds_dwordx4 %[lin], %[kh] offset:3072 sc1\n\t" \
  "v_mfma_f32_32x32x16_bf16 v[160:175], v[188:191], v[48:51], v[160:175]\n\t" \
  "ds_read_b64_tr_b16 v[188:189], v246 offset:25344\n\t" \
  "ds_read_b64_tr_b16 v[190:191], v246 offset:26368\n\t" \
  "s_waitcnt lgkmcnt(12)\n\t" \
  "v_mfma_f32_32x32x16_bf16 v[128:143], v[192:195], v[52:55], v[128:143]\n\t" \
  "ds_read_b64_tr_b16 v[192:193], v246 offset:28672\n\t" \
  "ds_read_b64_tr_b16 v[194:195], v246 offset:29696\n\t" \
  "v_mfma_f32_32x32x16_bf16 v[208:223], v[196:199], v[52:55], v[208:223]\n\t" \
  "ds_read_b64_tr_b16 v[196:197], v246 offset:28928\n\t" \
  "ds_read_b64_tr_b16 v[198:199], v246 offset:29952\n\t" \
  "s_waitcnt lgkmcnt(12)\n\t" \
  "v_mfma_f32_32x32x16_bf16 v[224:239], v[200:203], v[52:55], v[224:239]\n\t" \
  "ds_read_b64_tr_b16 v[200:201], v246 offset:29184\n\t" \
  "ds_read_b64_tr_b16 v[202:203], v246 offset:30208\n\t" \
  "v_mfma_f32_32x32x16_bf16 v[160:175], v[204:207], v[52:55], v[160:175]\n\t" \
  "ds_read_b64_tr_b16 v[204:205], v246 offset:29440\n\t" \
  "ds_read_b64_tr_b16 v[206:207], v246 offset:30464\n\t" \
  "s_waitcnt lgkmcnt(12)\n\t" \
  "v_mfma_f32_32x32x16_bf16 v[128:143], v[176:179], v[56:59], v[128:143]\n\t" \
  "ds_read_b64_tr_b16 v[176:177], %[wrd] offset:49152\n\t" \
  "ds_read_b64_tr_b16 v[178:179], %[wrd] offset:50176\n\t" \
  "v_mfma_f32_32x32x16_bf16 v[208:223], v[180:183], v[56:59], v[208:223]\n\t" \
  "ds_read_b64_tr_b16 v[180:181], %[wrd] offset:49408\n\t" \
  "ds_read_b64_tr_b16 v[182:183], %[wrd] offset:50432\n\t" \
  "s_waitcnt lgkmcnt(12)\n\t" \
  "v_mfma_f32_32x32x16_bf16 v[224:239], v[184:187], v[56:59], v[224:239]\n\t" \
  "ds_read_b64_tr_b16 v[184:185], %[wrd] offset:49664\n\t" \
  "ds_read_b64_tr_b16 v[186:187], %[wrd] offset:50688\n\t" \
  "v_mfma_f32_32x32x16_bf16 v[160:175], v[188:191], v[56:59], v[160:175]\n\t" \
  "ds_read_b64_tr_b16 v[188:189], %[wrd] offset:49920\n\t" \
  "ds_read_b64_tr_b16 v[190:191], %[wrd] offset:50944\n\t" \
  "s_waitcnt vmcnt(9) lgkmcnt(12)\n\t" \
  "v_mfma_f32_32x32x16_bf16 v[128:143], v[192:195], v[60:63], v[128:143]\n\t" \
  "ds_read_b64_tr_b16 v[192:193], %[wrd] offset:53248\n\t" \
  "ds_read_b64_tr_b16 v[194:195], %[wrd] offset:54272\n\t" \
  "v_mfma_f32_32x32x16_bf16 v[208:223], v[196:199], v[60:63], v[208:223]\n\t" \
  "ds_read_b64_tr_b16 v[196:197], %[wrd] offset:53504\n\t" \
  "ds_read_b64_tr_b16 v[198:199], %[wrd] offset:54528\n\t" \
  "s_waitcnt lgkmcnt(12)\n\t" \
  "v_mfma_f32_32x32x16_bf16 v[224:239], v[200:203], v[60:63], v[224:239]\n\t" \
  "ds_read_b64_tr_b16 v[200:201], %[wrd] offset:53760\n\t" \
  "ds_read_b64_tr_b16 v[202:203], %[wrd] offset:54784\n\t" \
  "v_mfma_f32_32x32x16_bf16 v[160:175], v[204:207], v[60:63], v[160:175]\n\t" \
  "ds_read_b64_tr_b16 v[204:205], %[wrd] offset:54016\n\t" \
  "ds_read_b64_tr_b16 v[206:207], %[wrd] offset:55040\n\t" \
  "s_waitcnt vmcnt(8) lgkmcnt(12)\n\t" \
  "v_mfma_f32_32x32x16_bf16 v[128:143], v[176:179], v[64:67], v[128:143]\n\t" \
  "ds_read_b64_tr_b16 v[176:177], %[wrd] offset:57344\n\t" \
  "ds_read_b64_tr_b16 v[178:179], %[wrd] offset:58368\n\t" \
  "v_mfma_f32_32x32x16_bf16 v[208:223], v[180:183], v[64:67], v[208:223]\n\t" \
  "ds_read_b64_tr_b16 v[180:181], %[wrd] offset:57600\n\t" \
  "ds_read_b64_tr_b16 v[182:183], %[wrd] offset:58624\n\t" \
  "s_waitcnt lgkmcnt(12)\n\t" \
  "v_mfma_f32_32x32x16_bf16 v[224:239], v[184:187], v[64:67], v[224:239]\n\t" \
  "ds_read_b64_tr_b16 v[184:185], %[wrd] offset:57856\n\t" \
  "ds_read_b64_tr_b16 v[186:187], %[wrd] offset:58880\n\t" \
  "v_mfma_f32_32x32x16_bf16 v[160:175], v[188:191], v[64:67], v[160:175]\n\t" \
  "ds_read_b64_tr_b16 v[188:189], %[wrd] offset:58112\n\t" \
  "ds_read_b64_tr_b16 v[190:191], %[wrd] offset:59136\n\t" \
  "s_waitcnt vmcnt(7) lgkmcnt(12)\n\t" \
  "v_mfma_f32_32x32x16_bf16 v[128:143], v[192:195], v[68:71], v[128:143]\n\t" \
  "ds_read_b64_tr_b16 v[192:193], %[wrd] offset:61440\n\t" \
  "ds_read_b64_tr_b16 v[194:195], %[wrd] offset:62464\n\t" \
  "v_mfma_f32_32x32x16_bf16 v[208:223], v[196:199], v[68:71], v[208:223]\n\t" \
  "ds_read_b64_tr_b16 v[196:197], %[wrd] offset:61696\n\t" \
  "ds_read_b64_tr_b16 v[198:199], %[wrd] offset:62720\n\t" \
  "s_waitcnt lgkmcnt(12)\n\t" \
  "v_mfma_f32_32x32x16_bf16 v[224:239], v[200:203], v[68:71], v[224:239]\n\t" \
  "ds_read_b64_tr_b16 v[200:201], %[wrd] offset:61952\n\t" \
  "ds_read_b64_tr_b16 v[202:203], %[wrd] offset:62976\n\t" \
  "v_mfma_f32_32x32x16_bf16 v[160:175], v[204:207], v[68:71], v[160:175]\n\t" \
  "ds_read_b64_tr_b16 v[204:205], %[wrd] offset:62208\n\t" \
  "ds_read_b64_tr_b16 v[206:207], %[wrd] offset:63232\n\t" \
  "s_waitcnt vmcnt(6) lgkmcnt(12)\n\t" \
  "v_mfma_f32_32x32x16_bf16 v[128:143], v[176:179], v[72:75], v[128:143]\n\t" \
  "v_mfma_f32_32x32x16_bf16 v[208:223], v[180:183], v[72:75], v[208:223]\n\t" \
  "s_waitcnt lgkmcnt(8)\n\t" \
  "v_mfma_f32_32x32x16_bf16 v[224:239], v[184:187], v[72:75], v[224:239]\n\t" \
  "v_mfma_f32_32x32x16_bf16 v[160:175], v[188:191], v[72:75], v[160:175]\n\t" \
  "s_waitcnt vmcnt(4) lgkmcnt(4)\n\t" \
  "v_mfma_f32_32x32x16_bf16 v[128:143], v[192:195], v[76:79], v[128:143]\n\t" \
  "v_mfma_f32_32x32x16_bf16 v[208:223], v[196:199], v[76:79], v[208:223]\n\t" \
  "s_waitcnt lgkmcnt(0)\n\t" \
  "v_mfma_f32_32x32x16_bf16 v[224:239], v[200:203], v[76:79], v[224:239]\n\t" \
  "global_load_dword v242, v240, %[cnv] sc1\n\t" \
  "v_mfma_f32_32x32x16_bf16 v[160:175], v[204:207], v[76:79], v[160:175]\n\t" \
  "s_nop 11\n\t" \
  "v_readlane_b32 s68, %[prm], 34\n\t" \
  "s_nop 1\n\t" \
  "v_mul_f32_e32 v128, s68, v128\n\t" \
  "v_mul_f32_e32 v129, s68, v129\n\t" \
  "v_mul_f32_e32 v130, s68, v130\n\t" \
  "v_mul_f32_e32 v131, s68, v131\n\t" \
  "v_mul_f32_e32 v132, s68, v132\n\t" \
  "v_mul_f32_e32 v133, s68, v133\n\t" \
  "v_mul_f32_e32 v134, s68, v134\n\t" \
  "v_mul_f32_e32 v135, s68, v135\n\t" \
  "v_mul_f32_e32 v136, s68, v136\n\t" \
  "v_mul_f32_e32 v137, s68, v137\n\t" \
  "v_mul_f32_e32 v138, s68, v138\n\t" \
  "v_mul_f32_e32 v139, s68, v139\n\t" \
  "v_mul_f32_e32 v140, s68, v140\n\t" \
  "v_mul_f32_e32 v141, s68, v141\n\t" \
  "v_mul_f32_e32 v142, s68, v142\n\t" \
  "v_mul_f32_e32 v143, s68, v143\n\t" \
  "v_cvt_pk_bf16_f32 v16, v128, v129\n\t" \
  "v_cvt_pk_bf16_f32 v17, v130, v131\n\t" \
  "v_cvt_pk_bf16_f32 v18, v132, v133\n\t" \
  "v_cvt_pk_bf16_f32 v19, v134, v135\n\t" \
  "v_cvt_pk_bf16_f32 v20, v136, v137\n\t" \
  "v_cvt_pk_bf16_f32 v21, v138, v139\n\t" \
  "v_cvt_pk_bf16_f32 v22, v140, v141\n\t" \
  "v_cvt_pk_bf16_f32 v23, v142, v143\n\t" \
  "v_mul_f32_e32 v208, s68, v208\n\t" \
  "v_mul_f32_e32 v209, s68, v209\n\t" \
  "v_mul_f32_e32 v210, s68, v210\n\t" \
  "v_mul_f32_e32 v211, s68, v211\n\t" \
  "v_mul_f32_e32 v212, s68, v212\n\t" \
  "v_mul_f32_e32 v213, s68, v213\n\t" \
  "v_mul_f32_e32 v214, s68, v214\n\t" \
  "v_mul_f32_e32 v215, s68, v215\n\t" \
  "v_mul_f32_e32 v216, s68, v216\n\t" \
  "v_mul_f32_e32 v217, s68, v217\n\t" \
  "v_mul_f32_e32 v218, s68, v218\n\t" \
  "v_mul_f32_e32 v219, s68, v219\n\t" \
  "v_mul_f32_e32 v220, s68, v220\n\t" \
  "v_mul_f32_e32 v221, s68, v221\n\t" \
  "v_mul_f32_e32 v222, s68, v222\n\t" \
  "v_mul_f32_e32 v223, s68, v223\n\t" \
  "v_cvt_pk_bf16_f32 v24, v208, v209\n\t" \
  "v_cvt_pk_bf16_f32 v25, v210, v211\n\t" \
  "v_cvt_pk_bf16_f32 v26, v212, v213\n\t" \
  "v_cvt_pk_bf16_f32 v27, v214, v215\n\t" \
  "v_cvt_pk_bf16_f32 v28, v216, v217\n\t" \
  "v_cvt_pk_bf16_f32 v29, v218, v219\n\t" \
  "v_cvt_pk_bf16_f32 v30, v220, v221\n\t" \
  "v_cvt_pk_bf16_f32 v31, v222, v223\n\t" \
  "v_mul_f32_e32 v224, s68, v224\n\t" \
  "v_mul_f32_e32 v225, s68, v225\n\t" \
  "v_mul_f32_e32 v226, s68, v226\n\t" \
  "v_mul_f32_e32 v227, s68, v227\n\t" \
  "v_mul_f32_e32 v228, s68, v228\n\t" \
  "v_mul_f32_e32 v229, s68, v229\n\t" \
  "v_mul_f32_e32 v230, s68, v230\n\t" \
  "v_mul_f32_e32 v231, s68, v231\n\t" \
  "v_mul_f32_e32 v232, s68, v232\n\t" \
  "v_mul_f32_e32 v233, s68, v233\n\t" \
  "v_mul_f32_e32 v234, s68, v234\n\t" \
  "v_mul_f32_e32 v235, s68, v235\n\t" \
  "v_mul_f32_e32 v236, s68, v236\n\t" \
  "v_mul_f32_e32 v237, s68, v237\n\t" \
  "v_mul_f32_e32 v238, s68, v238\n\t" \
  "v_mul_f32_e32 v239, s68, v239\n\t" \
  "v_cvt_pk_bf16_f32 v32, v224, v225\n\t" \
  "v_cvt_pk_bf16_f32 v33, v226, v227\n\t" \
  "v_cvt_pk_bf16_f32 v34, v228, v229\n\t" \
  "v_cvt_pk_bf16_f32 v35, v230, v231\n\t" \
  "v_cvt_pk_bf16_f32 v36, v232, v233\n\t" \
  "v_cvt_pk_bf16_f32 v37, v234, v235\n\t" \
  "v_cvt_pk_bf16_f32 v38, v236, v237\n\t" \
  "v_cvt_pk_bf16_f32 v39, v238, v239\n\t" \
  "v_mul_f32_e32 v160, s68, v160\n\t" \
  "v_mul_f32_e32 v161, s68, v161\n\t" \
  "v_mul_f32_e32 v162, s68, v162\n\t" \
  "v_mul_f32_e32 v163, s68, v163\n\t" \
  "v_mul_f32_e32 v164, s68, v164\n\t" \
  "v_mul_f32_e32 v165, s68, v165\n\t" \
  "v_mul_f32_e32 v166, s68, v166\n\t" \
  "v_mul_f32_e32 v167, s68, v167\n\t" \
  "v_mul_f32_e32 v168, s68, v168\n\t" \
  "v_mul_f32_e32 v169, s68, v169\n\t" \
  "v_mul_f32_e32 v170, s68, v170\n\t" \
  "v_mul_f32_e32 v171, s68, v171\n\t" \
  "v_mul_f32_e32 v172, s68, v172\n\t" \
  "v_mul_f32_e32 v173, s68, v173\n\t" \
  "v_mul_f32_e32 v174, s68, v174\n\t" \
  "v_mul_f32_e32 v175, s68, v175\n\t" \
  "v_cvt_pk_bf16_f32 v40, v160, v161\n\t" \
  "v_cvt_pk_bf16_f32 v41, v162, v163\n\t" \
  "v_cvt_pk_bf16_f32 v42, v164, v165\n\t" \
  "v_cvt_pk_bf16_f32 v43, v166, v167\n\t" \
  "v_cvt_pk_bf16_f32 v44, v168, v169\n\t" \
  "v_cvt_pk_bf16_f32 v45, v170, v171\n\t" \
  "v_cvt_pk_bf16_f32 v46, v172, v173\n\t" \
  "v_cvt_pk_bf16_f32 v47, v174, v175\n\t" \
  "s_waitcnt vmcnt(1)\n\t" \
  "s_barrier\n\t" \
  "s_waitcnt vmcnt(0)\n\t" \
  "s_cmp_lt_u32 %[wid], 4\n\t" \
  "s_cbranch_scc1 L_OKV%=\n\t" \
  "v_readfirstlane_b32 s81, v242\n\t" \
  "s_mov_b32 s80, 0\n\t" \
  "s_cmp_ge_u32 s81, 64\n\t" \
  "s_cbranch_scc1 L_OKV%=\n\t" \
  "L_SPINV%=:\n\t" \
  "s_sleep 2\n\t" \
  "global_load_dword v242, v240, %[cnv] sc1\n\t" \
  "s_waitcnt vmcnt(0)\n\t" \
  "v_readfirstlane_b32 s81, v242\n\t" \
  "s_add_u32 s80, s80, 1\n\t" \
  "s_cmp_ge_u32 s81, 64\n\t" \
  "s_cbranch_scc1 L_OKV%=\n\t" \
  "s_cmp_lt_u32 s80, 0x20000\n\t" \
  "s_cbranch_scc1 L_SPINV%=\n\t" \
  "L_OKV%=:\n\t"
#define FRONT_CLOB "v48", "v49", "v50", "v51", "v52", "v53", "v54", "v55", "v56", "v57", "v58", "v59", "v60", "v61", "v62", "v63", "v64", "v65", "v66", "v67", "v68", "v69", "v70", "v71", "v72", "v73", "v74", "v75", "v76", "v77", "v78", "v79", "v80", "v81", "v82", "v83", "v84", "v85", "v86", "v87", "v88", "v89", "v90", "v91", "v92", "v93", "v94", "v95", "v96", "v97", "v98", "v99", "v100", "v101", "v102", "v103", "v104", "v105", "v106", "v107", "v108", "v109", "v110", "v111", "v112", "v113", "v114", "v115", "v116", "v117", "v118", "v119", "v120", "v121", "v122", "v123", "v124", "v125", "v126", "v127", "v128", "v129", "v130", "v131", "v132", "v133", "v134", "v135", "v136", "v137", "v138", "v139", "v140", "v141", "v142", "v143", "v144", "v145", "v146", "v147", "v148", "v149", "v150", "v151", "v152", "v153", "v154", "v155", "v156", "v157", "v158", "v159", "v160", "v161", "v162", "v163", "v164", "v165", "v166", "v167", "v168", "v169", "v170", "v171", "v172", "v173", "v174", "v175", "v176", "v177", "v178", "v179", "v180", "v181", "v182", "v183", "v184", "v185", "v186", "v187", "v188", "v189", "v190", "v191", "v192", "v193", "v194", "v195", "v196", "v197", "v198", "v199", "v200", "v201", "v202", "v203", "v204", "v205", "v206", "v207", "v208", "v209", "v210", "v211", "v212", "v213", "v214", "v215", "v216", "v217", "v218", "v219", "v220", "v221", "v222", "v223", "v224", "v225", "v226", "v227", "v228", "v229", "v230", "v231", "v232", "v233", "v234", "v235", "v236", "v237", "v238", "v239", "v240", "v241", "v242", "v243", "v244", "v245", "v246", "v247", "v248", "v249", "v250", "v251", "v252", "v253", "v254", "v255", "s64", "s65", "s66", "s67", "s68", "s69", "s70", "s71", "s72", "s73", "s74", "s75", "s76", "s77", "s78", "s79", "s80", "s81", "scc", "memory"
#define FRONT_PRE_CLOB "v248", "v249", "v250", "v251", "s64", "s65", "s66", "s67", "scc", "memory"

__global__ __launch_bounds__(512, 2) void k2_attn(const u16* __restrict__ ckv, const u16* __restrict__ wuk, const u16* __restrict__ wuv,
                                                   const u16* __restrict__ wq, char* __restrict__ ws, const u16* __restrict__ wo, const u16* __restrict__ wg,
                                                   const u16* __restrict__ wu, const u16* __restrict__ wd) {
  extern __shared__ __attribute__((aligned(16))) char smem[];
  lptr lds = (lptr)smem;
  const unsigned lds0 = (unsigned)(uintptr_t)smem;
  const int tid = threadIdx.x, wid = __builtin_amdgcn_readfirstlane(tid >> 6), lane = tid & 63, r32 = lane & 31, hi = lane >> 5;
  const int G = wid >> 2, w4 = wid & 3;
  const int b = blockIdx.x, head = (b & 7) * 4 + (b >> 6), pr = (b >> 3) & 7;
  const char* Kh = ws + WS_KF + (size_t)head * (S * 256) + w4 * 4096;
  const char* Vh = ws + WS_VF + (size_t)head * (S * 256) + w4 * 4096;
  const unsigned lin = (unsigned)lane * 16u;
  const unsigned gbase = lds0 + (unsigned)G * 16384u + (unsigned)w4 * 4096u;
  u32x16 q0, q1; unsigned mimic, prm = 0, lin_ = lin; int dq = (w4 & 1) * 32 + r32 - 4 * hi;
  {
    const int qblk = G ? pr : 15 - pr;
    const int wm = wid >> 1, wn = wid & 1, fr = lane & 15, fq = lane >> 4, krow = (lane >> 2) & 3, pp = lane & 3;
    const unsigned va = (unsigned)((lane >> 3) * 1024 + (((lane & 7) ^ (lane >> 3)) * 16));
    const unsigned vb = (unsigned)(krow * (HD * 2) + fq * 64 + ((pp ^ ((wid & 1) << 1)) * 16));
    const char* ga = (const char*)ckv + (size_t)(pr * 256 + wid * 32) * (DC * 2);
    const char* gb0 = (const char*)wuk + head * 256 + (size_t)(8 * wid) * (HD * 2);
    const unsigned da = lds0 + (unsigned)wid * 4096u, db = lds0 + 32768u + (unsigned)wid * 2048u;
    {
      const int c0 = wid * 64 + lane, c1 = 512 + wid * 16 + (lane & 15);
      const unsigned hb = (unsigned)b * 10240u; const int kb = (b & 3) * 160;
      const u16* mp = (const u16*)(ws + WS_MODB);
      const u32x4 x0 = *(const u32x4*)(ws + WS_XN + hb + c0 * 16), x1 = *(const u32x4*)(ws + WS_XN + hb + c1 * 16);
      const u32x4 s0 = *(const u32x4*)(mp + kb + (c0 >> 5) * 8), s1 = *(const u32x4*)(mp + kb + (c1 >> 5) * 8);
      const u32x4 g0 = *(const u32x4*)(mp + M + kb + (c0 >> 5) * 8), g1 = *(const u32x4*)(mp + M + kb + (c1 >> 5) * 8);
      const float st0 = ((const float*)(ws + WS_RST))[lane < 40 ? lane : 0], st1 = ((const float*)(ws + WS_RST))[64 + (lane < 40 ? lane : 0)];
      SBAR();
      asm volatile(FRONT_PRE :: [va] "v"(va), [vb] "v"(vb), [ga] "s"(ga), [gb0] "s"(gb0), [da] "s"(da), [db] "s"(db) : FRONT_PRE_CLOB);
      SBAR();
    {
      const int nte = 2 * (qblk + 1) - (w4 < 2 ? 1 : 0), gw = b * 4 + w4;
      const int sg = ((7 - pr) * 32 + (b >> 6) * 8 + (b & 7)) * 4 + w4;
      const bool isd = sg >= 432; const int sv = isd ? min(sg - 432, 219) : sg;
      const int strm = isd ? sv / 22 : sv >> 3, k0 = isd ? min((sv - strm * 22) * 80, F - 80) : (sv & 7) * 80;
      const int mt = !isd && strm >= 27, cb = strm - mt * 27, ld = isd ? M * 2 : F * 2;
      const size_t w17 = (size_t)min(gw * 17, M * F * 2 / 128 - 20) * 128;
#define PUT(l, v) asm volatile("s_nop 1\n\tv_writelane_b32 %0, %1, %2" : "+v"(prm) : "s"(__builtin_amdgcn_readfirstlane((unsigned)(v))), "i"(l))
#define PUT2(l, p) do { const unsigned long long p_ = (unsigned long long)(p); PUT(l, p_); PUT((l) + 1, p_ >> 32); } while (0)
      PUT2(0, Kh + w4 * 4096); PUT2(2, Vh + w4 * 4096 - 16384); PUT2(4, Vh); PUT(6, lds0 + 32768u + (unsigned)w4 * 8192u); PUT(7, lds0 + 81920u + (unsigned)w4 * 4096u);
      PUT(8, 17 - pr); PUT(9, nte); PUT(10, G); PUT(11, 2);
      PUT2(12, ws + WS_CTX + (size_t)(qblk * 4 + w4) * 262144 + (size_t)head * 8192); PUT2(14, ws + WS_PART + (size_t)gw * 10240);
      PUT2(16, (const char*)(isd ? wd : mt ? wu : wg) + (size_t)k0 * ld + cb * 128);
      PUT2(18, ws + WS_WT + (isd ? (size_t)108 * 40960 + (size_t)cb * 4 * 55296 : (size_t)cb * 4 * 40960 + mt * 20480) + k0 * 32);
      PUT(20, sg < 652 ? 16 - pr - (pr < 7) : 999); PUT(21, ld); PUT(22, isd ? 55296 : 40960);
      PUT2(24, (const char*)wo + (size_t)gw * 5120); PUT2(26, (const char*)wo + (size_t)gw * 5120 + 2560);
      PUT2(28, (const char*)wg + w17); PUT2(30, (const char*)wu + w17); PUT2(32, (const char*)wd + w17);
    }
      SBAR();
      { const float est = wave_sum(lane < 40 ? st0 : 0.f) + 2.66f * sqrtf(wave_sum(lane < 40 ? st1 : 0.f));
        mimic = __builtin_amdgcn_readfirstlane(__float_as_uint(__builtin_fabsf(est) < 8.6f ? 1.f : QSCALE));
        PUT(34, mimic); }
#undef PUT
#undef PUT2
      u32x4 o0, o1;
#pragma unroll
      for (int e = 0; e < 4; ++e) {
        const unsigned p0 = cvtpk(bflo(x0[e]) * bflo(g0[e]), bfhi(x0[e]) * bfhi(g0[e])), p1 = cvtpk(bflo(x1[e]) * bflo(g1[e]), bfhi(x1[e]) * bfhi(g1[e]));
        o0[e] = cvtpk(bflo(p0) + bflo(s0[e]), bfhi(p0) + bfhi(s0[e])); o1[e] = cvtpk(bflo(p1) + bflo(s1[e]), bfhi(p1) + bfhi(s1[e]));
      }
      asm volatile("global_store_dwordx4 %0, %2, %4 sc1\n\ts_mov_b64 exec, 0xffff\n\tglobal_store_dwordx4 %1, %3, %4 sc1\n\ts_mov_b64 exec, -1\n\ts_nop 0"
                   :: "v"(hb + (unsigned)c0 * 16u), "v"(hb + (unsigned)c1 * 16u), "v"(o0), "v"(o1), "s"(ws + WS_XH) : "memory");
    }
    unsigned ar0 = lds0 + (unsigned)((wm * 8 + (fr >> 3)) * 1024 + (fr & 7) * 128 + ((fq ^ (fr & 7)) * 16)), ar1 = ar0 ^ 64u;
    unsigned br0 = lds0 + 32768u + (unsigned)(fq * 2048 + wn * 512 + krow * 64 + ((pp * 8) ^ ((fq & 1) << 5))), br1 = br0 ^ 32u;
    const unsigned kst = (unsigned)(wm * 16384 + (4 * wn + (fq >> 1)) * 1024 + (fq & 1) * 512 + (((fr >> 3) << 4) | (fr & 7)) * 16);
    const unsigned vst = (unsigned)((wm * 4 + (fq >> 1)) * 4096 + wn * 2048 + (fq & 1) * 512 + fr * 16);
    const unsigned wv0 = (unsigned)(krow * (HD * 2) + fq * 64 + pp * 16);
    const unsigned wrd = lds0 + (unsigned)(hi * 2048 + krow * 64 + 32 * (pp >> 1) + 16 * (fq & 1) + 8 * (pp & 1));
    asm volatile(FRONT_ASM
                 : "=&{v[16:31]}"(q0), "=&{v[32:47]}"(q1), [ar0] "+{v12}"(ar0), [ar1] "+{v13}"(ar1), [br0] "+{v14}"(br0), [br1] "+{v15}"(br1), [lin] "+{v4}"(lin_), "+{v1}"(dq), [prm] "+{v2}"(prm)
                 : [va] "{v5}"(va), [vb] "{v6}"(vb), [kst] "{v7}"(kst), [vst] "{v8}"(vst), [wv0] "{v9}"(wv0), [wrd] "{v10}"(wrd),
                   [wmv] "{v11}"((unsigned)((b * 4 + w4) * 32 + (lane & 31)) * 128u), [wmv2] "{v3}"((unsigned)((b * 4 + w4) * 40 + min(lane, 39)) * 128u),

                   [wm0] "s"((const char*)wuv), [wm1] "s"((const char*)wq), [wid] "s"(wid),
                   [ga] "s"(ga), [gb0] "s"(gb0), [gb1] "s"((const char*)wuv + head * 256 + (size_t)(8 * wid) * (HD * 2)),
                   [kd] "s"(ws + WS_KF + (size_t)head * (S * 256) + (size_t)pr * 65536), [vd] "s"(ws + WS_VF + (size_t)head * (S * 256) + (size_t)pr * 65536),
                   [sx] "s"(ws + WS_XH + (size_t)(qblk * 4 + w4) * 40960), [sw] "s"((const char*)wq + head * 256 + (size_t)(8 * wid) * (HD * 2)),
                   [cnt] "s"(ws + WS_CNT + head * 256), [cnv] "s"(ws + WS_CNT + (64 + head) * 256), [hcp] "s"(ws + WS_CNT + (32 + (b >> 4)) * 256), [hcc] "s"(ws + WS_CNT + (32 + qblk) * 256), [kh] "s"(Kh + G * 16384),
                   [da] "s"(da), [db] "s"(db), [dw] "s"(lds0 + (unsigned)wid * 2048u), [gbase] "s"(gbase)
                 : FRONT_CLOB);
  }
  {
    if (mimic != 0x3f800000u)
      asm volatile(ATTN_ASM :: "{v[16:31]}"(q0), "{v[32:47]}"(q1), [lin] "{v4}"(lin_), [dq] "{v1}"(dq), [prm] "{v2}"(prm) : ATTN_CLOB);
    else
      asm volatile(ATTNM_ASM :: "{v[16:31]}"(q0), "{v[32:47]}"(q1), [lin] "{v4}"(lin_), [dq] "{v1}"(dq), [prm] "{v2}"(prm) : ATTNM_CLOB);
  }
}

#define OP_PRE \
  "s_add_u32 m0, %[da], 0\n\t" \
  "s_mov_b64 s[64:65], %[ga]\n\t" \
  "global_load_lds_dwordx4 %[lin], s[64:65]\n\t" \
  "global_load_lds_dwordx4 %[lin], s[64:65] offset:1024\n\t" \
  "s_add_u32 m0, %[db], 0\n\t" \
  "s_mov_b64 s[66:67], %[gb]\n\t" \
  "global_load_lds_dwordx4 %[vb0], s[66:67]\n\t" \
  "s_add_u32 m0, %[db], 8192\n\t" \
  "s_nop 0\n\t" \
  "global_load_lds_dwordx4 %[vb1], s[66:67]\n\t" \
  "s_add_u32 m0, %[db2], 0\n\t" \
  "s_cmp_lg_u32 %[kh], 0\n\t" \
  "s_cbranch_scc1 L_S1%=\n\t" \
  "global_load_lds_dwordx4 %[vb2], s[66:67]\n\t" \
  "L_S1%=:\n\t" \
  "s_add_u32 m0, %[da], 36864\n\t" \
  "s_add_u32 s64, s64, 4096\n\t" \
  "s_addc_u32 s65, s65, 0\n\t" \
  "global_load_lds_dwordx4 %[lin], s[64:65]\n\t" \
  "global_load_lds_dwordx4 %[lin], s[64:65] offset:1024\n\t" \
  "s_add_u32 m0, %[db], 36864\n\t" \
  "s_add_u32 s66, s66, 81920\n\t" \
  "s_addc_u32 s67, s67, 0\n\t" \
  "global_load_lds_dwordx4 %[vb0], s[66:67]\n\t" \
  "s_add_u32 m0, %[db], 45056\n\t" \
  "s_nop 0\n\t" \
  "global_load_lds_dwordx4 %[vb1], s[66:67]\n\t" \
  "s_add_u32 m0, %[db2], 36864\n\t" \
  "s_cmp_lg_u32 %[kh], 1\n\t" \
  "s_cbranch_scc1 L_S2%=\n\t" \
  "global_load_lds_dwordx4 %[vb2], s[66:67]\n\t" \
  "L_S2%=:\n\t" \
  "s_add_u32 m0, %[da], 73728\n\t" \
  "s_add_u32 s64, s64, 4096\n\t" \
  "s_addc_u32 s65, s65, 0\n\t" \
  "global_load_lds_dwordx4 %[lin], s[64:65]\n\t" \
  "global_load_lds_dwordx4 %[lin], s[64:65] offset:1024\n\t" \
  "s_add_u32 m0, %[db], 73728\n\t" \
  "s_add_u32 s66, s66, 81920\n\t" \
  "s_addc_u32 s67, s67, 0\n\t" \
  "global_load_lds_dwordx4 %[vb0], s[66:67]\n\t" \
  "s_add_u32 m0, %[db], 81920\n\t" \
  "s_nop 0\n\t" \
  "global_load_lds_dwordx4 %[vb1], s[66:67]\n\t" \
  "s_add_u32 m0, %[db2], 73728\n\t" \
  "s_cmp_lg_u32 %[kh], 0\n\t" \
  "s_cbranch_scc1 L_S3%=\n\t" \
  "global_load_lds_dwordx4 %[vb2], s[66:67]\n\t" \
  "L_S3%=:\n\t" \
  "s_add_u32 m0, %[da], 110592\n\t" \
  "s_add_u32 s64, s64, 4096\n\t" \
  "s_addc_u32 s65, s65, 0\n\t" \
  "global_load_lds_dwordx4 %[lin], s[64:65]\n\t" \
  "global_load_lds_dwordx4 %[lin], s[64:65] offset:1024\n\t" \
  "s_add_u32 m0, %[db], 110592\n\t" \
  "s_add_u32 s66, s66, 81920\n\t" \
  "s_addc_u32 s67, s67, 0\n\t" \
  "global_load_lds_dwordx4 %[vb0], s[66:67]\n\t" \
  "s_add_u32 m0, %[db], 118784\n\t" \
  "s_nop 0\n\t" \
  "global_load_lds_dwordx4 %[vb1], s[66:67]\n\t" \
  "s_add_u32 m0, %[db2], 110592\n\t" \
  "s_cmp_lg_u32 %[kh], 1\n\t" \
  "s_cbranch_scc1 L_S4%=\n\t" \
  "global_load_lds_dwordx4 %[vb2], s[66:67]\n\t" \
  "L_S4%=:\n\t"
#define OP_ASM \
  "s_mov_b64 s[64:65], %[ga]\n\t" \
  "s_add_u32 s64, s64, 12288\n\t" \
  "s_addc_u32 s65, s65, 0\n\t" \
  "s_mov_b64 s[66:67], %[gb]\n\t" \
  "s_add_u32 s66, s66, 245760\n\t" \
  "s_addc_u32 s67, s67, 0\n\t" \
  "v_add_u32_e32 v184, 73728, %[ar]\n\t" \
  "v_add_u32_e32 v185, 73728, %[bp0]\n\t" \
  "v_add_u32_e32 v186, 73728, %[bp1]\n\t" \
  "s_waitcnt vmcnt(13) lgkmcnt(0)\n\t" \
  "s_barrier\n\t" \
  "ds_read_b128 v[112:115], %[ar] offset:0\n\t" \
  "ds_read_b64_tr_b16 v[128:129], %[bp0] offset:0\n\t" \
  "ds_read_b64_tr_b16 v[130:131], %[bp0] offset:2560\n\t" \
  "ds_read_b64_tr_b16 v[132:133], %[bp1] offset:0\n\t" \
  "ds_read_b64_tr_b16 v[134:135], %[bp1] offset:2560\n\t" \
  "ds_read_b64_tr_b16 v[136:137], %[bp0] offset:256\n\t" \
  "ds_read_b64_tr_b16 v[138:139], %[bp0] offset:2816\n\t" \
  "ds_read_b64_tr_b16 v[140:141], %[bp1] offset:256\n\t" \
  "ds_read_b64_tr_b16 v[142:143], %[bp1] offset:2816\n\t" \
  "ds_read_b64_tr_b16 v[144:145], %[bp0] offset:512\n\t" \
  "ds_read_b64_tr_b16 v[146:147], %[bp0] offset:3072\n\t" \
  "ds_read_b128 v[116:119], %[ar] offset:256\n\t" \
  "ds_read_b128 v[120:123], %[ar] offset:4096\n\t" \
  "ds_read_b128 v[124:127], %[ar] offset:4352\n\t" \
  "s_waitcnt vmcnt(9) lgkmcnt(0)\n\t" \
  "s_barrier\n\t" \
  "v_mfma_f32_16x16x32_bf16 v[32:35], v[128:131], v[112:115], 0\n\t" \
  "ds_read_b128 v[148:151], %[ar] offset:36864\n\t" \
  "v_mfma_f32_16x16x32_bf16 v[36:39], v[132:135], v[112:115], 0\n\t" \
  "ds_read_b64_tr_b16 v[164:165], %[bp0] offset:36864\n\t" \
  "v_mfma_f32_16x16x32_bf16 v[40:43], v[136:139], v[112:115], 0\n\t" \
  "ds_read_b64_tr_b16 v[166:167], %[bp0] offset:39424\n\t" \
  "s_add_u32 m0, %[da], 0\n\t" \
  "s_add_u32 s64, s64, 4096\n\t" \
  "s_addc_u32 s65, s65, 0\n\t" \
  "global_load_lds_dwordx4 %[lin], s[64:65]\n\t" \
  "v_mfma_f32_16x16x32_bf16 v[44:47], v[140:143], v[112:115], 0\n\t" \
  "ds_read_b64_tr_b16 v[168:169], %[bp1] offset:36864\n\t" \
  "v_mfma_f32_16x16x32_bf16 v[48:51], v[144:147], v[112:115], 0\n\t" \
  "ds_read_b64_tr_b16 v[170:171], %[bp1] offset:39424\n\t" \
  "v_mfma_f32_16x16x32_bf16 v[52:55], v[128:131], v[116:119], 0\n\t" \
  "ds_read_b64_tr_b16 v[172:173], %[bp0] offset:37120\n\t" \
  "global_load_lds_dwordx4 %[lin], s[64:65] offset:1024\n\t" \
  "v_mfma_f32_16x16x32_bf16 v[56:59], v[132:135], v[116:119], 0\n\t" \
  "ds_read_b64_tr_b16 v[174:175], %[bp0] offset:39680\n\t" \
  "v_mfma_f32_16x16x32_bf16 v[60:63], v[136:139], v[116:119], 0\n\t" \
  "ds_read_b64_tr_b16 v[176:177], %[bp1] offset:37120\n\t" \
  "v_mfma_f32_16x16x32_bf16 v[64:67], v[140:143], v[116:119], 0\n\t" \
  "ds_read_b64_tr_b16 v[178:179], %[bp1] offset:39680\n\t" \
  "s_add_u32 m0, %[db], 0\n\t" \
  "s_add_u32 s66, s66, 81920\n\t" \
  "s_addc_u32 s67, s67, 0\n\t" \
  "global_load_lds_dwordx4 %[vb0], s[66:67]\n\t" \
  "v_mfma_f32_16x16x32_bf16 v[68:71], v[144:147], v[116:119], 0\n\t" \
  "ds_read_b64_tr_b16 v[180:181], %[bp0] offset:37376\n\t" \
  "v_mfma_f32_16x16x32_bf16 v[72:75], v[128:131], v[120:123], 0\n\t" \
  "ds_read_b64_tr_b16 v[182:183], %[bp0] offset:39936\n\t" \
  "v_mfma_f32_16x16x32_bf16 v[76:79], v[132:135], v[120:123], 0\n\t" \
  "ds_read_b128 v[152:155], %[ar] offset:37120\n\t" \
  "s_add_u32 m0, %[db], 8192\n\t" \
  "s_nop 0\n\t" \
  "global_load_lds_dwordx4 %[vb1], s[66:67]\n\t" \
  "v_mfma_f32_16x16x32_bf16 v[80:83], v[136:139], v[120:123], 0\n\t" \
  "ds_read_b128 v[156:159], %[ar] offset:40960\n\t" \
  "v_mfma_f32_16x16x32_bf16 v[84:87], v[140:143], v[120:123], 0\n\t" \
  "ds_read_b128 v[160:163], %[ar] offset:41216\n\t" \
  "v_mfma_f32_16x16x32_bf16 v[88:91], v[144:147], v[120:123], 0\n\t" \
  "s_add_u32 m0, %[db2], 0\n\t" \
  "s_cmp_lg_u32 %[kh], 0\n\t" \
  "s_cbranch_scc1 L_S5%=\n\t" \
  "global_load_lds_dwordx4 %[vb2], s[66:67]\n\t" \
  "L_S5%=:\n\t" \
  "v_mfma_f32_16x16x32_bf16 v[92:95], v[128:131], v[124:127], 0\n\t" \
  "v_mfma_f32_16x16x32_bf16 v[96:99], v[132:135], v[124:127], 0\n\t" \
  "v_mfma_f32_16x16x32_bf16 v[100:103], v[136:139], v[124:127], 0\n\t" \
  "v_mfma_f32_16x16x32_bf16 v[104:107], v[140:143], v[124:127], 0\n\t" \
  "v_mfma_f32_16x16x32_bf16 v[108:111], v[144:147], v[124:127], 0\n\t" \
  "s_waitcnt vmcnt(9) lgkmcnt(0)\n\t" \
  "s_barrier\n\t" \
  "v_mfma_f32_16x16x32_bf16 v[32:35], v[164:167], v[148:151], v[32:35]\n\t" \
  "ds_read_b128 v[112:115], v184 offset:0\n\t" \
  "v_mfma_f32_16x16x32_bf16 v[36:39], v[168:171], v[148:151], v[36:39]\n\t" \
  "ds_read_b64_tr_b16 v[128:129], v185 offset:0\n\t" \
  "v_mfma_f32_16x16x32_bf16 v[40:43], v[172:175], v[148:151], v[40:43]\n\t" \
  "ds_read_b64_tr_b16 v[130:131], v185 offset:2560\n\t" \
  "s_add_u32 m0, %[da], 36864\n\t" \
  "s_add_u32 s64, s64, 4096\n\t" \
  "s_addc_u32 s65, s65, 0\n\t" \
  "global_load_lds_dwordx4 %[lin], s[64:65]\n\t" \
  "v_mfma_f32_16x16x32_bf16 v[44:47], v[176:179], v[148:151], v[44:47]\n\t" \
  "ds_read_b64_tr_b16 v[132:133], v186 offset:0\n\t" \
  "v_mfma_f32_16x16x32_bf16 v[48:51], v[180:183], v[148:151], v[48:51]\n\t" \
  "ds_read_b64_tr_b16 v[134:135], v186 offset:2560\n\t" \
  "v_mfma_f32_16x16x32_bf16 v[52:55], v[164:167], v[152:155], v[52:55]\n\t" \
  "ds_read_b64_tr_b16 v[136:137], v185 offset:256\n\t" \
  "global_load_lds_dwordx4 %[lin], s[64:65] offset:1024\n\t" \
  "v_mfma_f32_16x16x32_bf16 v[56:59], v[168:171], v[152:155], v[56:59]\n\t" \
  "ds_read_b64_tr_b16 v[138:139], v185 offset:2816\n\t" \
  "v_mfma_f32_16x16x32_bf16 v[60:63], v[172:175], v[152:155], v[60:63]\n\t" \
  "ds_read_b64_tr_b16 v[140:141], v186 offset:256\n\t" \
  "v_mfma_f32_16x16x32_bf16 v[64:67], v[176:179], v[152:155], v[64:67]\n\t" \
  "ds_read_b64_tr_b16 v[142:143], v186 offset:2816\n\t" \
  "s_add_u32 m0, %[db], 36864\n\t" \
  "s_add_u32 s66, s66, 81920\n\t" \
  "s_addc_u32 s67, s67, 0\n\t" \
  "global_load_lds_dwordx4 %[vb0], s[66:67]\n\t" \
  "v_mfma_f32_16x16x32_bf16 v[68:71], v[180:183], v[152:155], v[68:71]\n\t" \
  "ds_read_b64_tr_b16 v[144:145], v185 offset:512\n\t" \
  "v_mfma_f32_16x16x32_bf16 v[72:75], v[164:167], v[156:159], v[72:75]\n\t" \
  "ds_read_b64_tr_b16 v[146:147], v185 offset:3072\n\t" \
  "v_mfma_f32_16x16x32_bf16 v[76:79], v[168:171], v[156:159], v[76:79]\n\t" \
  "ds_read_b128 v[116:119], v184 offset:256\n\t" \
  "s_add_u32 m0, %[db], 45056\n\t" \
  "s_nop 0\n\t" \
  "global_load_lds_dwordx4 %[vb1], s[66:67]\n\t" \
  "v_mfma_f32_16x16x32_bf16 v[80:83], v[172:175], v[156:159], v[80:83]\n\t" \
  "ds_read_b128 v[120:123], v184 offset:4096\n\t" \
  "v_mfma_f32_16x16x32_bf16 v[84:87], v[176:179], v[156:159], v[84:87]\n\t" \
  "ds_read_b128 v[124:127], v184 offset:4352\n\t" \
  "v_mfma_f32_16x16x32_bf16 v[88:91], v[180:183], v[156:159], v[88:91]\n\t" \
  "s_add_u32 m0, %[db2], 36864\n\t" \
  "s_cmp_lg_u32 %[kh], 1\n\t" \
  "s_cbranch_scc1 L_S6%=\n\t" \
  "global_load_lds_dwordx4 %[vb2], s[66:67]\n\t" \
  "L_S6%=:\n\t" \
  "v_mfma_f32_16x16x32_bf16 v[92:95], v[164:167], v[160:163], v[92:95]\n\t" \
  "v_mfma_f32_16x16x32_bf16 v[96:99], v[168:171], v[160:163], v[96:99]\n\t" \
  "v_mfma_f32_16x16x32_bf16 v[100:103], v[172:175], v[160:163], v[100:103]\n\t" \
  "v_mfma_f32_16x16x32_bf16 v[104:107], v[176:179], v[160:163], v[104:107]\n\t" \
  "v_mfma_f32_16x16x32_bf16 v[108:111], v[180:183], v[160:163], v[108:111]\n\t" \
  "s_waitcnt vmcnt(9) lgkmcnt(0)\n\t" \
  "s_barrier\n\t" \
  "v_mfma_f32_16x16x32_bf16 v[32:35], v[128:131], v[112:115], v[32:35]\n\t" \
  "ds_read_b128 v[148:151], v184 offset:36864\n\t" \
  "v_mfma_f32_16x16x32_bf16 v[36:39], v[132:135], v[112:115], v[36:39]\n\t" \
  "ds_read_b64_tr_b16 v[164:165], v185 offset:36864\n\t" \
  "v_mfma_f32_16x16x32_bf16 v[40:43], v[136:139], v[112:115], v[40:43]\n\t" \
  "ds_read_b64_tr_b16 v[166:167], v185 offset:39424\n\t" \
  "s_add_u32 m0, %[da], 73728\n\t" \
  "s_add_u32 s64, s64, 4096\n\t" \
  "s_addc_u32 s65, s65, 0\n\t" \
  "global_load_lds_dwordx4 %[lin], s[64:65]\n\t" \
  "v_mfma_f32_16x16x32_bf16 v[44:47], v[140:143], v[112:115], v[44:47]\n\t" \
  "ds_read_b64_tr_b16 v[168:169], v186 offset:36864\n\t" \
  "v_mfma_f32_16x16x32_bf16 v[48:51], v[144:147], v[112:115], v[48:51]\n\t" \
  "ds_read_b64_tr_b16 v[170:171], v186 offset:39424\n\t" \
  "v_mfma_f32_16x16x32_bf16 v[52:55], v[128:131], v[116:119], v[52:55]\n\t" \
  "ds_read_b64_tr_b16 v[172:173], v185 offset:37120\n\t" \
  "global_load_lds_dwordx4 %[lin], s[64:65] offset:1024\n\t" \
  "v_mfma_f32_16x16x32_bf16 v[56:59], v[132:135], v[116:119], v[56:59]\n\t" \
  "ds_read_b64_tr_b16 v[174:175], v185 offset:39680\n\t" \
  "v_mfma_f32_16x16x32_bf16 v[60:63], v[136:139], v[116:119], v[60:63]\n\t" \
  "ds_read_b64_tr_b16 v[176:177], v186 offset:37120\n\t" \
  "v_mfma_f32_16x16x32_bf16 v[64:67], v[140:143], v[116:119], v[64:67]\n\t" \
  "ds_read_b64_tr_b16 v[178:179], v186 offset:39680\n\t" \
  "s_add_u32 m0, %[db], 73728\n\t" \
  "s_add_u32 s66, s66, 81920\n\t" \
  "s_addc_u32 s67, s67, 0\n\t" \
  "global_load_lds_dwordx4 %[vb0], s[66:67]\n\t" \
  "v_mfma_f32_16x16x32_bf16 v[68:71], v[144:147], v[116:119], v[68:71]\n\t" \
  "ds_read_b64_tr_b16 v[180:181], v185 offset:37376\n\t" \
  "v_mfma_f32_16x16x32_bf16 v[72:75], v[128:131], v[120:123], v[72:75]\n\t" \
  "ds_read_b64_tr_b16 v[182:183], v185 offset:39936\n\t" \
  "v_mfma_f32_16x16x32_bf16 v[76:79], v[132:135], v[120:123], v[76:79]\n\t" \
  "ds_read_b128 v[152:155], v184 offset:37120\n\t" \
  "s_add_u32 m0, %[db], 81920\n\t" \
  "s_nop 0\n\t" \
  "global_load_lds_dwordx4 %[vb1], s[66:67]\n\t" \
  "v_mfma_f32_16x16x32_bf16 v[80:83], v[136:139], v[120:123], v[80:83]\n\t" \
  "ds_read_b128 v[156:159], v184 offset:40960\n\t" \
  "v_mfma_f32_16x16x32_bf16 v[84:87], v[140:143], v[120:123], v[84:87]\n\t" \
  "ds_read_b128 v[160:163], v184 offset:41216\n\t" \
  "v_mfma_f32_16x16x32_bf16 v[88:91], v[144:147], v[120:123], v[88:91]\n\t" \
  "s_add_u32 m0, %[db2], 73728\n\t" \
  "s_cmp_lg_u32 %[kh], 0\n\t" \
  "s_cbranch_scc1 L_S7%=\n\t" \
  "global_load_lds_dwordx4 %[vb2], s[66:67]\n\t" \
  "L_S7%=:\n\t" \
  "v_mfma_f32_16x16x32_bf16 v[92:95], v[128:131], v[124:127], v[92:95]\n\t" \
  "v_mfma_f32_16x16x32_bf16 v[96:99], v[132:135], v[124:127], v[96:99]\n\t" \
  "v_mfma_f32_16x16x32_bf16 v[100:103], v[136:139], v[124:127], v[100:103]\n\t" \
  "v_mfma_f32_16x16x32_bf16 v[104:107], v[140:143], v[124:127], v[104:107]\n\t" \
  "v_mfma_f32_16x16x32_bf16 v[108:111], v[144:147], v[124:127], v[108:111]\n\t" \
  "s_waitcnt vmcnt(9) lgkmcnt(0)\n\t" \
  "s_barrier\n\t" \
  "v_mfma_f32_16x16x32_bf16 v[32:35], v[164:167], v[148:151], v[32:35]\n\t" \
  "ds_read_b128 v[112:115], %[ar] offset:0\n\t" \
  "v_mfma_f32_16x16x32_bf16 v[36:39], v[168:171], v[148:151], v[36:39]\n\t" \
  "ds_read_b64_tr_b16 v[128:129], %[bp0] offset:0\n\t" \
  "v_mfma_f32_16x16x32_bf16 v[40:43], v[172:175], v[148:151], v[40:43]\n\t" \
  "ds_read_b64_tr_b16 v[130:131], %[bp0] offset:2560\n\t" \
  "s_add_u32 m0, %[da], 110592\n\t" \
  "s_add_u32 s64, s64, 4096\n\t" \
  "s_addc_u32 s65, s65, 0\n\t" \
  "global_load_lds_dwordx4 %[lin], s[64:65]\n\t" \
  "v_mfma_f32_16x16x32_bf16 v[44:47], v[176:179], v[148:151], v[44:47]\n\t" \
  "ds_read_b64_tr_b16 v[132:133], %[bp1] offset:0\n\t" \
  "v_mfma_f32_16x16x32_bf16 v[48:51], v[180:183], v[148:151], v[48:51]\n\t" \
  "ds_read_b64_tr_b16 v[134:135], %[bp1] offset:2560\n\t" \
  "v_mfma_f32_16x16x32_bf16 v[52:55], v[164:167], v[152:155], v[52:55]\n\t" \
  "ds_read_b64_tr_b16 v[136:137], %[bp0] offset:256\n\t" \
  "global_load_lds_dwordx4 %[lin], s[64:65] offset:1024\n\t" \
  "v_mfma_f32_16x16x32_bf16 v[56:59], v[168:171], v[152:155], v[56:59]\n\t" \
  "ds_read_b64_tr_b16 v[138:139], %[bp0] offset:2816\n\t" \
  "v_mfma_f32_16x16x32_bf16 v[60:63], v[172:175], v[152:155], v[60:63]\n\t" \
  "ds_read_b64_tr_b16 v[140:141], %[bp1] offset:256\n\t" \
  "v_mfma_f32_16x16x32_bf16 v[64:67], v[176:179], v[152:155], v[64:67]\n\t" \
  "ds_read_b64_tr_b16 v[142:143], %[bp1] offset:2816\n\t" \
  "s_add_u32 m0, %[db], 110592\n\t" \
  "s_add_u32 s66, s66, 81920\n\t" \
  "s_addc_u32 s67, s67, 0\n\t" \
  "global_load_lds_dwordx4 %[vb0], s[66:67]\n\t" \
  "v_mfma_f32_16x16x32_bf16 v[68:71], v[180:183], v[152:155], v[68:71]\n\t" \
  "ds_read_b64_tr_b16 v[144:145], %[bp0] offset:512\n\t" \
  "v_mfma_f32_16x16x32_bf16 v[72:75], v[164:167], v[156:159], v[72:75]\n\t" \
  "ds_read_b64_tr_b16 v[146:147], %[bp0] offset:3072\n\t" \
  "v_mfma_f32_16x16x32_bf16 v[76:79], v[168:171], v[156:159], v[76:79]\n\t" \
  "ds_read_b128 v[116:119], %[ar] offset:256\n\t" \
  "s_add_u32 m0, %[db], 118784\n\t" \
  "s_nop 0\n\t" \
  "global_load_lds_dwordx4 %[vb1], s[66:67]\n\t" \
  "v_mfma_f32_16x16x32_bf16 v[80:83], v[172:175], v[156:159], v[80:83]\n\t" \
  "ds_read_b128 v[120:123], %[ar] offset:4096\n\t" \
  "v_mfma_f32_16x16x32_bf16 v[84:87], v[176:179], v[156:159], v[84:87]\n\t" \
  "ds_read_b128 v[124:127], %[ar] offset:4352\n\t" \
  "v_mfma_f32_16x16x32_bf16 v[88:91], v[180:183], v[156:159], v[88:91]\n\t" \
  "s_add_u32 m0, %[db2], 110592\n\t" \
  "s_cmp_lg_u32 %[kh], 1\n\t" \
  "s_cbranch_scc1 L_S8%=\n\t" \
  "global_load_lds_dwordx4 %[vb2], s[66:67]\n\t" \
  "L_S8%=:\n\t" \
  "v_mfma_f32_16x16x32_bf16 v[92:95], v[164:167], v[160:163], v[92:95]\n\t" \
  "v_mfma_f32_16x16x32_bf16 v[96:99], v[168:171], v[160:163], v[96:99]\n\t" \
  "v_mfma_f32_16x16x32_bf16 v[100:103], v[172:175], v[160:163], v[100:103]\n\t" \
  "v_mfma_f32_16x16x32_bf16 v[104:107], v[176:179], v[160:163], v[104:107]\n\t" \
  "v_mfma_f32_16x16x32_bf16 v[108:111], v[180:183], v[160:163], v[108:111]\n\t" \
  "s_waitcnt vmcnt(9) lgkmcnt(0)\n\t" \
  "s_barrier\n\t" \
  "v_mfma_f32_16x16x32_bf16 v[32:35], v[128:131], v[112:115], v[32:35]\n\t" \
  "ds_read_b128 v[148:151], %[ar] offset:36864\n\t" \
  "v_mfma_f32_16x16x32_bf16 v[36:39], v[132:135], v[112:115], v[36:39]\n\t" \
  "ds_read_b64_tr_b16 v[164:165], %[bp0] offset:36864\n\t" \
  "v_mfma_f32_16x16x32_bf16 v[40:43], v[136:139], v[112:115], v[40:43]\n\t" \
  "ds_read_b64_tr_b16 v[166:167], %[bp0] offset:39424\n\t" \
  "s_add_u32 m0, %[da], 0\n\t" \
  "s_add_u32 s64, s64, 4096\n\t" \
  "s_addc_u32 s65, s65, 0\n\t" \
  "global_load_lds_dwordx4 %[lin], s[64:65]\n\t" \
  "v_mfma_f32_16x16x32_bf16 v[44:47], v[140:143], v[112:115], v[44:47]\n\t" \
  "ds_read_b64_tr_b16 v[168:169], %[bp1] offset:36864\n\t" \
  "v_mfma_f32_16x16x32_bf16 v[48:51], v[144:147], v[112:115], v[48:51]\n\t" \
  "ds_read_b64_tr_b16 v[170:171], %[bp1] offset:39424\n\t" \
  "v_mfma_f32_16x16x32_bf16 v[52:55], v[128:131], v[116:119], v[52:55]\n\t" \
  "ds_read_b64_tr_b16 v[172:173], %[bp0] offset:37120\n\t" \
  "global_load_lds_dwordx4 %[lin], s[64:65] offset:1024\n\t" \
  "v_mfma_f32_16x16x32_bf16 v[56:59], v[132:135], v[116:119], v[56:59]\n\t" \
  "ds_read_b64_tr_b16 v[174:175], %[bp0] offset:39680\n\t" \
  "v_mfma_f32_16x16x32_bf16 v[60:63], v[136:139], v[116:119], v[60:63]\n\t" \
  "ds_read_b64_tr_b16 v[176:177], %[bp1] offset:37120\n\t" \
  "v_mfma_f32_16x16x32_bf16 v[64:67], v[140:143], v[116:119], v[64:67]\n\t" \
  "ds_read_b64_tr_b16 v[178:179], %[bp1] offset:39680\n\t" \
  "s_add_u32 m0, %[db], 0\n\t" \
  "s_add_u32 s66, s66, 81920\n\t" \
  "s_addc_u32 s67, s67, 0\n\t" \
  "global_load_lds_dwordx4 %[vb0], s[66:67]\n\t" \
  "v_mfma_f32_16x16x32_bf16 v[68:71], v[144:147], v[116:119], v[68:71]\n\t" \
  "ds_read_b64_tr_b16 v[180:181], %[bp0] offset:37376\n\t" \
  "v_mfma_f32_16x16x32_bf16 v[72:75], v[128:131], v[120:123], v[72:75]\n\t" \
  "ds_read_b64_tr_b16 v[182:183], %[bp0] offset:39936\n\t" \
  "v_mfma_f32_16x16x32_bf16 v[76:79], v[132:135], v[120:123], v[76:79]\n\t" \
  "ds_read_b128 v[152:155], %[ar] offset:37120\n\t" \
  "s_add_u32 m0, %[db], 8192\n\t" \
  "s_nop 0\n\t" \
  "global_load_lds_dwordx4 %[vb1], s[66:67]\n\t" \
  "v_mfma_f32_16x16x32_bf16 v[80:83], v[136:139], v[120:123], v[80:83]\n\t" \
  "ds_read_b128 v[156:159], %[ar] offset:40960\n\t" \
  "v_mfma_f32_16x16x32_bf16 v[84:87], v[140:143], v[120:123], v[84:87]\n\t" \
  "ds_read_b128 v[160:163], %[ar] offset:41216\n\t" \
  "v_mfma_f32_16x16x32_bf16 v[88:91], v[144:147], v[120:123], v[88:91]\n\t" \
  "s_add_u32 m0, %[db2], 0\n\t" \
  "s_cmp_lg_u32 %[kh], 0\n\t" \
  "s_cbranch_scc1 L_S9%=\n\t" \
  "global_load_lds_dwordx4 %[vb2], s[66:67]\n\t" \
  "L_S9%=:\n\t" \
  "v_mfma_f32_16x16x32_bf16 v[92:95], v[128:131], v[124:127], v[92:95]\n\t" \
  "v_mfma_f32_16x16x32_bf16 v[96:99], v[132:135], v[124:127], v[96:99]\n\t" \
  "v_mfma_f32_16x16x32_bf16 v[100:103], v[136:139], v[124:127], v[100:103]\n\t" \
  "v_mfma_f32_16x16x32_bf16 v[104:107], v[140:143], v[124:127], v[104:107]\n\t" \
  "v_mfma_f32_16x16x32_bf16 v[108:111], v[144:147], v[124:127], v[108:111]\n\t" \
  "s_waitcnt vmcnt(9) lgkmcnt(0)\n\t" \
  "s_barrier\n\t" \
  "v_mfma_f32_16x16x32_bf16 v[32:35], v[164:167], v[148:151], v[32:35]\n\t" \
  "ds_read_b128 v[112:115], v184 offset:0\n\t" \
  "v_mfma_f32_16x16x32_bf16 v[36:39], v[168:171], v[148:151], v[36:39]\n\t" \
  "ds_read_b64_tr_b16 v[128:129], v185 offset:0\n\t" \
  "v_mfma_f32_16x16x32_bf16 v[40:43], v[172:175], v[148:151], v[40:43]\n\t" \
  "ds_read_b64_tr_b16 v[130:131], v185 offset:2560\n\t" \
  "s_add_u32 m0, %[da], 36864\n\t" \
  "s_add_u32 s64, s64, 4096\n\t" \
  "s_addc_u32 s65, s65, 0\n\t" \
  "global_load_lds_dwordx4 %[lin], s[64:65]\n\t" \
  "v_mfma_f32_16x16x32_bf16 v[44:47], v[176:179], v[148:151], v[44:47]\n\t" \
  "ds_read_b64_tr_b16 v[132:133], v186 offset:0\n\t" \
  "v_mfma_f32_16x16x32_bf16 v[48:51], v[180:183], v[148:151], v[48:51]\n\t" \
  "ds_read_b64_tr_b16 v[134:135], v186 offset:2560\n\t" \
  "v_mfma_f32_16x16x32_bf16 v[52:55], v[164:167], v[152:155], v[52:55]\n\t" \
  "ds_read_b64_tr_b16 v[136:137], v185 offset:256\n\t" \
  "global_load_lds_dwordx4 %[lin], s[64:65] offset:1024\n\t" \
  "v_mfma_f32_16x16x32_bf16 v[56:59], v[168:171], v[152:155], v[56:59]\n\t" \
  "ds_read_b64_tr_b16 v[138:139], v185 offset:2816\n\t" \
  "v_mfma_f32_16x16x32_bf16 v[60:63], v[172:175], v[152:155], v[60:63]\n\t" \
  "ds_read_b64_tr_b16 v[140:141], v186 offset:256\n\t" \
  "v_mfma_f32_16x16x32_bf16 v[64:67], v[176:179], v[152:155], v[64:67]\n\t" \
  "ds_read_b64_tr_b16 v[142:143], v186 offset:2816\n\t" \
  "s_add_u32 m0, %[db], 36864\n\t" \
  "s_add_u32 s66, s66, 81920\n\t" \
  "s_addc_u32 s67, s67, 0\n\t" \
  "global_load_lds_dwordx4 %[vb0], s[66:67]\n\t" \
  "v_mfma_f32_16x16x32_bf16 v[68:71], v[180:183], v[152:155], v[68:71]\n\t" \
  "ds_read_b64_tr_b16 v[144:145], v185 offset:512\n\t" \
  "v_mfma_f32_16x16x32_bf16 v[72:75], v[164:167], v[156:159], v[72:75]\n\t" \
  "ds_read_b64_tr_b16 v[146:147], v185 offset:3072\n\t" \
  "v_mfma_f32_16x16x32_bf16 v[76:79], v[168:171], v[156:159], v[76:79]\n\t" \
  "ds_read_b128 v[116:119], v184 offset:256\n\t" \
  "s_add_u32 m0, %[db], 45056\n\t" \
  "s_nop 0\n\t" \
  "global_load_lds_dwordx4 %[vb1], s[66:67]\n\t" \
  "v_mfma_f32_16x16x32_bf16 v[80:83], v[172:175], v[156:159], v[80:83]\n\t" \
  "ds_read_b128 v[120:123], v184 offset:4096\n\t" \
  "v_mfma_f32_16x16x32_bf16 v[84:87], v[176:179], v[156:159], v[84:87]\n\t" \
  "ds_read_b128 v[124:127], v184 offset:4352\n\t" \
  "v_mfma_f32_16x16x32_bf16 v[88:91], v[180:183], v[156:159], v[88:91]\n\t" \
  "s_add_u32 m0, %[db2], 36864\n\t" \
  "s_cmp_lg_u32 %[kh], 1\n\t" \
  "s_cbranch_scc1 L_S10%=\n\t" \
  "global_load_lds_dwordx4 %[vb2], s[66:67]\n\t" \
  "L_S10%=:\n\t" \
  "v_mfma_f32_16x16x32_bf16 v[92:95], v[164:167], v[160:163], v[92:95]\n\t" \
  "v_mfma_f32_16x16x32_bf16 v[96:99], v[168:171], v[160:163], v[96:99]\n\t" \
  "v_mfma_f32_16x16x32_bf16 v[100:103], v[172:175], v[160:163], v[100:103]\n\t" \
  "v_mfma_f32_16x16x32_bf16 v[104:107], v[176:179], v[160:163], v[104:107]\n\t" \
  "v_mfma_f32_16x16x32_bf16 v[108:111], v[180:183], v[160:163], v[108:111]\n\t" \
  "s_waitcnt vmcnt(9) lgkmcnt(0)\n\t" \
  "s_barrier\n\t" \
  "v_mfma_f32_16x16x32_bf16 v[32:35], v[128:131], v[112:115], v[32:35]\n\t" \
  "ds_read_b128 v[148:151], v184 offset:36864\n\t" \
  "v_mfma_f32_16x16x32_bf16 v[36:39], v[132:135], v[112:115], v[36:39]\n\t" \
  "ds_read_b64_tr_b16 v[164:165], v185 offset:36864\n\t" \
  "v_mfma_f32_16x16x32_bf16 v[40:43], v[136:139], v[112:115], v[40:43]\n\t" \
  "ds_read_b64_tr_b16 v[166:167], v185 offset:39424\n\t" \
  "s_add_u32 m0, %[da], 73728\n\t" \
  "s_add_u32 s64, s64, 4096\n\t" \
  "s_addc_u32 s65, s65, 0\n\t" \
  "global_load_lds_dwordx4 %[lin], s[64:65]\n\t" \
  "v_mfma_f32_16x16x32_bf16 v[44:47], v[140:143], v[112:115], v[44:47]\n\t" \
  "ds_read_b64_tr_b16 v[168:169], v186 offset:36864\n\t" \
  "v_mfma_f32_16x16x32_bf16 v[48:51], v[144:147], v[112:115], v[48:51]\n\t" \
  "ds_read_b64_tr_b16 v[170:171], v186 offset:39424\n\t" \
  "v_mfma_f32_16x16x32_bf16 v[52:55], v[128:131], v[116:119], v[52:55]\n\t" \
  "ds_read_b64_tr_b16 v[172:173], v185 offset:37120\n\t" \
  "global_load_lds_dwordx4 %[lin], s[64:65] offset:1024\n\t" \
  "v_mfma_f32_16x16x32_bf16 v[56:59], v[132:135], v[116:119], v[56:59]\n\t" \
  "ds_read_b64_tr_b16 v[174:175], v185 offset:39680\n\t" \
  "v_mfma_f32_16x16x32_bf16 v[60:63], v[136:139], v[116:119], v[60:63]\n\t" \
  "ds_read_b64_tr_b16 v[176:177], v186 offset:37120\n\t" \
  "v_mfma_f32_16x16x32_bf16 v[64:67], v[140:143], v[116:119], v[64:67]\n\t" \
  "ds_read_b64_tr_b16 v[178:179], v186 offset:39680\n\t" \
  "s_add_u32 m0, %[db], 73728\n\t" \
  "s_add_u32 s66, s66, 81920\n\t" \
  "s_addc_u32 s67, s67, 0\n\t" \
  "global_load_lds_dwordx4 %[vb0], s[66:67]\n\t" \
  "v_mfma_f32_16x16x32_bf16 v[68:71], v[144:147], v[116:119], v[68:71]\n\t" \
  "ds_read_b64_tr_b16 v[180:181], v185 offset:37376\n\t" \
  "v_mfma_f32_16x16x32_bf16 v[72:75], v[128:131], v[120:123], v[72:75]\n\t" \
  "ds_read_b64_tr_b16 v[182:183], v185 offset:39936\n\t" \
  "v_mfma_f32_16x16x32_bf16 v[76:79], v[132:135], v[120:123], v[76:79]\n\t" \
  "ds_read_b128 v[152:155], v184 offset:37120\n\t" \
  "s_add_u32 m0, %[db], 81920\n\t" \
  "s_nop 0\n\t" \
  "global_load_lds_dwordx4 %[vb1], s[66:67]\n\t" \
  "v_mfma_f32_16x16x32_bf16 v[80:83], v[136:139], v[120:123], v[80:83]\n\t" \
  "ds_read_b128 v[156:159], v184 offset:40960\n\t" \
  "v_mfma_f32_16x16x32_bf16 v[84:87], v[140:143], v[120:123], v[84:87]\n\t" \
  "ds_read_b128 v[160:163], v184 offset:41216\n\t" \
  "v_mfma_f32_16x16x32_bf16 v[88:91], v[144:147], v[120:123], v[88:91]\n\t" \
  "s_add_u32 m0, %[db2], 73728\n\t" \
  "s_cmp_lg_u32 %[kh], 0\n\t" \
  "s_cbranch_scc1 L_S11%=\n\t" \
  "global_load_lds_dwordx4 %[vb2], s[66:67]\n\t" \
  "L_S11%=:\n\t" \
  "v_mfma_f32_16x16x32_bf16 v[92:95], v[128:131], v[124:127], v[92:95]\n\t" \
  "v_mfma_f32_16x16x32_bf16 v[96:99], v[132:135], v[124:127], v[96:99]\n\t" \
  "v_mfma_f32_16x16x32_bf16 v[100:103], v[136:139], v[124:127], v[100:103]\n\t" \
  "v_mfma_f32_16x16x32_bf16 v[104:107], v[140:143], v[124:127], v[104:107]\n\t" \
  "v_mfma_f32_16x16x32_bf16 v[108:111], v[144:147], v[124:127], v[108:111]\n\t" \
  "s_waitcnt vmcnt(9) lgkmcnt(0)\n\t" \
  "s_barrier\n\t" \
  "v_mfma_f32_16x16x32_bf16 v[32:35], v[164:167], v[148:151], v[32:35]\n\t" \
  "ds_read_b128 v[112:115], %[ar] offset:0\n\t" \
  "v_mfma_f32_16x16x32_bf16 v[36:39], v[168:171], v[148:151], v[36:39]\n\t" \
  "ds_read_b64_tr_b16 v[128:129], %[bp0] offset:0\n\t" \
  "v_mfma_f32_16x16x32_bf16 v[40:43], v[172:175], v[148:151], v[40:43]\n\t" \
  "ds_read_b64_tr_b16 v[130:131], %[bp0] offset:2560\n\t" \
  "s_add_u32 m0, %[da], 110592\n\t" \
  "s_add_u32 s64, s64, 4096\n\t" \
  "s_addc_u32 s65, s65, 0\n\t" \
  "global_load_lds_dwordx4 %[lin], s[64:65]\n\t" \
  "v_mfma_f32_16x16x32_bf16 v[44:47], v[176:179], v[148:151], v[44:47]\n\t" \
  "ds_read_b64_tr_b16 v[132:133], %[bp1] offset:0\n\t" \
  "v_mfma_f32_16x16x32_bf16 v[48:51], v[180:183], v[148:151], v[48:51]\n\t" \
  "ds_read_b64_tr_b16 v[134:135], %[bp1] offset:2560\n\t" \
  "v_mfma_f32_16x16x32_bf16 v[52:55], v[164:167], v[152:155], v[52:55]\n\t" \
  "ds_read_b64_tr_b16 v[136:137], %[bp0] offset:256\n\t" \
  "global_load_lds_dwordx4 %[lin], s[64:65] offset:1024\n\t" \
  "v_mfma_f32_16x16x32_bf16 v[56:59], v[168:171], v[152:155], v[56:59]\n\t" \
  "ds_read_b64_tr_b16 v[138:139], %[bp0] offset:2816\n\t" \
  "v_mfma_f32_16x16x32_bf16 v[60:63], v[172:175], v[152:155], v[60:63]\n\t" \
  "ds_read_b64_tr_b16 v[140:141], %[bp1] offset:256\n\t" \
  "v_mfma_f32_16x16x32_bf16 v[64:67], v[176:179], v[152:155], v[64:67]\n\t" \
  "ds_read_b64_tr_b16 v[142:143], %[bp1] offset:2816\n\t" \
  "s_add_u32 m0, %[db], 110592\n\t" \
  "s_add_u32 s66, s66, 81920\n\t" \
  "s_addc_u32 s67, s67, 0\n\t" \
  "global_load_lds_dwordx4 %[vb0], s[66:67]\n\t" \
  "v_mfma_f32_16x16x32_bf16 v[68:71], v[180:183], v[152:155], v[68:71]\n\t" \
  "ds_read_b64_tr_b16 v[144:145], %[bp0] offset:512\n\t" \
  "v_mfma_f32_16x16x32_bf16 v[72:75], v[164:167], v[156:159], v[72:75]\n\t" \
  "ds_read_b64_tr_b16 v[146:147], %[bp0] offset:3072\n\t" \
  "v_mfma_f32_16x16x32_bf16 v[76:79], v[168:171], v[156:159], v[76:79]\n\t" \
  "ds_read_b128 v[116:119], %[ar] offset:256\n\t" \
  "s_add_u32 m0, %[db], 118784\n\t" \
  "s_nop 0\n\t" \
  "global_load_lds_dwordx4 %[vb1], s[66:67]\n\t" \
  "v_mfma_f32_16x16x32_bf16 v[80:83], v[172:175], v[156:159], v[80:83]\n\t" \
  "ds_read_b128 v[120:123], %[ar] offset:4096\n\t" \
  "v_mfma_f32_16x16x32_bf16 v[84:87], v[176:179], v[156:159], v[84:87]\n\t" \
  "ds_read_b128 v[124:127], %[ar] offset:4352\n\t" \
  "v_mfma_f32_16x16x32_bf16 v[88:91], v[180:183], v[156:159], v[88:91]\n\t" \
  "s_add_u32 m0, %[db2], 110592\n\t" \
  "s_cmp_lg_u32 %[kh], 1\n\t" \
  "s_cbranch_scc1 L_S12%=\n\t" \
  "global_load_lds_dwordx4 %[vb2], s[66:67]\n\t" \
  "L_S12%=:\n\t" \
  "v_mfma_f32_16x16x32_bf16 v[92:95], v[164:167], v[160:163], v[92:95]\n\t" \
  "v_mfma_f32_16x16x32_bf16 v[96:99], v[168:171], v[160:163], v[96:99]\n\t" \
  "v_mfma_f32_16x16x32_bf16 v[100:103], v[172:175], v[160:163], v[100:103]\n\t" \
  "v_mfma_f32_16x16x32_bf16 v[104:107], v[176:179], v[160:163], v[104:107]\n\t" \
  "v_mfma_f32_16x16x32_bf16 v[108:111], v[180:183], v[160:163], v[108:111]\n\t" \
  "s_waitcnt vmcnt(9) lgkmcnt(0)\n\t" \
  "s_barrier\n\t" \
  "v_mfma_f32_16x16x32_bf16 v[32:35], v[128:131], v[112:115], v[32:35]\n\t" \
  "ds_read_b128 v[148:151], %[ar] offset:36864\n\t" \
  "v_mfma_f32_16x16x32_bf16 v[36:39], v[132:135], v[112:115], v[36:39]\n\t" \
  "ds_read_b64_tr_b16 v[164:165], %[bp0] offset:36864\n\t" \
  "v_mfma_f32_16x16x32_bf16 v[40:43], v[136:139], v[112:115], v[40:43]\n\t" \
  "ds_read_b64_tr_b16 v[166:167], %[bp0] offset:39424\n\t" \
  "s_add_u32 m0, %[da], 0\n\t" \
  "s_add_u32 s64, s64, 4096\n\t" \
  "s_addc_u32 s65, s65, 0\n\t" \
  "global_load_lds_dwordx4 %[lin], s[64:65]\n\t" \
  "v_mfma_f32_16x16x32_bf16 v[44:47], v[140:143], v[112:115], v[44:47]\n\t" \
  "ds_read_b64_tr_b16 v[168:169], %[bp1] offset:36864\n\t" \
  "v_mfma_f32_16x16x32_bf16 v[48:51], v[144:147], v[112:115], v[48:51]\n\t" \
  "ds_read_b64_tr_b16 v[170:171], %[bp1] offset:39424\n\t" \
  "v_mfma_f32_16x16x32_bf16 v[52:55], v[128:131], v[116:119], v[52:55]\n\t" \
  "ds_read_b64_tr_b16 v[172:173], %[bp0] offset:37120\n\t" \
  "global_load_lds_dwordx4 %[lin], s[64:65] offset:1024\n\t" \
  "v_mfma_f32_16x16x32_bf16 v[56:59], v[132:135], v[116:119], v[56:59]\n\t" \
  "ds_read_b64_tr_b16 v[174:175], %[bp0] offset:39680\n\t" \
  "v_mfma_f32_16x16x32_bf16 v[60:63], v[136:139], v[116:119], v[60:63]\n\t" \
  "ds_read_b64_tr_b16 v[176:177], %[bp1] offset:37120\n\t" \
  "v_mfma_f32_16x16x32_bf16 v[64:67], v[140:143], v[116:119], v[64:67]\n\t" \
  "ds_read_b64_tr_b16 v[178:179], %[bp1] offset:39680\n\t" \
  "s_add_u32 m0, %[db], 0\n\t" \
  "s_add_u32 s66, s66, 81920\n\t" \
  "s_addc_u32 s67, s67, 0\n\t" \
  "global_load_lds_dwordx4 %[vb0], s[66:67]\n\t" \
  "v_mfma_f32_16x16x32_bf16 v[68:71], v[144:147], v[116:119], v[68:71]\n\t" \
  "ds_read_b64_tr_b16 v[180:181], %[bp0] offset:37376\n\t" \
  "v_mfma_f32_16x16x32_bf16 v[72:75], v[128:131], v[120:123], v[72:75]\n\t" \
  "ds_read_b64_tr_b16 v[182:183], %[bp0] offset:39936\n\t" \
  "v_mfma_f32_16x16x32_bf16 v[76:79], v[132:135], v[120:123], v[76:79]\n\t" \
  "ds_read_b128 v[152:155], %[ar] offset:37120\n\t" \
  "s_add_u32 m0, %[db], 8192\n\t" \
  "s_nop 0\n\t" \
  "global_load_lds_dwordx4 %[vb1], s[66:67]\n\t" \
  "v_mfma_f32_16x16x32_bf16 v[80:83], v[136:139], v[120:123], v[80:83]\n\t" \
  "ds_read_b128 v[156:159], %[ar] offset:40960\n\t" \
  "v_mfma_f32_16x16x32_bf16 v[84:87], v[140:143], v[120:123], v[84:87]\n\t" \
  "ds_read_b128 v[160:163], %[ar] offset:41216\n\t" \
  "v_mfma_f32_16x16x32_bf16 v[88:91], v[144:147], v[120:123], v[88:91]\n\t" \
  "s_add_u32 m0, %[db2], 0\n\t" \
  "s_cmp_lg_u32 %[kh], 0\n\t" \
  "s_cbranch_scc1 L_S13%=\n\t" \
  "global_load_lds_dwordx4 %[vb2], s[66:67]\n\t" \
  "L_S13%=:\n\t" \
  "v_mfma_f32_16x16x32_bf16 v[92:95], v[128:131], v[124:127], v[92:95]\n\t" \
  "v_mfma_f32_16x16x32_bf16 v[96:99], v[132:135], v[124:127], v[96:99]\n\t" \
  "v_mfma_f32_16x16x32_bf16 v[100:103], v[136:139], v[124:127], v[100:103]\n\t" \
  "v_mfma_f32_16x16x32_bf16 v[104:107], v[140:143], v[124:127], v[104:107]\n\t" \
  "v_mfma_f32_16x16x32_bf16 v[108:111], v[144:147], v[124:127], v[108:111]\n\t" \
  "s_waitcnt vmcnt(9) lgkmcnt(0)\n\t" \
  "s_barrier\n\t" \
  "v_mfma_f32_16x16x32_bf16 v[32:35], v[164:167], v[148:151], v[32:35]\n\t" \
  "ds_read_b128 v[112:115], v184 offset:0\n\t" \
  "v_mfma_f32_16x16x32_bf16 v[36:39], v[168:171], v[148:151], v[36:39]\n\t" \
  "ds_read_b64_tr_b16 v[128:129], v185 offset:0\n\t" \
  "v_mfma_f32_16x16x32_bf16 v[40:43], v[172:175], v[148:151], v[40:43]\n\t" \
  "ds_read_b64_tr_b16 v[130:131], v185 offset:2560\n\t" \
  "s_add_u32 m0, %[da], 36864\n\t" \
  "s_add_u32 s64, s64, 4096\n\t" \
  "s_addc_u32 s65, s65, 0\n\t" \
  "global_load_lds_dwordx4 %[lin], s[64:65]\n\t" \
  "v_mfma_f32_16x16x32_bf16 v[44:47], v[176:179], v[148:151], v[44:47]\n\t" \
  "ds_read_b64_tr_b16 v[132:133], v186 offset:0\n\t" \
  "v_mfma_f32_16x16x32_bf16 v[48:51], v[180:183], v[148:151], v[48:51]\n\t" \
  "ds_read_b64_tr_b16 v[134:135], v186 offset:2560\n\t" \
  "v_mfma_f32_16x16x32_bf16 v[52:55], v[164:167], v[152:155], v[52:55]\n\t" \
  "ds_read_b64_tr_b16 v[136:137], v185 offset:256\n\t" \
  "global_load_lds_dwordx4 %[lin], s[64:65] offset:1024\n\t" \
  "v_mfma_f32_16x16x32_bf16 v[56:59], v[168:171], v[152:155], v[56:59]\n\t" \
  "ds_read_b64_tr_b16 v[138:139], v185 offset:2816\n\t" \
  "v_mfma_f32_16x16x32_bf16 v[60:63], v[172:175], v[152:155], v[60:63]\n\t" \
  "ds_read_b64_tr_b16 v[140:141], v186 offset:256\n\t" \
  "v_mfma_f32_16x16x32_bf16 v[64:67], v[176:179], v[152:155], v[64:67]\n\t" \
  "ds_read_b64_tr_b16 v[142:143], v186 offset:2816\n\t" \
  "s_add_u32 m0, %[db], 36864\n\t" \
  "s_add_u32 s66, s66, 81920\n\t" \
  "s_addc_u32 s67, s67, 0\n\t" \
  "global_load_lds_dwordx4 %[vb0], s[66:67]\n\t" \
  "v_mfma_f32_16x16x32_bf16 v[68:71], v[180:183], v[152:155], v[68:71]\n\t" \
  "ds_read_b64_tr_b16 v[144:145], v185 offset:512\n\t" \
  "v_mfma_f32_16x16x32_bf16 v[72:75], v[164:167], v[156:159], v[72:75]\n\t" \
  "ds_read_b64_tr_b16 v[146:147], v185 offset:3072\n\t" \
  "v_mfma_f32_16x16x32_bf16 v[76:79], v[168:171], v[156:159], v[76:79]\n\t" \
  "ds_read_b128 v[116:119], v184 offset:256\n\t" \
  "s_add_u32 m0, %[db], 45056\n\t" \
  "s_nop 0\n\t" \
  "global_load_lds_dwordx4 %[vb1], s[66:67]\n\t" \
  "v_mfma_f32_16x16x32_bf16 v[80:83], v[172:175], v[156:159], v[80:83]\n\t" \
  "ds_read_b128 v[120:123], v184 offset:4096\n\t" \
  "v_mfma_f32_16x16x32_bf16 v[84:87], v[176:179], v[156:159], v[84:87]\n\t" \
  "ds_read_b128 v[124:127], v184 offset:4352\n\t" \
  "v_mfma_f32_16x16x32_bf16 v[88:91], v[180:183], v[156:159], v[88:91]\n\t" \
  "s_add_u32 m0, %[db2], 36864\n\t" \
  "s_cmp_lg_u32 %[kh], 1\n\t" \
  "s_cbranch_scc1 L_S14%=\n\t" \
  "global_load_lds_dwordx4 %[vb2], s[66:67]\n\t" \
  "L_S14%=:\n\t" \
  "v_mfma_f32_16x16x32_bf16 v[92:95], v[164:167], v[160:163], v[92:95]\n\t" \
  "v_mfma_f32_16x16x32_bf16 v[96:99], v[168:171], v[160:163], v[96:99]\n\t" \
  "v_mfma_f32_16x16x32_bf16 v[100:103], v[172:175], v[160:163], v[100:103]\n\t" \
  "v_mfma_f32_16x16x32_bf16 v[104:107], v[176:179], v[160:163], v[104:107]\n\t" \
  "v_mfma_f32_16x16x32_bf16 v[108:111], v[180:183], v[160:163], v[108:111]\n\t" \
  "s_waitcnt vmcnt(9) lgkmcnt(0)\n\t" \
  "s_barrier\n\t" \
  "v_mfma_f32_16x16x32_bf16 v[32:35], v[128:131], v[112:115], v[32:35]\n\t" \
  "ds_read_b128 v[148:151], v184 offset:36864\n\t" \
  "v_mfma_f32_16x16x32_bf16 v[36:39], v[132:135], v[112:115], v[36:39]\n\t" \
  "ds_read_b64_tr_b16 v[164:165], v185 offset:36864\n\t" \
  "v_mfma_f32_16x16x32_bf16 v[40:43], v[136:139], v[112:115], v[40:43]\n\t" \
  "ds_read_b64_tr_b16 v[166:167], v185 offset:39424\n\t" \
  "s_add_u32 m0, %[da], 73728\n\t" \
  "s_add_u32 s64, s64, 4096\n\t" \
  "s_addc_u32 s65, s65, 0\n\t" \
  "global_load_lds_dwordx4 %[lin], s[64:65]\n\t" \
  "v_mfma_f32_16x16x32_bf16 v[44:47], v[140:143], v[112:115], v[44:47]\n\t" \
  "ds_read_b64_tr_b16 v[168:169], v186 offset:36864\n\t" \
  "v_mfma_f32_16x16x32_bf16 v[48:51], v[144:147], v[112:115], v[48:51]\n\t" \
  "ds_read_b64_tr_b16 v[170:171], v186 offset:39424\n\t" \
  "v_mfma_f32_16x16x32_bf16 v[52:55], v[128:131], v[116:119], v[52:55]\n\t" \
  "ds_read_b64_tr_b16 v[172:173], v185 offset:37120\n\t" \
  "global_load_lds_dwordx4 %[lin], s[64:65] offset:1024\n\t" \
  "v_mfma_f32_16x16x32_bf16 v[56:59], v[132:135], v[116:119], v[56:59]\n\t" \
  "ds_read_b64_tr_b16 v[174:175], v185 offset:39680\n\t" \
  "v_mfma_f32_16x16x32_bf16 v[60:63], v[136:139], v[116:119], v[60:63]\n\t" \
  "ds_read_b64_tr_b16 v[176:177], v186 offset:37120\n\t" \
  "v_mfma_f32_16x16x32_bf16 v[64:67], v[140:143], v[116:119], v[64:67]\n\t" \
  "ds_read_b64_tr_b16 v[178:179], v186 offset:39680\n\t" \
  "s_add_u32 m0, %[db], 73728\n\t" \
  "s_add_u32 s66, s66, 81920\n\t" \
  "s_addc_u32 s67, s67, 0\n\t" \
  "global_load_lds_dwordx4 %[vb0], s[66:67]\n\t" \
  "v_mfma_f32_16x16x32_bf16 v[68:71], v[144:147], v[116:119], v[68:71]\n\t" \
  "ds_read_b64_tr_b16 v[180:181], v185 offset:37376\n\t" \
  "v_mfma_f32_16x16x32_bf16 v[72:75], v[128:131], v[120:123], v[72:75]\n\t" \
  "ds_read_b64_tr_b16 v[182:183], v185 offset:39936\n\t" \
  "v_mfma_f32_16x16x32_bf16 v[76:79], v[132:135], v[120:123], v[76:79]\n\t" \
  "ds_read_b128 v[152:155], v184 offset:37120\n\t" \
  "s_add_u32 m0, %[db], 81920\n\t" \
  "s_nop 0\n\t" \
  "global_load_lds_dwordx4 %[vb1], s[66:67]\n\t" \
  "v_mfma_f32_16x16x32_bf16 v[80:83], v[136:139], v[120:123], v[80:83]\n\t" \
  "ds_read_b128 v[156:159], v184 offset:40960\n\t" \
  "v_mfma_f32_16x16x32_bf16 v[84:87], v[140:143], v[120:123], v[84:87]\n\t" \
  "ds_read_b128 v[160:163], v184 offset:41216\n\t" \
  "v_mfma_f32_16x16x32_bf16 v[88:91], v[144:147], v[120:123], v[88:91]\n\t" \
  "s_add_u32 m0, %[db2], 73728\n\t" \
  "s_cmp_lg_u32 %[kh], 0\n\t" \
  "s_cbranch_scc1 L_S15%=\n\t" \
  "global_load_lds_dwordx4 %[vb2], s[66:67]\n\t" \
  "L_S15%=:\n\t" \
  "v_mfma_f32_16x16x32_bf16 v[92:95], v[128:131], v[124:127], v[92:95]\n\t" \
  "v_mfma_f32_16x16x32_bf16 v[96:99], v[132:135], v[124:127], v[96:99]\n\t" \
  "v_mfma_f32_16x16x32_bf16 v[100:103], v[136:139], v[124:127], v[100:103]\n\t" \
  "v_mfma_f32_16x16x32_bf16 v[104:107], v[140:143], v[124:127], v[104:107]\n\t" \
  "v_mfma_f32_16x16x32_bf16 v[108:111], v[144:147], v[124:127], v[108:111]\n\t" \
  "s_waitcnt vmcnt(9) lgkmcnt(0)\n\t" \
  "s_barrier\n\t" \
  "v_mfma_f32_16x16x32_bf16 v[32:35], v[164:167], v[148:151], v[32:35]\n\t" \
  "ds_read_b128 v[112:115], %[ar] offset:0\n\t" \
  "v_mfma_f32_16x16x32_bf16 v[36:39], v[168:171], v[148:151], v[36:39]\n\t" \
  "ds_read_b64_tr_b16 v[128:129], %[bp0] offset:0\n\t" \
  "v_mfma_f32_16x16x32_bf16 v[40:43], v[172:175], v[148:151], v[40:43]\n\t" \
  "ds_read_b64_tr_b16 v[130:131], %[bp0] offset:2560\n\t" \
  "s_add_u32 m0, %[da], 110592\n\t" \
  "s_add_u32 s64, s64, 4096\n\t" \
  "s_addc_u32 s65, s65, 0\n\t" \
  "global_load_lds_dwordx4 %[lin], s[64:65]\n\t" \
  "v_mfma_f32_16x16x32_bf16 v[44:47], v[176:179], v[148:151], v[44:47]\n\t" \
  "ds_read_b64_tr_b16 v[132:133], %[bp1] offset:0\n\t" \
  "v_mfma_f32_16x16x32_bf16 v[48:51], v[180:183], v[148:151], v[48:51]\n\t" \
  "ds_read_b64_tr_b16 v[134:135], %[bp1] offset:2560\n\t" \
  "v_mfma_f32_16x16x32_bf16 v[52:55], v[164:167], v[152:155], v[52:55]\n\t" \
  "ds_read_b64_tr_b16 v[136:137], %[bp0] offset:256\n\t" \
  "global_load_lds_dwordx4 %[lin], s[64:65] offset:1024\n\t" \
  "v_mfma_f32_16x16x32_bf16 v[56:59], v[168:171], v[152:155], v[56:59]\n\t" \
  "ds_read_b64_tr_b16 v[138:139], %[bp0] offset:2816\n\t" \
  "v_mfma_f32_16x16x32_bf16 v[60:63], v[172:175], v[152:155], v[60:63]\n\t" \
  "ds_read_b64_tr_b16 v[140:141], %[bp1] offset:256\n\t" \
  "v_mfma_f32_16x16x32_bf16 v[64:67], v[176:179], v[152:155], v[64:67]\n\t" \
  "ds_read_b64_tr_b16 v[142:143], %[bp1] offset:2816\n\t" \
  "s_add_u32 m0, %[db], 110592\n\t" \
  "s_add_u32 s66, s66, 81920\n\t" \
  "s_addc_u32 s67, s67, 0\n\t" \
  "global_load_lds_dwordx4 %[vb0], s[66:67]\n\t" \
  "v_mfma_f32_16x16x32_bf16 v[68:71], v[180:183], v[152:155], v[68:71]\n\t" \
  "ds_read_b64_tr_b16 v[144:145], %[bp0] offset:512\n\t" \
  "v_mfma_f32_16x16x32_bf16 v[72:75], v[164:167], v[156:159], v[72:75]\n\t" \
  "ds_read_b64_tr_b16 v[146:147], %[bp0] offset:3072\n\t" \
  "v_mfma_f32_16x16x32_bf16 v[76:79], v[168:171], v[156:159], v[76:79]\n\t" \
  "ds_read_b128 v[116:119], %[ar] offset:256\n\t" \
  "s_add_u32 m0, %[db], 118784\n\t" \
  "s_nop 0\n\t" \
  "global_load_lds_dwordx4 %[vb1], s[66:67]\n\t" \
  "v_mfma_f32_16x16x32_bf16 v[80:83], v[172:175], v[156:159], v[80:83]\n\t" \
  "ds_read_b128 v[120:123], %[ar] offset:4096\n\t" \
  "v_mfma_f32_16x16x32_bf16 v[84:87], v[176:179], v[156:159], v[84:87]\n\t" \
  "ds_read_b128 v[124:127], %[ar] offset:4352\n\t" \
  "v_mfma_f32_16x16x32_bf16 v[88:91], v[180:183], v[156:159], v[88:91]\n\t" \
  "s_add_u32 m0, %[db2], 110592\n\t" \
  "s_cmp_lg_u32 %[kh], 1\n\t" \
  "s_cbranch_scc1 L_S16%=\n\t" \
  "global_load_lds_dwordx4 %[vb2], s[66:67]\n\t" \
  "L_S16%=:\n\t" \
  "v_mfma_f32_16x16x32_bf16 v[92:95], v[164:167], v[160:163], v[92:95]\n\t" \
  "v_mfma_f32_16x16x32_bf16 v[96:99], v[168:171], v[160:163], v[96:99]\n\t" \
  "v_mfma_f32_16x16x32_bf16 v[100:103], v[172:175], v[160:163], v[100:103]\n\t" \
  "v_mfma_f32_16x16x32_bf16 v[104:107], v[176:179], v[160:163], v[104:107]\n\t" \
  "v_mfma_f32_16x16x32_bf16 v[108:111], v[180:183], v[160:163], v[108:111]\n\t" \
  "s_waitcnt vmcnt(9) lgkmcnt(0)\n\t" \
  "s_barrier\n\t" \
  "v_mfma_f32_16x16x32_bf16 v[32:35], v[128:131], v[112:115], v[32:35]\n\t" \
  "ds_read_b128 v[148:151], %[ar] offset:36864\n\t" \
  "v_mfma_f32_16x16x32_bf16 v[36:39], v[132:135], v[112:115], v[36:39]\n\t" \
  "ds_read_b64_tr_b16 v[164:165], %[bp0] offset:36864\n\t" \
  "v_mfma_f32_16x16x32_bf16 v[40:43], v[136:139], v[112:115], v[40:43]\n\t" \
  "ds_read_b64_tr_b16 v[166:167], %[bp0] offset:39424\n\t" \
  "v_mfma_f32_16x16x32_bf16 v[44:47], v[140:143], v[112:115], v[44:47]\n\t" \
  "ds_read_b64_tr_b16 v[168:169], %[bp1] offset:36864\n\t" \
  "v_mfma_f32_16x16x32_bf16 v[48:51], v[144:147], v[112:115], v[48:51]\n\t" \
  "ds_read_b64_tr_b16 v[170:171], %[bp1] offset:39424\n\t" \
  "v_mfma_f32_16x16x32_bf16 v[52:55], v[128:131], v[116:119], v[52:55]\n\t" \
  "ds_read_b64_tr_b16 v[172:173], %[bp0] offset:37120\n\t" \
  "v_mfma_f32_16x16x32_bf16 v[56:59], v[132:135], v[116:119], v[56:59]\n\t" \
  "ds_read_b64_tr_b16 v[174:175], %[bp0] offset:39680\n\t" \
  "v_mfma_f32_16x16x32_bf16 v[60:63], v[136:139], v[116:119], v[60:63]\n\t" \
  "ds_read_b64_tr_b16 v[176:177], %[bp1] offset:37120\n\t" \
  "v_mfma_f32_16x16x32_bf16 v[64:67], v[140:143], v[116:119], v[64:67]\n\t" \
  "ds_read_b64_tr_b16 v[178:179], %[bp1] offset:39680\n\t" \
  "v_mfma_f32_16x16x32_bf16 v[68:71], v[144:147], v[116:119], v[68:71]\n\t" \
  "ds_read_b64_tr_b16 v[180:181], %[bp0] offset:37376\n\t" \
  "v_mfma_f32_16x16x32_bf16 v[72:75], v[128:131], v[120:123], v[72:75]\n\t" \
  "ds_read_b64_tr_b16 v[182:183], %[bp0] offset:39936\n\t" \
  "v_mfma_f32_16x16x32_bf16 v[76:79], v[132:135], v[120:123], v[76:79]\n\t" \
  "ds_read_b128 v[152:155], %[ar] offset:37120\n\t" \
  "v_mfma_f32_16x16x32_bf16 v[80:83], v[136:139], v[120:123], v[80:83]\n\t" \
  "ds_read_b128 v[156:159], %[ar] offset:40960\n\t" \
  "v_mfma_f32_16x16x32_bf16 v[84:87], v[140:143], v[120:123], v[84:87]\n\t" \
  "ds_read_b128 v[160:163], %[ar] offset:41216\n\t" \
  "v_mfma_f32_16x16x32_bf16 v[88:91], v[144:147], v[120:123], v[88:91]\n\t" \
  "v_mfma_f32_16x16x32_bf16 v[92:95], v[128:131], v[124:127], v[92:95]\n\t" \
  "v_mfma_f32_16x16x32_bf16 v[96:99], v[132:135], v[124:127], v[96:99]\n\t" \
  "v_mfma_f32_16x16x32_bf16 v[100:103], v[136:139], v[124:127], v[100:103]\n\t" \
  "v_mfma_f32_16x16x32_bf16 v[104:107], v[140:143], v[124:127], v[104:107]\n\t" \
  "v_mfma_f32_16x16x32_bf16 v[108:111], v[144:147], v[124:127], v[108:111]\n\t" \
  "s_waitcnt vmcnt(4) lgkmcnt(0)\n\t" \
  "s_barrier\n\t" \
  "v_mfma_f32_16x16x32_bf16 v[32:35], v[164:167], v[148:151], v[32:35]\n\t" \
  "ds_read_b128 v[112:115], v184 offset:0\n\t" \
  "v_mfma_f32_16x16x32_bf16 v[36:39], v[168:171], v[148:151], v[36:39]\n\t" \
  "ds_read_b64_tr_b16 v[128:129], v185 offset:0\n\t" \
  "v_mfma_f32_16x16x32_bf16 v[40:43], v[172:175], v[148:151], v[40:43]\n\t" \
  "ds_read_b64_tr_b16 v[130:131], v185 offset:2560\n\t" \
  "v_mfma_f32_16x16x32_bf16 v[44:47], v[176:179], v[148:151], v[44:47]\n\t" \
  "ds_read_b64_tr_b16 v[132:133], v186 offset:0\n\t" \
  "v_mfma_f32_16x16x32_bf16 v[48:51], v[180:183], v[148:151], v[48:51]\n\t" \
  "ds_read_b64_tr_b16 v[134:135], v186 offset:2560\n\t" \
  "v_mfma_f32_16x16x32_bf16 v[52:55], v[164:167], v[152:155], v[52:55]\n\t" \
  "ds_read_b64_tr_b16 v[136:137], v185 offset:256\n\t" \
  "v_mfma_f32_16x16x32_bf16 v[56:59], v[168:171], v[152:155], v[56:59]\n\t" \
  "ds_read_b64_tr_b16 v[138:139], v185 offset:2816\n\t" \
  "v_mfma_f32_16x16x32_bf16 v[60:63], v[172:175], v[152:155], v[60:63]\n\t" \
  "ds_read_b64_tr_b16 v[140:141], v186 offset:256\n\t" \
  "v_mfma_f32_16x16x32_bf16 v[64:67], v[176:179], v[152:155], v[64:67]\n\t" \
  "ds_read_b64_tr_b16 v[142:143], v186 offset:2816\n\t" \
  "v_mfma_f32_16x16x32_bf16 v[68:71], v[180:183], v[152:155], v[68:71]\n\t" \
  "ds_read_b64_tr_b16 v[144:145], v185 offset:512\n\t" \
  "v_mfma_f32_16x16x32_bf16 v[72:75], v[164:167], v[156:159], v[72:75]\n\t" \
  "ds_read_b64_tr_b16 v[146:147], v185 offset:3072\n\t" \
  "v_mfma_f32_16x16x32_bf16 v[76:79], v[168:171], v[156:159], v[76:79]\n\t" \
  "ds_read_b128 v[116:119], v184 offset:256\n\t" \
  "v_mfma_f32_16x16x32_bf16 v[80:83], v[172:175], v[156:159], v[80:83]\n\t" \
  "ds_read_b128 v[120:123], v184 offset:4096\n\t" \
  "v_mfma_f32_16x16x32_bf16 v[84:87], v[176:179], v[156:159], v[84:87]\n\t" \
  "ds_read_b128 v[124:127], v184 offset:4352\n\t" \
  "v_mfma_f32_16x16x32_bf16 v[88:91], v[180:183], v[156:159], v[88:91]\n\t" \
  "v_mfma_f32_16x16x32_bf16 v[92:95], v[164:167], v[160:163], v[92:95]\n\t" \
  "v_mfma_f32_16x16x32_bf16 v[96:99], v[168:171], v[160:163], v[96:99]\n\t" \
  "v_mfma_f32_16x16x32_bf16 v[100:103], v[172:175], v[160:163], v[100:103]\n\t" \
  "v_mfma_f32_16x16x32_bf16 v[104:107], v[176:179], v[160:163], v[104:107]\n\t" \
  "v_mfma_f32_16x16x32_bf16 v[108:111], v[180:183], v[160:163], v[108:111]\n\t" \
  "s_waitcnt vmcnt(0) lgkmcnt(0)\n\t" \
  "s_barrier\n\t" \
  "v_mfma_f32_16x16x32_bf16 v[32:35], v[128:131], v[112:115], v[32:35]\n\t" \
  "ds_read_b128 v[148:151], v184 offset:36864\n\t" \
  "v_mfma_f32_16x16x32_bf16 v[36:39], v[132:135], v[112:115], v[36:39]\n\t" \
  "ds_read_b64_tr_b16 v[164:165], v185 offset:36864\n\t" \
  "global_load_dwordx4 v[192:195], %[l16], %[xr] offset:0\n\t" \
  "v_mfma_f32_16x16x32_bf16 v[40:43], v[136:139], v[112:115], v[40:43]\n\t" \
  "ds_read_b64_tr_b16 v[166:167], v185 offset:39424\n\t" \
  "v_mfma_f32_16x16x32_bf16 v[44:47], v[140:143], v[112:115], v[44:47]\n\t" \
  "ds_read_b64_tr_b16 v[168:169], v186 offset:36864\n\t" \
  "global_load_dwordx4 v[196:199], %[l16], %[xr] offset:640\n\t" \
  "v_mfma_f32_16x16x32_bf16 v[48:51], v[144:147], v[112:115], v[48:51]\n\t" \
  "ds_read_b64_tr_b16 v[170:171], v186 offset:39424\n\t" \
  "v_mfma_f32_16x16x32_bf16 v[52:55], v[128:131], v[116:119], v[52:55]\n\t" \
  "ds_read_b64_tr_b16 v[172:173], v185 offset:37120\n\t" \
  "global_load_dwordx4 v[200:203], %[l16], %[wr] offset:0\n\t" \
  "v_mfma_f32_16x16x32_bf16 v[56:59], v[132:135], v[116:119], v[56:59]\n\t" \
  "ds_read_b64_tr_b16 v[174:175], v185 offset:39680\n\t" \
  "v_mfma_f32_16x16x32_bf16 v[60:63], v[136:139], v[116:119], v[60:63]\n\t" \
  "ds_read_b64_tr_b16 v[176:177], v186 offset:37120\n\t" \
  "global_load_dwordx4 v[204:207], %[l16], %[wr] offset:640\n\t" \
  "v_mfma_f32_16x16x32_bf16 v[64:67], v[140:143], v[116:119], v[64:67]\n\t" \
  "ds_read_b64_tr_b16 v[178:179], v186 offset:39680\n\t" \
  "v_mfma_f32_16x16x32_bf16 v[68:71], v[144:147], v[116:119], v[68:71]\n\t" \
  "ds_read_b64_tr_b16 v[180:181], v185 offset:37376\n\t" \
  "global_load_dwordx4 v[208:211], %[l16], %[mb] offset:-2240\n\t" \
  "v_mfma_f32_16x16x32_bf16 v[72:75], v[128:131], v[120:123], v[72:75]\n\t" \
  "ds_read_b64_tr_b16 v[182:183], v185 offset:39936\n\t" \
  "v_mfma_f32_16x16x32_bf16 v[76:79], v[132:135], v[120:123], v[76:79]\n\t" \
  "ds_read_b128 v[152:155], v184 offset:37120\n\t" \
  "global_load_dwordx4 v[212:215], %[l16], %[mb] offset:-1600\n\t" \
  "v_mfma_f32_16x16x32_bf16 v[80:83], v[136:139], v[120:123], v[80:83]\n\t" \
  "ds_read_b128 v[156:159], v184 offset:40960\n\t" \
  "v_mfma_f32_16x16x32_bf16 v[84:87], v[140:143], v[120:123], v[84:87]\n\t" \
  "ds_read_b128 v[160:163], v184 offset:41216\n\t" \
  "v_mfma_f32_16x16x32_bf16 v[88:91], v[144:147], v[120:123], v[88:91]\n\t" \
  "v_mfma_f32_16x16x32_bf16 v[92:95], v[128:131], v[124:127], v[92:95]\n\t" \
  "v_mfma_f32_16x16x32_bf16 v[96:99], v[132:135], v[124:127], v[96:99]\n\t" \
  "v_mfma_f32_16x16x32_bf16 v[100:103], v[136:139], v[124:127], v[100:103]\n\t" \
  "v_mfma_f32_16x16x32_bf16 v[104:107], v[140:143], v[124:127], v[104:107]\n\t" \
  "v_mfma_f32_16x16x32_bf16 v[108:111], v[144:147], v[124:127], v[108:111]\n\t" \
  "s_waitcnt vmcnt(63) lgkmcnt(0)\n\t" \
  "s_barrier\n\t" \
  "v_mfma_f32_16x16x32_bf16 v[32:35], v[164:167], v[148:151], v[32:35]\n\t" \
  "v_mfma_f32_16x16x32_bf16 v[36:39], v[168:171], v[148:151], v[36:39]\n\t" \
  "global_load_dwordx4 v[216:219], %[l16], %[mb] offset:-960\n\t" \
  "v_mfma_f32_16x16x32_bf16 v[40:43], v[172:175], v[148:151], v[40:43]\n\t" \
  "v_mfma_f32_16x16x32_bf16 v[44:47], v[176:179], v[148:151], v[44:47]\n\t" \
  "global_load_dwordx4 v[220:223], %[l16], %[mb] offset:-320\n\t" \
  "v_mfma_f32_16x16x32_bf16 v[48:51], v[180:183], v[148:151], v[48:51]\n\t" \
  "v_mfma_f32_16x16x32_bf16 v[52:55], v[164:167], v[152:155], v[52:55]\n\t" \
  "global_load_dwordx4 v[224:227], %[l16], %[mb] offset:320\n\t" \
  "v_mfma_f32_16x16x32_bf16 v[56:59], v[168:171], v[152:155], v[56:59]\n\t" \
  "v_mfma_f32_16x16x32_bf16 v[60:63], v[172:175], v[152:155], v[60:63]\n\t" \
  "global_load_dwordx4 v[228:231], %[l16], %[mb] offset:960\n\t" \
  "v_mfma_f32_16x16x32_bf16 v[64:67], v[176:179], v[152:155], v[64:67]\n\t" \
  "v_mfma_f32_16x16x32_bf16 v[68:71], v[180:183], v[152:155], v[68:71]\n\t" \
  "global_load_dwordx4 v[232:235], %[l16], %[mb] offset:1600\n\t" \
  "v_mfma_f32_16x16x32_bf16 v[72:75], v[164:167], v[156:159], v[72:75]\n\t" \
  "v_mfma_f32_16x16x32_bf16 v[76:79], v[168:171], v[156:159], v[76:79]\n\t" \
  "global_load_dwordx4 v[236:239], %[l16], %[mb] offset:2240\n\t" \
  "v_mfma_f32_16x16x32_bf16 v[80:83], v[172:175], v[156:159], v[80:83]\n\t" \
  "v_mfma_f32_16x16x32_bf16 v[84:87], v[176:179], v[156:159], v[84:87]\n\t" \
  "v_mfma_f32_16x16x32_bf16 v[88:91], v[180:183], v[156:159], v[88:91]\n\t" \
  "v_mfma_f32_16x16x32_bf16 v[92:95], v[164:167], v[160:163], v[92:95]\n\t" \
  "v_mfma_f32_16x16x32_bf16 v[96:99], v[168:171], v[160:163], v[96:99]\n\t" \
  "v_mfma_f32_16x16x32_bf16 v[100:103], v[172:175], v[160:163], v[100:103]\n\t" \
  "v_mfma_f32_16x16x32_bf16 v[104:107], v[176:179], v[160:163], v[104:107]\n\t" \
  "v_mfma_f32_16x16x32_bf16 v[108:111], v[180:183], v[160:163], v[108:111]\n\t" \
  "s_cmp_lg_u32 %[kh], 0\n\t" \
  "s_cbranch_scc1 L_K1%=\n\t" \
  "s_nop 7\n\t" \
  "ds_write_b128 %[xo], v[72:75] offset:0\n\t" \
  "ds_write_b128 %[xo], v[76:79] offset:1024\n\t" \
  "ds_write_b128 %[xo], v[80:83] offset:2048\n\t" \
  "ds_write_b128 %[xo], v[84:87] offset:3072\n\t" \
  "ds_write_b128 %[xo], v[88:91] offset:4096\n\t" \
  "ds_write_b128 %[xo], v[92:95] offset:5120\n\t" \
  "ds_write_b128 %[xo], v[96:99] offset:6144\n\t" \
  "ds_write_b128 %[xo], v[100:103] offset:7168\n\t" \
  "ds_write_b128 %[xo], v[104:107] offset:8192\n\t" \
  "ds_write_b128 %[xo], v[108:111] offset:9216\n\t" \
  "s_waitcnt lgkmcnt(0)\n\t" \
  "s_barrier\n\t" \
  "ds_read_b128 v[112:115], %[xi] offset:0\n\t" \
  "ds_read_b128 v[116:119], %[xi] offset:1024\n\t" \
  "ds_read_b128 v[120:123], %[xi] offset:2048\n\t" \
  "ds_read_b128 v[124:127], %[xi] offset:3072\n\t" \
  "ds_read_b128 v[128:131], %[xi] offset:4096\n\t" \
  "ds_read_b128 v[132:135], %[xi] offset:5120\n\t" \
  "ds_read_b128 v[136:139], %[xi] offset:6144\n\t" \
  "ds_read_b128 v[140:143], %[xi] offset:7168\n\t" \
  "ds_read_b128 v[144:147], %[xi] offset:8192\n\t" \
  "ds_read_b128 v[148:151], %[xi] offset:9216\n\t" \
  "s_waitcnt lgkmcnt(9)\n\t" \
  "v_add_f32_e32 v32, v32, v112\n\t" \
  "v_add_f32_e32 v33, v33, v113\n\t" \
  "v_add_f32_e32 v34, v34, v114\n\t" \
  "v_add_f32_e32 v35, v35, v115\n\t" \
  "v_cvt_pk_bf16_f32 v152, v32, v33\n\t" \
  "v_cvt_pk_bf16_f32 v153, v34, v35\n\t" \
  "s_waitcnt lgkmcnt(8)\n\t" \
  "v_add_f32_e32 v36, v36, v116\n\t" \
  "v_add_f32_e32 v37, v37, v117\n\t" \
  "v_add_f32_e32 v38, v38, v118\n\t" \
  "v_add_f32_e32 v39, v39, v119\n\t" \
  "v_cvt_pk_bf16_f32 v154, v36, v37\n\t" \
  "v_cvt_pk_bf16_f32 v155, v38, v39\n\t" \
  "s_waitcnt lgkmcnt(7)\n\t" \
  "v_add_f32_e32 v40, v40, v120\n\t" \
  "v_add_f32_e32 v41, v41, v121\n\t" \
  "v_add_f32_e32 v42, v42, v122\n\t" \
  "v_add_f32_e32 v43, v43, v123\n\t" \
  "v_cvt_pk_bf16_f32 v156, v40, v41\n\t" \
  "v_cvt_pk_bf16_f32 v157, v42, v43\n\t" \
  "s_waitcnt lgkmcnt(6)\n\t" \
  "v_add_f32_e32 v44, v44, v124\n\t" \
  "v_add_f32_e32 v45, v45, v125\n\t" \
  "v_add_f32_e32 v46, v46, v126\n\t" \
  "v_add_f32_e32 v47, v47, v127\n\t" \
  "v_cvt_pk_bf16_f32 v158, v44, v45\n\t" \
  "v_cvt_pk_bf16_f32 v159, v46, v47\n\t" \
  "s_waitcnt lgkmcnt(5)\n\t" \
  "v_add_f32_e32 v48, v48, v128\n\t" \
  "v_add_f32_e32 v49, v49, v129\n\t" \
  "v_add_f32_e32 v50, v50, v130\n\t" \
  "v_add_f32_e32 v51, v51, v131\n\t" \
  "v_cvt_pk_bf16_f32 v160, v48, v49\n\t" \
  "v_cvt_pk_bf16_f32 v161, v50, v51\n\t" \
  "s_waitcnt lgkmcnt(4)\n\t" \
  "v_add_f32_e32 v52, v52, v132\n\t" \
  "v_add_f32_e32 v53, v53, v133\n\t" \
  "v_add_f32_e32 v54, v54, v134\n\t" \
  "v_add_f32_e32 v55, v55, v135\n\t" \
  "v_cvt_pk_bf16_f32 v162, v52, v53\n\t" \
  "v_cvt_pk_bf16_f32 v163, v54, v55\n\t" \
  "s_waitcnt lgkmcnt(3)\n\t" \
  "v_add_f32_e32 v56, v56, v136\n\t" \
  "v_add_f32_e32 v57, v57, v137\n\t" \
  "v_add_f32_e32 v58, v58, v138\n\t" \
  "v_add_f32_e32 v59, v59, v139\n\t" \
  "v_cvt_pk_bf16_f32 v164, v56, v57\n\t" \
  "v_cvt_pk_bf16_f32 v165, v58, v59\n\t" \
  "s_waitcnt lgkmcnt(2)\n\t" \
  "v_add_f32_e32 v60, v60, v140\n\t" \
  "v_add_f32_e32 v61, v61, v141\n\t" \
  "v_add_f32_e32 v62, v62, v142\n\t" \
  "v_add_f32_e32 v63, v63, v143\n\t" \
  "v_cvt_pk_bf16_f32 v166, v60, v61\n\t" \
  "v_cvt_pk_bf16_f32 v167, v62, v63\n\t" \
  "s_waitcnt lgkmcnt(1)\n\t" \
  "v_add_f32_e32 v64, v64, v144\n\t" \
  "v_add_f32_e32 v65, v65, v145\n\t" \
  "v_add_f32_e32 v66, v66, v146\n\t" \
  "v_add_f32_e32 v67, v67, v147\n\t" \
  "v_cvt_pk_bf16_f32 v168, v64, v65\n\t" \
  "v_cvt_pk_bf16_f32 v169, v66, v67\n\t" \
  "s_waitcnt lgkmcnt(0)\n\t" \
  "v_add_f32_e32 v68, v68, v148\n\t" \
  "v_add_f32_e32 v69, v69, v149\n\t" \
  "v_add_f32_e32 v70, v70, v150\n\t" \
  "v_add_f32_e32 v71, v71, v151\n\t" \
  "v_cvt_pk_bf16_f32 v170, v68, v69\n\t" \
  "v_cvt_pk_bf16_f32 v171, v70, v71\n\t" \
  "s_branch L_J%=\n\t" \
  "L_K1%=:\n\t" \
  "s_nop 7\n\t" \
  "ds_write_b128 %[xo], v[32:35] offset:0\n\t" \
  "ds_write_b128 %[xo], v[36:39] offset:1024\n\t" \
  "ds_write_b128 %[xo], v[40:43] offset:2048\n\t" \
  "ds_write_b128 %[xo], v[44:47] offset:3072\n\t" \
  "ds_write_b128 %[xo], v[48:51] offset:4096\n\t" \
  "ds_write_b128 %[xo], v[52:55] offset:5120\n\t" \
  "ds_write_b128 %[xo], v[56:59] offset:6144\n\t" \
  "ds_write_b128 %[xo], v[60:63] offset:7168\n\t" \
  "ds_write_b128 %[xo], v[64:67] offset:8192\n\t" \
  "ds_write_b128 %[xo], v[68:71] offset:9216\n\t" \
  "s_waitcnt lgkmcnt(0)\n\t" \
  "s_barrier\n\t" \
  "ds_read_b128 v[112:115], %[xi] offset:0\n\t" \
  "ds_read_b128 v[116:119], %[xi] offset:1024\n\t" \
  "ds_read_b128 v[120:123], %[xi] offset:2048\n\t" \
  "ds_read_b128 v[124:127], %[xi] offset:3072\n\t" \
  "ds_read_b128 v[128:131], %[xi] offset:4096\n\t" \
  "ds_read_b128 v[132:135], %[xi] offset:5120\n\t" \
  "ds_read_b128 v[136:139], %[xi] offset:6144\n\t" \
  "ds_read_b128 v[140:143], %[xi] offset:7168\n\t" \
  "ds_read_b128 v[144:147], %[xi] offset:8192\n\t" \
  "ds_read_b128 v[148:151], %[xi] offset:9216\n\t" \
  "s_waitcnt lgkmcnt(9)\n\t" \
  "v_add_f32_e32 v72, v72, v112\n\t" \
  "v_add_f32_e32 v73, v73, v113\n\t" \
  "v_add_f32_e32 v74, v74, v114\n\t" \
  "v_add_f32_e32 v75, v75, v115\n\t" \
  "v_cvt_pk_bf16_f32 v152, v72, v73\n\t" \
  "v_cvt_pk_bf16_f32 v153, v74, v75\n\t" \
  "s_waitcnt lgkmcnt(8)\n\t" \
  "v_add_f32_e32 v76, v76, v116\n\t" \
  "v_add_f32_e32 v77, v77, v117\n\t" \
  "v_add_f32_e32 v78, v78, v118\n\t" \
  "v_add_f32_e32 v79, v79, v119\n\t" \
  "v_cvt_pk_bf16_f32 v154, v76, v77\n\t" \
  "v_cvt_pk_bf16_f32 v155, v78, v79\n\t" \
  "s_waitcnt lgkmcnt(7)\n\t" \
  "v_add_f32_e32 v80, v80, v120\n\t" \
  "v_add_f32_e32 v81, v81, v121\n\t" \
  "v_add_f32_e32 v82, v82, v122\n\t" \
  "v_add_f32_e32 v83, v83, v123\n\t" \
  "v_cvt_pk_bf16_f32 v156, v80, v81\n\t" \
  "v_cvt_pk_bf16_f32 v157, v82, v83\n\t" \
  "s_waitcnt lgkmcnt(6)\n\t" \
  "v_add_f32_e32 v84, v84, v124\n\t" \
  "v_add_f32_e32 v85, v85, v125\n\t" \
  "v_add_f32_e32 v86, v86, v126\n\t" \
  "v_add_f32_e32 v87, v87, v127\n\t" \
  "v_cvt_pk_bf16_f32 v158, v84, v85\n\t" \
  "v_cvt_pk_bf16_f32 v159, v86, v87\n\t" \
  "s_waitcnt lgkmcnt(5)\n\t" \
  "v_add_f32_e32 v88, v88, v128\n\t" \
  "v_add_f32_e32 v89, v89, v129\n\t" \
  "v_add_f32_e32 v90, v90, v130\n\t" \
  "v_add_f32_e32 v91, v91, v131\n\t" \
  "v_cvt_pk_bf16_f32 v160, v88, v89\n\t" \
  "v_cvt_pk_bf16_f32 v161, v90, v91\n\t" \
  "s_waitcnt lgkmcnt(4)\n\t" \
  "v_add_f32_e32 v92, v92, v132\n\t" \
  "v_add_f32_e32 v93, v93, v133\n\t" \
  "v_add_f32_e32 v94, v94, v134\n\t" \
  "v_add_f32_e32 v95, v95, v135\n\t" \
  "v_cvt_pk_bf16_f32 v162, v92, v93\n\t" \
  "v_cvt_pk_bf16_f32 v163, v94, v95\n\t" \
  "s_waitcnt lgkmcnt(3)\n\t" \
  "v_add_f32_e32 v96, v96, v136\n\t" \
  "v_add_f32_e32 v97, v97, v137\n\t" \
  "v_add_f32_e32 v98, v98, v138\n\t" \
  "v_add_f32_e32 v99, v99, v139\n\t" \
  "v_cvt_pk_bf16_f32 v164, v96, v97\n\t" \
  "v_cvt_pk_bf16_f32 v165, v98, v99\n\t" \
  "s_waitcnt lgkmcnt(2)\n\t" \
  "v_add_f32_e32 v100, v100, v140\n\t" \
  "v_add_f32_e32 v101, v101, v141\n\t" \
  "v_add_f32_e32 v102, v102, v142\n\t" \
  "v_add_f32_e32 v103, v103, v143\n\t" \
  "v_cvt_pk_bf16_f32 v166, v100, v101\n\t" \
  "v_cvt_pk_bf16_f32 v167, v102, v103\n\t" \
  "s_waitcnt lgkmcnt(1)\n\t" \
  "v_add_f32_e32 v104, v104, v144\n\t" \
  "v_add_f32_e32 v105, v105, v145\n\t" \
  "v_add_f32_e32 v106, v106, v146\n\t" \
  "v_add_f32_e32 v107, v107, v147\n\t" \
  "v_cvt_pk_bf16_f32 v168, v104, v105\n\t" \
  "v_cvt_pk_bf16_f32 v169, v106, v107\n\t" \
  "s_waitcnt lgkmcnt(0)\n\t" \
  "v_add_f32_e32 v108, v108, v148\n\t" \
  "v_add_f32_e32 v109, v109, v149\n\t" \
  "v_add_f32_e32 v110, v110, v150\n\t" \
  "v_add_f32_e32 v111, v111, v151\n\t" \
  "v_cvt_pk_bf16_f32 v170, v108, v109\n\t" \
  "v_cvt_pk_bf16_f32 v171, v110, v111\n\t" \
  "L_J%=:\n\t" \
  "ds_write_b64 %[sw], v[152:153] offset:0\n\t" \
  "ds_write_b64 %[sw], v[154:155] offset:32\n\t" \
  "ds_write_b64 %[sw], v[156:157] offset:64\n\t" \
  "ds_write_b64 %[sw], v[158:159] offset:96\n\t" \
  "ds_write_b64 %[sw], v[160:161] offset:128\n\t" \
  "ds_write_b64 %[sw], v[162:163] offset:2816\n\t" \
  "ds_write_b64 %[sw], v[164:165] offset:2848\n\t" \
  "ds_write_b64 %[sw], v[166:167] offset:2880\n\t" \
  "ds_write_b64 %[sw], v[168:169] offset:2912\n\t" \
  "ds_write_b64 %[sw], v[170:171] offset:2944\n\t" \
  "ds_read_b128 v[172:175], %[sr0]\n\t" \
  "ds_read_b128 v[176:179], %[sr1]\n\t" \
  "ds_read_b128 v[180:183], %[sr2]\n\t" \
  "ds_read_b128 v[184:187], %[sr3]\n\t" \
  "ds_read_b128 v[188:191], %[sr4]\n\t" \
  "s_mov_b64 s[64:65], %[dst]\n\t" \
  "s_add_u32 s64, s64, 2048\n\t" \
  "s_addc_u32 s65, s65, 0\n\t" \
  "s_waitcnt lgkmcnt(4)\n\t" \
  "global_store_dwordx4 %[lin], v[172:175], s[64:65] offset:-2048 sc1\n\t" \
  "s_waitcnt lgkmcnt(3)\n\t" \
  "global_store_dwordx4 %[lin], v[176:179], s[64:65] offset:-1024 sc1\n\t" \
  "s_waitcnt lgkmcnt(2)\n\t" \
  "global_store_dwordx4 %[lin], v[180:183], s[64:65] offset:0 sc1\n\t" \
  "s_waitcnt lgkmcnt(1)\n\t" \
  "global_store_dwordx4 %[lin], v[184:187], s[64:65] offset:1024 sc1\n\t" \
  "s_waitcnt lgkmcnt(0)\n\t" \
  "global_store_dwordx4 %[lin], v[188:191], s[64:65] offset:2048 sc1\n\t" \
  "s_waitcnt vmcnt(5)\n\t" \
  "s_nop 0\n\t"
#define OP_CLOB "v32", "v33", "v34", "v35", "v36", "v37", "v38", "v39", "v40", "v41", "v42", "v43", "v44", "v45", "v46", "v47", "v48", "v49", "v50", "v51", "v52", "v53", "v54", "v55", "v56", "v57", "v58", "v59", "v60", "v61", "v62", "v63", "v64", "v65", "v66", "v67", "v68", "v69", "v70", "v71", "v72", "v73", "v74", "v75", "v76", "v77", "v78", "v79", "v80", "v81", "v82", "v83", "v84", "v85", "v86", "v87", "v88", "v89", "v90", "v91", "v92", "v93", "v94", "v95", "v96", "v97", "v98", "v99", "v100", "v101", "v102", "v103", "v104", "v105", "v106", "v107", "v108", "v109", "v110", "v111", "v112", "v113", "v114", "v115", "v116", "v117", "v118", "v119", "v120", "v121", "v122", "v123", "v124", "v125", "v126", "v127", "v128", "v129", "v130", "v131", "v132", "v133", "v134", "v135", "v136", "v137", "v138", "v139", "v140", "v141", "v142", "v143", "v144", "v145", "v146", "v147", "v148", "v149", "v150", "v151", "v152", "v153", "v154", "v155", "v156", "v157", "v158", "v159", "v160", "v161", "v162", "v163", "v164", "v165", "v166", "v167", "v168", "v169", "v170", "v171", "v172", "v173", "v174", "v175", "v176", "v177", "v178", "v179", "v180", "v181", "v182", "v183", "v184", "v185", "v186", "v187", "v188", "v189", "v190", "v191", "s64", "s65", "s66", "s67", "scc", "memory"

constexpr int NSPLIT = 4;
constexpr int K3_STAGE = 73728, K3_LDS = 2 * K3_STAGE;
constexpr unsigned SPIN_CAP = 1u << 15;
DI void st16_sc1(const char* sbase, unsigned voff, u32x4 v) { asm volatile("global_store_dwordx4 %0, %1, %2 sc1\n\ts_nop 1" :: "v"(voff), "v"(v), "s"(sbase) : "memory"); }
#define KSLEEP 8
#define PSLEEP 6
#define AFIRST 24
#define PFIRST 0
#define KFIRST 40
#define ASLEEP 2
__global__ __launch_bounds__(512, 2) void k3_tail(const u16* __restrict__ wo, char* __restrict__ ws, const u16* __restrict__ x, const u16* __restrict__ wr,
                                                  u16* __restrict__ out) {
  extern __shared__ __attribute__((aligned(16))) char smem[];
  lptr lds = (lptr)smem;
  const unsigned lds0 = (unsigned)(uintptr_t)smem;
  const int tid = threadIdx.x, wid = __builtin_amdgcn_readfirstlane(tid >> 6), lane = tid & 63, fr = lane & 15, fq = lane >> 4;
  const int wm = (wid >> 1) & 1, wn = wid & 1, kh = wid >> 2;
  const int b = blockIdx.x, ksp = (b & 7) >> 1, rb = (b & 1) * 8 + ((b >> 3) & 7), ct = b >> 6;
  const unsigned lin = (unsigned)lane * 16u;
  const int row = rb * 128 + (ksp * 4 + ct) * 8 + wid;
  u32x16 lop0, lop1, lop2;
  const bool gu = b < 112;
  const int cg = gu ? (b & 7) * 14 + (b >> 3) : ((b - 112) & 7) * 5 + ((b - 112) >> 3);
  const bool role = gu ? cg < 108 : b < 152;
  {
    const char* gA = ws + WS_CTX + (size_t)(rb * 4 + (wid >> 1)) * 262144 + (size_t)ksp * 65536 + (wid & 1) * 2048;
    const char* gB = (const char*)wo + (size_t)ksp * 1024 * (M * 2) + ct * 320;
    unsigned vB[3];
#pragma unroll
    for (int i = 0; i < 3; ++i) { const int c = (i < 2 ? wid + 8 * i : 16 + (wid & 3)) * 64 + lane, qd = c / 80, r = c - qd * 80;
      vB[i] = (unsigned)((qd * 4 + ((r >> 2) & 3)) * (M * 2) + (r >> 4) * 64 + (((r & 3) ^ ((qd & 1) << 1)) * 16)); }
    const unsigned dA = lds0 + (unsigned)wid * 2048u, dB = lds0 + 16384u + (unsigned)wid * 1024u;
    const unsigned dB2 = lds0 + 32768u + (unsigned)(wid & 3) * 1024u;
    asm volatile(OP_PRE :: [lin] "v"(lin), [vb0] "v"(vB[0]), [vb1] "v"(vB[1]), [vb2] "v"(vB[2]), [ga] "s"(gA), [gb] "s"(gB), [da] "s"(dA), [db] "s"(dB), [db2] "s"(dB2), [kh] "s"(kh)
                 : "s64", "s65", "s66", "s67", "scc", "memory");
    SBAR();
    const unsigned ar = lds0 + (unsigned)(wm * 8192 + kh * 2048 + (fq >> 1) * 1024 + (fq & 1) * 512 + fr * 16);
    const unsigned brd0 = lds0 + 16384u + (unsigned)((kh * 8 + 4 * (fq >> 1) + (fq & 1)) * 1280 + (fr >> 2) * 64 + (lane & 3) * 8);
    const unsigned sw_ = (unsigned)(fq & 1) << 5, bE = brd0 + sw_, bO = brd0 + (32u ^ sw_);
    const unsigned bp0 = wn ? bO + 512u : bE, bp1 = wn ? bE + 768u : bO;
    const unsigned stb = lds0 + 58368u + (unsigned)(wid ^ 4) * 10240u;
    unsigned srd[5];
#pragma unroll
    for (int i = 0; i < 5; ++i) { const int c = i * 64 + lane, r = c / 10; srd[i] = stb + (unsigned)(r * 176 + (c - r * 10) * 16); }
    asm volatile(OP_ASM
                 : "=&{v[192:207]}"(lop0), "=&{v[208:223]}"(lop1), "=&{v[224:239]}"(lop2)
                 : [l16] "v"((unsigned)(lane < 40 ? lane : 0) * 16u), [xr] "s"(x + (size_t)row * M), [wr] "s"(wr), [mb] "s"(ws + WS_MODB + 2 * M * 2 + 2240),
                   [ar] "v"(ar), [bp0] "v"(bp0), [bp1] "v"(bp1), [lin] "v"(lin), [vb0] "v"(vB[0]), [vb1] "v"(vB[1]), [vb2] "v"(vB[2]),
                   [db2] "s"(dB2),
                   [xo] "v"(lds0 + 58368u + (unsigned)wid * 10240u + lin), [xi] "v"(stb + lin), [sw] "v"(stb + (unsigned)(fr * 176 + fq * 8)),
                   [sr0] "v"(srd[0]), [sr1] "v"(srd[1]), [sr2] "v"(srd[2]), [sr3] "v"(srd[3]), [sr4] "v"(srd[4]),
                   [ga] "s"(gA), [gb] "s"(gB), [da] "s"(dA), [db] "s"(dB), [kh] "s"(kh),
                   [dst] "s"(ws + WS_PART + (size_t)(((rb * 4 + wm * 2 + kh) * 4 + ksp) * 8 + ct * 2 + wn) * 5120)
                 : OP_CLOB);
  }
  {
    const int l40 = lane < 40 ? lane : 0; const bool act = lane < 40;
#define SUB4(v, i) (u32x4){v[4 * (i)], v[4 * (i) + 1], v[4 * (i) + 2], v[4 * (i) + 3]}
    const u32x4 xa = SUB4(lop0, 0), xb = SUB4(lop0, 1), wa = SUB4(lop0, 2), wb = SUB4(lop0, 3);
    const u32x4 mv[4][2] = {{SUB4(lop1, 0), SUB4(lop1, 1)}, {SUB4(lop1, 2), SUB4(lop1, 3)}, {SUB4(lop2, 0), SUB4(lop2, 1)}, {SUB4(lop2, 2), SUB4(lop2, 3)}};
    asm volatile("s_barrier" ::: "memory");
    unsigned* pcnt = (unsigned*)(ws + WS_CNT + (128 + rb) * 256);
    if (tid == 0) __hip_atomic_fetch_add(pcnt, 1u, __ATOMIC_RELAXED, __HIP_MEMORY_SCOPE_AGENT);
    u32x4 pv[NSPLIT][2]; f32x16 xf, wf_, gaf, sbf, cbf, gbf;
    { const int sg = l40 / 10; const unsigned po = (unsigned)((row >> 5) * 163840 + sg * 5120 + (row & 31) * 160 + (l40 - sg * 10) * 16); const char* pb = ws + WS_PART;
#define UNP16(dst, lo, hi) _Pragma("unroll") for (int e_ = 0; e_ < 4; ++e_) { dst[2 * e_] = bflo(lo[e_]); dst[2 * e_ + 1] = bfhi(lo[e_]); dst[8 + 2 * e_] = bflo(hi[e_]); dst[9 + 2 * e_] = bfhi(hi[e_]); }
      UNP16(xf, xa, xb); UNP16(wf_, wa, wb); UNP16(gaf, mv[0][0], mv[0][1]); UNP16(sbf, mv[1][0], mv[1][1]); UNP16(cbf, mv[2][0], mv[2][1]); UNP16(gbf, mv[3][0], mv[3][1]);
      asm volatile("" : "+v"(xf), "+v"(wf_), "+v"(gaf), "+v"(sbf), "+v"(cbf), "+v"(gbf));
      if (wid == 0) {
        for (unsigned spin = 0; spin < SPIN_CAP; ++spin) {
          if ((unsigned)__builtin_amdgcn_readfirstlane(__hip_atomic_load(pcnt, __ATOMIC_RELAXED, __HIP_MEMORY_SCOPE_AGENT)) >= 16u) break;
          __builtin_amdgcn_s_sleep(2);
        }
        __builtin_amdgcn_s_sleep(PFIRST);
      }
      asm volatile("s_barrier" ::: "memory");
      for (unsigned spin = 0; spin < SPIN_CAP; ++spin) {
        asm volatile("global_load_dwordx4 %0, %8, %10 sc1\n\tglobal_load_dwordx4 %1, %9, %10 sc1\n\t"
                     "global_load_dwordx4 %2, %8, %11 sc1\n\tglobal_load_dwordx4 %3, %9, %11 sc1\n\t"
                     "global_load_dwordx4 %4, %8, %12 sc1\n\tglobal_load_dwordx4 %5, %9, %12 sc1\n\t"
                     "global_load_dwordx4 %6, %8, %13 sc1\n\tglobal_load_dwordx4 %7, %9, %13 sc1\n\ts_waitcnt vmcnt(0)"
                     : "=&v"(pv[0][0]), "=&v"(pv[0][1]), "=&v"(pv[1][0]), "=&v"(pv[1][1]), "=&v"(pv[2][0]), "=&v"(pv[2][1]), "=&v"(pv[3][0]), "=&v"(pv[3][1])
                     : "v"(po), "v"(po + 20480u), "s"(pb), "s"(pb + 40960), "s"(pb + 81920), "s"(pb + 122880) : "memory");
        unsigned mx = 0;
#pragma unroll
        for (int kq = 0; kq < NSPLIT; ++kq)
#pragma unroll
          for (int hh = 0; hh < 2; ++hh) mx = max(max(mx, max(pv[kq][hh][0], pv[kq][hh][1])), max(pv[kq][hh][2], pv[kq][hh][3]));
        if (!__any(mx == 0xffffffffu)) break;
        __builtin_amdgcn_s_sleep(PSLEEP);
      } }
    float v[16], h[16]; float s = 0.f;
#pragma unroll
    for (int p2 = 0; p2 < 8; ++p2) {
      const int hh = p2 >> 2, e = p2 & 3;
      const float a0 = (bflo(pv[0][hh][e]) + bflo(pv[1][hh][e])) + (bflo(pv[2][hh][e]) + bflo(pv[3][hh][e])), a1 = (bfhi(pv[0][hh][e]) + bfhi(pv[1][hh][e])) + (bfhi(pv[2][hh][e]) + bfhi(pv[3][hh][e]));
      const unsigned ra = cvtpk(a0, a1);
      const unsigned rg = cvtpk(gaf[2 * p2] * bflo(ra), gaf[2 * p2 + 1] * bfhi(ra));
      const unsigned rx = cvtpk(xf[2 * p2] + bflo(rg), xf[2 * p2 + 1] + bfhi(rg));
      v[2 * p2] = bflo(rx); v[2 * p2 + 1] = bfhi(rx); s += v[2 * p2] + v[2 * p2 + 1];
    }
    if (!act) s = 0.f;
    const float mean = wave_sum(s) * (1.f / M); float q = 0.f;
#pragma unroll
    for (int e = 0; e < 16; ++e) { const float dd = v[e] - mean; q += dd * dd; }
    if (!act) q = 0.f;
    const float var = wave_sum(q) * (1.f / M), mb = rbf(mean), rden = 1.f / rbf(sqrtf(rbf(rbf(var) + 1e-5f)));
    float rr = 0.f; u32x4 hw[2], ow[2];
#pragma unroll
    for (int p2 = 0; p2 < 8; ++p2) {
      const int hh = p2 >> 2, e = p2 & 3;
      const unsigned r1 = cvtpk(v[2 * p2] - mb, v[2 * p2 + 1] - mb), r2 = cvtpk(bflo(r1) * rden, bfhi(r1) * rden);
      const unsigned r3 = cvtpk(bflo(r2) * cbf[2 * p2], bfhi(r2) * cbf[2 * p2 + 1]), r4 = cvtpk(bflo(r3) + sbf[2 * p2], bfhi(r3) + sbf[2 * p2 + 1]);
      h[0] = bflo(r4); h[1] = bfhi(r4); hw[hh][e] = r4; rr += h[0] * wf_[2 * p2] + h[1] * wf_[2 * p2 + 1];
      const unsigned r5 = cvtpk(gbf[2 * p2] * h[0], gbf[2 * p2 + 1] * h[1]);
      ow[hh][e] = cvtpk(v[2 * p2] + bflo(r5), v[2 * p2 + 1] + bfhi(r5));
    }
    asm volatile("s_mov_b32 exec_hi, 0xff\n\t"
                 "global_store_dwordx4 %0, %1, %5 sc1\n\tglobal_store_dwordx4 %0, %2, %5 offset:640 sc1\n\t"
                 "global_store_dwordx4 %0, %3, %6 sc1\n\tglobal_store_dwordx4 %0, %4, %6 offset:640 sc1\n\t"
                 "s_mov_b32 exec_hi, -1\n\ts_nop 0"
                 :: "v"((unsigned)(row * (M * 2) + lane * 16)), "v"(hw[0]), "v"(hw[1]), "v"(ow[0]), "v"(ow[1]), "s"(ws + WS_H2), "s"(out) : "memory");
    if (!act) rr = 0.f;
    rr = wave_sum(rr);
    const unsigned ru = __float_as_uint(rr), ord = (ru & 0x80000000u) ? ~ru : (ru | 0x80000000u);
    unsigned keyv = max((ord & 0xfffff800u) | (2047u - (unsigned)row), 1u);
    asm volatile("s_waitcnt vmcnt(0)" : "+v"(keyv) :: "memory");
    if (lane == 0) __hip_atomic_store((unsigned*)(ws + WS_KEY) + row, keyv, __ATOMIC_RELAXED, __HIP_MEMORY_SCOPE_AGENT);
  }
  if (!role) { return; }
  LAS unsigned* cand = (LAS unsigned*)(lds + 141312);
  unsigned selkey;
  {
    if (tid < 17) cand[tid < 16 ? 64 + tid : 32] = 0u;
    asm volatile("s_barrier" ::: "memory");
    {
      const int np = gu ? 40 : 54;
      const char* src = ws + WS_WT + (gu ? (size_t)cg * 40960 : (size_t)108 * 40960 + (size_t)cg * 55296);
      for (int pc = wid; pc < np; pc += 8) dma1<0, 0>(src + pc * 1024, lin, lds0 + (unsigned)pc * 1024u);
    }
    u32x4 kq;
    __builtin_amdgcn_s_sleep(KFIRST);
    for (unsigned spin = 0; spin < SPIN_CAP; ++spin) {
      asm volatile("global_load_dwordx4 %0, %1, %2 sc1\n\ts_waitcnt vmcnt(0)" : "=&v"(kq) : "v"((unsigned)tid * 16u), "s"(ws + WS_KEY) : "memory");
      if (!__any(min(min(kq[0], kq[1]), min(kq[2], kq[3])) == 0u)) break;
      __builtin_amdgcn_s_sleep(KSLEEP);
    }
    unsigned gm = max(max(kq[0], kq[1]), max(kq[2], kq[3]));
    gm = max(gm, (unsigned)__builtin_amdgcn_update_dpp(0, (int)gm, 0xB1, 0xf, 0xf, true)); gm = max(gm, (unsigned)__builtin_amdgcn_update_dpp(0, (int)gm, 0x4E, 0xf, 0xf, true));
    gm = max(gm, (unsigned)__builtin_amdgcn_update_dpp(0, (int)gm, 0x141, 0xf, 0xf, true)); gm = max(gm, (unsigned)__builtin_amdgcn_update_dpp(0, (int)gm, 0x140, 0xf, 0xf, true));
    if (fr == 0) cand[wid * 4 + fq] = gm;
    asm volatile("s_waitcnt lgkmcnt(0)\n\ts_barrier" ::: "memory");
    const unsigned km = max(max(kq[0], kq[1]), max(kq[2], kq[3]));
    { const u32x2 g2 = *(LAS const u32x2*)(cand + 2 * fr); int rk = (g2[0] > gm) + (g2[1] > gm);
      rk += __builtin_amdgcn_update_dpp(0, rk, 0xB1, 0xf, 0xf, true); rk += __builtin_amdgcn_update_dpp(0, rk, 0x4E, 0xf, 0xf, true);
      rk += __builtin_amdgcn_update_dpp(0, rk, 0x124, 0xf, 0xf, true); rk += __builtin_amdgcn_update_dpp(0, rk, 0x128, 0xf, 0xf, true);
      if (rk == 7 && fr == 0) cand[33] = gm; }
    asm volatile("s_waitcnt lgkmcnt(0)\n\ts_barrier" ::: "memory");
    const unsigned tau = cand[33];
    if (km >= tau) {
#pragma unroll
      for (int i = 0; i < 4; ++i) if (kq[i] >= tau) cand[64 + __hip_atomic_fetch_add(cand + 32, 1u, __ATOMIC_RELAXED, __HIP_MEMORY_SCOPE_WORKGROUP)] = kq[i];
    }
    asm volatile("s_waitcnt lgkmcnt(0)\n\ts_barrier" ::: "memory");
    const int n = __builtin_amdgcn_readfirstlane((int)cand[32]);
    LAS unsigned* sel = cand + 1024 + wid * 8;
    if (n <= 16) {
      const unsigned ck = cand[64 + fr]; const u32x4 o4 = *(LAS const u32x4*)(cand + 64 + 4 * fq);
      unsigned rank = (o4[0] > ck) + (o4[1] > ck) + (o4[2] > ck) + (o4[3] > ck);
      { auto r = __builtin_amdgcn_permlane16_swap(rank, rank, false, false); rank = r[0] + r[1]; }
      { auto r = __builtin_amdgcn_permlane32_swap(rank, rank, false, false); rank = r[0] + r[1]; }
      if (rank < 8u && fq == 0) sel[rank] = ck;
    } else {
      for (int c0 = 0; c0 < n; c0 += 64) {
        const unsigned ck = c0 + lane < n ? cand[64 + c0 + lane] : 0u; int rank = 0;
        for (int j = 0; j < n; ++j) rank += cand[64 + j] > ck ? 1 : 0;
        if (ck != 0u && rank < 8) sel[rank] = ck;
      }
    }
    asm volatile("s_waitcnt lgkmcnt(0)" ::: "memory");
    selkey = sel[lane & 7];
  }
  const int srow = 2047 - (int)(selkey & 2047u);
  if (gu) {
    u32x4 xa[5];
    asm volatile("global_load_dwordx4 %0, %5, %6 sc1\n\tglobal_load_dwordx4 %1, %5, %6 offset:64 sc1\n\tglobal_load_dwordx4 %2, %5, %6 offset:128 sc1\n\t"
                 "global_load_dwordx4 %3, %5, %6 offset:192 sc1\n\tglobal_load_dwordx4 %4, %5, %6 offset:256 sc1"
                 : "=&v"(xa[0]), "=&v"(xa[1]), "=&v"(xa[2]), "=&v"(xa[3]), "=&v"(xa[4])
                 : "v"((unsigned)(srow * (M * 2) + (160 * (wid & 3) + 8 * fq) * 2)), "s"(ws + WS_H2) : "memory");
    lcptr wb = (lcptr)lds + wid * 5120 + (8 * fq + (fr >> 2)) * 32 + (lane & 3) * 8;
    bf16x8 wf[5];
#pragma unroll
    for (int s = 0; s < 5; ++s) wf[s] = cat8(ldtr(wb + s * 1024), ldtr(wb + s * 1024 + 128));
    asm volatile("s_waitcnt vmcnt(0)" : "+v"(xa[0]), "+v"(xa[1]), "+v"(xa[2]), "+v"(xa[3]), "+v"(xa[4]) :: "memory");
    f32x4 ag = {0.f, 0.f, 0.f, 0.f};
#pragma unroll
    for (int s = 0; s < 5; ++s) ag = MFMA16(__builtin_bit_cast(bf16x8, xa[s]), wf[s], ag);
    LAS f32x4* red = (LAS f32x4*)(lds + 58368);
    red[wid * 64 + lane] = ag;
    asm volatile("s_waitcnt lgkmcnt(0)\n\ts_barrier" ::: "memory");
    if (tid < 128) {
      const int r = tid >> 4, c = tid & 15; LAS const float* rf = (LAS const float*)(lds + 58368) + ((r >> 2) * 16 + c) * 4 + (r & 3);
      float g = (rf[0] + rf[256]) + (rf[512] + rf[768]), u = (rf[1024] + rf[1280]) + (rf[1536] + rf[1792]);
      g = rbf(g); u = rbf(u); g = rbf(g * __builtin_amdgcn_rcpf(1.f + __expf(-g)));
      const float o = g * u, on = dppf<0xB1>(o);
      unsigned wv = cvtpk(o, on); if (wv == 0xffffffffu) wv = 0x7fc07fc0u;
      const u32x4 w4 = {wv, (unsigned)__builtin_amdgcn_update_dpp(0, (int)wv, 0x102, 0xf, 0xf, true), (unsigned)__builtin_amdgcn_update_dpp(0, (int)wv, 0x104, 0xf, 0xf, true),
                        (unsigned)__builtin_amdgcn_update_dpp(0, (int)wv, 0x106, 0xf, 0xf, true)};
      if ((c & 7) == 0) st16_sc1(ws + WS_ACT, (unsigned)(cg * 256 + r * 32 + c * 2), w4);
    }
  } else {
    const int r = (tid >> 4) & 7, c = cg * 16 + (tid & 15);
    const unsigned kr = (cand + 1024 + wid * 8)[r];
    const int orow = 2047 - (int)(kr & 2047u);
    unsigned hv, p0, p1, p2, p3;
    { const int sg = c / 80; const unsigned po = (unsigned)((orow >> 5) * 163840 + sg * 5120 + (orow & 31) * 160 + (c - sg * 80) * 2); const char* pb = ws + WS_PART;
      asm volatile("global_load_ushort %0, %5, %7 sc1\n\tglobal_load_ushort %1, %6, %8 sc1\n\tglobal_load_ushort %2, %6, %9 sc1\n\tglobal_load_ushort %3, %6, %10 sc1\n\tglobal_load_ushort %4, %6, %11 sc1"
                   : "=&v"(hv), "=&v"(p0), "=&v"(p1), "=&v"(p2), "=&v"(p3)
                   : "v"((unsigned)((orow * M + c) * 2)), "v"(po), "s"(ws + WS_H2), "s"(pb), "s"(pb + 40960), "s"(pb + 81920), "s"(pb + 122880) : "memory"); }
    const float gbv = bf2f(((const u16*)(ws + WS_MODB))[5 * M + c]), gav = bf2f(((const u16*)(ws + WS_MODB))[2 * M + c]), xv = bf2f(x[orow * M + c]);
    const int s0 = min(7 * wid, 47);
    lcptr wb = (lcptr)lds + s0 * 1024 + (8 * fq + (fr >> 2)) * 32 + (lane & 3) * 8;
    bf16x8 wf[7];
#pragma unroll
    for (int s = 0; s < 7; ++s) wf[s] = cat8(ldtr(wb + s * 1024), ldtr(wb + s * 1024 + 128));
    if (wid == 7) { wf[0] = bf16x8{}; wf[1] = bf16x8{}; }
    u32x4 xa[7];
    const unsigned ao = (unsigned)((2 * s0 + (fq >> 1)) * 256 + (lane & 7) * 32 + (fq & 1) * 16);
    __builtin_amdgcn_s_sleep(AFIRST);
    for (unsigned spin = 0; spin < SPIN_CAP; ++spin) {
      asm volatile("global_load_dwordx4 %0, %7, %8 sc1\n\tglobal_load_dwordx4 %1, %7, %8 offset:512 sc1\n\tglobal_load_dwordx4 %2, %7, %8 offset:1024 sc1\n\t"
                   "global_load_dwordx4 %3, %7, %8 offset:1536 sc1\n\tglobal_load_dwordx4 %4, %7, %8 offset:2048 sc1\n\tglobal_load_dwordx4 %5, %7, %8 offset:2560 sc1\n\t"
                   "global_load_dwordx4 %6, %7, %8 offset:3072 sc1\n\ts_waitcnt vmcnt(0)"
                   : "=&v"(xa[0]), "=&v"(xa[1]), "=&v"(xa[2]), "=&v"(xa[3]), "=&v"(xa[4]), "=&v"(xa[5]), "=&v"(xa[6]) : "v"(ao), "s"(ws + WS_ACT) : "memory");
      unsigned mx = 0;
#pragma unroll
      for (int s = 0; s < 7; ++s) mx = max(max(mx, max(xa[s][0], xa[s][1])), max(xa[s][2], xa[s][3]));
      if (!__any(mx == 0xffffffffu)) break;
      __builtin_amdgcn_s_sleep(ASLEEP);
    }
    asm volatile("" : "+v"(hv), "+v"(p0), "+v"(p1), "+v"(p2), "+v"(p3));
    f32x4 ay = {0.f, 0.f, 0.f, 0.f};
#pragma unroll
    for (int s = 0; s < 7; ++s) ay = MFMA16(__builtin_bit_cast(bf16x8, xa[s]), wf[s], ay);
    LAS f32x4* red = (LAS f32x4*)(lds + 58368);
    red[wid * 64 + lane] = ay;
    const float x1v = rbf(xv + rbf(gav * rbf((bflo(p0) + bflo(p1)) + (bflo(p2) + bflo(p3)))));
    const unsigned od = (kr & 0xfffff800u) | 0x400u, rbits = (od & 0x80000000u) ? (od & 0x7fffffffu) : ~od;
    const float rs = rbf(1.f / (1.f + __expf(-rbf(__uint_as_float(rbits)))));
    asm volatile("s_waitcnt lgkmcnt(0)\n\ts_barrier" ::: "memory");
    if (tid < 128) {
      LAS const float* rf = (LAS const float*)(lds + 58368) + ((r >> 2) * 16 + (tid & 15)) * 4 + (r & 3);
      float y = ((rf[0] + rf[256]) + (rf[512] + rf[768])) + ((rf[1024] + rf[1280]) + (rf[1536] + rf[1792]));
      y = rbf(rbf(y) * rs);
      out[orow * M + c] = f2bf(x1v + rbf(gbv * rbf(bflo(hv) + y)));
    }
  }
}

extern "C" void kernel_launch(void* const* d_in, const int* in_sizes, int n_in, void* d_out, int out_size, void* d_ws, size_t ws_size, hipStream_t st) {
  const u16* x = (const u16*)d_in[0]; const u16* ckv = (const u16*)d_in[1]; const u16* wq = (const u16*)d_in[2];
  const u16* wuk = (const u16*)d_in[3]; const u16* wuv = (const u16*)d_in[4]; const u16* wo = (const u16*)d_in[5];
  const u16* wr = (const u16*)d_in[7]; const u16* wg = (const u16*)d_in[8]; const u16* wu = (const u16*)d_in[9]; const u16* wd = (const u16*)d_in[10];
  const u16* cvec = (const u16*)d_in[11]; const u16* wmod = (const u16*)d_in[12];
  char* ws = (char*)d_ws;
  (void)hipFuncSetAttribute((const void*)k2_attn, hipFuncAttributeMaxDynamicSharedMemorySize, K2_LDS);
  (void)hipFuncSetAttribute((const void*)k3_tail, hipFuncAttributeMaxDynamicSharedMemorySize, K3_LDS);
  k0_pre<<<256, 512, 0, st>>>(x, cvec, wmod, ws, wr);
  k2_attn<<<256, 512, K2_LDS, st>>>(ckv, wuk, wuv, wq, ws, wo, wg, wu, wd);
  k3_tail<<<256, 512, K3_LDS, st>>>(wo, ws, x, wr, (u16*)d_out);
}
```
